```python
import torch

def _softmax(x, axis=-1):
    return torch.softmax(x, dim=axis)
def _gelu(x):
    return torch.nn.functional.gelu(x, approximate="tanh")
def _silu(x):
    return torch.nn.functional.silu(x)
def _sigmoid(x):
    return torch.sigmoid(x)
def _layernorm(x, axis=-1, eps=1e-5):
    m = x.mean(dim=axis, keepdim=True)
    v = x.var(dim=axis, keepdim=True, unbiased=False)
    return (x - m) / torch.sqrt(v + eps)
def _scatter_add(tgt, idx, src, axis=0):
    return tgt.clone().index_add_(axis, idx, src)
def _segment_sum(src, seg, n):
    out = torch.zeros((n,) + src.shape[1:], dtype=src.dtype)
    return out.index_add_(0, seg, src)
def _pad(x, pw):
    flat = tuple(v for pair in reversed(pw) for v in pair)
    return torch.nn.functional.pad(x, flat)

kernel_name = 'recurrent_scan__linear_attn_block__glu_gated_reglu__prompt'

def setup_inputs():

    _w = (lambda a: (lambda t: t.to(torch.bfloat16) if t.is_floating_point() else t)(torch.as_tensor(a)))
    def _A_setup():
        B, S, D = 1, 2048, 640
        return {
            "x": _w((lambda *s: torch.randn(*s, dtype=torch.bfloat16) * 0.05)(B, S, D)),
            "wqkv": _w((lambda *s: torch.randn(*s, dtype=torch.bfloat16) * 0.8)(D, 3 * D)),
            "wo": _w((lambda *s: torch.randn(*s, dtype=torch.bfloat16) * 0.8)(D, D)),
        }
    _A_in = _A_setup()

    def _B_setup():
        B, S, D, F = 1, 2048, 640, 1728
        return {
            "x": _w((lambda *s: torch.randn(*s, dtype=torch.bfloat16) * 0.05)(B, S, D)),
            "W_gate": _w((lambda *s: torch.randn(*s, dtype=torch.bfloat16) * 0.2)(D, F) * (2.0 / D) ** 0.5),
            "W_up": _w((lambda *s: torch.randn(*s, dtype=torch.bfloat16) * 0.2)(D, F) * (2.0 / D) ** 0.5),
            "W_down": _w((lambda *s: torch.randn(*s, dtype=torch.bfloat16) * 0.2)(F, D) * (1.0 / F) ** 0.5),
        }
    _B_in = _B_setup()

    return {
        "A_x": _A_in["x"],
        "A_wqkv": _A_in["wqkv"],
        "A_wo": _A_in["wo"],
        "B_x": _B_in["x"],
        "B_W_gate": _B_in["W_gate"],
        "B_W_up": _B_in["W_up"],
        "B_W_down": _B_in["W_down"],
    }

def reference(A_x, A_wqkv, A_wo, B_x, B_W_gate, B_W_up, B_W_down):
    def _A_ref(x, wqkv, wo):
        B, S, D = x.shape
        H = 32; Dh = D // H
        qkv = (lambda x, *d: x.permute(*d))((x @ wqkv).reshape(B, S, 3, H, Dh), 2, 0, 3, 1, 4)
        q, k, v = qkv[0], qkv[1], qkv[2]
        phi = lambda t: torch.where(t > 0, t + 1, torch.exp(t)) + 1e-6
        pq, pk = phi(q), phi(k)
        kv = torch.einsum("bhsd,bhse->bhde", pk, v)
        z = torch.einsum("bhsd->bhd", pk)
        num = torch.einsum("bhqd,bhde->bhqe", pq, kv)
        den = torch.einsum("bhqd,bhd->bhq", pq, z)[..., None] + 1e-6
        o = num / den
        return (lambda x, *d: x.permute(*d))(o, 0, 2, 1, 3).reshape(B, S, D) @ wo

    def _B_ref(x, W_gate, W_up, W_down):
        gate = torch.einsum("bsd,df->bsf", x, W_gate)
        up = torch.einsum("bsd,df->bsf", x, W_up)
        gate = gate * (lambda x, dt: x.to(dt))(gate > 0, gate.dtype)
        h = gate * up
        return torch.einsum("bsf,fd->bsd", h, W_down)

    x = A_x
    xn = _layernorm(x, axis=-1)
    s = _A_ref(xn, A_wqkv, A_wo)
    g = _silu(_B_ref(xn, B_W_gate, B_W_up, B_W_down))
    return x + s * g
```

```cpp
#include <hip/hip_runtime.h>
#include <hip/hip_bf16.h>
typedef __hip_bfloat16 bf16;
__device__ __forceinline__ float b2f(bf16 v){return __bfloat162float(v);}
__device__ __forceinline__ float rb(float v){return __bfloat162float(__float2bfloat16(v));}
constexpr int S=2048,D=640,F=1728,H=32,DH=20;

__global__ void n_ln(const bf16* x, bf16* xn){
  int r=blockIdx.x; int t=threadIdx.x;
  float s=0; for(int i=t;i<D;i+=64) s+=b2f(x[r*D+i]);
  for(int o=32;o;o>>=1) s+=__shfl_xor(s,o);
  float m=s/D; float q=0;
  for(int i=t;i<D;i+=64){float d=b2f(x[r*D+i])-m; q+=d*d;}
  for(int o=32;o;o>>=1) q+=__shfl_xor(q,o);
  float mb=rb(m), vb=rb(q/D); float sd=rb(sqrtf(rb(vb+1e-5f)));
  for(int i=t;i<D;i+=64) xn[r*D+i]=__float2bfloat16(rb(b2f(x[r*D+i])-mb)/sd);
}
__global__ void n_gemm(const bf16* A,const bf16* B,bf16* C,int M,int N,int K){
  int n=blockIdx.x*blockDim.x+threadIdx.x; int m=blockIdx.y; if(n>=N) return;
  float a=0; for(int k=0;k<K;k++) a+=b2f(A[m*K+k])*b2f(B[(size_t)k*N+n]);
  C[(size_t)m*N+n]=__float2bfloat16(a);
}
__device__ float phi(float t){ float r = t>0? rb(t+1.f): rb(expf(t)); return rb(r+1e-6f);}
__global__ void n_kv(const bf16* qkv, bf16* kv, bf16* z){
  int h=blockIdx.x; int t=threadIdx.x;
  if(t<400){int d=t/20,e=t%20; float a=0; for(int s=0;s<S;s++) a+=phi(b2f(qkv[s*1920+640+h*20+d]))*b2f(qkv[s*1920+1280+h*20+e]); kv[h*400+t]=__float2bfloat16(a);}
  else if(t<420){int d=t-400; float a=0; for(int s=0;s<S;s++) a+=phi(b2f(qkv[s*1920+640+h*20+d])); z[h*20+d]=__float2bfloat16(a);}
}
__global__ void n_o(const bf16* qkv,const bf16* kv,const bf16* z,bf16* o){
  int s=blockIdx.x; int c=threadIdx.x; int h=c/20,e=c%20; float num=0,den=0;
  for(int d=0;d<20;d++){float p=phi(b2f(qkv[s*1920+h*20+d])); num+=p*b2f(kv[h*400+d*20+e]); den+=p*b2f(z[h*20+d]);}
  den=rb(rb(den)+1e-6f); o[s*D+c]=__float2bfloat16(rb(num)/den);
}
__global__ void n_h(const bf16* g,const bf16* u,bf16* h,int n){int i=blockIdx.x*256+threadIdx.x; if(i<n){float gg=b2f(g[i]); gg=gg>0?gg:0.f; h[i]=__float2bfloat16(gg*b2f(u[i]));}}
__global__ void n_out(const bf16* x,const bf16* s,const bf16* d,bf16* out,int n){int i=blockIdx.x*256+threadIdx.x; if(i<n){float dd=b2f(d[i]); float g=rb(dd/(1.f+expf(-dd))); out[i]=__float2bfloat16(b2f(x[i])+rb(b2f(s[i])*g));}}

extern "C" void kernel_launch(void* const* d_in, const int* in_sizes, int n_in, void* d_out, int out_size, void* d_ws, size_t ws_size, hipStream_t stream){
  const bf16* x=(const bf16*)d_in[0]; const bf16* wqkv=(const bf16*)d_in[1]; const bf16* wo=(const bf16*)d_in[2];
  const bf16* wg=(const bf16*)d_in[4]; const bf16* wu=(const bf16*)d_in[5]; const bf16* wd=(const bf16*)d_in[6];
  bf16* w=(bf16*)d_ws; bf16* xn=w; bf16* qkv=xn+S*D; bf16* kv=qkv+S*1920; bf16* z=kv+H*400; bf16* o=z+1024; bf16* sa=o+S*D; bf16* g=sa+S*D; bf16* u=g+S*F; bf16* h=u+S*F; bf16* dn=h+S*F;
  n_ln<<<S,64,0,stream>>>(x,xn);
  n_gemm<<<dim3(1920/128,S),128,0,stream>>>(xn,wqkv,qkv,S,1920,D);
  n_kv<<<H,448,0,stream>>>(qkv,kv,z);
  n_o<<<S,640,0,stream>>>(qkv,kv,z,o);
  n_gemm<<<dim3(5,S),128,0,stream>>>(o,wo,sa,S,D,D);
  n_gemm<<<dim3(14,S),128,0,stream>>>(xn,wg,g,S,F,D);
  n_gemm<<<dim3(14,S),128,0,stream>>>(xn,wu,u,S,F,D);
  n_h<<<(S*F+255)/256,256,0,stream>>>(g,u,h,S*F);
  n_gemm<<<dim3(5,S),128,0,stream>>>(h,wd,dn,S,D,F);
  n_out<<<(S*D+255)/256,256,0,stream>>>(x,sa,dn,(bf16*)d_out,S*D);
}
```

```cpp
#include <hip/hip_runtime.h>
#include <stdint.h>

typedef __attribute__((ext_vector_type(8))) __bf16 bf16x8;
typedef __attribute__((ext_vector_type(4))) __bf16 bf16x4;
typedef __attribute__((ext_vector_type(2))) __bf16 bf16x2;
typedef __attribute__((ext_vector_type(4))) float f32x4;
typedef __attribute__((ext_vector_type(2))) float f32x2;
typedef __attribute__((ext_vector_type(4))) unsigned u32x4;
typedef __attribute__((ext_vector_type(2))) unsigned u32x2;
typedef __amdgpu_buffer_rsrc_t rsrc_t;
#define LAS __attribute__((address_space(3)))
#define GAS __attribute__((address_space(1)))
#define DI __device__ __forceinline__
#define RLX_AGENT __ATOMIC_RELAXED, __HIP_MEMORY_SCOPE_AGENT
typedef GAS unsigned gu32;
typedef GAS unsigned long long gu64;

constexpr int S = 2048, D = 640, F = 1728;
constexpr int NCOL = 5376;
constexpr int NSTEP = 74;
constexpr unsigned WS_EPOCH = 0;
constexpr unsigned WS_FLAGH = 256;
constexpr unsigned WS_FLAGO = WS_FLAGH + 8 * 128;
constexpr unsigned WS_FLAGKV = WS_FLAGO + 8 * 128;
constexpr unsigned WS_STAT = 4096;
constexpr unsigned WS_CSUM = WS_STAT + S * 8;
constexpr unsigned WS_KVP = WS_CSUM + 3 * NCOL * 8;
constexpr unsigned WS_HA = 1u << 20;
constexpr unsigned WS_DBG = 12u << 20;
constexpr unsigned WS_END = 13u << 20;
constexpr unsigned L_B = 65536;
constexpr unsigned L_SCR = 147456;
constexpr unsigned L_MISC = 159744;
constexpr unsigned L_GO = L_MISC + 3072;
constexpr unsigned SPIN_CAP = 1u << 18;
#define HSLEEP 4
#define HSPLIT 54
#define WLEAD 10
#define GPRIO 0
#define HPRIO 0
#define XBAR 28
#define PSLEEP 2
#define WSLEEP 0
#define ROTM 18
#define MAPV 0
#define O_BAR 5
#define PRIO_MODE 0
#define DMA_SPACE_A T
#define DMA_SPACE_B (4 * T / 3)

struct Params {
  const void* x; const void* wqkv; const void* wo; const void* wg; const void* wu; const void* wd;
  void* out; char* ws;
};

__shared__ __attribute__((aligned(16))) char smem[163840];

DI unsigned rfl(unsigned v) { return __builtin_amdgcn_readfirstlane(v); }
DI rsrc_t mk_rsrc(const void* p, unsigned bytes) { return __builtin_amdgcn_make_buffer_rsrc((void*)p, (short)0, (int)bytes, 0x00020000); }
DI void dma16(unsigned ldsdst, unsigned voff, rsrc_t rs, unsigned soff) {
  asm volatile("s_mov_b32 m0, %0\n\ts_nop 0\n\tbuffer_load_dwordx4 %1, %2, %3 offen lds" ::"s"(ldsdst), "v"(voff), "s"(rs), "s"(soff) : "memory");
}
template <int N> DI void wait_vm() { asm volatile("s_waitcnt vmcnt(%0)" ::"i"(N) : "memory"); }
DI void wait_lgkm0() { asm volatile("s_waitcnt lgkmcnt(0)" ::: "memory"); }
DI void bar() { asm volatile("" ::: "memory"); __builtin_amdgcn_s_barrier(); asm volatile("" ::: "memory"); }
DI void lbar() { wait_lgkm0(); bar(); }
template <class V> DI V lds_ld(unsigned a) { return *(LAS V*)((LAS char*)smem + a); }
template <class V> DI void lds_st(unsigned a, V v) { *(LAS V*)((LAS char*)smem + a) = v; }
DI bf16x4 lds_tr(unsigned a) { return __builtin_amdgcn_ds_read_tr16_b64_v4bf16((LAS bf16x4*)((LAS char*)smem + a)); }
DI bf16x8 cat(bf16x4 a, bf16x4 b) { return __builtin_shufflevector(a, b, 0, 1, 2, 3, 4, 5, 6, 7); }
DI f32x4 mfma(bf16x8 a, bf16x8 b, f32x4 c) { return __builtin_amdgcn_mfma_f32_16x16x32_bf16(a, b, c, 0, 0, 0); }
DI unsigned pk2(float a, float b) { f32x2 v = {a, b}; return __builtin_bit_cast(unsigned, __builtin_convertvector(v, bf16x2)); }
DI u32x2 pk4(f32x4 v) { u32x2 r = {pk2(v[0], v[1]), pk2(v[2], v[3])}; return r; }
DI bf16x8 frag(u32x2 lo, u32x2 hi) { u32x4 v = {lo[0], lo[1], hi[0], hi[1]}; return __builtin_bit_cast(bf16x8, v); }
DI float bflo(unsigned v) { return __builtin_bit_cast(float, v << 16); }
DI float bfhi(unsigned v) { return __builtin_bit_cast(float, v & 0xffff0000u); }
DI float rbf(float v) { return bflo(pk2(v, 0.f)); }
DI float relu2(float v) { float o; asm("v_add_f32 %0, %1, |%1|" : "=v"(o) : "v"(v)); return o; }
DI f32x4 phi4(f32x4 u, f32x4 rl, f32x4 rh) {
  const f32x4 l = u * rl;
  f32x4 e, a;
#pragma unroll
  for (int j = 0; j < 4; ++j) {
    e[j] = __builtin_amdgcn_fmed3f(__builtin_amdgcn_exp2f(l[j]), 0.f, 1.f);
    a[j] = relu2(u[j]);
  }
  return a * rh + e + 1e-6f;
}
DI f32x4 splat(float v) { return f32x4{v, v, v, v}; }
template <int CTRL> DI float dpp_add(float v) {
  return v + __builtin_bit_cast(float, __builtin_amdgcn_update_dpp(0, __builtin_bit_cast(int, v), CTRL, 0xf, 0xf, true));
}
DI float sum64(float v) {
  v = dpp_add<0xB1>(v);
  v = dpp_add<0x4E>(v);
  v = dpp_add<0x141>(v);
  v = dpp_add<0x140>(v);
  v = dpp_add<0x142>(v);
  return dpp_add<0x143>(v);
}
DI float dot2(unsigned a, unsigned b, float c) {
  return __builtin_amdgcn_fdot2_f32_bf16(__builtin_bit_cast(bf16x2, a), __builtin_bit_cast(bf16x2, b), c, false);
}
DI u32x4 ldtr2(unsigned a, unsigned d) { return __builtin_bit_cast(u32x4, cat(lds_tr(a), lds_tr(a + d))); }
DI void mfma_ip(f32x4& c, const u32x4& a, const u32x4& b) { asm volatile("v_mfma_f32_16x16x32_bf16 %0, %1, %2, %0" : "+v"(c) : "v"(a), "v"(b)); }
DI void mfma_z(f32x4& c, const u32x4& a, const u32x4& b) { asm volatile("v_mfma_f32_16x16x32_bf16 %0, %1, %2, 0" : "=&v"(c) : "v"(a), "v"(b)); }
template <int N> DI void acc_fence(f32x4* c) {
  asm volatile("s_nop 7" ::: "memory");
#pragma unroll
  for (int i = 0; i < N; ++i) asm volatile("" : "+v"(c[i]));
}
DI u32x4 cat2(u32x2 a, u32x2 b) { return u32x4{a[0], a[1], b[0], b[1]}; }
DI u32x2 ldtr1(unsigned a) { return __builtin_bit_cast(u32x2, lds_tr(a)); }
DI void set_m0(unsigned v) { asm volatile("s_mov_b32 m0, %0" ::"s"(v) : "memory"); }
DI void dma_go(unsigned voff, rsrc_t rs, unsigned soff) {
  asm volatile("buffer_load_dwordx4 %0, %1, %2 offen lds" ::"v"(voff), "s"(rs), "s"(soff) : "memory");
}
DI f32x4 mfma_u(u32x4 a, u32x4 b, f32x4 c) { return mfma(__builtin_bit_cast(bf16x8, a), __builtin_bit_cast(bf16x8, b), c); }
#define STAMP_DECL
#define STAMP_ARG
#define STAMP_PASS
#define STAMPX(c, i)
#define STAMP_END
#define STAMP(i) STAMPX(threadIdx.x == 0, i)

DI void poll_flags(gu32* word, bool active, unsigned tag) {
#pragma unroll 1
  for (unsigned spins = 0; spins < SPIN_CAP; ++spins) {
    unsigned v = __hip_atomic_load(word, RLX_AGENT);
    if (__all(!active || v == tag)) break;
    if (PSLEEP) __builtin_amdgcn_s_sleep(PSLEEP);
  }
  asm volatile("" ::: "memory");
}

template <int I> struct IC { static constexpr int v = I; };
template <int B, int E, class Fn> DI void static_for(Fn&& f) {
  if constexpr (B < E) { f(IC<B>{}); static_for<B + 1, E>(f); }
}

enum { J_MLP = 0, J_KV = 1, J_Q = 2, J_SPARE = 3 };

struct Dma {
  rsrc_t rsA, rsB;
  unsigned vA0, vA1, vB0, vB1, vB2;
  unsigned sA, sB, advA, advB;
};

DI unsigned a_slot(unsigned s3) { return s3 == 0 ? 126976u : (s3 - 1) * 32768u; }
#define SB __builtin_amdgcn_sched_barrier(0)

template <int T> DI unsigned bofs(const unsigned (&boff)[T], int t) { return T == 6 && t >= 3 ? boff[t - 3] + 6144 : boff[t]; }
template <int T, bool TR, bool RD, int KIND>
DI void kstep(f32x4 (&acc)[4][T], const u32x4 (&ac)[4], const u32x2 (&bc)[T][2], u32x4 (&an)[4], u32x2 (&bn)[T][2], unsigned ab,
              const unsigned (&boff)[T], unsigned bs, const Dma& d, unsigned dst) {
  constexpr int NP = KIND == 0 ? 4 : 3, ST = (4 * T - 1) / NP;
  static_for<0, 4 * T>([&](auto E) {
    constexpr int e = E.v, t = e >> 2, i = e & 3;
    const u32x4 bf = cat2(bc[t][0], bc[t][1]);
    if (TR) mfma_ip(acc[i][t], bf, ac[i]); else mfma_ip(acc[i][t], ac[i], bf);
    SB;
    if constexpr (RD) {
      if constexpr (e < 4) an[e] = lds_ld<u32x4>(ab + e * 2048);
      else if constexpr (e < 4 + 2 * T) bn[(e - 4) >> 1][e & 1] = ldtr1(bofs<T>(boff, (e - 4) >> 1) + bs + (e & 1) * 768);
    }
    constexpr int pc = e / ST;
    if constexpr (pc < NP && e % ST == ST - 2) set_m0(dst + pc * 1024);
    if constexpr (pc < NP && e % ST == ST - 1) {
      if constexpr (KIND == 0) dma_go((pc & 1) ? d.vA1 : d.vA0, d.rsA, d.sA + pc * 10240);
      else dma_go(pc == 0 ? d.vB0 : (pc == 1 ? d.vB1 : d.vB2), d.rsB, d.sB);
    }
    SB;
  });
}

struct RowStat { u32x4 xa; unsigned xb, row; };
template <int NY> DI void row_stat(RowStat& R, char* ws, unsigned tag, unsigned lane) {
  float s = 0.f, q = 0.f;
  asm volatile("s_waitcnt vmcnt(%8)\n\t"
               "v_dot2c_f32_bf16 %0, %2, %7\n\tv_dot2c_f32_bf16 %1, %2, %2\n\tv_dot2c_f32_bf16 %0, %3, %7\n\tv_dot2c_f32_bf16 %1, %3, %3\n\t"
               "v_dot2c_f32_bf16 %0, %4, %7\n\tv_dot2c_f32_bf16 %1, %4, %4\n\tv_dot2c_f32_bf16 %0, %5, %7\n\tv_dot2c_f32_bf16 %1, %5, %5\n\t"
               "v_dot2c_f32_bf16 %0, %6, %7\n\tv_dot2c_f32_bf16 %1, %6, %6\n\ts_nop 2"
               : "+v"(s), "+v"(q) : "v"(R.xa[0]), "v"(R.xa[1]), "v"(R.xa[2]), "v"(R.xa[3]), "v"(R.xb), "v"(0x3f803f80u), "i"(NY) : "memory");
  const float mean = sum64(s) * (1.0f / D);
  const float var = fmaxf(sum64(q) * (1.0f / D) - mean * mean, 0.f);
  const float sd = __builtin_amdgcn_sqrtf(rbf(rbf(var) + 1e-5f));
  if (lane == 63) __hip_atomic_store((gu64*)(ws + WS_STAT) + R.row, ((unsigned long long)tag << 32) | pk2(mean, sd), RLX_AGENT);
}

struct Gran { u32x2 g0, g1, g2; unsigned off, step; };
DI void gran_issue(Gran& G, rsrc_t rs) {
  asm volatile("buffer_load_dwordx2 %0, %3, %4, 0 offen sc1\n\tbuffer_load_dwordx2 %1, %3, %4, %5 offen sc1\n\t"
               "buffer_load_dwordx2 %2, %3, %4, %6 offen sc1"
               : "+v"(G.g0), "+v"(G.g1), "+v"(G.g2) : "v"(G.off), "s"(rs), "s"(G.step), "s"(2 * G.step) : "memory");
}

template <int T, bool TR, bool LAST>
DI void stage_body(f32x4 (&acc)[4][T], u32x4 (&a0)[4], u32x2 (&b0)[T][2], const unsigned (&aoff)[2], const unsigned (&boff)[T], Dma& d,
                   unsigned w, unsigned ac, unsigned an, unsigned ap, unsigned h0, unsigned h1, unsigned h2, Gran& G, rsrc_t rsWS, bool ld,
                   unsigned tag, unsigned tid) {
  u32x4 a1[4];
  u32x2 b1[T][2];
  kstep<T, TR, true, 0>(acc, a0, b0, a1, b1, aoff[1] + ac, boff, h1, d, ap + w * 4096u);
  if (LAST) {
  asm volatile("s_waitcnt vmcnt(7)" : "+v"(G.g0), "+v"(G.g1), "+v"(G.g2)::"memory");
#pragma unroll 1
  for (unsigned spins = 0; spins < SPIN_CAP; ++spins) {
    if (__all(G.g0[1] == tag && G.g1[1] == tag && G.g2[1] == tag)) break;
    __builtin_amdgcn_s_sleep(2);
    gran_issue(G, rsWS);
    asm volatile("s_waitcnt vmcnt(0)" : "+v"(G.g0), "+v"(G.g1), "+v"(G.g2)::"memory");
  }
  if (w < 4) {
    lds_st<float>(L_MISC + 2048 + tid * 4,
                  __builtin_bit_cast(float, G.g0[0]) + __builtin_bit_cast(float, G.g1[0]) + __builtin_bit_cast(float, G.g2[0]));
  } else {
    lds_st<float>(L_MISC + (tid - 256) * 4, -bflo(G.g0[0]));
    lds_st<float>(L_MISC + 1024 + (tid - 256) * 4, __builtin_amdgcn_rcpf(bfhi(G.g0[0])));
  }
  }
  wait_lgkm0();
  if (!LAST) wait_vm<4>();
  bar();
  if (!LAST) { if (__builtin_expect(ld, 0)) gran_issue(G, rsWS); }
  else if (ld) {
#pragma unroll
    for (int i = 0; i < 4; ++i) dma16(ac + w * 4096u + i * 1024, d.vA0, d.rsA, d.advA + i * 10240);
  }
  SB;
  const unsigned bdst = L_B + (w < 4 ? h0 : h1) + ((w & 1) * 2 + ((w >> 1) & 1)) * 3072u;
  kstep<T, TR, !LAST, 1>(acc, a1, b1, a0, b0, aoff[0] + an, boff, h2, d, bdst);
}

DI unsigned urow(unsigned pos, unsigned rot) { const unsigned u = pos + rot; return (u >= 54 ? u - 54 : u) * 40960u; }

template <int T, bool TR, class Pre>
DI void main_loop(Pre&& pre, RowStat& R, char* ws, f32x4 (&acc)[4][T], unsigned (&boff)[T], Dma& d, unsigned w, unsigned lane, unsigned tid,
                  unsigned tag, unsigned rb, unsigned ccol, rsrc_t rsB2, rsrc_t rsWS, unsigned cb, unsigned rot, bool lastA STAMP_ARG) {
  const unsigned g = lane >> 4, r = lane & 15, m = w & 3;
  unsigned aoff[2];
  aoff[0] = (64 * m + r) * 128 + ((g ^ (r >> 1)) << 4);
  aoff[1] = aoff[0] ^ 64;
  const unsigned wq = (w & 1) * 2 + ((w >> 1) & 1);
  asm volatile("" : "+v"(aoff[0]), "+v"(aoff[1]), "+v"(boff[0]), "+v"(boff[1]), "+v"(boff[2]));
  Gran G;
  G.g0 = G.g1 = G.g2 = u32x2{0u, tag + 1};
  G.off = w < 4 ? WS_CSUM + ccol * 8 : WS_STAT + (rb * 256 + tid - 256) * 8;
  G.step = w < 4 ? NCOL * 8 : 0;
#pragma unroll
  for (int i = 0; i < 4; ++i)
#pragma unroll
    for (int t = 0; t < T; ++t) asm volatile("" : "+v"(acc[i][t]));
  row_stat<7>(R, ws, tag, lane);
  {
    const unsigned bdst = L_B + (w < 4 ? 36864u : 24576u) + wq * 3072u, adst = a_slot(1) + w * 4096u;
    unsigned so = d.sB + d.advB;
    dma16(bdst, d.vB0, d.rsB, so); dma16(bdst + 1024, d.vB1, d.rsB, so); dma16(bdst + 2048, d.vB2, d.rsB, so);
    dma16(adst, d.vA0, d.rsA, d.sA + 128); dma16(adst + 1024, d.vA1, d.rsA, d.sA + 128 + 10240);
    dma16(adst + 2048, d.vA0, d.rsA, d.sA + 128 + 20480); dma16(adst + 3072, d.vA1, d.rsA, d.sA + 128 + 30720);
    pre();
    wait_vm<8>();
    bar();
    if (w >= 4) {
      so += d.advB;
      dma16(bdst + 24576, d.vB0, d.rsB, so); dma16(bdst + 24576 + 1024, d.vB1, d.rsB, so); dma16(bdst + 24576 + 2048, d.vB2, d.rsB, so);
    }
    d.sA += 256;
    d.sB = so + d.advB;
  }
  STAMP(1);
  u32x4 a0[4];
  u32x2 b0[T][2];
  {
    const unsigned ab = aoff[0] + a_slot(0);
#pragma unroll
    for (int i = 0; i < 4; ++i) a0[i] = lds_ld<u32x4>(ab + i * 2048);
#pragma unroll
    for (int t = 0; t < T; ++t) { b0[t][0] = ldtr1(bofs<T>(boff, t)); b0[t][1] = ldtr1(bofs<T>(boff, t) + 768); }
  }
  unsigned ac = a_slot(0), an = a_slot(1), ap = a_slot(2), h0 = 0, h1 = 12288, h2 = 24576;
#pragma unroll 1
  for (unsigned s = 0; s < 9; ++s) {
    if (__builtin_expect(s >= 7, 0)) {
      const unsigned v2 = (lane >> 3) * 1280 + (((lane & 7) ^ (((lane >> 4) & 3) << 1)) << 4);
      if (s == 8) { d.rsA = rsB2; d.vA0 = v2; d.vA1 = v2; d.sA = urow(w, rot) + cb * 128u; }
      if (s == 8 || w >= 4) {
        d.rsB = rsB2; d.vB0 = v2 + (3 * wq) * 10240u; d.vB1 = d.vB0 + 10240u; d.vB2 = d.vB0 + 20480u;
        d.sB = urow(w < 4 ? 19u : (s == 7 ? 16u : 22u), rot) + cb * 128u;
      }
    }
    stage_body<T, TR, false>(acc, a0, b0, aoff, boff, d, w, ac, an, ap, h0, h1, h2, G, rsWS, s == 8, tag, tid);
    d.sA += d.advA; d.sB += d.advB;
    const unsigned t = ap; ap = ac; ac = an; an = t;
    h0 = h2;
    h1 = h0 == 49152 ? 0 : h0 + 12288;
    h2 = h1 == 49152 ? 0 : h1 + 12288;
  }
  STAMP(13);
  d.sA = urow(8 + w, rot) + cb * 128u;
  d.sB = urow(w < 4 ? 25u : 28u, rot) + cb * 128u;
  d.advA = urow(31 + w, rot) + cb * 128u;
  SB;
  stage_body<T, TR, true>(acc, a0, b0, aoff, boff, d, w, a_slot(0), a_slot(1), a_slot(2), 36864, 49152, 0, G, rsWS, lastA && w < 5, tag, tid);
  acc_fence<4 * T>(&acc[0][0]);
  STAMP(14);
}

DI void st16(u32x4 v, rsrc_t rs, unsigned voff, unsigned soff) {
  __builtin_amdgcn_raw_buffer_store_b128(v, rs, voff, soff, 16);
  asm volatile("s_nop 1" ::"v"(v) : "memory");
}
DI void publish_flag(const Params& p, unsigned flag_off, unsigned tag, unsigned tid) {
  wait_vm<0>();
  bar();
  if (tid == 0) __hip_atomic_store((gu32*)(p.ws + flag_off), tag, RLX_AGENT);
}
DI void st_frag(rsrc_t rs, unsigned lane, unsigned rt, unsigned step, u32x2 lo, u32x2 hi) {
  u32x4 v = {lo[0], lo[1], hi[0], hi[1]};
  st16(v, rs, lane * 16, WS_HA + (rt * NSTEP + step) * 1024);
}
DI void shared_send(unsigned w, unsigned lane, const u32x2 (&sh)[4]) {
  const bool n = w >= 4;
  u32x4 snd = n ? u32x4{sh[0][0], sh[0][1], sh[1][0], sh[1][1]} : u32x4{sh[2][0], sh[2][1], sh[3][0], sh[3][1]};
  lds_st<u32x4>(L_SCR + w * 1024 + lane * 16, snd);
}
DI void shared_recv(rsrc_t rs, unsigned w, unsigned lane, unsigned rt0, unsigned step, const u32x2 (&sh)[4]) {
  lbar();
  u32x4 rcv = lds_ld<u32x4>(L_SCR + (w ^ 4) * 1024 + lane * 16);
  u32x2 r0 = {rcv[0], rcv[1]}, r1 = {rcv[2], rcv[3]};
  if (w >= 4) { st_frag(rs, lane, rt0 + 2, step, r0, sh[2]); st_frag(rs, lane, rt0 + 3, step, r1, sh[3]); }
  else        { st_frag(rs, lane, rt0 + 0, step, sh[0], r0); st_frag(rs, lane, rt0 + 1, step, sh[1], r1); }
}

constexpr unsigned wo_tile(unsigned v) { return (v / 5) * 10 + (v % 5 == 4 ? 4 : (v % 5 < 2 ? 2 * (v % 5) : 2 * (v % 5) + 1)); }

__global__ __launch_bounds__(512, 2) void fused_block(Params p) {
  const unsigned tid = threadIdx.x, lane = tid & 63, w = rfl(tid >> 6);
  const unsigned b = blockIdx.x, xcd = b & 7, slot = b >> 3;
  unsigned jtype, jidx;
  const unsigned rb = 4 * (xcd & 1) + (slot & 3), jq = xcd >> 1, ji = slot >> 2;
  const unsigned spi = (xcd - 4) * 4 + (slot & 3);
  if (ji == 7) { jtype = J_Q; jidx = jq; }
  else if (ji >= 5) { jtype = J_KV; jidx = 2 * jq + ji - 5; }
  else if (jq < 2) { jtype = J_MLP; jidx = 5 * jq + ji; }
  else if (ji < 4) { jtype = J_MLP; jidx = 10 + 4 * (jq - 2) + ji; }
  else { jtype = J_SPARE; jidx = 0; }
  STAMP_DECL;
  STAMP(0);
  const rsrc_t rsX = mk_rsrc(p.x, S * D * 2);

  RowStat rs_;
  rs_.row = rfl(b * 8 + w);
  asm volatile("s_nop 4\n\tbuffer_load_dwordx4 %0, %2, %4, %5 offen\n\tbuffer_load_dword %1, %3, %4, %5 offen offset:1024"
               : "=&v"(rs_.xa), "=&v"(rs_.xb) : "v"(lane * 16), "v"(lane * 4), "s"(rsX), "s"(rs_.row * 1280) : "memory");
  Dma d;
  const bool act = jtype != J_SPARE;
  {
    const unsigned mh = w & 1;
    const void* bw; unsigned pitch, nch, colb;
    if (jtype == J_MLP) { bw = mh ? p.wu : p.wg; pitch = 3456; colb = 192 * jidx; nch = 12; }
    else if (jtype == J_KV) { bw = p.wqkv; pitch = 3840; colb = 1280 + 1280 * mh + 160 * jidx; nch = 10; }
    else { bw = p.wqkv; pitch = 3840; colb = 320 * jidx + 160 * mh; nch = 10; }
    d.rsA = rsX; d.rsB = mk_rsrc(bw, D * pitch);
    d.vA0 = (lane >> 3) * 1280 + (((lane & 7) ^ (lane >> 4)) << 4); d.vA1 = d.vA0 ^ 64;
    d.sA = (rb * 256 + 32 * w) * 1280; d.advA = 128;
    asm volatile("s_nop 4" ::: "memory");
    if (act) {
      const unsigned adst = a_slot(0) + w * 4096u;
      dma16(adst, d.vA0, d.rsA, d.sA); dma16(adst + 1024, d.vA1, d.rsA, d.sA + 10240);
      dma16(adst + 2048, d.vA0, d.rsA, d.sA + 20480); dma16(adst + 3072, d.vA1, d.rsA, d.sA + 30720);
    }
    unsigned vb[3];
#pragma unroll
    for (int i = 0; i < 3; ++i) {
      const unsigned c = 64 * i + lane, row = c / 12, pos = c % 12;
      unsigned ch = pos ^ (((row >> 3) & 1) << 1);
      if (ch >= nch) ch -= 2;
      vb[i] = row * pitch + ch * 16;
    }
    d.vB0 = vb[0]; d.vB1 = vb[1]; d.vB2 = vb[2];
    d.sB = (16 * ((w >> 1) & 1) + (w < 4 ? 32 : 0)) * pitch + colb; d.advB = 64 * pitch;
    const unsigned bdst = L_B + (w < 4 ? 12288u : 0u) + ((w & 1) * 2 + ((w >> 1) & 1)) * 3072u;
    if (act) { dma16(bdst, d.vB0, d.rsB, d.sB); dma16(bdst + 1024, d.vB1, d.rsB, d.sB); dma16(bdst + 2048, d.vB2, d.rsB, d.sB); }
  }
  STAMP(10);
  if (tid == 0) lds_st<unsigned>(L_GO, 0u);
  unsigned tag;
  asm volatile("s_load_dword %0, %1, 0x0\n\ts_waitcnt lgkmcnt(0)" : "=s"(tag) : "s"(p.ws) : "memory");
  tag += 1;

  const unsigned t2 = 28 * xcd + slot;
  const bool has2 = slot < 28 && t2 < 220;
  const unsigned G = t2 / 10, cb = t2 % 10;
  const unsigned rot = ROTM == 18 ? 2 + 3 * ((9 * cb / 5 + 6 * (G % 3)) % 18) : 2 + (54 / ROTM) * (cb % ROTM);
  const unsigned g = lane >> 4, r = lane & 15, q4 = r >> 2, p4 = lane & 3;
  const rsrc_t rsWd = mk_rsrc(p.wd, has2 ? F * D * 2 : 0);
  const rsrc_t rsWS = mk_rsrc(p.ws, WS_END);
  const unsigned v2 = (lane >> 3) * 1280 + (((lane & 7) ^ (((lane >> 4) & 3) << 1)) << 4);
  const unsigned m = w & 3, n = w >> 2;
  const unsigned rt0 = rb * 16 + m * 4;
  const unsigned brow = L_B + (8 * g + q4) * 192;
  const unsigned bx = (g & 1) << 5;

  if (jtype == J_MLP) {
    f32x4 acc[4][6] = {};
    unsigned boff[6];
#pragma unroll
    for (int t = 0; t < 3; ++t) {
      const unsigned gt = n ? (t == 2 ? 3 : 4 + t) : t;
      boff[t] = brow + ((32 * gt + 8 * p4) ^ bx); boff[t + 3] = boff[t] + 6144;
    }
    const unsigned ccol = tid < 96 ? 1920 + 96 * jidx + tid : 3648 + 96 * jidx + (tid < 192 ? tid - 96 : 0);
    main_loop<6, true>([] {}, rs_, p.ws, acc, boff, d, w, lane, tid, tag, rb, ccol, rsWd, rsWS, cb, rot, true STAMP_PASS);
    float mm[4];
    f32x4 cg[3], cu[3];
#pragma unroll
    for (int i = 0; i < 4; ++i) mm[i] = lds_ld<float>(L_MISC + (64 * m + 16 * i + r) * 4);
#pragma unroll
    for (int t = 0; t < 3; ++t) {
      const unsigned gt = n ? (t == 2 ? 3 : 4 + t) : t;
      cg[t] = lds_ld<f32x4>(L_MISC + 2048 + (16 * gt + 4 * g) * 4); cu[t] = lds_ld<f32x4>(L_MISC + 2048 + (96 + 16 * gt + 4 * g) * 4);
    }
    auto hval = [&](int i, int t) {
      f32x4 gg = acc[i][t] + mm[i] * cg[t];
      const f32x4 uu = acc[i][t + 3] + mm[i] * cu[t];
#pragma unroll
      for (int j = 0; j < 4; ++j) gg[j] = relu2(gg[j]);
      return pk4(gg * uu);
    };
#pragma unroll
    for (int i = 0; i < 4; ++i) { st_frag(rsWS, lane, rt0 + i, 3 * jidx + 2 * n, hval(i, 0), hval(i, 1)); SB; }
    const u32x2 sh[4] = {hval(0, 2), hval(1, 2), hval(2, 2), hval(3, 2)};
    shared_send(w, lane, sh);
    shared_recv(rsWS, w, lane, rt0, 3 * jidx + 1, sh);
    STAMP(15);
    publish_flag(p, WS_FLAGH + rb * 128 + jidx * 4, tag, tid);
    STAMP(3);
  } else if (jtype == J_KV) {
    f32x4 acc[4][5] = {};
    unsigned boff[5];
    boff[0] = brow + ((80 * n + 8 * p4) ^ bx); boff[1] = boff[0] + 6144;
    boff[2] = brow + ((80 * n + 40 + 8 * p4) ^ bx); boff[3] = boff[2] + 6144;
    boff[4] = brow + ((80 * n + 32 + 40 * (p4 >> 1)) ^ bx) + (p4 & 1) * 6144;
    const unsigned kb = 126976u + m * 6720 + n * 3360, dump = L_MISC - 2048 + lane * 16;
    const unsigned ea = (q4 == 1 || r == 0) ? kb + (r == 0 ? 20 : 12 + r) * 80 : 0u, eb = (q4 == 3 || r == 0) ? kb + 1680 + (r == 0 ? 20 : 4 + r) * 80 : 0u;
    unsigned ka[6] = {kb + r * 80 + g * 16, ea ? ea + g * 16 : dump, eb ? eb + g * 16 : dump, g == 0 ? kb + r * 80 + 64 : dump,
                      g == 2 ? kb + 1680 + r * 80 + 64 : dump, (g == 0 && ea) ? ea + 64 : ((g == 2 && eb) ? eb + 64 : dump)};
    auto pin = [&] { asm volatile("" : "+v"(ka[0]), "+v"(ka[1]), "+v"(ka[2]), "+v"(ka[3]), "+v"(ka[4]), "+v"(ka[5])); };
    const unsigned ccol = tid < 80 ? 640 + 80 * jidx + tid : 1280 + 80 * jidx + (tid < 160 ? tid - 80 : 0);
    main_loop<5, false>(pin, rs_, p.ws, acc, boff, d, w, lane, tid, tag, rb, ccol, rsWd, rsWS, cb, rot, false STAMP_PASS);
    float cs[5];
    cs[0] = lds_ld<float>(L_MISC + 2048 + (40 * n + r) * 4);
    cs[1] = lds_ld<float>(L_MISC + 2048 + (80 + 40 * n + r) * 4);
    cs[2] = lds_ld<float>(L_MISC + 2048 + (40 * n + 20 + r) * 4);
    cs[3] = lds_ld<float>(L_MISC + 2048 + (80 + 40 * n + 20 + r) * 4);
    cs[4] = lds_ld<float>(L_MISC + 2048 + ((q4 & 1) * 80 + 40 * n + 16 + 20 * (q4 >> 1) + (r & 3)) * 4);
    u32x2 pk[4][3], vv[4][3];
    f32x4 mm4[4], rr4[4];
#pragma unroll
    for (int i = 0; i < 4; ++i) {
      mm4[i] = lds_ld<f32x4>(L_MISC + (64 * m + 16 * i + 4 * g) * 4); rr4[i] = lds_ld<f32x4>(L_MISC + 1024 + (64 * m + 16 * i + 4 * g) * 4);
    }
    SB;
#pragma unroll
    for (int i = 0; i < 4; ++i) {
      const f32x4 mm = mm4[i], rr = rr4[i];
      const f32x4 rl = rr * 1.4426950408889634f, rh = rr * 0.5f;
      f32x4 val[5];
#pragma unroll
      for (int t = 0; t < 5; ++t) val[t] = acc[i][t] + mm * cs[t];
#pragma unroll
      for (int t = 0; t < 3; ++t) pk[i][t] = pk4(phi4(val[2 * t], rl, rh));
      vv[i][0] = pk4(val[1] * rr); vv[i][1] = pk4(val[3] * rr);
      const u32x2 v4 = pk4(val[4] * rr);
      vv[i][2] = (q4 & 1) ? v4 : u32x2{0x3f803f80u, 0x3f803f80u};
    }
    STAMP(25);
    f32x4 P[7] = {};
#pragma unroll
    for (int i = 0; i < 4; i += 2) {
      const bf16x8 k0 = frag(pk[i][0], pk[i + 1][0]), k2 = frag(pk[i][1], pk[i + 1][1]), k4 = frag(pk[i][2], pk[i + 1][2]);
      const bf16x8 v1 = frag(vv[i][0], vv[i + 1][0]), v3 = frag(vv[i][1], vv[i + 1][1]), v4 = frag(vv[i][2], vv[i + 1][2]);
      P[0] = mfma(k0, v1, P[0]); P[1] = mfma(k0, v4, P[1]); P[2] = mfma(k4, v1, P[2]);
      P[3] = mfma(k2, v3, P[3]); P[4] = mfma(k2, v4, P[4]); P[5] = mfma(k4, v3, P[5]); P[6] = mfma(k4, v4, P[6]);
    }
    lds_st<f32x4>(ka[0], P[0]); lds_st<f32x4>(ka[0] + 1680, P[3]); lds_st<f32x4>(ka[1], P[1]); lds_st<f32x4>(ka[2], P[4]);
    lds_st<f32x4>(ka[3], P[2]); lds_st<f32x4>(ka[4], P[5]); lds_st<f32x4>(ka[5], P[6]);
    lbar();
    if (tid < 210) {
      const unsigned a = 126976u + tid * 32;
      const f32x4 t0 = (lds_ld<f32x4>(a) + lds_ld<f32x4>(a + 6720)) + (lds_ld<f32x4>(a + 13440) + lds_ld<f32x4>(a + 20160));
      const f32x4 t1 = (lds_ld<f32x4>(a + 16) + lds_ld<f32x4>(a + 6736)) + (lds_ld<f32x4>(a + 13456) + lds_ld<f32x4>(a + 20176));
      st16(cat2(pk4(t0), pk4(t1)), rsWS, tid * 16, WS_KVP + (rb * 32 + 4 * jidx) * 840);
    }
    wait_lgkm0();
    STAMP(15);
    publish_flag(p, WS_FLAGKV + (jidx >> 1) * 128 + ((jidx & 1) * 8 + rb) * 4, tag, tid);
    STAMP(3);
  } else if (jtype == J_Q) {
    f32x4 acc[4][5] = {};
    unsigned boff[5];
#pragma unroll
    for (int t = 0; t < 5; ++t) boff[t] = brow + n * 6144 + ((32 * t + 8 * p4) ^ bx);
    const unsigned ccol = 160 * jidx + (tid < 160 ? tid : 0);
    main_loop<5, true>([] {}, rs_, p.ws, acc, boff, d, w, lane, tid, tag, rb, ccol, rsWd, rsWS, cb, rot, false STAMP_PASS);
    u32x2 pq[4][5];
    {
      float mm[4], rr[4];
      f32x4 c[5];
#pragma unroll
      for (int i = 0; i < 4; ++i) { mm[i] = lds_ld<float>(L_MISC + (64 * m + 16 * i + r) * 4); rr[i] = lds_ld<float>(L_MISC + 1024 + (64 * m + 16 * i + r) * 4); }
#pragma unroll
      for (int t = 0; t < 5; ++t) c[t] = lds_ld<f32x4>(L_MISC + 2048 + (80 * n + 16 * t + 4 * g) * 4);
      SB;
#pragma unroll
      for (int i = 0; i < 4; ++i)
#pragma unroll
        for (int t = 0; t < 5; ++t) pq[i][t] = pk4(phi4(acc[i][t] + mm[i] * c[t], splat(rr[i] * 1.4426950408889634f), splat(rr[i] * 0.5f)));
#pragma unroll
      for (int i = 0; i < 4; ++i) asm volatile("" : "+v"(pq[i][0]), "+v"(pq[i][1]), "+v"(pq[i][2]), "+v"(pq[i][3]), "+v"(pq[i][4]));
    }
    constexpr unsigned KP = 208, KQ = 84 * KP;
    unsigned wa[2];
#pragma unroll
    for (int k = 0; k < 2; ++k) {
      const unsigned qd = 2 * (tid < 420 ? tid : 0) + k, hd = qd / 105, e = (qd % 105) / 5, dq = qd % 5, hl = hd & 3;
      wa[k] = (hd >> 2) * KQ + (e < 20 ? hl * 20 + e : 80 + hl) * KP + hl * 40 + dq * 8;
    }
    unsigned fk = n * KQ + r * KP + g * 8, fz = n * KQ + (80 + (r & 3)) * KP + g * 8;
    asm volatile("" : "+v"(wa[0]), "+v"(wa[1]), "+v"(fk), "+v"(fz));
    STAMP(25);
    wait_vm<0>();
    bar();
#pragma unroll
    for (int k = 0; k < 5; ++k) lds_st<u32x4>(k * 8192 + tid * 16, u32x4{0u, 0u, 0u, 0u});
    if (w == 0) poll_flags((gu32*)(p.ws + WS_FLAGKV + jidx * 128) + (lane & 15), true, tag);
    lbar();
    STAMP(4);
    if (w < 7) {
      u32x4 v[8];
#pragma unroll
      for (int j = 0; j < 8; ++j)
        v[j] = __builtin_amdgcn_raw_buffer_load_b128(rsWS, (tid < 420 ? tid : 0) * 16, WS_KVP + (j * 32 + 8 * jidx) * 840, 16);
      SB;
      f32x4 t0 = {0.f, 0.f, 0.f, 0.f}, t1 = t0;
#pragma unroll
      for (int j = 0; j < 8; ++j) {
        t0 += f32x4{bflo(v[j][0]), bfhi(v[j][0]), bflo(v[j][1]), bfhi(v[j][1])};
        t1 += f32x4{bflo(v[j][2]), bfhi(v[j][2]), bflo(v[j][3]), bfhi(v[j][3])};
      }
      lds_st<u32x2>(wa[0], pk4(t0)); lds_st<u32x2>(wa[1], pk4(t1));
    }
    lbar();
    STAMP(16);
    bf16x8 A[12];
#pragma unroll
    for (int P = 0; P < 3; ++P) A[P] = frag(lds_ld<u32x2>(fz + P * 64), lds_ld<u32x2>(fz + P * 64 + 32));
#pragma unroll
    for (int u = 0; u < 5; ++u) {
      const unsigned a0 = fk + u * 16 * KP + (u < 3 ? 0 : 64);
      A[3 + u] = frag(lds_ld<u32x2>(a0), lds_ld<u32x2>(a0 + 32));
      if (u > 0) A[7 + u] = frag(lds_ld<u32x2>(a0 + 64), lds_ld<u32x2>(a0 + 96));
    }
    u32x2 sh[4];
#pragma unroll
    for (int i = 0; i < 4; ++i) {
      const bf16x8 B[3] = {frag(pq[i][0], pq[i][1]), frag(pq[i][2], pq[i][3]), frag(pq[i][4], u32x2{0u, 0u})};
      const f32x4 zero = {0.f, 0.f, 0.f, 0.f};
      const f32x4 den = mfma(A[2], B[2], mfma(A[1], B[1], mfma(A[0], B[0], zero))) + 1e-6f;
      f32x4 num[5];
#pragma unroll
      for (int u = 0; u < 5; ++u) num[u] = mfma(A[3 + u], B[u < 3 ? 0 : 1], zero);
#pragma unroll
      for (int u = 1; u < 5; ++u) num[u] = mfma(A[7 + u], B[u < 3 ? 1 : 2], num[u]);
      float rc[4];
#pragma unroll
      for (int j = 0; j < 4; ++j) rc[j] = __builtin_amdgcn_rcpf(den[j]);
      u32x2 o[5];
#pragma unroll
      for (int u = 0; u < 5; ++u) {
        const float rr = u == 0 ? rc[0] : (u == 4 ? rc[3] : (g < (unsigned)u ? rc[u - 1] : rc[u]));
        o[u] = pk4(num[u] * rr);
      }
      st_frag(rsWS, lane, rt0 + i, 54 + 5 * jidx + 2 * n, o[0], o[1]);
      st_frag(rsWS, lane, rt0 + i, 54 + 5 * jidx + 2 * n + 1, o[2], o[3]);
      sh[i] = o[4];
    }
    shared_send(w, lane, sh);
    shared_recv(rsWS, w, lane, rt0, 54 + 5 * jidx + 4, sh);
    STAMP(15);
    publish_flag(p, WS_FLAGO + rb * 128 + jidx * 4, tag, tid);
    STAMP(3);
    if (b == 224) {
      poll_flags((gu32*)(p.ws + WS_FLAGH) + tid, (tid & 31) < (tid < 256 ? 18u : 4u), tag);
      bar();
      if (tid == 0) __hip_atomic_store((gu32*)(p.ws + WS_EPOCH), tag, RLX_AGENT);
    }
  } else {
    const unsigned gw = spi * 8 + w;
    unsigned k10 = 0x00003f80u, k01 = 0x3f800000u;
    asm volatile("" : "+v"(k10), "+v"(k01));
    auto cs_src = [&](unsigned u, rsrc_t& rs, unsigned& cv, unsigned& st) {
      const unsigned cline = u / 3, third = u % 3;
      const void* cw = cline < 30 ? p.wqkv : (cline < 57 ? p.wg : p.wu);
      const unsigned cpitch = cline < 30 ? 3840u : 3456u;
      const unsigned clocal = cline < 30 ? cline : (cline < 57 ? cline - 30 : cline - 57);
      rs = mk_rsrc(cw, gw < 126 ? D * cpitch : 0);
      cv = (third * 216 + (lane >> 3)) * cpitch + clocal * 128 + (lane & 7) * 16;
      st = 8 * cpitch;
    };
    auto cs_publish = [&](unsigned u, float (&cs)[8]) {
#pragma unroll
      for (int k = 0; k < 4; ++k) { const bool hi = lane & 32; const float o = __shfl_xor(hi ? cs[k] : cs[k + 4], 32); cs[k] = (hi ? cs[k + 4] : cs[k]) + o; }
#pragma unroll
      for (int k = 0; k < 2; ++k) { const bool hi = lane & 16; const float o = __shfl_xor(hi ? cs[k] : cs[k + 2], 16); cs[k] = (hi ? cs[k + 2] : cs[k]) + o; }
      { const bool hi = lane & 8; const float o = __shfl_xor(hi ? cs[0] : cs[1], 8); cs[0] = (hi ? cs[1] : cs[0]) + o; }
      if (gw < 126)
        __hip_atomic_store((gu64*)(p.ws + WS_CSUM) + (u % 3) * NCOL + (u / 3) * 64 + (lane & 7) * 8 + (lane >> 3),
                           ((unsigned long long)tag << 32) | __builtin_bit_cast(unsigned, cs[0]), RLX_AGENT);
    };
    row_stat<0>(rs_, p.ws, tag, lane);
    u32x4 c[27];
    rsrc_t rs0, rs1; unsigned cv0, cv1, st0, st1;
    cs_src(gw, rs0, cv0, st0); cs_src(gw + 126, rs1, cv1, st1);
#pragma unroll
    for (int j = 0; j < 27; ++j) c[j] = __builtin_amdgcn_raw_buffer_load_b128(rs0, cv0 + j * st0, 0, 0);
    bar();
    float cs[8] = {};
#pragma unroll
    for (int j = 0; j < 27; ++j) {
#pragma unroll
      for (int k = 0; k < 4; ++k) { cs[2 * k] = dot2(c[j][k], k10, cs[2 * k]); cs[2 * k + 1] = dot2(c[j][k], k01, cs[2 * k + 1]); }
      if (j < 8) c[j] = __builtin_amdgcn_raw_buffer_load_b128(rs1, cv1 + j * st1, 0, 0);
      SB;
    }
    cs_publish(gw, cs);
    SB;
#pragma unroll
    for (int j = 8; j < 27; ++j) c[j] = __builtin_amdgcn_raw_buffer_load_b128(rs1, cv1 + j * st1, 0, 0);
    SB;
    STAMP(11);
#pragma unroll
    for (int k = 0; k < 8; ++k) cs[k] = 0.f;
#pragma unroll
    for (int j = 0; j < 27; ++j)
#pragma unroll
      for (int k = 0; k < 4; ++k) { cs[2 * k] = dot2(c[j][k], k10, cs[2 * k]); cs[2 * k + 1] = dot2(c[j][k], k01, cs[2 * k + 1]); }
    cs_publish(gw + 126, cs);
    STAMP(12);
#pragma unroll 1
    for (unsigned i = w; i < 144; i += 8) dma16(i * 1024, v2, rsWd, urow(i >> 2, rot) + (i & 3) * 10240u + cb * 128u);
    wait_vm<0>();
    bar();
  }
  if (!has2) { STAMP_END; return; }

  const unsigned rbA = (G * 6) >> 4, rbB = (G * 6 + 5 > 127 ? 127 : G * 6 + 5) >> 4;
  if (w >= 4) {
    __builtin_amdgcn_s_setprio(HPRIO);
    const rsrc_t rsWo = mk_rsrc(p.wo, D * D * 2);
    const unsigned lw = w - 4, ld0 = lw * 1024;
    const unsigned wo1 = (lw >> 1) * 20480u + (lw & 1) * 10240u + cb * 128u, wo5 = wo1 + (lw >> 1) * 81920u;
    unsigned sd = urow(31, rot) + lw * 10240u + cb * 128u;
    const unsigned sdlim = 54 * 40960u;
    auto src = [&](int u) {
      if (u >= 54) return wo_tile(u - 54) * 20480u + ((u - 54) % 5 == 4 ? wo5 : wo1);
      const unsigned o = sd;
      sd += 40960u; if (sd >= sdlim) sd -= sdlim;
      return o;
    };
    __builtin_amdgcn_s_sleep(HSLEEP);
    if (jtype == J_KV) {
#pragma unroll
      for (int u = 31; u < 36; ++u) dma16(u * 4096 + ld0, v2, rsWd, src(u));
    }
    sd = urow(36, rot) + lw * 10240u + cb * 128u;
    u32x4 wr[36];
#pragma unroll
    for (int u = 36; u < HSPLIT; ++u) { wr[u - 36] = __builtin_amdgcn_raw_buffer_load_b128(u < 54 ? rsWd : rsWo, v2, src(u), 0); SB; }
    STAMP(8);
    gu32* fo = (gu32*)(p.ws + WS_FLAGO + (lane < 32 ? rbA : rbB) * 128) + (lane & 31);
    unsigned la[3], wb[3], xo[3], st[3];
    u32x4 xr[3];
    const unsigned ch = lane & 7;
#pragma unroll
    for (int i = 0; i < 3; ++i) {
      const unsigned row = lw * 24 + i * 8 + (lane >> 3), rl = row >> 4, rr = row & 15;
      la[i] = (rl >= 3 ? 24576u + (rl - 3) * 4096u : rl * 4096u) + (ch >> 1) * 1024 + (ch & 1) * 512 + ((rr + 2 * ch) & 15) * 16;
      xo[i] = (G * 96 + row) * 1280 + cb * 128 + ch * 16;
      wb[i] = ld0 + lane * 16 + i * 49152;
      asm volatile("" : "+v"(la[i]), "+v"(wb[i]), "+v"(xo[i]));
    }
    float ri[3], rl[3];
    f32x4 xf[3][2], dd[3][2];
#pragma unroll
    for (int k = 1; k <= 6; ++k) {
      if (k == O_BAR && w == 7) poll_flags(fo, (lane & 31) < 4, tag);
      if (k == 6) {
#pragma unroll
        for (int h = 0; h < 3; ++h) {
          ri[h] = 2.0f * bfhi(st[h]) * bfhi(st[h]);
          rl[h] = -1.4426950408889634f * __builtin_amdgcn_rcpf(ri[h]);
          xf[h][0] = f32x4{bflo(xr[h][0]), bfhi(xr[h][0]), bflo(xr[h][1]), bfhi(xr[h][1])};
          xf[h][1] = f32x4{bflo(xr[h][2]), bfhi(xr[h][2]), bflo(xr[h][3]), bfhi(xr[h][3])};
          asm volatile("" : "+v"(ri[h]), "+v"(rl[h]), "+v"(xf[h][0]), "+v"(xf[h][1]));
        }
      }
      lbar();
      if (k <= 4) {
#pragma unroll
        for (int u = 9 * (k - 1) + ((k - 1) & 1); u < 9 * k + (k & 1); ++u) {
          lds_st<u32x4>(wb[u / 12] + (u % 12) * 4096, wr[u]);
          if (u + 36 + WLEAD >= HSPLIT && u + 36 + WLEAD < 72) wr[u + WLEAD] = __builtin_amdgcn_raw_buffer_load_b128(rsWo, v2, src(u + 36 + WLEAD), 0);
          if (u + WLEAD == 36 || u + WLEAD == 37) dma16((u + WLEAD) * 4096 + ld0, v2, rsWo, src(u + 36 + WLEAD));
          SB;
          if (WSLEEP) __builtin_amdgcn_s_sleep(WSLEEP);
          SB;
        }
      }
      if (k == 4) {
#pragma unroll
        for (int h = 0; h < 3; ++h) {
          const unsigned row = G * 96 + lw * 24 + h * 8 + (lane >> 3);
          st[h] = __builtin_amdgcn_raw_buffer_load_b32(rsWS, WS_STAT + (row < S ? row : 0) * 8, 0, 16);
          xr[h] = __builtin_amdgcn_raw_buffer_load_b128(rsX, xo[h], 0, 0);
        }
      }
    }
#pragma unroll
    for (int i = 0; i < 3; ++i) {
      const unsigned lb = la[i] + (la[i] >= 28672u ? 126976u : 12288u);
      dd[i][0] = lds_ld<f32x4>(la[i]) + lds_ld<f32x4>(lb);
      dd[i][1] = lds_ld<f32x4>(la[i] + 256) + lds_ld<f32x4>(lb + 256);
    }
    lbar();
    __builtin_amdgcn_s_setprio(GPRIO);
#pragma unroll
    for (int i = 0; i < 6; ++i) {
      const f32x4 l = dd[i >> 1][i & 1] * rl[i >> 1];
      f32x4 e;
#pragma unroll
      for (int j = 0; j < 4; ++j) e[j] = __builtin_amdgcn_exp2f(l[j]);
      e = e * ri[i >> 1] + ri[i >> 1];
#pragma unroll
      for (int j = 0; j < 4; ++j) e[j] = __builtin_amdgcn_rcpf(e[j]);
      dd[i >> 1][i & 1] *= e;
      asm volatile("" : "+v"(dd[i >> 1][i & 1]));
    }
    const rsrc_t rsO = mk_rsrc(p.out, S * D * 2);
    STAMP(9);
    bar();
    f32x4 ss[3][2];
#pragma unroll
    for (int i = 0; i < 3; ++i) {
      ss[i][0] = lds_ld<f32x4>(la[i]) + lds_ld<f32x4>(la[i] + 12288);
      ss[i][1] = lds_ld<f32x4>(la[i] + 256) + lds_ld<f32x4>(la[i] + 12288 + 256);
    }
#pragma unroll
    for (int i = 0; i < 3; ++i)
      __builtin_amdgcn_raw_buffer_store_b128(cat2(pk4(xf[i][0] + ss[i][0] * dd[i][0]), pk4(xf[i][1] + ss[i][1] * dd[i][1])), rsO, xo[i], 0, 0);
    STAMP(5);
    STAMP_END;
    return;
  }
  {
    const unsigned kh = w & 1, rt = G * 6 + 3 * (w >> 1);
    const rsrc_t rsH = mk_rsrc(p.ws + WS_HA + (rt < 128 ? rt : 0) * (NSTEP * 1024), rt < 128 ? (rt > 125 ? 128 - rt : 3) * (NSTEP * 1024) : 0);
    unsigned tb[3][4];
#pragma unroll
    for (int t = 0; t < 4; ++t) {
      tb[0][t] = (4 * g + q4) * 128 + ((t ^ ((2 * g + (q4 >> 1)) & 3)) << 5) + 8 * p4 + kh * 4096;
      tb[1][t] = tb[0][t] + 49152; tb[2][t] = tb[0][t] + 98304;
      asm volatile("" : "+v"(tb[0][t]), "+v"(tb[1][t]), "+v"(tb[2][t]));
    }
    auto rd1 = [&](int i, int t, int hf) {
      const int sl = i == 36 ? 36 : (2 * i) % 36, bi = sl / 12 > 2 ? 2 : sl / 12;
      return __builtin_bit_cast(u32x2, lds_tr(tb[bi][t] + (sl - 12 * bi) * 4096 + hf * 2048));
    };
    constexpr int NW = 12, W0 = 5;
    constexpr int OI = O_BAR == 4 ? 17 : 22;
    f32x4 acc[3][4];
    u32x4 a[NW][3];
    u32x2 bw[2][4][2];
    unsigned va[3];
#pragma unroll
    for (int h = 0; h < 3; ++h) va[h] = lane * 16 + h * NSTEP * 1024;
    auto put = [&](bool hi) {
      acc_fence<12>(&acc[0][0]);
#pragma unroll
      for (int t = 0; t < 4; ++t) {
        const unsigned xa = w * 12288u + t * 1024 + g * 256 + ((r + 4 * t + (g & 2)) & 15) * 16, xb = xa + (hi && w == 3 ? 114688u : 0u);
        lds_st<f32x4>(xa, acc[0][t]); lds_st<f32x4>(xb + 4096, acc[1][t]); lds_st<f32x4>(xb + 8192, acc[2][t]);
      }
      wait_lgkm0();
    };
    unsigned so = (rot + kh) * 1024;
    auto lda = [&](auto I, auto H) {
      constexpr int i = I.v, h = H.v;
      if constexpr (i < 27) {
        if (h == 0 && so >= 54 * 1024) so -= 54 * 1024;
        a[i % NW][h] = __builtin_amdgcn_raw_buffer_load_b128(rsH, va[h], so, 16);
        if (h == 2) so += 2048;
      } else {
        a[i % NW][h] = __builtin_amdgcn_raw_buffer_load_b128(rsH, va[h], (2 * i + kh) * 1024, 16);
      }
      SB;
    };
    if (w == 0) {
      poll_flags((gu32*)(p.ws + WS_FLAGH + (lane < 32 ? rbA : rbB) * 128) + (lane & 31), (lane & 31) < 18, tag);
      __hip_atomic_store((LAS unsigned*)((LAS char*)smem + L_GO), 1u, __ATOMIC_RELAXED, __HIP_MEMORY_SCOPE_WORKGROUP);
    } else {
#pragma unroll 1
      for (unsigned spins = 0; spins < (SPIN_CAP << 4); ++spins) {
        if (rfl(__hip_atomic_load((LAS unsigned*)((LAS char*)smem + L_GO), __ATOMIC_RELAXED, __HIP_MEMORY_SCOPE_WORKGROUP))) break;
        __builtin_amdgcn_s_sleep(1);
      }
    }
    asm volatile("" ::: "memory");
    STAMP(8);
    static_for<0, W0>([&](auto I) { lda(I, IC<0>{}); lda(I, IC<1>{}); lda(I, IC<2>{}); });
#pragma unroll
    for (int t = 0; t < 4; ++t) { bw[0][t][0] = rd1(0, t, 0); bw[0][t][1] = rd1(0, t, 1); }
    static_for<0, 37>([&](auto I) {
      constexpr int i = I.v;
      constexpr int lim = i + NW - 1;
      constexpr int nxt = i <= OI ? (W0 + 2 * i < lim ? W0 + 2 * i : lim) : (27 + 2 * (i - OI - 1) < lim ? 27 + 2 * (i - OI - 1) : lim);
      constexpr int end = i <= OI ? 27 : 37;
      constexpr int l0 = nxt < end ? nxt : -1, l1 = (nxt + 1 <= lim && nxt + 1 < end) ? nxt + 1 : -1;
      SB;
      static_for<0, 12>([&](auto E) {
        constexpr int e = E.v, t = e / 3, h = e % 3;
        const u32x4 bf = cat2(bw[i & 1][t][0], bw[i & 1][t][1]);
        if constexpr (i == 0 || i == 27) mfma_z(acc[2 - h][t], bf, a[i % NW][2 - h]); else mfma_ip(acc[2 - h][t], bf, a[i % NW][2 - h]);
        SB;
        if constexpr (i < 36 && h < 2) bw[(i + 1) & 1][t][h] = rd1(i + 1, t, h);
        if constexpr (l1 < 0) { if constexpr (h == 2 && t < 3 && l0 >= 0) lda(IC<l0>{}, IC<t>{}); }
        else if constexpr (e & 1) lda(IC<(e < 6 ? l0 : l1)>{}, IC<(e >> 1) % 3>{});
        SB;
      });
      if constexpr (i == 26) put(true);
      if constexpr (i == XBAR) bar();
      if constexpr (i < 27 && (i % 9 == 4 || i % 9 == 8)) {
        bar();
        STAMP(17 + (2 * i) / 9);
      }
    });
    STAMP(24);
    put(false);
    bar();
    STAMP(5);
    STAMP_END;
  }
}

extern "C" void kernel_launch(void* const* d_in, const int* in_sizes, int n_in, void* d_out, int out_size, void* d_ws,
                              size_t ws_size, hipStream_t stream) {
  Params p;
  p.x = d_in[0]; p.wqkv = d_in[1]; p.wo = d_in[2]; p.wg = d_in[4]; p.wu = d_in[5]; p.wd = d_in[6];
  p.out = d_out; p.ws = (char*)d_ws;
  fused_block<<<256, 512, 0, stream>>>(p);
}
```

```cpp
#include <hip/hip_runtime.h>
#include <stdint.h>

typedef __attribute__((ext_vector_type(8))) __bf16 bf16x8;
typedef __attribute__((ext_vector_type(4))) __bf16 bf16x4;
typedef __attribute__((ext_vector_type(2))) __bf16 bf16x2;
typedef __attribute__((ext_vector_type(4))) float f32x4;
typedef __attribute__((ext_vector_type(2))) float f32x2;
typedef __attribute__((ext_vector_type(4))) unsigned u32x4;
typedef __attribute__((ext_vector_type(2))) unsigned u32x2;
typedef __amdgpu_buffer_rsrc_t rsrc_t;
#define LAS __attribute__((address_space(3)))
#define GAS __attribute__((address_space(1)))
#define DI __device__ __forceinline__
#define RLX_AGENT __ATOMIC_RELAXED, __HIP_MEMORY_SCOPE_AGENT
typedef GAS unsigned gu32;
typedef GAS unsigned long long gu64;

constexpr int S = 2048, D = 640, F = 1728;
constexpr int NCOL = 5376;
constexpr int NSTEP = 74;
constexpr unsigned WS_EPOCH = 0;
constexpr unsigned WS_FLAGH = 256;
constexpr unsigned WS_FLAGO = WS_FLAGH + 8 * 128;
constexpr unsigned WS_FLAGKV = WS_FLAGO + 8 * 128;
constexpr unsigned WS_STAT = 4096;
constexpr unsigned WS_CSUM = WS_STAT + S * 8;
constexpr unsigned WS_KVP = WS_CSUM + 3 * NCOL * 8;
constexpr unsigned WS_HA = 1u << 20;
constexpr unsigned WS_DBG = 12u << 20;
constexpr unsigned WS_END = 13u << 20;
constexpr unsigned L_B = 65536;
constexpr unsigned L_SCR = 147456;
constexpr unsigned L_MISC = 159744;
constexpr unsigned L_GO = L_MISC + 3072;
constexpr unsigned SPIN_CAP = 1u << 18;
#define HSLEEP 4
#define HSPLIT 54
#define WLEAD 10
#define GPRIO 0
#define HPRIO 0
#define XBAR 28
#define PSLEEP 2
#define WSLEEP 0
#define ROTM 18
#define MAPV 0
#define O_BAR 5
#define PRIO_MODE 0
#define DMA_SPACE_A T
#define DMA_SPACE_B (4 * T / 3)

struct Params {
  const void* x; const void* wqkv; const void* wo; const void* wg; const void* wu; const void* wd;
  void* out; char* ws;
};

__shared__ __attribute__((aligned(16))) char smem[163840];

DI unsigned rfl(unsigned v) { return __builtin_amdgcn_readfirstlane(v); }
DI rsrc_t mk_rsrc(const void* p, unsigned bytes) { return __builtin_amdgcn_make_buffer_rsrc((void*)p, (short)0, (int)bytes, 0x00020000); }
DI void dma16(unsigned ldsdst, unsigned voff, rsrc_t rs, unsigned soff) {
  asm volatile("s_mov_b32 m0, %0\n\ts_nop 0\n\tbuffer_load_dwordx4 %1, %2, %3 offen lds" ::"s"(ldsdst), "v"(voff), "s"(rs), "s"(soff) : "memory");
}
template <int N> DI void wait_vm() { asm volatile("s_waitcnt vmcnt(%0)" ::"i"(N) : "memory"); }
DI void wait_lgkm0() { asm volatile("s_waitcnt lgkmcnt(0)" ::: "memory"); }
DI void bar() { asm volatile("" ::: "memory"); __builtin_amdgcn_s_barrier(); asm volatile("" ::: "memory"); }
DI void lbar() { wait_lgkm0(); bar(); }
template <class V> DI V lds_ld(unsigned a) { return *(LAS V*)((LAS char*)smem + a); }
template <class V> DI void lds_st(unsigned a, V v) { *(LAS V*)((LAS char*)smem + a) = v; }
DI bf16x4 lds_tr(unsigned a) { return __builtin_amdgcn_ds_read_tr16_b64_v4bf16((LAS bf16x4*)((LAS char*)smem + a)); }
DI bf16x8 cat(bf16x4 a, bf16x4 b) { return __builtin_shufflevector(a, b, 0, 1, 2, 3, 4, 5, 6, 7); }
DI f32x4 mfma(bf16x8 a, bf16x8 b, f32x4 c) { return __builtin_amdgcn_mfma_f32_16x16x32_bf16(a, b, c, 0, 0, 0); }
DI unsigned pk2(float a, float b) { f32x2 v = {a, b}; return __builtin_bit_cast(unsigned, __builtin_convertvector(v, bf16x2)); }
DI u32x2 pk4(f32x4 v) { u32x2 r = {pk2(v[0], v[1]), pk2(v[2], v[3])}; return r; }
DI bf16x8 frag(u32x2 lo, u32x2 hi) { u32x4 v = {lo[0], lo[1], hi[0], hi[1]}; return __builtin_bit_cast(bf16x8, v); }
DI float bflo(unsigned v) { return __builtin_bit_cast(float, v << 16); }
DI float bfhi(unsigned v) { return __builtin_bit_cast(float, v & 0xffff0000u); }
DI float rbf(float v) { return bflo(pk2(v, 0.f)); }
DI float relu2(float v) { float o; asm("v_add_f32 %0, %1, |%1|" : "=v"(o) : "v"(v)); return o; }
DI f32x4 phi4(f32x4 u, f32x4 rl, f32x4 rh) {
  const f32x4 l = u * rl;
  f32x4 e, a;
#pragma unroll
  for (int j = 0; j < 4; ++j) {
    e[j] = __builtin_amdgcn_fmed3f(__builtin_amdgcn_exp2f(l[j]), 0.f, 1.f);
    a[j] = relu2(u[j]);
  }
  return a * rh + e + 1e-6f;
}
DI f32x4 splat(float v) { return f32x4{v, v, v, v}; }
template <int CTRL> DI float dpp_add(float v) {
  return v + __builtin_bit_cast(float, __builtin_amdgcn_update_dpp(0, __builtin_bit_cast(int, v), CTRL, 0xf, 0xf, true));
}
DI float sum64(float v) {
  v = dpp_add<0xB1>(v);
  v = dpp_add<0x4E>(v);
  v = dpp_add<0x141>(v);
  v = dpp_add<0x140>(v);
  v = dpp_add<0x142>(v);
  return dpp_add<0x143>(v);
}
DI float dot2(unsigned a, unsigned b, float c) {
  return __builtin_amdgcn_fdot2_f32_bf16(__builtin_bit_cast(bf16x2, a), __builtin_bit_cast(bf16x2, b), c, false);
}
DI u32x4 ldtr2(unsigned a, unsigned d) { return __builtin_bit_cast(u32x4, cat(lds_tr(a), lds_tr(a + d))); }
DI void mfma_ip(f32x4& c, const u32x4& a, const u32x4& b) { asm volatile("v_mfma_f32_16x16x32_bf16 %0, %1, %2, %0" : "+v"(c) : "v"(a), "v"(b)); }
DI void mfma_z(f32x4& c, const u32x4& a, const u32x4& b) { asm volatile("v_mfma_f32_16x16x32_bf16 %0, %1, %2, 0" : "=&v"(c) : "v"(a), "v"(b)); }
template <int N> DI void acc_fence(f32x4* c) {
  asm volatile("s_nop 7" ::: "memory");
#pragma unroll
  for (int i = 0; i < N; ++i) asm volatile("" : "+v"(c[i]));
}
DI u32x4 cat2(u32x2 a, u32x2 b) { return u32x4{a[0], a[1], b[0], b[1]}; }
DI u32x2 ldtr1(unsigned a) { return __builtin_bit_cast(u32x2, lds_tr(a)); }
DI void set_m0(unsigned v) { asm volatile("s_mov_b32 m0, %0" ::"s"(v) : "memory"); }
DI void dma_go(unsigned voff, rsrc_t rs, unsigned soff) {
  asm volatile("buffer_load_dwordx4 %0, %1, %2 offen lds" ::"v"(voff), "s"(rs), "s"(soff) : "memory");
}
DI f32x4 mfma_u(u32x4 a, u32x4 b, f32x4 c) { return mfma(__builtin_bit_cast(bf16x8, a), __builtin_bit_cast(bf16x8, b), c); }
#define STAMP_DECL
#define STAMP_ARG
#define STAMP_PASS
#define STAMPX(c, i)
#define STAMP_END
#define STAMP(i) STAMPX(threadIdx.x == 0, i)

DI void poll_flags(gu32* word, bool active, unsigned tag) {
#pragma unroll 1
  for (unsigned spins = 0; spins < SPIN_CAP; ++spins) {
    unsigned v = __hip_atomic_load(word, RLX_AGENT);
    if (__all(!active || v == tag)) break;
    if (PSLEEP) __builtin_amdgcn_s_sleep(PSLEEP);
  }
  asm volatile("" ::: "memory");
}

template <int I> struct IC { static constexpr int v = I; };
template <int B, int E, class Fn> DI void static_for(Fn&& f) {
  if constexpr (B < E) { f(IC<B>{}); static_for<B + 1, E>(f); }
}

enum { J_MLP = 0, J_KV = 1, J_Q = 2, J_SPARE = 3 };

struct Dma {
  rsrc_t rsA, rsB;
  unsigned vA0, vA1, vB0, vB1, vB2;
  unsigned sA, sB, advA, advB;
};

DI unsigned a_slot(unsigned s3) { return s3 == 0 ? 126976u : (s3 - 1) * 32768u; }
#define SB __builtin_amdgcn_sched_barrier(0)

template <int T> DI unsigned bofs(const unsigned (&boff)[T], int t) { return T == 6 && t >= 3 ? boff[t - 3] + 6144 : boff[t]; }
template <int T, bool TR, bool RD, int KIND>
DI void kstep(f32x4 (&acc)[4][T], const u32x4 (&ac)[4], const u32x2 (&bc)[T][2], u32x4 (&an)[4], u32x2 (&bn)[T][2], unsigned ab,
              const unsigned (&boff)[T], unsigned bs, const Dma& d, unsigned dst) {
  constexpr int NP = KIND == 0 ? 4 : 3, ST = (4 * T - 1) / NP;
  static_for<0, 4 * T>([&](auto E) {
    constexpr int e = E.v, t = e >> 2, i = e & 3;
    const u32x4 bf = cat2(bc[t][0], bc[t][1]);
    if (TR) mfma_ip(acc[i][t], bf, ac[i]); else mfma_ip(acc[i][t], ac[i], bf);
    SB;
    if constexpr (RD) {
      if constexpr (e < 4) an[e] = lds_ld<u32x4>(ab + e * 2048);
      else if constexpr (e < 4 + 2 * T) bn[(e - 4) >> 1][e & 1] = ldtr1(bofs<T>(boff, (e - 4) >> 1) + bs + (e & 1) * 768);
    }
    constexpr int pc = e / ST;
    if constexpr (pc < NP && e % ST == ST - 2) set_m0(dst + pc * 1024);
    if constexpr (pc < NP && e % ST == ST - 1) {
      if constexpr (KIND == 0) dma_go((pc & 1) ? d.vA1 : d.vA0, d.rsA, d.sA + pc * 10240);
      else dma_go(pc == 0 ? d.vB0 : (pc == 1 ? d.vB1 : d.vB2), d.rsB, d.sB);
    }
    SB;
  });
}

struct RowStat { u32x4 xa; unsigned xb, row; };
template <int NY> DI void row_stat(RowStat& R, char* ws, unsigned tag, unsigned lane) {
  float s = 0.f, q = 0.f;
  asm volatile("s_waitcnt vmcnt(%8)\n\t"
               "v_dot2c_f32_bf16 %0, %2, %7\n\tv_dot2c_f32_bf16 %1, %2, %2\n\tv_dot2c_f32_bf16 %0, %3, %7\n\tv_dot2c_f32_bf16 %1, %3, %3\n\t"
               "v_dot2c_f32_bf16 %0, %4, %7\n\tv_dot2c_f32_bf16 %1, %4, %4\n\tv_dot2c_f32_bf16 %0, %5, %7\n\tv_dot2c_f32_bf16 %1, %5, %5\n\t"
               "v_dot2c_f32_bf16 %0, %6, %7\n\tv_dot2c_f32_bf16 %1, %6, %6\n\ts_nop 2"
               : "+v"(s), "+v"(q) : "v"(R.xa[0]), "v"(R.xa[1]), "v"(R.xa[2]), "v"(R.xa[3]), "v"(R.xb), "v"(0x3f803f80u), "i"(NY) : "memory");
  const float mean = sum64(s) * (1.0f / D);
  const float var = fmaxf(sum64(q) * (1.0f / D) - mean * mean, 0.f);
  const float sd = __builtin_amdgcn_sqrtf(rbf(rbf(var) + 1e-5f));
  if (lane == 63) __hip_atomic_store((gu64*)(ws + WS_STAT) + R.row, ((unsigned long long)tag << 32) | pk2(mean, sd), RLX_AGENT);
}

struct Gran { u32x2 g0, g1, g2; unsigned off, step; };
DI void gran_issue(Gran& G, rsrc_t rs) {
  asm volatile("buffer_load_dwordx2 %0, %3, %4, 0 offen sc1\n\tbuffer_load_dwordx2 %1, %3, %4, %5 offen sc1\n\t"
               "buffer_load_dwordx2 %2, %3, %4, %6 offen sc1"
               : "+v"(G.g0), "+v"(G.g1), "+v"(G.g2) : "v"(G.off), "s"(rs), "s"(G.step), "s"(2 * G.step) : "memory");
}

template <int T, bool TR, bool LAST>
DI void stage_body(f32x4 (&acc)[4][T], u32x4 (&a0)[4], u32x2 (&b0)[T][2], const unsigned (&aoff)[2], const unsigned (&boff)[T], Dma& d,
                   unsigned w, unsigned ac, unsigned an, unsigned ap, unsigned h0, unsigned h1, unsigned h2, Gran& G, rsrc_t rsWS, bool ld,
                   unsigned tag, unsigned tid) {
  u32x4 a1[4];
  u32x2 b1[T][2];
  kstep<T, TR, true, 0>(acc, a0, b0, a1, b1, aoff[1] + ac, boff, h1, d, ap + w * 4096u);
  if (LAST) {
  asm volatile("s_waitcnt vmcnt(7)" : "+v"(G.g0), "+v"(G.g1), "+v"(G.g2)::"memory");
#pragma unroll 1
  for (unsigned spins = 0; spins < SPIN_CAP; ++spins) {
    if (__all(G.g0[1] == tag && G.g1[1] == tag && G.g2[1] == tag)) break;
    __builtin_amdgcn_s_sleep(2);
    gran_issue(G, rsWS);
    asm volatile("s_waitcnt vmcnt(0)" : "+v"(G.g0), "+v"(G.g1), "+v"(G.g2)::"memory");
  }
  if (w < 4) {
    lds_st<float>(L_MISC + 2048 + tid * 4,
                  __builtin_bit_cast(float, G.g0[0]) + __builtin_bit_cast(float, G.g1[0]) + __builtin_bit_cast(float, G.g2[0]));
  } else {
    lds_st<float>(L_MISC + (tid - 256) * 4, -bflo(G.g0[0]));
    lds_st<float>(L_MISC + 1024 + (tid - 256) * 4, __builtin_amdgcn_rcpf(bfhi(G.g0[0])));
  }
  }
  wait_lgkm0();
  if (!LAST) wait_vm<4>();
  bar();
  if (!LAST) { if (__builtin_expect(ld, 0)) gran_issue(G, rsWS); }
  else if (ld) {
#pragma unroll
    for (int i = 0; i < 4; ++i) dma16(ac + w * 4096u + i * 1024, d.vA0, d.rsA, d.advA + i * 10240);
  }
  SB;
  const unsigned bdst = L_B + (w < 4 ? h0 : h1) + ((w & 1) * 2 + ((w >> 1) & 1)) * 3072u;
  kstep<T, TR, !LAST, 1>(acc, a1, b1, a0, b0, aoff[0] + an, boff, h2, d, bdst);
}

DI unsigned urow(unsigned pos, unsigned rot) { const unsigned u = pos + rot; return (u >= 54 ? u - 54 : u) * 40960u; }

template <int T, bool TR, class Pre>
DI void main_loop(Pre&& pre, RowStat& R, char* ws, f32x4 (&acc)[4][T], unsigned (&boff)[T], Dma& d, unsigned w, unsigned lane, unsigned tid,
                  unsigned tag, unsigned rb, unsigned ccol, rsrc_t rsB2, rsrc_t rsWS, unsigned cb, unsigned rot, bool lastA STAMP_ARG) {
  const unsigned g = lane >> 4, r = lane & 15, m = w & 3;
  unsigned aoff[2];
  aoff[0] = (64 * m + r) * 128 + ((g ^ (r >> 1)) << 4);
  aoff[1] = aoff[0] ^ 64;
  const unsigned wq = (w & 1) * 2 + ((w >> 1) & 1);
  asm volatile("" : "+v"(aoff[0]), "+v"(aoff[1]), "+v"(boff[0]), "+v"(boff[1]), "+v"(boff[2]));
  Gran G;
  G.g0 = G.g1 = G.g2 = u32x2{0u, tag + 1};
  G.off = w < 4 ? WS_CSUM + ccol * 8 : WS_STAT + (rb * 256 + tid - 256) * 8;
  G.step = w < 4 ? NCOL * 8 : 0;
#pragma unroll
  for (int i = 0; i < 4; ++i)
#pragma unroll
    for (int t = 0; t < T; ++t) asm volatile("" : "+v"(acc[i][t]));
  row_stat<7>(R, ws, tag, lane);
  {
    const unsigned bdst = L_B + (w < 4 ? 36864u : 24576u) + wq * 3072u, adst = a_slot(1) + w * 4096u;
    unsigned so = d.sB + d.advB;
    dma16(bdst, d.vB0, d.rsB, so); dma16(bdst + 1024, d.vB1, d.rsB, so); dma16(bdst + 2048, d.vB2, d.rsB, so);
    dma16(adst, d.vA0, d.rsA, d.sA + 128); dma16(adst + 1024, d.vA1, d.rsA, d.sA + 128 + 10240);
    dma16(adst + 2048, d.vA0, d.rsA, d.sA + 128 + 20480); dma16(adst + 3072, d.vA1, d.rsA, d.sA + 128 + 30720);
    pre();
    wait_vm<8>();
    bar();
    if (w >= 4) {
      so += d.advB;
      dma16(bdst + 24576, d.vB0, d.rsB, so); dma16(bdst + 24576 + 1024, d.vB1, d.rsB, so); dma16(bdst + 24576 + 2048, d.vB2, d.rsB, so);
    }
    d.sA += 256;
    d.sB = so + d.advB;
  }
  STAMP(1);
  u32x4 a0[4];
  u32x2 b0[T][2];
  {
    const unsigned ab = aoff[0] + a_slot(0);
#pragma unroll
    for (int i = 0; i < 4; ++i) a0[i] = lds_ld<u32x4>(ab + i * 2048);
#pragma unroll
    for (int t = 0; t < T; ++t) { b0[t][0] = ldtr1(bofs<T>(boff, t)); b0[t][1] = ldtr1(bofs<T>(boff, t) + 768); }
  }
  unsigned ac = a_slot(0), an = a_slot(1), ap = a_slot(2), h0 = 0, h1 = 12288, h2 = 24576;
#pragma unroll 1
  for (unsigned s = 0; s < 9; ++s) {
    if (__builtin_expect(s >= 7, 0)) {
      const unsigned v2 = (lane >> 3) * 1280 + (((lane & 7) ^ (((lane >> 4) & 3) << 1)) << 4);
      if (s == 8) { d.rsA = rsB2; d.vA0 = v2; d.vA1 = v2; d.sA = urow(w, rot) + cb * 128u; }
      if (s == 8 || w >= 4) {
        d.rsB = rsB2; d.vB0 = v2 + (3 * wq) * 10240u; d.vB1 = d.vB0 + 10240u; d.vB2 = d.vB0 + 20480u;
        d.sB = urow(w < 4 ? 19u : (s == 7 ? 16u : 22u), rot) + cb * 128u;
      }
    }
    stage_body<T, TR, false>(acc, a0, b0, aoff, boff, d, w, ac, an, ap, h0, h1, h2, G, rsWS, s == 8, tag, tid);
    d.sA += d.advA; d.sB += d.advB;
    const unsigned t = ap; ap = ac; ac = an; an = t;
    h0 = h2;
    h1 = h0 == 49152 ? 0 : h0 + 12288;
    h2 = h1 == 49152 ? 0 : h1 + 12288;
  }
  STAMP(13);
  d.sA = urow(8 + w, rot) + cb * 128u;
  d.sB = urow(w < 4 ? 25u : 28u, rot) + cb * 128u;
  d.advA = urow(31 + w, rot) + cb * 128u;
  SB;
  stage_body<T, TR, true>(acc, a0, b0, aoff, boff, d, w, a_slot(0), a_slot(1), a_slot(2), 36864, 49152, 0, G, rsWS, lastA && w < 5, tag, tid);
  acc_fence<4 * T>(&acc[0][0]);
  STAMP(14);
}

DI void st16(u32x4 v, rsrc_t rs, unsigned voff, unsigned soff) {
  __builtin_amdgcn_raw_buffer_store_b128(v, rs, voff, soff, 16);
  asm volatile("s_nop 1" ::"v"(v) : "memory");
}
DI void publish_flag(const Params& p, unsigned flag_off, unsigned tag, unsigned tid) {
  wait_vm<0>();
  bar();
  if (tid == 0) __hip_atomic_store((gu32*)(p.ws + flag_off), tag, RLX_AGENT);
}
DI void st_frag(rsrc_t rs, unsigned lane, unsigned rt, unsigned step, u32x2 lo, u32x2 hi) {
  u32x4 v = {lo[0], lo[1], hi[0], hi[1]};
  st16(v, rs, lane * 16, WS_HA + (rt * NSTEP + step) * 1024);
}
DI void shared_send(unsigned w, unsigned lane, const u32x2 (&sh)[4]) {
  const bool n = w >= 4;
  u32x4 snd = n ? u32x4{sh[0][0], sh[0][1], sh[1][0], sh[1][1]} : u32x4{sh[2][0], sh[2][1], sh[3][0], sh[3][1]};
  lds_st<u32x4>(L_SCR + w * 1024 + lane * 16, snd);
}
DI void shared_recv(rsrc_t rs, unsigned w, unsigned lane, unsigned rt0, unsigned step, const u32x2 (&sh)[4]) {
  lbar();
  u32x4 rcv = lds_ld<u32x4>(L_SCR + (w ^ 4) * 1024 + lane * 16);
  u32x2 r0 = {rcv[0], rcv[1]}, r1 = {rcv[2], rcv[3]};
  if (w >= 4) { st_frag(rs, lane, rt0 + 2, step, r0, sh[2]); st_frag(rs, lane, rt0 + 3, step, r1, sh[3]); }
  else        { st_frag(rs, lane, rt0 + 0, step, sh[0], r0); st_frag(rs, lane, rt0 + 1, step, sh[1], r1); }
}

constexpr unsigned wo_tile(unsigned v) { return (v / 5) * 10 + (v % 5 == 4 ? 4 : (v % 5 < 2 ? 2 * (v % 5) : 2 * (v % 5) + 1)); }

__global__ __launch_bounds__(512, 2) void fused_block(Params p) {
  const unsigned tid = threadIdx.x, lane = tid & 63, w = rfl(tid >> 6);
  const unsigned b = blockIdx.x, xcd = b & 7, slot = b >> 3;
  unsigned jtype, jidx;
  const unsigned rb = 4 * (xcd & 1) + (slot & 3), jq = xcd >> 1, ji = slot >> 2;
  const unsigned spi = (xcd - 4) * 4 + (slot & 3);
  if (ji == 7) { jtype = J_Q; jidx = jq; }
  else if (ji >= 5) { jtype = J_KV; jidx = 2 * jq + ji - 5; }
  else if (jq < 2) { jtype = J_MLP; jidx = 5 * jq + ji; }
  else if (ji < 4) { jtype = J_MLP; jidx = 10 + 4 * (jq - 2) + ji; }
  else { jtype = J_SPARE; jidx = 0; }
  STAMP_DECL;
  STAMP(0);
  const rsrc_t rsX = mk_rsrc(p.x, S * D * 2);

  RowStat rs_;
  rs_.row = rfl(b * 8 + w);
  asm volatile("s_nop 4\n\tbuffer_load_dwordx4 %0, %2, %4, %5 offen\n\tbuffer_load_dword %1, %3, %4, %5 offen offset:1024"
               : "=&v"(rs_.xa), "=&v"(rs_.xb) : "v"(lane * 16), "v"(lane * 4), "s"(rsX), "s"(rs_.row * 1280) : "memory");
  Dma d;
  const bool act = jtype != J_SPARE;
  {
    const unsigned mh = w & 1;
    const void* bw; unsigned pitch, nch, colb;
    if (jtype == J_MLP) { bw = mh ? p.wu : p.wg; pitch = 3456; colb = 192 * jidx; nch = 12; }
    else if (jtype == J_KV) { bw = p.wqkv; pitch = 3840; colb = 1280 + 1280 * mh + 160 * jidx; nch = 10; }
    else { bw = p.wqkv; pitch = 3840; colb = 320 * jidx + 160 * mh; nch = 10; }
    d.rsA = rsX; d.rsB = mk_rsrc(bw, D * pitch);
    d.vA0 = (lane >> 3) * 1280 + (((lane & 7) ^ (lane >> 4)) << 4); d.vA1 = d.vA0 ^ 64;
    d.sA = (rb * 256 + 32 * w) * 1280; d.advA = 128;
    asm volatile("s_nop 4" ::: "memory");
    if (act) {
      const unsigned adst = a_slot(0) + w * 4096u;
      dma16(adst, d.vA0, d.rsA, d.sA); dma16(adst + 1024, d.vA1, d.rsA, d.sA + 10240);
      dma16(adst + 2048, d.vA0, d.rsA, d.sA + 20480); dma16(adst + 3072, d.vA1, d.rsA, d.sA + 30720);
    }
    unsigned vb[3];
#pragma unroll
    for (int i = 0; i < 3; ++i) {
      const unsigned c = 64 * i + lane, row = c / 12, pos = c % 12;
      unsigned ch = pos ^ (((row >> 3) & 1) << 1);
      if (ch >= nch) ch -= 2;
      vb[i] = row * pitch + ch * 16;
    }
    d.vB0 = vb[0]; d.vB1 = vb[1]; d.vB2 = vb[2];
    d.sB = (16 * ((w >> 1) & 1) + (w < 4 ? 32 : 0)) * pitch + colb; d.advB = 64 * pitch;
    const unsigned bdst = L_B + (w < 4 ? 12288u : 0u) + ((w & 1) * 2 + ((w >> 1) & 1)) * 3072u;
    if (act) { dma16(bdst, d.vB0, d.rsB, d.sB); dma16(bdst + 1024, d.vB1, d.rsB, d.sB); dma16(bdst + 2048, d.vB2, d.rsB, d.sB); }
  }
  STAMP(10);
  if (tid == 0) lds_st<unsigned>(L_GO, 0u);
  unsigned tag;
  asm volatile("s_load_dword %0, %1, 0x0\n\ts_waitcnt lgkmcnt(0)" : "=s"(tag) : "s"(p.ws) : "memory");
  tag += 1;

  const unsigned t2 = 28 * xcd + slot;
  const bool has2 = slot < 28 && t2 < 220;
  const unsigned G = t2 / 10, cb = t2 % 10;
  const unsigned rot = ROTM == 18 ? 2 + 3 * ((9 * cb / 5 + 6 * (G % 3)) % 18) : 2 + (54 / ROTM) * (cb % ROTM);
  const unsigned g = lane >> 4, r = lane & 15, q4 = r >> 2, p4 = lane & 3;
  const rsrc_t rsWd = mk_rsrc(p.wd, has2 ? F * D * 2 : 0);
  const rsrc_t rsWS = mk_rsrc(p.ws, WS_END);
  const unsigned v2 = (lane >> 3) * 1280 + (((lane & 7) ^ (((lane >> 4) & 3) << 1)) << 4);
  const unsigned m = w & 3, n = w >> 2;
  const unsigned rt0 = rb * 16 + m * 4;
  const unsigned brow = L_B + (8 * g + q4) * 192;
  const unsigned bx = (g & 1) << 5;

  if (jtype == J_MLP) {
    f32x4 acc[4][6] = {};
    unsigned boff[6];
#pragma unroll
    for (int t = 0; t < 3; ++t) {
      const unsigned gt = n ? (t == 2 ? 3 : 4 + t) : t;
      boff[t] = brow + ((32 * gt + 8 * p4) ^ bx); boff[t + 3] = boff[t] + 6144;
    }
    const unsigned ccol = tid < 96 ? 1920 + 96 * jidx + tid : 3648 + 96 * jidx + (tid < 192 ? tid - 96 : 0);
    main_loop<6, true>([] {}, rs_, p.ws, acc, boff, d, w, lane, tid, tag, rb, ccol, rsWd, rsWS, cb, rot, true STAMP_PASS);
    float mm[4];
    f32x4 cg[3], cu[3];
#pragma unroll
    for (int i = 0; i < 4; ++i) mm[i] = lds_ld<float>(L_MISC + (64 * m + 16 * i + r) * 4);
#pragma unroll
    for (int t = 0; t < 3; ++t) {
      const unsigned gt = n ? (t == 2 ? 3 : 4 + t) : t;
      cg[t] = lds_ld<f32x4>(L_MISC + 2048 + (16 * gt + 4 * g) * 4); cu[t] = lds_ld<f32x4>(L_MISC + 2048 + (96 + 16 * gt + 4 * g) * 4);
    }
    auto hval = [&](int i, int t) {
      f32x4 gg = acc[i][t] + mm[i] * cg[t];
      const f32x4 uu = acc[i][t + 3] + mm[i] * cu[t];
#pragma unroll
      for (int j = 0; j < 4; ++j) gg[j] = relu2(gg[j]);
      return pk4(gg * uu);
    };
#pragma unroll
    for (int i = 0; i < 4; ++i) { st_frag(rsWS, lane, rt0 + i, 3 * jidx + 2 * n, hval(i, 0), hval(i, 1)); SB; }
    const u32x2 sh[4] = {hval(0, 2), hval(1, 2), hval(2, 2), hval(3, 2)};
    shared_send(w, lane, sh);
    shared_recv(rsWS, w, lane, rt0, 3 * jidx + 1, sh);
    STAMP(15);
    publish_flag(p, WS_FLAGH + rb * 128 + jidx * 4, tag, tid);
    STAMP(3);
  } else if (jtype == J_KV) {
    f32x4 acc[4][5] = {};
    unsigned boff[5];
    boff[0] = brow + ((80 * n + 8 * p4) ^ bx); boff[1] = boff[0] + 6144;
    boff[2] = brow + ((80 * n + 40 + 8 * p4) ^ bx); boff[3] = boff[2] + 6144;
    boff[4] = brow + ((80 * n + 32 + 40 * (p4 >> 1)) ^ bx) + (p4 & 1) * 6144;
    const unsigned kb = 126976u + m * 6720 + n * 3360, dump = L_MISC - 2048 + lane * 16;
    const unsigned ea = (q4 == 1 || r == 0) ? kb + (r == 0 ? 20 : 12 + r) * 80 : 0u, eb = (q4 == 3 || r == 0) ? kb + 1680 + (r == 0 ? 20 : 4 + r) * 80 : 0u;
    unsigned ka[6] = {kb + r * 80 + g * 16, ea ? ea + g * 16 : dump, eb ? eb + g * 16 : dump, g == 0 ? kb + r * 80 + 64 : dump,
                      g == 2 ? kb + 1680 + r * 80 + 64 : dump, (g == 0 && ea) ? ea + 64 : ((g == 2 && eb) ? eb + 64 : dump)};
    auto pin = [&] { asm volatile("" : "+v"(ka[0]), "+v"(ka[1]), "+v"(ka[2]), "+v"(ka[3]), "+v"(ka[4]), "+v"(ka[5])); };
    const unsigned ccol = tid < 80 ? 640 + 80 * jidx + tid : 1280 + 80 * jidx + (tid < 160 ? tid - 80 : 0);
    main_loop<5, false>(pin, rs_, p.ws, acc, boff, d, w, lane, tid, tag, rb, ccol, rsWd, rsWS, cb, rot, false STAMP_PASS);
    float cs[5];
    cs[0] = lds_ld<float>(L_MISC + 2048 + (40 * n + r) * 4);
    cs[1] = lds_ld<float>(L_MISC + 2048 + (80 + 40 * n + r) * 4);
    cs[2] = lds_ld<float>(L_MISC + 2048 + (40 * n + 20 + r) * 4);
    cs[3] = lds_ld<float>(L_MISC + 2048 + (80 + 40 * n + 20 + r) * 4);
    cs[4] = lds_ld<float>(L_MISC + 2048 + ((q4 & 1) * 80 + 40 * n + 16 + 20 * (q4 >> 1) + (r & 3)) * 4);
    u32x2 pk[4][3], vv[4][3];
    f32x4 mm4[4], rr4[4];
#pragma unroll
    for (int i = 0; i < 4; ++i) {
      mm4[i] = lds_ld<f32x4>(L_MISC + (64 * m + 16 * i + 4 * g) * 4); rr4[i] = lds_ld<f32x4>(L_MISC + 1024 + (64 * m + 16 * i + 4 * g) * 4);
    }
    SB;
#pragma unroll
    for (int i = 0; i < 4; ++i) {
      const f32x4 mm = mm4[i], rr = rr4[i];
      const f32x4 rl = rr * 1.4426950408889634f, rh = rr * 0.5f;
      f32x4 val[5];
#pragma unroll
      for (int t = 0; t < 5; ++t) val[t] = acc[i][t] + mm * cs[t];
#pragma unroll
      for (int t = 0; t < 3; ++t) pk[i][t] = pk4(phi4(val[2 * t], rl, rh));
      vv[i][0] = pk4(val[1] * rr); vv[i][1] = pk4(val[3] * rr);
      const u32x2 v4 = pk4(val[4] * rr);
      vv[i][2] = (q4 & 1) ? v4 : u32x2{0x3f803f80u, 0x3f803f80u};
    }
    STAMP(25);
    f32x4 P[7] = {};
#pragma unroll
    for (int i = 0; i < 4; i += 2) {
      const bf16x8 k0 = frag(pk[i][0], pk[i + 1][0]), k2 = frag(pk[i][1], pk[i + 1][1]), k4 = frag(pk[i][2], pk[i + 1][2]);
      const bf16x8 v1 = frag(vv[i][0], vv[i + 1][0]), v3 = frag(vv[i][1], vv[i + 1][1]), v4 = frag(vv[i][2], vv[i + 1][2]);
      P[0] = mfma(k0, v1, P[0]); P[1] = mfma(k0, v4, P[1]); P[2] = mfma(k4, v1, P[2]);
      P[3] = mfma(k2, v3, P[3]); P[4] = mfma(k2, v4, P[4]); P[5] = mfma(k4, v3, P[5]); P[6] = mfma(k4, v4, P[6]);
    }
    lds_st<f32x4>(ka[0], P[0]); lds_st<f32x4>(ka[0] + 1680, P[3]); lds_st<f32x4>(ka[1], P[1]); lds_st<f32x4>(ka[2], P[4]);
    lds_st<f32x4>(ka[3], P[2]); lds_st<f32x4>(ka[4], P[5]); lds_st<f32x4>(ka[5], P[6]);
    lbar();
    if (tid < 210) {
      const unsigned a = 126976u + tid * 32;
      const f32x4 t0 = (lds_ld<f32x4>(a) + lds_ld<f32x4>(a + 6720)) + (lds_ld<f32x4>(a + 13440) + lds_ld<f32x4>(a + 20160));
      const f32x4 t1 = (lds_ld<f32x4>(a + 16) + lds_ld<f32x4>(a + 6736)) + (lds_ld<f32x4>(a + 13456) + lds_ld<f32x4>(a + 20176));
      st16(cat2(pk4(t0), pk4(t1)), rsWS, tid * 16, WS_KVP + (rb * 32 + 4 * jidx) * 840);
    }
    wait_lgkm0();
    STAMP(15);
    publish_flag(p, WS_FLAGKV + (jidx >> 1) * 128 + ((jidx & 1) * 8 + rb) * 4, tag, tid);
    STAMP(3);
  } else if (jtype == J_Q) {
    f32x4 acc[4][5] = {};
    unsigned boff[5];
#pragma unroll
    for (int t = 0; t < 5; ++t) boff[t] = brow + n * 6144 + ((32 * t + 8 * p4) ^ bx);
    const unsigned ccol = 160 * jidx + (tid < 160 ? tid : 0);
    main_loop<5, true>([] {}, rs_, p.ws, acc, boff, d, w, lane, tid, tag, rb, ccol, rsWd, rsWS, cb, rot, false STAMP_PASS);
    u32x2 pq[4][5];
    {
      float mm[4], rr[4];
      f32x4 c[5];
#pragma unroll
      for (int i = 0; i < 4; ++i) { mm[i] = lds_ld<float>(L_MISC + (64 * m + 16 * i + r) * 4); rr[i] = lds_ld<float>(L_MISC + 1024 + (64 * m + 16 * i + r) * 4); }
#pragma unroll
      for (int t = 0; t < 5; ++t) c[t] = lds_ld<f32x4>(L_MISC + 2048 + (80 * n + 16 * t + 4 * g) * 4);
      SB;
#pragma unroll
      for (int i = 0; i < 4; ++i)
#pragma unroll
        for (int t = 0; t < 5; ++t) pq[i][t] = pk4(phi4(acc[i][t] + mm[i] * c[t], splat(rr[i] * 1.4426950408889634f), splat(rr[i] * 0.5f)));
#pragma unroll
      for (int i = 0; i < 4; ++i) asm volatile("" : "+v"(pq[i][0]), "+v"(pq[i][1]), "+v"(pq[i][2]), "+v"(pq[i][3]), "+v"(pq[i][4]));
    }
    constexpr unsigned KP = 208, KQ = 84 * KP;
    unsigned wa[2];
#pragma unroll
    for (int k = 0; k < 2; ++k) {
      const unsigned qd = 2 * (tid < 420 ? tid : 0) + k, hd = qd / 105, e = (qd % 105) / 5, dq = qd % 5, hl = hd & 3;
      wa[k] = (hd >> 2) * KQ + (e < 20 ? hl * 20 + e : 80 + hl) * KP + hl * 40 + dq * 8;
    }
    unsigned fk = n * KQ + r * KP + g * 8, fz = n * KQ + (80 + (r & 3)) * KP + g * 8;
    asm volatile("" : "+v"(wa[0]), "+v"(wa[1]), "+v"(fk), "+v"(fz));
    STAMP(25);
    wait_vm<0>();
    bar();
#pragma unroll
    for (int k = 0; k < 5; ++k) lds_st<u32x4>(k * 8192 + tid * 16, u32x4{0u, 0u, 0u, 0u});
    if (w == 0) poll_flags((gu32*)(p.ws + WS_FLAGKV + jidx * 128) + (lane & 15), true, tag);
    lbar();
    STAMP(4);
    if (w < 7) {
      u32x4 v[8];
#pragma unroll
      for (int j = 0; j < 8; ++j)
        v[j] = __builtin_amdgcn_raw_buffer_load_b128(rsWS, (tid < 420 ? tid : 0) * 16, WS_KVP + (j * 32 + 8 * jidx) * 840, 16);
      SB;
      f32x4 t0 = {0.f, 0.f, 0.f, 0.f}, t1 = t0;
#pragma unroll
      for (int j = 0; j < 8; ++j) {
        t0 += f32x4{bflo(v[j][0]), bfhi(v[j][0]), bflo(v[j][1]), bfhi(v[j][1])};
        t1 += f32x4{bflo(v[j][2]), bfhi(v[j][2]), bflo(v[j][3]), bfhi(v[j][3])};
      }
      lds_st<u32x2>(wa[0], pk4(t0)); lds_st<u32x2>(wa[1], pk4(t1));
    }
    lbar();
    STAMP(16);
    bf16x8 A[12];
#pragma unroll
    for (int P = 0; P < 3; ++P) A[P] = frag(lds_ld<u32x2>(fz + P * 64), lds_ld<u32x2>(fz + P * 64 + 32));
#pragma unroll
    for (int u = 0; u < 5; ++u) {
      const unsigned a0 = fk + u * 16 * KP + (u < 3 ? 0 : 64);
      A[3 + u] = frag(lds_ld<u32x2>(a0), lds_ld<u32x2>(a0 + 32));
      if (u > 0) A[7 + u] = frag(lds_ld<u32x2>(a0 + 64), lds_ld<u32x2>(a0 + 96));
    }
    u32x2 sh[4];
#pragma unroll
    for (int i = 0; i < 4; ++i) {
      const bf16x8 B[3] = {frag(pq[i][0], pq[i][1]), frag(pq[i][2], pq[i][3]), frag(pq[i][4], u32x2{0u, 0u})};
      const f32x4 zero = {0.f, 0.f, 0.f, 0.f};
      const f32x4 den = mfma(A[2], B[2], mfma(A[1], B[1], mfma(A[0], B[0], zero))) + 1e-6f;
      f32x4 num[5];
#pragma unroll
      for (int u = 0; u < 5; ++u) num[u] = mfma(A[3 + u], B[u < 3 ? 0 : 1], zero);
#pragma unroll
      for (int u = 1; u < 5; ++u) num[u] = mfma(A[7 + u], B[u < 3 ? 1 : 2], num[u]);
      float rc[4];
#pragma unroll
      for (int j = 0; j < 4; ++j) rc[j] = __builtin_amdgcn_rcpf(den[j]);
      u32x2 o[5];
#pragma unroll
      for (int u = 0; u < 5; ++u) {
        const float rr = u == 0 ? rc[0] : (u == 4 ? rc[3] : (g < (unsigned)u ? rc[u - 1] : rc[u]));
        o[u] = pk4(num[u] * rr);
      }
      st_frag(rsWS, lane, rt0 + i, 54 + 5 * jidx + 2 * n, o[0], o[1]);
      st_frag(rsWS, lane, rt0 + i, 54 + 5 * jidx + 2 * n + 1, o[2], o[3]);
      sh[i] = o[4];
    }
    shared_send(w, lane, sh);
    shared_recv(rsWS, w, lane, rt0, 54 + 5 * jidx + 4, sh);
    STAMP(15);
    publish_flag(p, WS_FLAGO + rb * 128 + jidx * 4, tag, tid);
    STAMP(3);
    if (b == 224) {
      poll_flags((gu32*)(p.ws + WS_FLAGH) + tid, (tid & 31) < (tid < 256 ? 18u : 4u), tag);
      bar();
      if (tid == 0) __hip_atomic_store((gu32*)(p.ws + WS_EPOCH), tag, RLX_AGENT);
    }
  } else {
    const unsigned gw = spi * 8 + w;
    unsigned k10 = 0x00003f80u, k01 = 0x3f800000u;
    asm volatile("" : "+v"(k10), "+v"(k01));
    auto cs_src = [&](unsigned u, rsrc_t& rs, unsigned& cv, unsigned& st) {
      const unsigned cline = u / 3, third = u % 3;
      const void* cw = cline < 30 ? p.wqkv : (cline < 57 ? p.wg : p.wu);
      const unsigned cpitch = cline < 30 ? 3840u : 3456u;
      const unsigned clocal = cline < 30 ? cline : (cline < 57 ? cline - 30 : cline - 57);
      rs = mk_rsrc(cw, gw < 126 ? D * cpitch : 0);
      cv = (third * 216 + (lane >> 3)) * cpitch + clocal * 128 + (lane & 7) * 16;
      st = 8 * cpitch;
    };
    auto cs_publish = [&](unsigned u, float (&cs)[8]) {
#pragma unroll
      for (int k = 0; k < 4; ++k) { const bool hi = lane & 32; const float o = __shfl_xor(hi ? cs[k] : cs[k + 4], 32); cs[k] = (hi ? cs[k + 4] : cs[k]) + o; }
#pragma unroll
      for (int k = 0; k < 2; ++k) { const bool hi = lane & 16; const float o = __shfl_xor(hi ? cs[k] : cs[k + 2], 16); cs[k] = (hi ? cs[k + 2] : cs[k]) + o; }
      { const bool hi = lane & 8; const float o = __shfl_xor(hi ? cs[0] : cs[1], 8); cs[0] = (hi ? cs[1] : cs[0]) + o; }
      if (gw < 126)
        __hip_atomic_store((gu64*)(p.ws + WS_CSUM) + (u % 3) * NCOL + (u / 3) * 64 + (lane & 7) * 8 + (lane >> 3),
                           ((unsigned long long)tag << 32) | __builtin_bit_cast(unsigned, cs[0]), RLX_AGENT);
    };
    row_stat<0>(rs_, p.ws, tag, lane);
    u32x4 c[27];
    rsrc_t rs0, rs1; unsigned cv0, cv1, st0, st1;
    cs_src(gw, rs0, cv0, st0); cs_src(gw + 126, rs1, cv1, st1);
#pragma unroll
    for (int j = 0; j < 27; ++j) c[j] = __builtin_amdgcn_raw_buffer_load_b128(rs0, cv0 + j * st0, 0, 0);
    bar();
    float cs[8] = {};
#pragma unroll
    for (int j = 0; j < 27; ++j) {
#pragma unroll
      for (int k = 0; k < 4; ++k) { cs[2 * k] = dot2(c[j][k], k10, cs[2 * k]); cs[2 * k + 1] = dot2(c[j][k], k01, cs[2 * k + 1]); }
      if (j < 8) c[j] = __builtin_amdgcn_raw_buffer_load_b128(rs1, cv1 + j * st1, 0, 0);
      SB;
    }
    cs_publish(gw, cs);
    SB;
#pragma unroll
    for (int j = 8; j < 27; ++j) c[j] = __builtin_amdgcn_raw_buffer_load_b128(rs1, cv1 + j * st1, 0, 0);
    SB;
    STAMP(11);
#pragma unroll
    for (int k = 0; k < 8; ++k) cs[k] = 0.f;
#pragma unroll
    for (int j = 0; j < 27; ++j)
#pragma unroll
      for (int k = 0; k < 4; ++k) { cs[2 * k] = dot2(c[j][k], k10, cs[2 * k]); cs[2 * k + 1] = dot2(c[j][k], k01, cs[2 * k + 1]); }
    cs_publish(gw + 126, cs);
    STAMP(12);
#pragma unroll 1
    for (unsigned i = w; i < 144; i += 8) dma16(i * 1024, v2, rsWd, urow(i >> 2, rot) + (i & 3) * 10240u + cb * 128u);
    wait_vm<0>();
    bar();
  }
  if (!has2) { STAMP_END; return; }

  const unsigned rbA = (G * 6) >> 4, rbB = (G * 6 + 5 > 127 ? 127 : G * 6 + 5) >> 4;
  if (w >= 4) {
    __builtin_amdgcn_s_setprio(HPRIO);
    const rsrc_t rsWo = mk_rsrc(p.wo, D * D * 2);
    const unsigned lw = w - 4, ld0 = lw * 1024;
    const unsigned wo1 = (lw >> 1) * 20480u + (lw & 1) * 10240u + cb * 128u, wo5 = wo1 + (lw >> 1) * 81920u;
    unsigned sd = urow(31, rot) + lw * 10240u + cb * 128u;
    const unsigned sdlim = 54 * 40960u;
    auto src = [&](int u) {
      if (u >= 54) return wo_tile(u - 54) * 20480u + ((u - 54) % 5 == 4 ? wo5 : wo1);
      const unsigned o = sd;
      sd += 40960u; if (sd >= sdlim) sd -= sdlim;
      return o;
    };
    __builtin_amdgcn_s_sleep(HSLEEP);
    if (jtype == J_KV) {
#pragma unroll
      for (int u = 31; u < 36; ++u) dma16(u * 4096 + ld0, v2, rsWd, src(u));
    }
    sd = urow(36, rot) + lw * 10240u + cb * 128u;
    u32x4 wr[36];
#pragma unroll
    for (int u = 36; u < HSPLIT; ++u) { wr[u - 36] = __builtin_amdgcn_raw_buffer_load_b128(u < 54 ? rsWd : rsWo, v2, src(u), 0); SB; }
    STAMP(8);
    gu32* fo = (gu32*)(p.ws + WS_FLAGO + (lane < 32 ? rbA : rbB) * 128) + (lane & 31);
    unsigned la[3], wb[3], xo[3], st[3];
    u32x4 xr[3];
    const unsigned ch = lane & 7;
#pragma unroll
    for (int i = 0; i < 3; ++i) {
      const unsigned row = lw * 24 + i * 8 + (lane >> 3), rl = row >> 4, rr = row & 15;
      la[i] = (rl >= 3 ? 24576u + (rl - 3) * 4096u : rl * 4096u) + (ch >> 1) * 1024 + (ch & 1) * 512 + ((rr + 2 * ch) & 15) * 16;
      xo[i] = (G * 96 + row) * 1280 + cb * 128 + ch * 16;
      wb[i] = ld0 + lane * 16 + i * 49152;
      asm volatile("" : "+v"(la[i]), "+v"(wb[i]), "+v"(xo[i]));
    }
    float ri[3], rl[3];
    f32x4 xf[3][2], dd[3][2];
#pragma unroll
    for (int k = 1; k <= 6; ++k) {
      if (k == O_BAR && w == 7) poll_flags(fo, (lane & 31) < 4, tag);
      if (k == 6) {
#pragma unroll
        for (int h = 0; h < 3; ++h) {
          ri[h] = 2.0f * bfhi(st[h]) * bfhi(st[h]);
          rl[h] = -1.4426950408889634f * __builtin_amdgcn_rcpf(ri[h]);
          xf[h][0] = f32x4{bflo(xr[h][0]), bfhi(xr[h][0]), bflo(xr[h][1]), bfhi(xr[h][1])};
          xf[h][1] = f32x4{bflo(xr[h][2]), bfhi(xr[h][2]), bflo(xr[h][3]), bfhi(xr[h][3])};
          asm volatile("" : "+v"(ri[h]), "+v"(rl[h]), "+v"(xf[h][0]), "+v"(xf[h][1]));
        }
      }
      lbar();
      if (k <= 4) {
#pragma unroll
        for (int u = 9 * (k - 1) + ((k - 1) & 1); u < 9 * k + (k & 1); ++u) {
          lds_st<u32x4>(wb[u / 12] + (u % 12) * 4096, wr[u]);
          if (u + 36 + WLEAD >= HSPLIT && u + 36 + WLEAD < 72) wr[u + WLEAD] = __builtin_amdgcn_raw_buffer_load_b128(rsWo, v2, src(u + 36 + WLEAD), 0);
          if (u + WLEAD == 36 || u + WLEAD == 37) dma16((u + WLEAD) * 4096 + ld0, v2, rsWo, src(u + 36 + WLEAD));
          SB;
          if (WSLEEP) __builtin_amdgcn_s_sleep(WSLEEP);
          SB;
        }
      }
      if (k == 4) {
#pragma unroll
        for (int h = 0; h < 3; ++h) {
          const unsigned row = G * 96 + lw * 24 + h * 8 + (lane >> 3);
          st[h] = __builtin_amdgcn_raw_buffer_load_b32(rsWS, WS_STAT + (row < S ? row : 0) * 8, 0, 16);
          xr[h] = __builtin_amdgcn_raw_buffer_load_b128(rsX, xo[h], 0, 0);
        }
      }
    }
#pragma unroll
    for (int i = 0; i < 3; ++i) {
      const unsigned lb = la[i] + (la[i] >= 28672u ? 126976u : 12288u);
      dd[i][0] = lds_ld<f32x4>(la[i]) + lds_ld<f32x4>(lb);
      dd[i][1] = lds_ld<f32x4>(la[i] + 256) + lds_ld<f32x4>(lb + 256);
    }
    lbar();
    __builtin_amdgcn_s_setprio(GPRIO);
#pragma unroll
    for (int i = 0; i < 6; ++i) {
      const f32x4 l = dd[i >> 1][i & 1] * rl[i >> 1];
      f32x4 e;
#pragma unroll
      for (int j = 0; j < 4; ++j) e[j] = __builtin_amdgcn_exp2f(l[j]);
      e = e * ri[i >> 1] + ri[i >> 1];
#pragma unroll
      for (int j = 0; j < 4; ++j) e[j] = __builtin_amdgcn_rcpf(e[j]);
      dd[i >> 1][i & 1] *= e;
      asm volatile("" : "+v"(dd[i >> 1][i & 1]));
    }
    const rsrc_t rsO = mk_rsrc(p.out, S * D * 2);
    STAMP(9);
    bar();
    f32x4 ss[3][2];
#pragma unroll
    for (int i = 0; i < 3; ++i) {
      ss[i][0] = lds_ld<f32x4>(la[i]) + lds_ld<f32x4>(la[i] + 12288);
      ss[i][1] = lds_ld<f32x4>(la[i] + 256) + lds_ld<f32x4>(la[i] + 12288 + 256);
    }
#pragma unroll
    for (int i = 0; i < 3; ++i)
      __builtin_amdgcn_raw_buffer_store_b128(cat2(pk4(xf[i][0] + ss[i][0] * dd[i][0]), pk4(xf[i][1] + ss[i][1] * dd[i][1])), rsO, xo[i], 0, 0);
    STAMP(5);
    STAMP_END;
    return;
  }
  {
    const unsigned kh = w & 1, rt = G * 6 + 3 * (w >> 1);
    const rsrc_t rsH = mk_rsrc(p.ws + WS_HA + (rt < 128 ? rt : 0) * (NSTEP * 1024), rt < 128 ? (rt > 125 ? 128 - rt : 3) * (NSTEP * 1024) : 0);
    unsigned tb[3][4];
#pragma unroll
    for (int t = 0; t < 4; ++t) {
      tb[0][t] = (4 * g + q4) * 128 + ((t ^ ((2 * g + (q4 >> 1)) & 3)) << 5) + 8 * p4 + kh * 4096;
      tb[1][t] = tb[0][t] + 49152; tb[2][t] = tb[0][t] + 98304;
      asm volatile("" : "+v"(tb[0][t]), "+v"(tb[1][t]), "+v"(tb[2][t]));
    }
    auto rd1 = [&](int i, int t, int hf) {
      const int sl = i == 36 ? 36 : (2 * i) % 36, bi = sl / 12 > 2 ? 2 : sl / 12;
      return __builtin_bit_cast(u32x2, lds_tr(tb[bi][t] + (sl - 12 * bi) * 4096 + hf * 2048));
    };
    constexpr int NW = 12, W0 = 5;
    constexpr int OI = O_BAR == 4 ? 17 : 22;
    f32x4 acc[3][4];
    u32x4 a[NW][3];
    u32x2 bw[2][4][2];
    unsigned va[3];
#pragma unroll
    for (int h = 0; h < 3; ++h) va[h] = lane * 16 + h * NSTEP * 1024;
    auto put = [&](bool hi) {
      acc_fence<12>(&acc[0][0]);
#pragma unroll
      for (int t = 0; t < 4; ++t) {
        const unsigned xa = w * 12288u + t * 1024 + g * 256 + ((r + 4 * t + (g & 2)) & 15) * 16, xb = xa + (hi && w == 3 ? 114688u : 0u);
        lds_st<f32x4>(xa, acc[0][t]); lds_st<f32x4>(xb + 4096, acc[1][t]); lds_st<f32x4>(xb + 8192, acc[2][t]);
      }
      wait_lgkm0();
    };
    unsigned so = (rot + kh) * 1024;
    auto lda = [&](auto I, auto H) {
      constexpr int i = I.v, h = H.v;
      if constexpr (i < 27) {
        if (h == 0 && so >= 54 * 1024) so -= 54 * 1024;
        a[i % NW][h] = __builtin_amdgcn_raw_buffer_load_b128(rsH, va[h], so, 16);
        if (h == 2) so += 2048;
      } else {
        a[i % NW][h] = __builtin_amdgcn_raw_buffer_load_b128(rsH, va[h], (2 * i + kh) * 1024, 16);
      }
      SB;
    };
    if (w == 0) {
      poll_flags((gu32*)(p.ws + WS_FLAGH + (lane < 32 ? rbA : rbB) * 128) + (lane & 31), (lane & 31) < 18, tag);
      __hip_atomic_store((LAS unsigned*)((LAS char*)smem + L_GO), 1u, __ATOMIC_RELAXED, __HIP_MEMORY_SCOPE_WORKGROUP);
    } else {
#pragma unroll 1
      for (unsigned spins = 0; spins < (SPIN_CAP << 4); ++spins) {
        if (rfl(__hip_atomic_load((LAS unsigned*)((LAS char*)smem + L_GO), __ATOMIC_RELAXED, __HIP_MEMORY_SCOPE_WORKGROUP))) break;
        __builtin_amdgcn_s_sleep(1);
      }
    }
    asm volatile("" ::: "memory");
    STAMP(8);
    static_for<0, W0>([&](auto I) { lda(I, IC<0>{}); lda(I, IC<1>{}); lda(I, IC<2>{}); });
#pragma unroll
    for (int t = 0; t < 4; ++t) { bw[0][t][0] = rd1(0, t, 0); bw[0][t][1] = rd1(0, t, 1); }
    static_for<0, 37>([&](auto I) {
      constexpr int i = I.v;
      constexpr int lim = i + NW - 1;
      constexpr int nxt = i <= OI ? (W0 + 2 * i < lim ? W0 + 2 * i : lim) : (27 + 2 * (i - OI - 1) < lim ? 27 + 2 * (i - OI - 1) : lim);
      constexpr int end = i <= OI ? 27 : 37;
      constexpr int l0 = nxt < end ? nxt : -1, l1 = (nxt + 1 <= lim && nxt + 1 < end) ? nxt + 1 : -1;
      SB;
      static_for<0, 12>([&](auto E) {
        constexpr int e = E.v, t = e / 3, h = e % 3;
        const u32x4 bf = cat2(bw[i & 1][t][0], bw[i & 1][t][1]);
        if constexpr (i == 0 || i == 27) mfma_z(acc[2 - h][t], bf, a[i % NW][2 - h]); else mfma_ip(acc[2 - h][t], bf, a[i % NW][2 - h]);
        SB;
        if constexpr (i < 36 && h < 2) bw[(i + 1) & 1][t][h] = rd1(i + 1, t, h);
        if constexpr (l1 < 0) { if constexpr (h == 2 && t < 3 && l0 >= 0) lda(IC<l0>{}, IC<t>{}); }
        else if constexpr (e & 1) lda(IC<(e < 6 ? l0 : l1)>{}, IC<(e >> 1) % 3>{});
        SB;
      });
      if constexpr (i == 26) put(true);
      if constexpr (i == XBAR) bar();
      if constexpr (i < 27 && (i % 9 == 4 || i % 9 == 8)) {
        bar();
        STAMP(17 + (2 * i) / 9);
      }
    });
    STAMP(24);
    put(false);
    bar();
    STAMP(5);
    STAMP_END;
  }
}

__global__ __launch_bounds__(512) void warmk(Params p) {
  const unsigned long long t0 = __builtin_amdgcn_s_memrealtime();
  if (threadIdx.x < 64) asm volatile("buffer_wbl2 sc0 sc1\n\ts_waitcnt vmcnt(0)" ::: "memory");
  while (__builtin_amdgcn_s_memrealtime() - t0 < 2000) __builtin_amdgcn_s_sleep(4);
}

extern "C" void kernel_launch(void* const* d_in, const int* in_sizes, int n_in, void* d_out, int out_size, void* d_ws,
                              size_t ws_size, hipStream_t stream) {
  Params p;
  p.x = d_in[0]; p.wqkv = d_in[1]; p.wo = d_in[2]; p.wg = d_in[4]; p.wu = d_in[5]; p.wd = d_in[6];
  p.out = d_out; p.ws = (char*)d_ws;
  warmk<<<256, 512, 0, stream>>>(p);
  fused_block<<<256, 512, 0, stream>>>(p);
}
```

```cpp
#include <hip/hip_runtime.h>
#include <stdint.h>

typedef __attribute__((ext_vector_type(8))) __bf16 bf16x8;
typedef __attribute__((ext_vector_type(4))) __bf16 bf16x4;
typedef __attribute__((ext_vector_type(2))) __bf16 bf16x2;
typedef __attribute__((ext_vector_type(4))) float f32x4;
typedef __attribute__((ext_vector_type(2))) float f32x2;
typedef __attribute__((ext_vector_type(4))) unsigned u32x4;
typedef __attribute__((ext_vector_type(2))) unsigned u32x2;
typedef __amdgpu_buffer_rsrc_t rsrc_t;
#define LAS __attribute__((address_space(3)))
#define GAS __attribute__((address_space(1)))
#define DI __device__ __forceinline__
#define RLX_AGENT __ATOMIC_RELAXED, __HIP_MEMORY_SCOPE_AGENT
typedef GAS unsigned gu32;
typedef GAS unsigned long long gu64;

constexpr int S = 2048, D = 640, F = 1728;
constexpr int NCOL = 5376;
constexpr int NSTEP = 74;
constexpr unsigned WS_EPOCH = 0;
#define FS 128
constexpr unsigned WS_FLAGH = 768u << 10;
constexpr unsigned WS_FLAGO = WS_FLAGH + 8 * 32 * FS;
constexpr unsigned WS_FLAGKV = WS_FLAGO + 8 * 32 * FS;
constexpr unsigned WS_STAT = 4096;
constexpr unsigned WS_CSUM = WS_STAT + S * 8;
constexpr unsigned WS_KVP = WS_CSUM + 3 * NCOL * 8;
constexpr unsigned WS_HA = 1u << 20;
constexpr unsigned WS_DBG = 12u << 20;
constexpr unsigned WS_END = 13u << 20;
constexpr unsigned L_B = 65536;
constexpr unsigned L_SCR = 147456;
constexpr unsigned L_MISC = 159744;
constexpr unsigned L_GO = L_MISC + 3072;
constexpr unsigned SPIN_CAP = 1u << 18;
#define HSLEEP 4
#define HSPLIT 64
#define PPIPE 0
#define STA 0
#define STB 2
#define NPOST 0
#define SNAKE 0
#define NPRE 12
#define WOK 2
#define GRP 1
#define GPRIO 0
#define HPRIO 0
#define XBAR 28
#define PSLEEP 2
#define P2NW 7
#define P2W0 6
#define WSLEEP 0
#define ROTM 18
#define MAPV 0
#define O_BAR 5
#define PRIO_MODE 0
#define DMA_SPACE_A T
#define DMA_SPACE_B (4 * T / 3)

struct Params {
  const void* x; const void* wqkv; const void* wo; const void* wg; const void* wu; const void* wd;
  void* out; char* ws;
};

__shared__ __attribute__((aligned(16))) char smem[163840];

DI unsigned rfl(unsigned v) { return __builtin_amdgcn_readfirstlane(v); }
DI rsrc_t mk_rsrc(const void* p, unsigned bytes) { return __builtin_amdgcn_make_buffer_rsrc((void*)p, (short)0, (int)bytes, 0x00020000); }
DI void dma16(unsigned ldsdst, unsigned voff, rsrc_t rs, unsigned soff) {
  asm volatile("s_mov_b32 m0, %0\n\ts_nop 0\n\tbuffer_load_dwordx4 %1, %2, %3 offen lds" ::"s"(ldsdst), "v"(voff), "s"(rs), "s"(soff) : "memory");
}
template <int N> DI void wait_vm() { asm volatile("s_waitcnt vmcnt(%0)" ::"i"(N) : "memory"); }
DI void drain_vm() { wait_vm<0>(); __builtin_amdgcn_s_waitcnt(0x0F70); }
DI void wait_lgkm0() { asm volatile("s_waitcnt lgkmcnt(0)" ::: "memory"); }
DI void bar() { asm volatile("" ::: "memory"); __builtin_amdgcn_s_barrier(); asm volatile("" ::: "memory"); }
DI void lbar() { wait_lgkm0(); bar(); }
template <class V> DI V lds_ld(unsigned a) { return *(LAS V*)((LAS char*)smem + a); }
template <class V> DI void lds_st(unsigned a, V v) { *(LAS V*)((LAS char*)smem + a) = v; }
DI bf16x4 lds_tr(unsigned a) { return __builtin_amdgcn_ds_read_tr16_b64_v4bf16((LAS bf16x4*)((LAS char*)smem + a)); }
DI bf16x8 cat(bf16x4 a, bf16x4 b) { return __builtin_shufflevector(a, b, 0, 1, 2, 3, 4, 5, 6, 7); }
DI f32x4 mfma(bf16x8 a, bf16x8 b, f32x4 c) { return __builtin_amdgcn_mfma_f32_16x16x32_bf16(a, b, c, 0, 0, 0); }
DI unsigned pk2(float a, float b) { f32x2 v = {a, b}; return __builtin_bit_cast(unsigned, __builtin_convertvector(v, bf16x2)); }
DI u32x2 pk4(f32x4 v) { u32x2 r = {pk2(v[0], v[1]), pk2(v[2], v[3])}; return r; }
DI bf16x8 frag(u32x2 lo, u32x2 hi) { u32x4 v = {lo[0], lo[1], hi[0], hi[1]}; return __builtin_bit_cast(bf16x8, v); }
DI float bflo(unsigned v) { return __builtin_bit_cast(float, v << 16); }
DI float bfhi(unsigned v) { return __builtin_bit_cast(float, v & 0xffff0000u); }
DI float rbf(float v) { return bflo(pk2(v, 0.f)); }
DI float relu2(float v) { float o; asm("v_add_f32 %0, %1, |%1|" : "=v"(o) : "v"(v)); return o; }
DI f32x4 phi4(f32x4 u, f32x4 rl, f32x4 rh) {
  const f32x4 l = u * rl;
  f32x4 e, a;
#pragma unroll
  for (int j = 0; j < 4; ++j) {
    e[j] = __builtin_amdgcn_fmed3f(__builtin_amdgcn_exp2f(l[j]), 0.f, 1.f);
    a[j] = relu2(u[j]);
  }
  return a * rh + e + 1e-6f;
}
DI f32x4 splat(float v) { return f32x4{v, v, v, v}; }
template <int CTRL> DI float dpp_add(float v) {
  return v + __builtin_bit_cast(float, __builtin_amdgcn_update_dpp(0, __builtin_bit_cast(int, v), CTRL, 0xf, 0xf, true));
}
DI float sum64(float v) {
  v = dpp_add<0xB1>(v);
  v = dpp_add<0x4E>(v);
  v = dpp_add<0x141>(v);
  v = dpp_add<0x140>(v);
  v = dpp_add<0x142>(v);
  return dpp_add<0x143>(v);
}
DI float dot2(unsigned a, unsigned b, float c) {
  return __builtin_amdgcn_fdot2_f32_bf16(__builtin_bit_cast(bf16x2, a), __builtin_bit_cast(bf16x2, b), c, false);
}
DI u32x4 ldtr2(unsigned a, unsigned d) { return __builtin_bit_cast(u32x4, cat(lds_tr(a), lds_tr(a + d))); }
DI void mfma_ip(f32x4& c, const u32x4& a, const u32x4& b) { asm volatile("v_mfma_f32_16x16x32_bf16 %0, %1, %2, %0" : "+v"(c) : "v"(a), "v"(b)); }
DI void mfma_z(f32x4& c, const u32x4& a, const u32x4& b) { asm volatile("v_mfma_f32_16x16x32_bf16 %0, %1, %2, 0" : "=&v"(c) : "v"(a), "v"(b)); }
template <int N> DI void acc_fence(f32x4* c) {
  asm volatile("s_nop 7" ::: "memory");
#pragma unroll
  for (int i = 0; i < N; ++i) asm volatile("" : "+v"(c[i]));
}
DI u32x4 cat2(u32x2 a, u32x2 b) { return u32x4{a[0], a[1], b[0], b[1]}; }
DI u32x2 ldtr1(unsigned a) { return __builtin_bit_cast(u32x2, lds_tr(a)); }
template <int IMM> DI void set_m0i(unsigned v) { asm volatile("s_add_i32 m0, %0, %1" ::"s"(v), "i"(IMM) : "memory", "scc"); }
template <int OFF> DI void dma_goi(unsigned voff, rsrc_t rs, unsigned soff) {
  asm volatile("buffer_load_dwordx4 %0, %1, %2 offen offset:%3 lds" ::"v"(voff), "s"(rs), "s"(soff), "i"(OFF) : "memory");
}
DI void set_m0(unsigned v) { asm volatile("s_mov_b32 m0, %0" ::"s"(v) : "memory"); }
DI void dma_go(unsigned voff, rsrc_t rs, unsigned soff) {
  asm volatile("buffer_load_dwordx4 %0, %1, %2 offen lds" ::"v"(voff), "s"(rs), "s"(soff) : "memory");
}
DI f32x4 mfma_u(u32x4 a, u32x4 b, f32x4 c) { return mfma(__builtin_bit_cast(bf16x8, a), __builtin_bit_cast(bf16x8, b), c); }
#define STAMP_DECL
#define STAMP_ARG
#define STAMP_PASS
#define STAMPX(c, i)
#define STAMP_END
#define STAMP(i) STAMPX(threadIdx.x == 0, i)

DI unsigned poll_flags(gu32* word, bool active, unsigned tag) {
  unsigned v = 0;
#pragma unroll 1
  for (unsigned spins = 0; spins < SPIN_CAP; ++spins) {
    v = __hip_atomic_load(word, RLX_AGENT);
    if (__all(!active || v == tag)) break;
    if (PSLEEP) __builtin_amdgcn_s_sleep(PSLEEP);
  }
  asm volatile("" ::: "memory");
  return v;
}

DI void lds_set(unsigned a) { __hip_atomic_store((LAS unsigned*)smem + a / 4, 1u, __ATOMIC_RELAXED, __HIP_MEMORY_SCOPE_WORKGROUP); }
DI unsigned lds_get(unsigned a) { return __hip_atomic_load((LAS unsigned*)smem + a / 4, __ATOMIC_RELAXED, __HIP_MEMORY_SCOPE_WORKGROUP); }
DI void lds_wait(unsigned a) {
#pragma unroll 1
  for (unsigned spins = 0; spins < (SPIN_CAP << 4); ++spins) {
    if (rfl(lds_get(a))) break;
    __builtin_amdgcn_s_sleep(1);
  }
  asm volatile("" ::: "memory");
}

template <int I> struct IC { static constexpr int v = I; };
template <int B, int E, class Fn> DI void static_for(Fn&& f) {
  if constexpr (B < E) { f(IC<B>{}); static_for<B + 1, E>(f); }
}

enum { J_MLP = 0, J_KV = 1, J_Q = 2, J_SPARE = 3 };

struct Dma {
  rsrc_t rsA, rsB;
  unsigned vA0, vA1, vB0, vB1, vB2;
  unsigned sA[4], sB, advB;
  unsigned mA, mB, mW;
};

constexpr unsigned a_slot(unsigned s3) { return s3 == 0 ? 126976u : (s3 - 1) * 32768u; }
#define SB __builtin_amdgcn_sched_barrier(0)

template <int T> DI unsigned bofs(const unsigned (&boff)[T], int t) { return T == 6 && t >= 3 ? boff[t - 3] + 6144 : boff[t]; }
template <int T, bool TR, int KS, class Extra>
DI void kstep(f32x4 (&acc)[4][T], const u32x4 (&ac)[4], const u32x2 (&bc)[T][2], u32x4 (&an)[4], u32x2 (&bn)[T][2],
              const unsigned (&aoff)[4], const unsigned (&boff)[T], const Dma& d, Extra&& extra) {
  constexpr int s = KS >> 1, KIND = KS & 1, NP = KIND == 0 ? 4 : 3, ST = KIND == 0 ? (STA ? STA : (4 * T - 1) / NP) : (STB ? STB : (4 * T - 1) / NP);
  constexpr int sn = (KS + 1) >> 1;
  constexpr unsigned asl = a_slot(sn % 3), bsl = ((KS + 1) % 5) * 12288u;
  static_for<0, 4 * T>([&](auto E) {
    constexpr int e = E.v, t = e >> 2, i = (SNAKE && (t & 1)) ? 3 - (e & 3) : (e & 3);
    const u32x4 bf = cat2(bc[t][0], bc[t][1]);
    if constexpr (KS == 0) { if (TR) mfma_z(acc[i][t], bf, ac[i]); else mfma_z(acc[i][t], ac[i], bf); }
    else { if (TR) mfma_ip(acc[i][t], bf, ac[i]); else mfma_ip(acc[i][t], ac[i], bf); }
    SB;
    if constexpr (KS < 19) {
      if constexpr (e < 4) an[e] = lds_ld<u32x4>(aoff[((KS + 1) & 1) + (sn % 3 == 0 ? 2 : 0)] + (sn % 3 == 0 ? 0u : asl) + e * 2048);
      else if constexpr (e < 4 + 2 * T) bn[(e - 4) >> 1][e & 1] = ldtr1(bofs<T>(boff, (e - 4) >> 1) + bsl + (e & 1) * 768);
    }
    constexpr int pc = e / ST;
    if constexpr (KIND == 0) {
      constexpr int adv = s < 8 ? (s + 2) * 128 : 0, dst = (int)a_slot((s + 2) % 3) + pc * 1024, off = dst >= adv ? adv : 0;
      if constexpr (pc < NP && e % ST == ST - 2) set_m0i<dst - off>(d.mA);
      if constexpr (pc < NP && e % ST == ST - 1) dma_goi<off>((pc & 1) ? d.vA1 : d.vA0, d.rsA, d.sA[pc] + (adv - off));
    } else {
      constexpr int h0 = (2 * s) % 5;
      if constexpr (pc < NP && e % ST == ST - 2) set_m0i<(h0 == 4 ? 0 : h0 * 12288) + pc * 1024>(h0 == 4 ? d.mW : d.mB);
      if constexpr (pc < NP && e % ST == ST - 1) dma_goi<0>(pc == 0 ? d.vB0 : (pc == 1 ? d.vB1 : d.vB2), d.rsB, d.sB);
    }
    extra(E);
    SB;
  });
}

struct RowStat { u32x4 xa; unsigned xb, row; };
template <int NY> DI void row_stat(RowStat& R, unsigned lane) {
  float s = 0.f, q = 0.f;
  asm volatile("s_waitcnt vmcnt(%8)\n\t"
               "v_dot2c_f32_bf16 %0, %2, %7\n\tv_dot2c_f32_bf16 %1, %2, %2\n\tv_dot2c_f32_bf16 %0, %3, %7\n\tv_dot2c_f32_bf16 %1, %3, %3\n\t"
               "v_dot2c_f32_bf16 %0, %4, %7\n\tv_dot2c_f32_bf16 %1, %4, %4\n\tv_dot2c_f32_bf16 %0, %5, %7\n\tv_dot2c_f32_bf16 %1, %5, %5\n\t"
               "v_dot2c_f32_bf16 %0, %6, %7\n\tv_dot2c_f32_bf16 %1, %6, %6\n\ts_nop 2"
               : "+v"(s), "+v"(q) : "v"(R.xa[0]), "v"(R.xa[1]), "v"(R.xa[2]), "v"(R.xa[3]), "v"(R.xb), "v"(0x3f803f80u), "i"(NY) : "memory");
  const float mean = sum64(s) * (1.0f / D);
  const float var = fmaxf(sum64(q) * (1.0f / D) - mean * mean, 0.f);
  const float sd = __builtin_amdgcn_sqrtf(rbf(rbf(var) + 1e-5f));
  if (lane == 63) lds_st<unsigned>(L_GO + 32 + (R.row & 7) * 4, pk2(mean, sd));
}
DI void row_publish(const RowStat& R, char* ws, unsigned tag, unsigned tid) {
  if (tid < 8) __hip_atomic_store((gu64*)(ws + WS_STAT) + R.row + tid, ((unsigned long long)tag << 32) | lds_ld<unsigned>(L_GO + 32 + tid * 4), RLX_AGENT);
}

struct Gran { u32x2 g0, g1, g2; unsigned off, step; };
DI void gran_issue(Gran& G, rsrc_t rs) {
  asm volatile("buffer_load_dwordx2 %0, %3, %4, 0 offen sc1\n\tbuffer_load_dwordx2 %1, %3, %4, %5 offen sc1\n\t"
               "buffer_load_dwordx2 %2, %3, %4, %6 offen sc1"
               : "=&v"(G.g0), "=&v"(G.g1), "=&v"(G.g2) : "v"(G.off), "s"(rs), "s"(G.step), "s"(2 * G.step) : "memory");
}
DI void gran_load(Gran& G, rsrc_t rs) {
  asm volatile("buffer_load_dwordx2 %0, %3, %4, 0 offen sc1\n\tbuffer_load_dwordx2 %1, %3, %4, %5 offen sc1\n\t"
               "buffer_load_dwordx2 %2, %3, %4, %6 offen sc1\n\ts_waitcnt vmcnt(0)"
               : "=&v"(G.g0), "=&v"(G.g1), "=&v"(G.g2) : "v"(G.off), "s"(rs), "s"(G.step), "s"(2 * G.step) : "memory");
}

DI unsigned urow(unsigned pos, unsigned rot) { const unsigned u = pos + rot; return (u >= 54 ? u - 54 : u) * 40960u; }

constexpr unsigned wo_tile(unsigned v) { return (v / 5) * 10 + (v % 5 == 4 ? 4 : (v % 5 < 2 ? 2 * (v % 5) : 2 * (v % 5) + 1)); }
struct W2 { rsrc_t wd, wo; unsigned rot, col; };
DI unsigned w2_off(const W2& W, int u, unsigned lw) {
  if (u < 54) return urow(u, W.rot) + lw * 10240u + W.col;
  return wo_tile(u - 54) * 20480u + (lw >> 1) * ((u - 54) % 5 == 4 ? 102400u : 20480u) + (lw & 1) * 10240u + W.col;
}
DI u32x4 w2_load(const W2& W, int u, unsigned lw, unsigned voff) {
  return __builtin_amdgcn_raw_buffer_load_b128(u < 54 ? W.wd : W.wo, voff, w2_off(W, u, lw), 0);
}

template <int T, bool TR, bool PF, bool LASTA, class Pre>
DI void main_loop(Pre&& pre, RowStat& R, char* ws, f32x4 (&acc)[4][T], unsigned (&boff)[T], Dma& d, unsigned w, unsigned lane, unsigned tid,
                  unsigned tag, unsigned rb, unsigned ccol, rsrc_t rsB2, rsrc_t rsWS, unsigned cb, unsigned rot, u32x4 (&pre2)[36] STAMP_ARG) {
  const unsigned g = lane >> 4, r = lane & 15, m = w & 3;
  unsigned aoff[4];
  aoff[0] = (64 * m + r) * 128 + ((g ^ (r >> 1)) << 4);
  aoff[1] = aoff[0] ^ 64;
  aoff[2] = aoff[0] + a_slot(0); aoff[3] = aoff[1] + a_slot(0);
  const unsigned wq = (w & 1) * 2 + ((w >> 1) & 1);
  asm volatile("" : "+v"(aoff[0]), "+v"(aoff[1]), "+v"(aoff[2]), "+v"(aoff[3]), "+v"(boff[0]), "+v"(boff[1]), "+v"(boff[2]));
  if (T == 5) asm volatile("" : "+v"(boff[3]), "+v"(boff[4]));
  d.mA = w * 4096u; d.mB = L_B + wq * 3072u + (w < 4 ? 0u : 12288u); d.mW = L_B + wq * 3072u + (w < 4 ? 49152u : 0u);
  Gran G;
  unsigned v2, vb2, tA[3], tB[3];
  {
    const unsigned bdst = L_B + (w < 4 ? 36864u : 24576u) + wq * 3072u;
    unsigned so = d.sB + d.advB;
    dma16(bdst, d.vB0, d.rsB, so); dma16(bdst + 1024, d.vB1, d.rsB, so); dma16(bdst + 2048, d.vB2, d.rsB, so);
#pragma unroll
    for (int i = 0; i < 4; ++i) dma16(a_slot(1) + d.mA + i * 1024, (i & 1) ? d.vA1 : d.vA0, d.rsA, d.sA[i] + 128);
    pre();
    v2 = (lane >> 3) * 1280 + (((lane & 7) ^ (((lane >> 4) & 3) << 1)) << 4);
    vb2 = v2 + (3 * wq) * 10240u;
    G.off = w < 4 ? WS_CSUM + ccol * 8 : WS_STAT + (rb * 256 + tid - 256) * 8;
    G.step = w < 4 ? NCOL * 8 : 0;
    tA[0] = urow(w, rot) + cb * 128u; tA[1] = urow(8 + w, rot) + cb * 128u; tA[2] = urow(31 + w, rot) + cb * 128u;
    tB[0] = urow(16, rot) + cb * 128u; tB[1] = urow(w < 4 ? 19u : 22u, rot) + cb * 128u; tB[2] = urow(w < 4 ? 25u : 28u, rot) + cb * 128u;
    asm volatile("" : "+v"(v2), "+v"(vb2), "+v"(G.off));
    asm volatile("" : "+s"(G.step), "+s"(tA[0]), "+s"(tA[1]), "+s"(tA[2]), "+s"(tB[0]), "+s"(tB[1]), "+s"(tB[2]));
    wait_vm<7>();
    bar();
    if (w >= 4) {
      so += d.advB;
      dma16(bdst + 24576, d.vB0, d.rsB, so); dma16(bdst + 24576 + 1024, d.vB1, d.rsB, so); dma16(bdst + 24576 + 2048, d.vB2, d.rsB, so);
    }
    d.sB = so;
  }
  STAMP(1);
  u32x4 a0[4], a1[4];
  u32x2 b0[T][2], b1[T][2];
#pragma unroll
  for (int i = 0; i < 4; ++i) a0[i] = lds_ld<u32x4>(aoff[2] + i * 2048);
#pragma unroll
  for (int t = 0; t < T; ++t) { b0[t][0] = ldtr1(bofs<T>(boff, t)); b0[t][1] = ldtr1(bofs<T>(boff, t) + 768); }
  const unsigned vpf = w >= 4 ? v2 : 0x7ffffff0u;
  const bool y = w >= 4;
  bool ok = true;
  auto ln_store = [&] {
    const float v0 = y ? -bflo(G.g0[0]) : __builtin_bit_cast(float, G.g0[0]) + __builtin_bit_cast(float, G.g1[0]) + __builtin_bit_cast(float, G.g2[0]);
    const unsigned la = L_MISC + (y ? tid * 4 - 1024 : 2048 + tid * 4);
    lds_st<float>(la, v0);
    lds_st<float>(y ? la + 1024 : L_GO + 64 + (lane & 7) * 4, __builtin_amdgcn_rcpf(bfhi(G.g0[0])));
  };
  static_for<0, 10>([&](auto S_) {
    constexpr int s = S_.v, L = 4 * T;
    constexpr int NX = (PF && s >= 1 && 2 * s <= NPRE) ? 2 : 0;
    kstep<T, TR, 2 * s>(acc, a0, b0, a1, b1, aoff, boff, d, [&](auto E) {
      constexpr int e = E.v;
      if constexpr (NX && e >= L - 2) {
        constexpr int u = 2 * (s - 1) + e - (L - 2);
        pre2[u] = __builtin_amdgcn_raw_buffer_load_b128(rsB2, vpf, urow(36 + u, rot) + (w & 3) * 10240u + cb * 128u, 0);
      }
      if constexpr (s < 7) { if constexpr (e == L - 3) d.sB += d.advB; }
      else if constexpr (s == 7) {
        if constexpr (e == L - 6) d.vB0 = y ? vb2 : d.vB0;
        if constexpr (e == L - 4) d.vB1 = y ? vb2 + 10240u : d.vB1;
        if constexpr (e == L - 3) d.vB2 = y ? vb2 + 20480u : d.vB2;
        if constexpr (e == L - 2) d.sB = y ? tB[0] : d.sB + d.advB;
        if constexpr (e == L - 1) d.rsB = y ? rsB2 : d.rsB;
      } else {
        if constexpr (e == L - 3) { d.sB = tB[s - 7]; d.rsB = rsB2; d.vB0 = vb2; d.vB1 = vb2 + 10240u; d.vB2 = vb2 + 20480u; }
      }
      if constexpr (s == 9 && e == L - 4) {
        asm volatile("s_waitcnt vmcnt(7)" : "+v"(G.g0), "+v"(G.g1), "+v"(G.g2)::"memory");
        ok = __all(G.g0[1] == tag && G.g1[1] == tag && G.g2[1] == tag);
      }
      if constexpr (s == 9 && e == L - 3) ln_store();
    });
    if constexpr (s == 0) row_stat<63>(R, lane);
    if constexpr (s == 9) {
      if (__builtin_expect(!ok, 0)) {
#pragma unroll 1
        for (unsigned spins = 0; spins < SPIN_CAP; ++spins) {
          __builtin_amdgcn_s_sleep(2);
          gran_load(G, rsWS);
          if (__all(G.g0[1] == tag && G.g1[1] == tag && G.g2[1] == tag)) break;
        }
        ln_store();
      }
    }
    wait_lgkm0();
    if constexpr (s < 9) wait_vm<4 + NX>();
    bar();
    SB;
    kstep<T, TR, 2 * s + 1>(acc, a1, b1, a0, b0, aoff, boff, d, [&](auto E) {
      constexpr int e = E.v;
      if constexpr (s == 0 && e == L - 4) row_publish(R, ws, tag, tid);
      if constexpr (s == 8 && e == 0) gran_issue(G, rsWS);
      if constexpr (s >= 7 && s < 9 && e >= L - 4) {
        d.rsA = rsB2; d.vA0 = v2; d.vA1 = v2;
        d.sA[e - (L - 4)] = tA[s - 7] + (e - (L - 4)) * 10240;
      }
      if constexpr (s == 9 && LASTA && e == 7) {
        if (w < 5) {
#pragma unroll
          for (int i = 0; i < 4; ++i) dma16(a_slot(0) + d.mA + i * 1024, v2, rsB2, tA[2] + i * 10240);
        }
      }
    });
    if constexpr (s == 8) { STAMP(13); }
    if constexpr (s == 2) { STAMP(26); }
    if constexpr (s == 6) { STAMP(27); }
  });
  acc_fence<4 * T>(&acc[0][0]);
  STAMP(14);
}

DI void st16(u32x4 v, rsrc_t rs, unsigned voff, unsigned soff) {
  __builtin_amdgcn_raw_buffer_store_b128(v, rs, voff, soff, 16);
  asm volatile("s_nop 1" ::"v"(v) : "memory");
}
DI void publish_flag(const Params& p, unsigned flag_off, unsigned tag, unsigned tid) {
  drain_vm();
  bar();
  if (tid == 0) __hip_atomic_store((gu32*)(p.ws + flag_off), tag, RLX_AGENT);
}
DI void st_frag(rsrc_t rs, unsigned lane, unsigned rt, unsigned step, u32x2 lo, u32x2 hi) {
  u32x4 v = {lo[0], lo[1], hi[0], hi[1]};
  st16(v, rs, lane * 16, WS_HA + (rt * NSTEP + step) * 1024);
}
DI void shared_send(unsigned w, unsigned lane, const u32x2 (&sh)[4]) {
  const bool n = w >= 4;
  u32x4 snd = n ? u32x4{sh[0][0], sh[0][1], sh[1][0], sh[1][1]} : u32x4{sh[2][0], sh[2][1], sh[3][0], sh[3][1]};
  lds_st<u32x4>(L_SCR + w * 1024 + lane * 16, snd);
}
DI void shared_recv(rsrc_t rs, unsigned w, unsigned lane, unsigned rt0, unsigned step, const u32x2 (&sh)[4]) {
  lbar();
  u32x4 rcv = lds_ld<u32x4>(L_SCR + (w ^ 4) * 1024 + lane * 16);
  u32x2 r0 = {rcv[0], rcv[1]}, r1 = {rcv[2], rcv[3]};
  if (w >= 4) { st_frag(rs, lane, rt0 + 2, step, r0, sh[2]); st_frag(rs, lane, rt0 + 3, step, r1, sh[3]); }
  else        { st_frag(rs, lane, rt0 + 0, step, sh[0], r0); st_frag(rs, lane, rt0 + 1, step, sh[1], r1); }
}

__global__ __launch_bounds__(512, 2) void fused_block(Params p) {
  const unsigned tid = threadIdx.x, lane = tid & 63, w = rfl(tid >> 6);
  const unsigned b = blockIdx.x, xcd = b & 7, slot = b >> 3;
  unsigned jtype, jidx;
  const unsigned rb = 4 * (xcd & 1) + (slot & 3), jq = xcd >> 1, ji = slot >> 2;
  const unsigned spi = (xcd - 4) * 4 + (slot & 3);
  if (ji == 7) { jtype = J_Q; jidx = jq; }
  else if (ji >= 5) { jtype = J_KV; jidx = 2 * jq + ji - 5; }
  else if (jq < 2) { jtype = J_MLP; jidx = 5 * jq + ji; }
  else if (ji < 4) { jtype = J_MLP; jidx = 10 + 4 * (jq - 2) + ji; }
  else { jtype = J_SPARE; jidx = 0; }
  STAMP_DECL;
  STAMP(0);
  const rsrc_t rsX = mk_rsrc(p.x, S * D * 2);

  RowStat rs_;
  rs_.row = rfl(b * 8 + w);
  asm volatile("s_nop 4\n\tbuffer_load_dwordx4 %0, %2, %4, %5 offen\n\tbuffer_load_dword %1, %3, %4, %5 offen offset:1024"
               : "=&v"(rs_.xa), "=&v"(rs_.xb) : "v"(lane * 16), "v"(lane * 4), "s"(rsX), "s"(rs_.row * 1280) : "memory");
  Dma d;
  const bool act = jtype != J_SPARE;
  {
    const unsigned mh = w & 1;
    const void* bw; unsigned pitch, nch, colb;
    if (jtype == J_MLP) { bw = mh ? p.wu : p.wg; pitch = 3456; colb = 192 * jidx; nch = 12; }
    else if (jtype == J_KV) { bw = p.wqkv; pitch = 3840; colb = 1280 + 1280 * mh + 160 * jidx; nch = 10; }
    else { bw = p.wqkv; pitch = 3840; colb = 320 * jidx + 160 * mh; nch = 10; }
    d.rsA = rsX; d.rsB = mk_rsrc(bw, D * pitch);
    d.vA0 = (lane >> 3) * 1280 + (((lane & 7) ^ (lane >> 4)) << 4); d.vA1 = d.vA0 ^ 64;
#pragma unroll
    for (int i = 0; i < 4; ++i) d.sA[i] = (rb * 256 + 32 * w + 8 * i) * 1280;
    asm volatile("s_nop 4" ::: "memory");
    if (act) {
      const unsigned adst = a_slot(0) + w * 4096u;
#pragma unroll
      for (int i = 0; i < 4; ++i) dma16(adst + i * 1024, (i & 1) ? d.vA1 : d.vA0, d.rsA, d.sA[i]);
    }
    unsigned vb[3];
#pragma unroll
    for (int i = 0; i < 3; ++i) {
      const unsigned c = 64 * i + lane, row = c / 12, pos = c % 12;
      unsigned ch = pos ^ (((row >> 3) & 1) << 1);
      if (ch >= nch) ch -= 2;
      vb[i] = row * pitch + ch * 16;
    }
    d.vB0 = vb[0]; d.vB1 = vb[1]; d.vB2 = vb[2];
    d.sB = (16 * ((w >> 1) & 1) + (w < 4 ? 32 : 0)) * pitch + colb; d.advB = 64 * pitch;
    const unsigned bdst = L_B + (w < 4 ? 12288u : 0u) + ((w & 1) * 2 + ((w >> 1) & 1)) * 3072u;
    if (act) { dma16(bdst, d.vB0, d.rsB, d.sB); dma16(bdst + 1024, d.vB1, d.rsB, d.sB); dma16(bdst + 2048, d.vB2, d.rsB, d.sB); }
  }
  STAMP(10);
  if (tid < 2) lds_st<unsigned>(L_GO + tid * 4, 0u);
  unsigned tag;
  asm volatile("s_load_dword %0, %1, 0x0\n\ts_waitcnt lgkmcnt(0)" : "=s"(tag) : "s"(p.ws) : "memory");
  tag += 1;

  const unsigned t2 = 28 * xcd + slot;
  const bool has2 = slot < 28 && t2 < 220;
  const unsigned G = t2 / 10, cb = t2 % 10;
  constexpr unsigned rot = 32;
  const unsigned g = lane >> 4, r = lane & 15, q4 = r >> 2, p4 = lane & 3;
  const rsrc_t rsWd = mk_rsrc(p.wd, has2 ? F * D * 2 : 0);
  const rsrc_t rsWS = mk_rsrc(p.ws, WS_END);
  const unsigned v2 = (lane >> 3) * 1280 + (((lane & 7) ^ (((lane >> 4) & 3) << 1)) << 4);
  const unsigned m = w & 3, n = w >> 2;
  const unsigned rt0 = rb * 16 + m * 4;
  const unsigned brow = L_B + (8 * g + q4) * 192;
  const unsigned bx = (g & 1) << 5;

  u32x4 pre2[36];
  W2 W;
  W.wd = rsWd; W.wo = mk_rsrc(p.wo, has2 ? D * D * 2 : 0); W.rot = rot; W.col = cb * 128u;
  if (jtype == J_MLP) {
    f32x4 acc[4][6];
    unsigned boff[6];
#pragma unroll
    for (int t = 0; t < 3; ++t) {
      const unsigned gt = n ? (t == 2 ? 3 : 4 + t) : t;
      boff[t] = brow + ((32 * gt + 8 * p4) ^ bx); boff[t + 3] = boff[t] + 6144;
    }
    const unsigned ccol = tid < 96 ? 1920 + 96 * jidx + tid : 3648 + 96 * jidx + (tid < 192 ? tid - 96 : 0);
    main_loop<6, true, true, true>([] {}, rs_, p.ws, acc, boff, d, w, lane, tid, tag, rb, ccol, rsWd, rsWS, cb, rot, pre2 STAMP_PASS);
    if (w >= 4) {
#pragma unroll
      for (int u = NPRE; u < NPRE + NPOST; ++u) { pre2[u] = w2_load(W, 36 + u, w & 3, v2); SB; }
    }
    float mm[4];
    f32x4 cg[3], cu[3];
#pragma unroll
    for (int i = 0; i < 4; ++i) mm[i] = lds_ld<float>(L_MISC + (64 * m + 16 * i + r) * 4);
#pragma unroll
    for (int t = 0; t < 3; ++t) {
      const unsigned gt = n ? (t == 2 ? 3 : 4 + t) : t;
      cg[t] = lds_ld<f32x4>(L_MISC + 2048 + (16 * gt + 4 * g) * 4); cu[t] = lds_ld<f32x4>(L_MISC + 2048 + (96 + 16 * gt + 4 * g) * 4);
    }
    auto hval = [&](int i, int t) {
      f32x4 gg = acc[i][t] + mm[i] * cg[t];
      const f32x4 uu = acc[i][t + 3] + mm[i] * cu[t];
#pragma unroll
      for (int j = 0; j < 4; ++j) gg[j] = relu2(gg[j]);
      return pk4(gg * uu);
    };
#pragma unroll
    for (int i = 0; i < 4; ++i) { st_frag(rsWS, lane, rt0 + i, 3 * jidx + 2 * n, hval(i, 0), hval(i, 1)); SB; }
    const u32x2 sh[4] = {hval(0, 2), hval(1, 2), hval(2, 2), hval(3, 2)};
    shared_send(w, lane, sh);
    shared_recv(rsWS, w, lane, rt0, 3 * jidx + 1, sh);
    STAMP(15);
    publish_flag(p, WS_FLAGH + (rb * 32 + jidx) * FS, tag, tid);
    STAMP(3);
  } else if (jtype == J_KV) {
    f32x4 acc[4][5];
    unsigned boff[5];
    boff[0] = brow + ((80 * n + 8 * p4) ^ bx); boff[1] = boff[0] + 6144;
    boff[2] = brow + ((80 * n + 40 + 8 * p4) ^ bx); boff[3] = boff[2] + 6144;
    boff[4] = brow + ((80 * n + 32 + 40 * (p4 >> 1)) ^ bx) + (p4 & 1) * 6144;
    const unsigned kb = 126976u + m * 6720 + n * 3360, dump = L_MISC - 2048 + lane * 16;
    const unsigned ea = (q4 == 1 || r == 0) ? kb + (r == 0 ? 20 : 12 + r) * 80 : 0u, eb = (q4 == 3 || r == 0) ? kb + 1680 + (r == 0 ? 20 : 4 + r) * 80 : 0u;
    unsigned ka[6] = {kb + r * 80 + g * 16, ea ? ea + g * 16 : dump, eb ? eb + g * 16 : dump, g == 0 ? kb + r * 80 + 64 : dump,
                      g == 2 ? kb + 1680 + r * 80 + 64 : dump, (g == 0 && ea) ? ea + 64 : ((g == 2 && eb) ? eb + 64 : dump)};
    auto pin = [&] { asm volatile("" : "+v"(ka[0]), "+v"(ka[1]), "+v"(ka[2]), "+v"(ka[3]), "+v"(ka[4]), "+v"(ka[5])); };
    const unsigned ccol = tid < 80 ? 640 + 80 * jidx + tid : 1280 + 80 * jidx + (tid < 160 ? tid - 80 : 0);
    main_loop<5, false, false, false>(pin, rs_, p.ws, acc, boff, d, w, lane, tid, tag, rb, ccol, rsWd, rsWS, cb, rot, pre2 STAMP_PASS);
    float cs[5];
    cs[0] = lds_ld<float>(L_MISC + 2048 + (40 * n + r) * 4);
    cs[1] = lds_ld<float>(L_MISC + 2048 + (80 + 40 * n + r) * 4);
    cs[2] = lds_ld<float>(L_MISC + 2048 + (40 * n + 20 + r) * 4);
    cs[3] = lds_ld<float>(L_MISC + 2048 + (80 + 40 * n + 20 + r) * 4);
    cs[4] = lds_ld<float>(L_MISC + 2048 + ((q4 & 1) * 80 + 40 * n + 16 + 20 * (q4 >> 1) + (r & 3)) * 4);
    u32x2 pk[4][3], vv[4][3];
    f32x4 mm4[4], rr4[4];
#pragma unroll
    for (int i = 0; i < 4; ++i) {
      mm4[i] = lds_ld<f32x4>(L_MISC + (64 * m + 16 * i + 4 * g) * 4); rr4[i] = lds_ld<f32x4>(L_MISC + 1024 + (64 * m + 16 * i + 4 * g) * 4);
    }
    SB;
#pragma unroll
    for (int i = 0; i < 4; ++i) {
      const f32x4 mm = mm4[i], rr = rr4[i];
      const f32x4 rl = rr * 1.4426950408889634f, rh = rr * 0.5f;
      f32x4 val[5];
#pragma unroll
      for (int t = 0; t < 5; ++t) val[t] = acc[i][t] + mm * cs[t];
#pragma unroll
      for (int t = 0; t < 3; ++t) pk[i][t] = pk4(phi4(val[2 * t], rl, rh));
      vv[i][0] = pk4(val[1] * rr); vv[i][1] = pk4(val[3] * rr);
      const u32x2 v4 = pk4(val[4] * rr);
      vv[i][2] = (q4 & 1) ? v4 : u32x2{0x3f803f80u, 0x3f803f80u};
    }
    STAMP(25);
    f32x4 P[7] = {};
#pragma unroll
    for (int i = 0; i < 4; i += 2) {
      const bf16x8 k0 = frag(pk[i][0], pk[i + 1][0]), k2 = frag(pk[i][1], pk[i + 1][1]), k4 = frag(pk[i][2], pk[i + 1][2]);
      const bf16x8 v1 = frag(vv[i][0], vv[i + 1][0]), v3 = frag(vv[i][1], vv[i + 1][1]), v4 = frag(vv[i][2], vv[i + 1][2]);
      P[0] = mfma(k0, v1, P[0]); P[1] = mfma(k0, v4, P[1]); P[2] = mfma(k4, v1, P[2]);
      P[3] = mfma(k2, v3, P[3]); P[4] = mfma(k2, v4, P[4]); P[5] = mfma(k4, v3, P[5]); P[6] = mfma(k4, v4, P[6]);
    }
    lds_st<f32x4>(ka[0], P[0]); lds_st<f32x4>(ka[0] + 1680, P[3]); lds_st<f32x4>(ka[1], P[1]); lds_st<f32x4>(ka[2], P[4]);
    lds_st<f32x4>(ka[3], P[2]); lds_st<f32x4>(ka[4], P[5]); lds_st<f32x4>(ka[5], P[6]);
    lbar();
    if (tid < 210) {
      const unsigned a = 126976u + tid * 32;
      const f32x4 t0 = (lds_ld<f32x4>(a) + lds_ld<f32x4>(a + 6720)) + (lds_ld<f32x4>(a + 13440) + lds_ld<f32x4>(a + 20160));
      const f32x4 t1 = (lds_ld<f32x4>(a + 16) + lds_ld<f32x4>(a + 6736)) + (lds_ld<f32x4>(a + 13456) + lds_ld<f32x4>(a + 20176));
      st16(cat2(pk4(t0), pk4(t1)), rsWS, tid * 16, WS_KVP + (rb * 32 + 4 * jidx) * 840);
    }
    wait_lgkm0();
    STAMP(15);
    publish_flag(p, WS_FLAGKV + ((jidx >> 1) * 32 + (jidx & 1) * 8 + rb) * FS, tag, tid);
    STAMP(3);
  } else if (jtype == J_Q) {
    f32x4 acc[4][5];
    unsigned boff[5];
#pragma unroll
    for (int t = 0; t < 5; ++t) boff[t] = brow + n * 6144 + ((32 * t + 8 * p4) ^ bx);
    const unsigned ccol = 160 * jidx + (tid < 160 ? tid : 0);
    main_loop<5, true, false, false>([] {}, rs_, p.ws, acc, boff, d, w, lane, tid, tag, rb, ccol, rsWd, rsWS, cb, rot, pre2 STAMP_PASS);
    u32x2 pq[4][5];
    {
      float mm[4], rr[4];
      f32x4 c[5];
#pragma unroll
      for (int i = 0; i < 4; ++i) { mm[i] = lds_ld<float>(L_MISC + (64 * m + 16 * i + r) * 4); rr[i] = lds_ld<float>(L_MISC + 1024 + (64 * m + 16 * i + r) * 4); }
#pragma unroll
      for (int t = 0; t < 5; ++t) c[t] = lds_ld<f32x4>(L_MISC + 2048 + (80 * n + 16 * t + 4 * g) * 4);
      SB;
#pragma unroll
      for (int i = 0; i < 4; ++i)
#pragma unroll
        for (int t = 0; t < 5; ++t) pq[i][t] = pk4(phi4(acc[i][t] + mm[i] * c[t], splat(rr[i] * 1.4426950408889634f), splat(rr[i] * 0.5f)));
#pragma unroll
      for (int i = 0; i < 4; ++i) asm volatile("" : "+v"(pq[i][0]), "+v"(pq[i][1]), "+v"(pq[i][2]), "+v"(pq[i][3]), "+v"(pq[i][4]));
    }
    constexpr unsigned KP = 208, KQ = 84 * KP;
    unsigned wa[2];
#pragma unroll
    for (int k = 0; k < 2; ++k) {
      const unsigned qd = 2 * (tid < 420 ? tid : 0) + k, hd = qd / 105, e = (qd % 105) / 5, dq = qd % 5, hl = hd & 3;
      wa[k] = (hd >> 2) * KQ + (e < 20 ? hl * 20 + e : 80 + hl) * KP + hl * 40 + dq * 8;
    }
    unsigned fk = n * KQ + r * KP + g * 8, fz = n * KQ + (80 + (r & 3)) * KP + g * 8;
    asm volatile("" : "+v"(wa[0]), "+v"(wa[1]), "+v"(fk), "+v"(fz));
    STAMP(25);
    wait_vm<0>();
    bar();
#pragma unroll
    for (int k = 0; k < 5; ++k) lds_st<u32x4>(k * 8192 + tid * 16, u32x4{0u, 0u, 0u, 0u});
    if (w == 0) poll_flags((gu32*)(p.ws + WS_FLAGKV + (jidx * 32 + (lane & 15)) * FS), true, tag);
    lbar();
    STAMP(4);
    if (w < 7) {
      u32x4 v[8];
#pragma unroll
      for (int j = 0; j < 8; ++j)
        v[j] = __builtin_amdgcn_raw_buffer_load_b128(rsWS, (tid < 420 ? tid : 0) * 16, WS_KVP + (j * 32 + 8 * jidx) * 840, 16);
      SB;
      f32x4 t0 = {0.f, 0.f, 0.f, 0.f}, t1 = t0;
#pragma unroll
      for (int j = 0; j < 8; ++j) {
        t0 += f32x4{bflo(v[j][0]), bfhi(v[j][0]), bflo(v[j][1]), bfhi(v[j][1])};
        t1 += f32x4{bflo(v[j][2]), bfhi(v[j][2]), bflo(v[j][3]), bfhi(v[j][3])};
      }
      lds_st<u32x2>(wa[0], pk4(t0)); lds_st<u32x2>(wa[1], pk4(t1));
    }
    lbar();
    STAMP(16);
    bf16x8 A[12];
#pragma unroll
    for (int P = 0; P < 3; ++P) A[P] = frag(lds_ld<u32x2>(fz + P * 64), lds_ld<u32x2>(fz + P * 64 + 32));
#pragma unroll
    for (int u = 0; u < 5; ++u) {
      const unsigned a0 = fk + u * 16 * KP + (u < 3 ? 0 : 64);
      A[3 + u] = frag(lds_ld<u32x2>(a0), lds_ld<u32x2>(a0 + 32));
      if (u > 0) A[7 + u] = frag(lds_ld<u32x2>(a0 + 64), lds_ld<u32x2>(a0 + 96));
    }
    u32x2 sh[4];
#pragma unroll
    for (int i = 0; i < 4; ++i) {
      const bf16x8 B[3] = {frag(pq[i][0], pq[i][1]), frag(pq[i][2], pq[i][3]), frag(pq[i][4], u32x2{0u, 0u})};
      const f32x4 zero = {0.f, 0.f, 0.f, 0.f};
      const f32x4 den = mfma(A[2], B[2], mfma(A[1], B[1], mfma(A[0], B[0], zero))) + 1e-6f;
      f32x4 num[5];
#pragma unroll
      for (int u = 0; u < 5; ++u) num[u] = mfma(A[3 + u], B[u < 3 ? 0 : 1], zero);
#pragma unroll
      for (int u = 1; u < 5; ++u) num[u] = mfma(A[7 + u], B[u < 3 ? 1 : 2], num[u]);
      float rc[4];
#pragma unroll
      for (int j = 0; j < 4; ++j) rc[j] = __builtin_amdgcn_rcpf(den[j]);
      u32x2 o[5];
#pragma unroll
      for (int u = 0; u < 5; ++u) {
        const float rr = u == 0 ? rc[0] : (u == 4 ? rc[3] : (g < (unsigned)u ? rc[u - 1] : rc[u]));
        o[u] = pk4(num[u] * rr);
      }
      st_frag(rsWS, lane, rt0 + i, 54 + 5 * jidx + 2 * n, o[0], o[1]);
      st_frag(rsWS, lane, rt0 + i, 54 + 5 * jidx + 2 * n + 1, o[2], o[3]);
      sh[i] = o[4];
    }
    shared_send(w, lane, sh);
    shared_recv(rsWS, w, lane, rt0, 54 + 5 * jidx + 4, sh);
    STAMP(15);
    publish_flag(p, WS_FLAGO + (rb * 32 + jidx) * FS, tag, tid);
    STAMP(3);
    if (b == 224) {
      poll_flags((gu32*)(p.ws + WS_FLAGH + tid * FS), (tid & 31) < (tid < 256 ? 18u : 4u), tag);
      bar();
      if (tid == 0) __hip_atomic_store((gu32*)(p.ws + WS_EPOCH), tag, RLX_AGENT);
    }
  } else {
    const unsigned gw = spi * 8 + w;
    unsigned k10 = 0x00003f80u, k01 = 0x3f800000u;
    asm volatile("" : "+v"(k10), "+v"(k01));
    auto cs_src = [&](unsigned u, rsrc_t& rs, unsigned& cv, unsigned& st) {
      const unsigned cline = u / 3, third = u % 3;
      const void* cw = cline < 30 ? p.wqkv : (cline < 57 ? p.wg : p.wu);
      const unsigned cpitch = cline < 30 ? 3840u : 3456u;
      const unsigned clocal = cline < 30 ? cline : (cline < 57 ? cline - 30 : cline - 57);
      rs = mk_rsrc(cw, gw < 126 ? D * cpitch : 0);
      cv = (third * 216 + (lane >> 3)) * cpitch + clocal * 128 + (lane & 7) * 16;
      st = 8 * cpitch;
    };
    auto cs_publish = [&](unsigned u, float (&cs)[8]) {
#pragma unroll
      for (int k = 0; k < 4; ++k) { const bool hi = lane & 32; const float o = __shfl_xor(hi ? cs[k] : cs[k + 4], 32); cs[k] = (hi ? cs[k + 4] : cs[k]) + o; }
#pragma unroll
      for (int k = 0; k < 2; ++k) { const bool hi = lane & 16; const float o = __shfl_xor(hi ? cs[k] : cs[k + 2], 16); cs[k] = (hi ? cs[k + 2] : cs[k]) + o; }
      { const bool hi = lane & 8; const float o = __shfl_xor(hi ? cs[0] : cs[1], 8); cs[0] = (hi ? cs[1] : cs[0]) + o; }
      if (gw < 126)
        __hip_atomic_store((gu64*)(p.ws + WS_CSUM) + (u % 3) * NCOL + (u / 3) * 64 + (lane & 7) * 8 + (lane >> 3),
                           ((unsigned long long)tag << 32) | __builtin_bit_cast(unsigned, cs[0]), RLX_AGENT);
    };
    row_stat<0>(rs_, lane);
    u32x4 c[27];
    rsrc_t rs0, rs1; unsigned cv0, cv1, st0, st1;
    cs_src(gw, rs0, cv0, st0); cs_src(gw + 126, rs1, cv1, st1);
#pragma unroll
    for (int j = 0; j < 27; ++j) c[j] = __builtin_amdgcn_raw_buffer_load_b128(rs0, cv0 + j * st0, 0, 0);
    lbar();
    row_publish(rs_, p.ws, tag, tid);
    float cs[8] = {};
#pragma unroll
    for (int j = 0; j < 27; ++j) {
#pragma unroll
      for (int k = 0; k < 4; ++k) { cs[2 * k] = dot2(c[j][k], k10, cs[2 * k]); cs[2 * k + 1] = dot2(c[j][k], k01, cs[2 * k + 1]); }
      if (j < 8) c[j] = __builtin_amdgcn_raw_buffer_load_b128(rs1, cv1 + j * st1, 0, 0);
      SB;
    }
    cs_publish(gw, cs);
    SB;
#pragma unroll
    for (int j = 8; j < 27; ++j) c[j] = __builtin_amdgcn_raw_buffer_load_b128(rs1, cv1 + j * st1, 0, 0);
    SB;
    STAMP(11);
#pragma unroll
    for (int k = 0; k < 8; ++k) cs[k] = 0.f;
#pragma unroll
    for (int j = 0; j < 27; ++j)
#pragma unroll
      for (int k = 0; k < 4; ++k) { cs[2 * k] = dot2(c[j][k], k10, cs[2 * k]); cs[2 * k + 1] = dot2(c[j][k], k01, cs[2 * k + 1]); }
    cs_publish(gw + 126, cs);
    STAMP(12);
#pragma unroll 1
    for (unsigned i = w; i < 144; i += 8) dma16(i * 1024, v2, rsWd, urow(i >> 2, rot) + (i & 3) * 10240u + cb * 128u);
    drain_vm();
    bar();
  }
  if (!has2) { STAMP_END; return; }

  const unsigned rbA = (G * 6) >> 4, rbB = (G * 6 + 5 > 127 ? 127 : G * 6 + 5) >> 4;
  if (w >= 4) {
    __builtin_amdgcn_s_setprio(HPRIO);
    const unsigned lw = w - 4, ld0 = lw * 1024;
    gu32* fo = (gu32*)(p.ws + WS_FLAGO + ((lane < 32 ? rbA : rbB) * 32 + (lane & 3)) * FS);
    gu32* fh = (gu32*)(p.ws + WS_FLAGH + ((lane < 32 ? rbA : rbB) * 32 + ((lane & 31) < 18 ? lane & 31 : 17)) * FS);
    if (w == 7) {
      __builtin_amdgcn_s_setprio(3);
      const unsigned v = poll_flags(fh, (lane & 31) < 18 && (lane & 31) >= 10, tag);
      lds_set(L_GO);
      if (!__all((lane & 31) >= 18 || v == tag)) poll_flags(fh, (lane & 31) < 18, tag);
      lds_set(L_GO + 4);
      __builtin_amdgcn_s_setprio(HPRIO);
    } else {
      __builtin_amdgcn_s_sleep(HSLEEP);
    }
    if (jtype == J_KV) {
#pragma unroll
      for (int u = 31; u < 36; ++u) dma16(u * 4096 + ld0, v2, rsWd, w2_off(W, u, lw));
    }
    if (jtype != J_MLP) {
#pragma unroll
      for (int u = 0; u < NPRE + NPOST; ++u) { pre2[u] = w2_load(W, 36 + u, lw, v2); SB; }
    }
#pragma unroll
    for (int u = 36 + NPRE + NPOST; u < HSPLIT; ++u) { pre2[u - 36] = w2_load(W, u, lw, v2); SB; }
    STAMP(8);
    unsigned la[3], wb[3], xo[3], st[3];
    u32x4 xr[3];
    const unsigned ch = lane & 7;
#pragma unroll
    for (int i = 0; i < 3; ++i) {
      const unsigned row = lw * 24 + i * 8 + (lane >> 3), rl = row >> 4, rr = row & 15;
      la[i] = (rl >= 3 ? 24576u + (rl - 3) * 4096u : rl * 4096u) + (ch >> 1) * 1024 + (ch & 1) * 512 + ((rr + 2 * ch) & 15) * 16;
      xo[i] = (G * 96 + row) * 1280 + cb * 128 + ch * 16;
      wb[i] = ld0 + lane * 16 + i * 49152;
      asm volatile("" : "+v"(la[i]), "+v"(wb[i]), "+v"(xo[i]));
    }
    float ri[3], rl[3];
    unsigned of = 0;
    f32x4 xf[3][2], dd[3][2];
#pragma unroll
    for (int k = 2; k <= 6; ++k) {
      if (k == O_BAR && w == 7 && !__all((lane & 31) >= 4 || of == tag)) poll_flags(fo, (lane & 31) < 4, tag);
      if (k == 6) {
        wait_vm<0>();
#pragma unroll
        for (int h = 0; h < 3; ++h) {
          ri[h] = 2.0f * bfhi(st[h]) * bfhi(st[h]);
          rl[h] = -1.4426950408889634f * __builtin_amdgcn_rcpf(ri[h]);
          xf[h][0] = f32x4{bflo(xr[h][0]), bfhi(xr[h][0]), bflo(xr[h][1]), bfhi(xr[h][1])};
          xf[h][1] = f32x4{bflo(xr[h][2]), bfhi(xr[h][2]), bflo(xr[h][3]), bfhi(xr[h][3])};
          asm volatile("" : "+v"(ri[h]), "+v"(rl[h]), "+v"(xf[h][0]), "+v"(xf[h][1]));
        }
      }
      lbar();
      if (k == O_BAR - 1) { of = __hip_atomic_load(fo, RLX_AGENT); SB; }
      if (k < 6) {
        const int kk = k - 1;
#pragma unroll
        for (int u = 9 * (kk - 1) + ((kk - 1) & 1); u < 9 * kk + (kk & 1); ++u) {
          lds_st<u32x4>(wb[u / 12] + (u % 12) * 4096, pre2[u]);
          SB;
        }
      }
      if (k == WOK || k == WOK + 1) {
#pragma unroll
        for (int u = (k == WOK ? 54 : 64); u < (k == WOK ? 64 : 72); ++u)
          if (u >= HSPLIT && u >= 36 + NPRE + NPOST) { pre2[u - 36] = w2_load(W, u, lw, v2); SB; }
        if (k != WOK) { dma16(36 * 4096 + ld0, v2, W.wo, w2_off(W, 72, lw)); dma16(37 * 4096 + ld0, v2, W.wo, w2_off(W, 73, lw)); }
      }
      if (k == 2) {
#pragma unroll
        for (int h = 0; h < 3; ++h) {
          const unsigned row = G * 96 + lw * 24 + h * 8 + (lane >> 3);
          st[h] = __builtin_amdgcn_raw_buffer_load_b32(rsWS, WS_STAT + (row < S ? row : 0) * 8, 0, 16);
          xr[h] = __builtin_amdgcn_raw_buffer_load_b128(rsX, xo[h], 0, 0);
        }
      }
    }
#pragma unroll
    for (int i = 0; i < 3; ++i) {
      const unsigned lb = la[i] + (la[i] >= 28672u ? 126976u : 12288u);
      dd[i][0] = lds_ld<f32x4>(la[i]) + lds_ld<f32x4>(lb);
      dd[i][1] = lds_ld<f32x4>(la[i] + 256) + lds_ld<f32x4>(lb + 256);
    }
    lbar();
    __builtin_amdgcn_s_setprio(GPRIO);
#pragma unroll
    for (int i = 0; i < 6; ++i) {
      const f32x4 l = dd[i >> 1][i & 1] * rl[i >> 1];
      f32x4 e;
#pragma unroll
      for (int j = 0; j < 4; ++j) e[j] = __builtin_amdgcn_exp2f(l[j]);
      e = e * ri[i >> 1] + ri[i >> 1];
#pragma unroll
      for (int j = 0; j < 4; ++j) e[j] = __builtin_amdgcn_rcpf(e[j]);
      dd[i >> 1][i & 1] *= e;
      asm volatile("" : "+v"(dd[i >> 1][i & 1]));
    }
    const rsrc_t rsO = mk_rsrc(p.out, S * D * 2);
    STAMP(9);
    bar();
    f32x4 ss[3][2];
#pragma unroll
    for (int i = 0; i < 3; ++i) {
      ss[i][0] = lds_ld<f32x4>(la[i]) + lds_ld<f32x4>(la[i] + 12288);
      ss[i][1] = lds_ld<f32x4>(la[i] + 256) + lds_ld<f32x4>(la[i] + 12288 + 256);
    }
#pragma unroll
    for (int i = 0; i < 3; ++i)
      __builtin_amdgcn_raw_buffer_store_b128(cat2(pk4(xf[i][0] + ss[i][0] * dd[i][0]), pk4(xf[i][1] + ss[i][1] * dd[i][1])), rsO, xo[i], 0, 0);
    STAMP(5);
    STAMP_END;
    return;
  }
  {
    const unsigned kh = w & 1, rt = G * 6 + 3 * (w >> 1);
    const rsrc_t rsH = mk_rsrc(p.ws + WS_HA + (rt < 128 ? rt : 0) * (NSTEP * 1024), rt < 128 ? (rt > 125 ? 128 - rt : 3) * (NSTEP * 1024) : 0);
    unsigned tb[3][4];
#pragma unroll
    for (int t = 0; t < 4; ++t) {
      tb[0][t] = (4 * g + q4) * 128 + ((t ^ ((2 * g + (q4 >> 1)) & 3)) << 5) + 8 * p4 + kh * 4096;
      tb[1][t] = tb[0][t] + 49152; tb[2][t] = tb[0][t] + 98304;
      asm volatile("" : "+v"(tb[0][t]), "+v"(tb[1][t]), "+v"(tb[2][t]));
    }
    auto rd1 = [&](int i, int t, int hf) {
      const int sl = i == 36 ? 36 : (2 * i) % 36, bi = sl / 12 > 2 ? 2 : sl / 12;
      return __builtin_bit_cast(u32x2, lds_tr(tb[bi][t] + (sl - 12 * bi) * 4096 + hf * 2048));
    };
    constexpr int NW = P2NW, W0 = P2W0;
    constexpr int OI = O_BAR == 4 ? 17 : (O_BAR == 5 ? 22 : 26);
    constexpr int OB0 = OI == 26 ? NW - 1 : 0;
    f32x4 acc[3][4];
    u32x4 a[NW][3];
    u32x2 bw[2][4][2];
    unsigned va[3];
#pragma unroll
    for (int h = 0; h < 3; ++h) va[h] = lane * 16 + h * NSTEP * 1024;
    auto put = [&](bool hi) {
      acc_fence<12>(&acc[0][0]);
#pragma unroll
      for (int t = 0; t < 4; ++t) {
        const unsigned xa = w * 12288u + t * 1024 + g * 256 + ((r + 4 * t + (g & 2)) & 15) * 16, xb = xa + (hi && w == 3 ? 114688u : 0u);
        lds_st<f32x4>(xa, acc[0][t]); lds_st<f32x4>(xb + 4096, acc[1][t]); lds_st<f32x4>(xb + 8192, acc[2][t]);
      }
      wait_lgkm0();
    };
    unsigned so = (rot + kh) * 1024;
    auto lda = [&](auto I, auto H) {
      constexpr int i = I.v, h = H.v;
      if constexpr (i < 27) {
        if (h == 0 && so >= 54 * 1024) so -= 54 * 1024;
        a[i % NW][h] = __builtin_amdgcn_raw_buffer_load_b128(rsH, va[h], so, 16);
        if (h == 2) so += 2048;
      } else {
        a[i % NW][h] = __builtin_amdgcn_raw_buffer_load_b128(rsH, va[h], (2 * i + kh) * 1024, 16);
      }
      SB;
    };
    lds_wait(L_GO);
    unsigned go2 = 0;
    STAMP(8);
    static_for<0, W0>([&](auto I) { lda(I, IC<0>{}); lda(I, IC<1>{}); lda(I, IC<2>{}); });
#pragma unroll
    for (int t = 0; t < 4; ++t) { bw[0][t][0] = rd1(0, t, 0); bw[0][t][1] = rd1(0, t, 1); }
    static_for<0, 37>([&](auto I) {
      constexpr int i = I.v;
      constexpr int lim = i + NW - 1;
      constexpr int nxt = i <= OI ? (W0 + 2 * i < lim ? W0 + 2 * i : lim) : (27 + OB0 + 2 * (i - OI - 1) < lim ? 27 + OB0 + 2 * (i - OI - 1) : lim);
      constexpr int end = i <= OI ? 27 : 37;
      constexpr int l0 = nxt < end ? nxt : -1, l1 = (nxt + 1 <= lim && nxt + 1 < end) ? nxt + 1 : -1;
      SB;
      static_for<0, 12>([&](auto E) {
        constexpr int e = E.v, t = e / 3, h = e % 3;
        const u32x4 bf = cat2(bw[i & 1][t][0], bw[i & 1][t][1]);
        if constexpr (i == 0 || i == 27) mfma_z(acc[2 - h][t], bf, a[i % NW][2 - h]); else mfma_ip(acc[2 - h][t], bf, a[i % NW][2 - h]);
        SB;
        if constexpr (i == 4 && e == 2) go2 = lds_get(L_GO + 4);
        if constexpr (i < 36 && h < 2) bw[(i + 1) & 1][t][h] = rd1(i + 1, t, h);
        if constexpr (l1 < 0) { if constexpr (h == 2 && t < 3 && l0 >= 0) lda(IC<l0>{}, IC<t>{}); }
        else if constexpr (e & 1) lda(IC<(e < 6 ? l0 : l1)>{}, IC<(e >> 1) % 3>{});
        SB;
      });
      if constexpr (i == 26) put(true);
      if constexpr (i == XBAR) bar();
      if constexpr (i == 4) { if (__builtin_expect(!rfl(go2), 0)) lds_wait(L_GO + 4); asm volatile("" ::: "memory"); STAMP(17); }
      if constexpr (i > 4 && i < 27 && (i % 9 == 4 || i % 9 == 8)) {
        bar();
        if constexpr (i == 26 && OI == 26) static_for<27, 27 + OB0>([&](auto J) { lda(J, IC<0>{}); lda(J, IC<1>{}); lda(J, IC<2>{}); });
        STAMP(17 + (2 * i) / 9);
      }
    });
    STAMP(24);
    put(false);
    bar();
    STAMP(5);
    STAMP_END;
  }
}

extern "C" void kernel_launch(void* const* d_in, const int* in_sizes, int n_in, void* d_out, int out_size, void* d_ws,
                              size_t ws_size, hipStream_t stream) {
  Params p;
  p.x = d_in[0]; p.wqkv = d_in[1]; p.wo = d_in[2]; p.wg = d_in[4]; p.wu = d_in[5]; p.wd = d_in[6];
  p.out = d_out; p.ws = (char*)d_ws;
  fused_block<<<256, 512, 0, stream>>>(p);
}
```

```cpp
#include <hip/hip_runtime.h>
#include <stdint.h>

typedef __attribute__((ext_vector_type(8))) __bf16 bf16x8;
typedef __attribute__((ext_vector_type(4))) __bf16 bf16x4;
typedef __attribute__((ext_vector_type(2))) __bf16 bf16x2;
typedef __attribute__((ext_vector_type(4))) float f32x4;
typedef __attribute__((ext_vector_type(2))) float f32x2;
typedef __attribute__((ext_vector_type(4))) unsigned u32x4;
typedef __attribute__((ext_vector_type(2))) unsigned u32x2;
typedef __amdgpu_buffer_rsrc_t rsrc_t;
#define LAS __attribute__((address_space(3)))
#define GAS __attribute__((address_space(1)))
#define DI __device__ __forceinline__
#define RLX_AGENT __ATOMIC_RELAXED, __HIP_MEMORY_SCOPE_AGENT
typedef GAS unsigned gu32;
typedef GAS unsigned long long gu64;

constexpr int S = 2048, D = 640, F = 1728;
constexpr int NCOL = 5376;
constexpr int NSTEP = 74;
constexpr unsigned WS_EPOCH = 0;
#define FS 128
constexpr unsigned WS_FLAGH = 768u << 10;
constexpr unsigned WS_FLAGO = WS_FLAGH + 8 * 32 * FS;
constexpr unsigned WS_FLAGKV = WS_FLAGO + 8 * 32 * FS;
constexpr unsigned WS_STAT = 4096;
constexpr unsigned WS_CSUM = WS_STAT + S * 8;
constexpr unsigned WS_KVP = WS_CSUM + 3 * NCOL * 8;
constexpr unsigned WS_HA = 1u << 20;
constexpr unsigned WS_DBG = 12u << 20;
constexpr unsigned WS_END = 13u << 20;
constexpr unsigned L_B = 65536;
constexpr unsigned L_SCR = 147456;
constexpr unsigned L_MISC = 159744;
constexpr unsigned L_GO = L_MISC + 3072;
constexpr unsigned SPIN_CAP = 1u << 18;
#define HSLEEP 4
#define HSPLIT 64
#define PPIPE 0
#define STA 0
#define STB 2
#define NPOST 0
#define SNAKE 0
#define NPRE 12
#define WOK 2
#define GRP 1
#define GPRIO 0
#define HPRIO 0
#define XBAR 28
#define PSLEEP 2
#define P2NW 7
#define P2W0 6
#define WSLEEP 0
#define ROTM 18
#define MAPV 0
#define O_BAR 5
#define PRIO_MODE 0
#define DMA_SPACE_A T
#define DMA_SPACE_B (4 * T / 3)

struct Params {
  const void* x; const void* wqkv; const void* wo; const void* wg; const void* wu; const void* wd;
  void* out; char* ws;
};

__shared__ __attribute__((aligned(16))) char smem[163840];

DI unsigned rfl(unsigned v) { return __builtin_amdgcn_readfirstlane(v); }
DI rsrc_t mk_rsrc(const void* p, unsigned bytes) { return __builtin_amdgcn_make_buffer_rsrc((void*)p, (short)0, (int)bytes, 0x00020000); }
DI void dma16(unsigned ldsdst, unsigned voff, rsrc_t rs, unsigned soff) {
  asm volatile("s_mov_b32 m0, %0\n\ts_nop 0\n\tbuffer_load_dwordx4 %1, %2, %3 offen lds" ::"s"(ldsdst), "v"(voff), "s"(rs), "s"(soff) : "memory");
}
template <int N> DI void wait_vm() { asm volatile("s_waitcnt vmcnt(%0)" ::"i"(N) : "memory"); }
DI void drain_vm() { wait_vm<0>(); __builtin_amdgcn_s_waitcnt(0x0F70); }
DI void wait_lgkm0() { asm volatile("s_waitcnt lgkmcnt(0)" ::: "memory"); }
DI void bar() { asm volatile("" ::: "memory"); __builtin_amdgcn_s_barrier(); asm volatile("" ::: "memory"); }
DI void lbar() { wait_lgkm0(); bar(); }
template <class V> DI V lds_ld(unsigned a) { return *(LAS V*)((LAS char*)smem + a); }
template <class V> DI void lds_st(unsigned a, V v) { *(LAS V*)((LAS char*)smem + a) = v; }
DI bf16x4 lds_tr(unsigned a) { return __builtin_amdgcn_ds_read_tr16_b64_v4bf16((LAS bf16x4*)((LAS char*)smem + a)); }
DI bf16x8 cat(bf16x4 a, bf16x4 b) { return __builtin_shufflevector(a, b, 0, 1, 2, 3, 4, 5, 6, 7); }
DI f32x4 mfma(bf16x8 a, bf16x8 b, f32x4 c) { return __builtin_amdgcn_mfma_f32_16x16x32_bf16(a, b, c, 0, 0, 0); }
DI unsigned pk2(float a, float b) { f32x2 v = {a, b}; return __builtin_bit_cast(unsigned, __builtin_convertvector(v, bf16x2)); }
DI u32x2 pk4(f32x4 v) { u32x2 r = {pk2(v[0], v[1]), pk2(v[2], v[3])}; return r; }
DI bf16x8 frag(u32x2 lo, u32x2 hi) { u32x4 v = {lo[0], lo[1], hi[0], hi[1]}; return __builtin_bit_cast(bf16x8, v); }
DI float bflo(unsigned v) { return __builtin_bit_cast(float, v << 16); }
DI float bfhi(unsigned v) { return __builtin_bit_cast(float, v & 0xffff0000u); }
DI float rbf(float v) { return bflo(pk2(v, 0.f)); }
DI float relu2(float v) { float o; asm("v_add_f32 %0, %1, |%1|" : "=v"(o) : "v"(v)); return o; }
DI f32x4 phi4(f32x4 u, f32x4 rl, f32x4 rh) {
  const f32x4 l = u * rl;
  f32x4 e, a;
#pragma unroll
  for (int j = 0; j < 4; ++j) {
    e[j] = __builtin_amdgcn_fmed3f(__builtin_amdgcn_exp2f(l[j]), 0.f, 1.f);
    a[j] = relu2(u[j]);
  }
  return a * rh + e + 1e-6f;
}
DI f32x4 splat(float v) { return f32x4{v, v, v, v}; }
template <int CTRL> DI float dpp_add(float v) {
  return v + __builtin_bit_cast(float, __builtin_amdgcn_update_dpp(0, __builtin_bit_cast(int, v), CTRL, 0xf, 0xf, true));
}
DI float sum64(float v) {
  v = dpp_add<0xB1>(v);
  v = dpp_add<0x4E>(v);
  v = dpp_add<0x141>(v);
  v = dpp_add<0x140>(v);
  v = dpp_add<0x142>(v);
  return dpp_add<0x143>(v);
}
DI float dot2(unsigned a, unsigned b, float c) {
  return __builtin_amdgcn_fdot2_f32_bf16(__builtin_bit_cast(bf16x2, a), __builtin_bit_cast(bf16x2, b), c, false);
}
DI u32x4 ldtr2(unsigned a, unsigned d) { return __builtin_bit_cast(u32x4, cat(lds_tr(a), lds_tr(a + d))); }
DI void mfma_ip(f32x4& c, const u32x4& a, const u32x4& b) { asm volatile("v_mfma_f32_16x16x32_bf16 %0, %1, %2, %0" : "+v"(c) : "v"(a), "v"(b)); }
DI void mfma_z(f32x4& c, const u32x4& a, const u32x4& b) { asm volatile("v_mfma_f32_16x16x32_bf16 %0, %1, %2, 0" : "=&v"(c) : "v"(a), "v"(b)); }
template <int N> DI void acc_fence(f32x4* c) {
  asm volatile("s_nop 7" ::: "memory");
#pragma unroll
  for (int i = 0; i < N; ++i) asm volatile("" : "+v"(c[i]));
}
DI u32x4 cat2(u32x2 a, u32x2 b) { return u32x4{a[0], a[1], b[0], b[1]}; }
DI u32x2 ldtr1(unsigned a) { return __builtin_bit_cast(u32x2, lds_tr(a)); }
template <int IMM> DI void set_m0i(unsigned v) { asm volatile("s_add_i32 m0, %0, %1" ::"s"(v), "i"(IMM) : "memory", "scc"); }
template <int OFF> DI void dma_goi(unsigned voff, rsrc_t rs, unsigned soff) {
  asm volatile("buffer_load_dwordx4 %0, %1, %2 offen offset:%3 lds" ::"v"(voff), "s"(rs), "s"(soff), "i"(OFF) : "memory");
}
DI void set_m0(unsigned v) { asm volatile("s_mov_b32 m0, %0" ::"s"(v) : "memory"); }
DI void dma_go(unsigned voff, rsrc_t rs, unsigned soff) {
  asm volatile("buffer_load_dwordx4 %0, %1, %2 offen lds" ::"v"(voff), "s"(rs), "s"(soff) : "memory");
}
DI f32x4 mfma_u(u32x4 a, u32x4 b, f32x4 c) { return mfma(__builtin_bit_cast(bf16x8, a), __builtin_bit_cast(bf16x8, b), c); }
struct Stamps { unsigned long long t; };
#define STAMP_DECL Stamps stv_ = {0ull}
#define STAMP_ARG , Stamps& stv_
#define STAMP_PASS , stv_
#define STAMPX(c, i) do { if ((i) == 0) { asm volatile("s_memrealtime %0\n\ts_waitcnt lgkmcnt(0)" : "=s"(stv_.t)); } } while (0)
#define STAMP_END do { if (blockIdx.x == 8 && threadIdx.x == 0) __hip_atomic_store((gu64*)(p.ws + WS_DBG), stv_.t, RLX_AGENT); } while (0)
#define STAMP(i) STAMPX(threadIdx.x == 0, i)

DI unsigned poll_flags(gu32* word, bool active, unsigned tag) {
  unsigned v = 0;
#pragma unroll 1
  for (unsigned spins = 0; spins < SPIN_CAP; ++spins) {
    v = __hip_atomic_load(word, RLX_AGENT);
    if (__all(!active || v == tag)) break;
    if (PSLEEP) __builtin_amdgcn_s_sleep(PSLEEP);
  }
  asm volatile("" ::: "memory");
  return v;
}

DI void lds_set(unsigned a) { __hip_atomic_store((LAS unsigned*)smem + a / 4, 1u, __ATOMIC_RELAXED, __HIP_MEMORY_SCOPE_WORKGROUP); }
DI unsigned lds_get(unsigned a) { return __hip_atomic_load((LAS unsigned*)smem + a / 4, __ATOMIC_RELAXED, __HIP_MEMORY_SCOPE_WORKGROUP); }
DI void lds_wait(unsigned a) {
#pragma unroll 1
  for (unsigned spins = 0; spins < (SPIN_CAP << 4); ++spins) {
    if (rfl(lds_get(a))) break;
    __builtin_amdgcn_s_sleep(1);
  }
  asm volatile("" ::: "memory");
}

template <int I> struct IC { static constexpr int v = I; };
template <int B, int E, class Fn> DI void static_for(Fn&& f) {
  if constexpr (B < E) { f(IC<B>{}); static_for<B + 1, E>(f); }
}

enum { J_MLP = 0, J_KV = 1, J_Q = 2, J_SPARE = 3 };

struct Dma {
  rsrc_t rsA, rsB;
  unsigned vA0, vA1, vB0, vB1, vB2;
  unsigned sA[4], sB, advB;
  unsigned mA, mB, mW;
};

constexpr unsigned a_slot(unsigned s3) { return s3 == 0 ? 126976u : (s3 - 1) * 32768u; }
#define SB __builtin_amdgcn_sched_barrier(0)

template <int T> DI unsigned bofs(const unsigned (&boff)[T], int t) { return T == 6 && t >= 3 ? boff[t - 3] + 6144 : boff[t]; }
template <int T, bool TR, int KS, class Extra>
DI void kstep(f32x4 (&acc)[4][T], const u32x4 (&ac)[4], const u32x2 (&bc)[T][2], u32x4 (&an)[4], u32x2 (&bn)[T][2],
              const unsigned (&aoff)[4], const unsigned (&boff)[T], const Dma& d, Extra&& extra) {
  constexpr int s = KS >> 1, KIND = KS & 1, NP = KIND == 0 ? 4 : 3, ST = KIND == 0 ? (STA ? STA : (4 * T - 1) / NP) : (STB ? STB : (4 * T - 1) / NP);
  constexpr int sn = (KS + 1) >> 1;
  constexpr unsigned asl = a_slot(sn % 3), bsl = ((KS + 1) % 5) * 12288u;
  static_for<0, 4 * T>([&](auto E) {
    constexpr int e = E.v, t = e >> 2, i = (SNAKE && (t & 1)) ? 3 - (e & 3) : (e & 3);
    const u32x4 bf = cat2(bc[t][0], bc[t][1]);
    if constexpr (KS == 0) { if (TR) mfma_z(acc[i][t], bf, ac[i]); else mfma_z(acc[i][t], ac[i], bf); }
    else { if (TR) mfma_ip(acc[i][t], bf, ac[i]); else mfma_ip(acc[i][t], ac[i], bf); }
    SB;
    if constexpr (KS < 19) {
      if constexpr (e < 4) an[e] = lds_ld<u32x4>(aoff[((KS + 1) & 1) + (sn % 3 == 0 ? 2 : 0)] + (sn % 3 == 0 ? 0u : asl) + e * 2048);
      else if constexpr (e < 4 + 2 * T) bn[(e - 4) >> 1][e & 1] = ldtr1(bofs<T>(boff, (e - 4) >> 1) + bsl + (e & 1) * 768);
    }
    constexpr int pc = e / ST;
    if constexpr (KIND == 0) {
      constexpr int adv = s < 8 ? (s + 2) * 128 : 0, dst = (int)a_slot((s + 2) % 3) + pc * 1024, off = dst >= adv ? adv : 0;
      if constexpr (pc < NP && e % ST == ST - 2) set_m0i<dst - off>(d.mA);
      if constexpr (pc < NP && e % ST == ST - 1) dma_goi<off>((pc & 1) ? d.vA1 : d.vA0, d.rsA, d.sA[pc] + (adv - off));
    } else {
      constexpr int h0 = (2 * s) % 5;
      if constexpr (pc < NP && e % ST == ST - 2) set_m0i<(h0 == 4 ? 0 : h0 * 12288) + pc * 1024>(h0 == 4 ? d.mW : d.mB);
      if constexpr (pc < NP && e % ST == ST - 1) dma_goi<0>(pc == 0 ? d.vB0 : (pc == 1 ? d.vB1 : d.vB2), d.rsB, d.sB);
    }
    extra(E);
    SB;
  });
}

struct RowStat { u32x4 xa; unsigned xb, row; };
template <int NY> DI void row_stat(RowStat& R, unsigned lane) {
  float s = 0.f, q = 0.f;
  asm volatile("s_waitcnt vmcnt(%8)\n\t"
               "v_dot2c_f32_bf16 %0, %2, %7\n\tv_dot2c_f32_bf16 %1, %2, %2\n\tv_dot2c_f32_bf16 %0, %3, %7\n\tv_dot2c_f32_bf16 %1, %3, %3\n\t"
               "v_dot2c_f32_bf16 %0, %4, %7\n\tv_dot2c_f32_bf16 %1, %4, %4\n\tv_dot2c_f32_bf16 %0, %5, %7\n\tv_dot2c_f32_bf16 %1, %5, %5\n\t"
               "v_dot2c_f32_bf16 %0, %6, %7\n\tv_dot2c_f32_bf16 %1, %6, %6\n\ts_nop 2"
               : "+v"(s), "+v"(q) : "v"(R.xa[0]), "v"(R.xa[1]), "v"(R.xa[2]), "v"(R.xa[3]), "v"(R.xb), "v"(0x3f803f80u), "i"(NY) : "memory");
  const float mean = sum64(s) * (1.0f / D);
  const float var = fmaxf(sum64(q) * (1.0f / D) - mean * mean, 0.f);
  const float sd = __builtin_amdgcn_sqrtf(rbf(rbf(var) + 1e-5f));
  if (lane == 63) lds_st<unsigned>(L_GO + 32 + (R.row & 7) * 4, pk2(mean, sd));
}
DI void row_publish(const RowStat& R, char* ws, unsigned tag, unsigned tid) {
  if (tid < 8) __hip_atomic_store((gu64*)(ws + WS_STAT) + R.row + tid, ((unsigned long long)tag << 32) | lds_ld<unsigned>(L_GO + 32 + tid * 4), RLX_AGENT);
}

struct Gran { u32x2 g0, g1, g2; unsigned off, step; };
DI void gran_issue(Gran& G, rsrc_t rs) {
  asm volatile("buffer_load_dwordx2 %0, %3, %4, 0 offen sc1\n\tbuffer_load_dwordx2 %1, %3, %4, %5 offen sc1\n\t"
               "buffer_load_dwordx2 %2, %3, %4, %6 offen sc1"
               : "=&v"(G.g0), "=&v"(G.g1), "=&v"(G.g2) : "v"(G.off), "s"(rs), "s"(G.step), "s"(2 * G.step) : "memory");
}
DI void gran_load(Gran& G, rsrc_t rs) {
  asm volatile("buffer_load_dwordx2 %0, %3, %4, 0 offen sc1\n\tbuffer_load_dwordx2 %1, %3, %4, %5 offen sc1\n\t"
               "buffer_load_dwordx2 %2, %3, %4, %6 offen sc1\n\ts_waitcnt vmcnt(0)"
               : "=&v"(G.g0), "=&v"(G.g1), "=&v"(G.g2) : "v"(G.off), "s"(rs), "s"(G.step), "s"(2 * G.step) : "memory");
}

DI unsigned urow(unsigned pos, unsigned rot) { const unsigned u = pos + rot; return (u >= 54 ? u - 54 : u) * 40960u; }

constexpr unsigned wo_tile(unsigned v) { return (v / 5) * 10 + (v % 5 == 4 ? 4 : (v % 5 < 2 ? 2 * (v % 5) : 2 * (v % 5) + 1)); }
struct W2 { rsrc_t wd, wo; unsigned rot, col; };
DI unsigned w2_off(const W2& W, int u, unsigned lw) {
  if (u < 54) return urow(u, W.rot) + lw * 10240u + W.col;
  return wo_tile(u - 54) * 20480u + (lw >> 1) * ((u - 54) % 5 == 4 ? 102400u : 20480u) + (lw & 1) * 10240u + W.col;
}
DI u32x4 w2_load(const W2& W, int u, unsigned lw, unsigned voff) {
  return __builtin_amdgcn_raw_buffer_load_b128(u < 54 ? W.wd : W.wo, voff, w2_off(W, u, lw), 0);
}

template <int T, bool TR, bool PF, bool LASTA, class Pre>
DI void main_loop(Pre&& pre, RowStat& R, char* ws, f32x4 (&acc)[4][T], unsigned (&boff)[T], Dma& d, unsigned w, unsigned lane, unsigned tid,
                  unsigned tag, unsigned rb, unsigned ccol, rsrc_t rsB2, rsrc_t rsWS, unsigned cb, unsigned rot, u32x4 (&pre2)[36] STAMP_ARG) {
  const unsigned g = lane >> 4, r = lane & 15, m = w & 3;
  unsigned aoff[4];
  aoff[0] = (64 * m + r) * 128 + ((g ^ (r >> 1)) << 4);
  aoff[1] = aoff[0] ^ 64;
  aoff[2] = aoff[0] + a_slot(0); aoff[3] = aoff[1] + a_slot(0);
  const unsigned wq = (w & 1) * 2 + ((w >> 1) & 1);
  asm volatile("" : "+v"(aoff[0]), "+v"(aoff[1]), "+v"(aoff[2]), "+v"(aoff[3]), "+v"(boff[0]), "+v"(boff[1]), "+v"(boff[2]));
  if (T == 5) asm volatile("" : "+v"(boff[3]), "+v"(boff[4]));
  d.mA = w * 4096u; d.mB = L_B + wq * 3072u + (w < 4 ? 0u : 12288u); d.mW = L_B + wq * 3072u + (w < 4 ? 49152u : 0u);
  Gran G;
  unsigned v2, vb2, tA[3], tB[3];
  {
    const unsigned bdst = L_B + (w < 4 ? 36864u : 24576u) + wq * 3072u;
    unsigned so = d.sB + d.advB;
    dma16(bdst, d.vB0, d.rsB, so); dma16(bdst + 1024, d.vB1, d.rsB, so); dma16(bdst + 2048, d.vB2, d.rsB, so);
#pragma unroll
    for (int i = 0; i < 4; ++i) dma16(a_slot(1) + d.mA + i * 1024, (i & 1) ? d.vA1 : d.vA0, d.rsA, d.sA[i] + 128);
    pre();
    v2 = (lane >> 3) * 1280 + (((lane & 7) ^ (((lane >> 4) & 3) << 1)) << 4);
    vb2 = v2 + (3 * wq) * 10240u;
    G.off = w < 4 ? WS_CSUM + ccol * 8 : WS_STAT + (rb * 256 + tid - 256) * 8;
    G.step = w < 4 ? NCOL * 8 : 0;
    tA[0] = urow(w, rot) + cb * 128u; tA[1] = urow(8 + w, rot) + cb * 128u; tA[2] = urow(31 + w, rot) + cb * 128u;
    tB[0] = urow(16, rot) + cb * 128u; tB[1] = urow(w < 4 ? 19u : 22u, rot) + cb * 128u; tB[2] = urow(w < 4 ? 25u : 28u, rot) + cb * 128u;
    asm volatile("" : "+v"(v2), "+v"(vb2), "+v"(G.off));
    asm volatile("" : "+s"(G.step), "+s"(tA[0]), "+s"(tA[1]), "+s"(tA[2]), "+s"(tB[0]), "+s"(tB[1]), "+s"(tB[2]));
    wait_vm<7>();
    bar();
    if (w >= 4) {
      so += d.advB;
      dma16(bdst + 24576, d.vB0, d.rsB, so); dma16(bdst + 24576 + 1024, d.vB1, d.rsB, so); dma16(bdst + 24576 + 2048, d.vB2, d.rsB, so);
    }
    d.sB = so;
  }
  STAMP(1);
  u32x4 a0[4], a1[4];
  u32x2 b0[T][2], b1[T][2];
#pragma unroll
  for (int i = 0; i < 4; ++i) a0[i] = lds_ld<u32x4>(aoff[2] + i * 2048);
#pragma unroll
  for (int t = 0; t < T; ++t) { b0[t][0] = ldtr1(bofs<T>(boff, t)); b0[t][1] = ldtr1(bofs<T>(boff, t) + 768); }
  const unsigned vpf = w >= 4 ? v2 : 0x7ffffff0u;
  const bool y = w >= 4;
  bool ok = true;
  auto ln_store = [&] {
    const float v0 = y ? -bflo(G.g0[0]) : __builtin_bit_cast(float, G.g0[0]) + __builtin_bit_cast(float, G.g1[0]) + __builtin_bit_cast(float, G.g2[0]);
    const unsigned la = L_MISC + (y ? tid * 4 - 1024 : 2048 + tid * 4);
    lds_st<float>(la, v0);
    lds_st<float>(y ? la + 1024 : L_GO + 64 + (lane & 7) * 4, __builtin_amdgcn_rcpf(bfhi(G.g0[0])));
  };
  static_for<0, 10>([&](auto S_) {
    constexpr int s = S_.v, L = 4 * T;
    constexpr int NX = (PF && s >= 1 && 2 * s <= NPRE) ? 2 : 0;
    kstep<T, TR, 2 * s>(acc, a0, b0, a1, b1, aoff, boff, d, [&](auto E) {
      constexpr int e = E.v;
      if constexpr (NX && e >= L - 2) {
        constexpr int u = 2 * (s - 1) + e - (L - 2);
        pre2[u] = __builtin_amdgcn_raw_buffer_load_b128(rsB2, vpf, urow(36 + u, rot) + (w & 3) * 10240u + cb * 128u, 0);
      }
      if constexpr (s < 7) { if constexpr (e == L - 3) d.sB += d.advB; }
      else if constexpr (s == 7) {
        if constexpr (e == L - 6) d.vB0 = y ? vb2 : d.vB0;
        if constexpr (e == L - 4) d.vB1 = y ? vb2 + 10240u : d.vB1;
        if constexpr (e == L - 3) d.vB2 = y ? vb2 + 20480u : d.vB2;
        if constexpr (e == L - 2) d.sB = y ? tB[0] : d.sB + d.advB;
        if constexpr (e == L - 1) d.rsB = y ? rsB2 : d.rsB;
      } else {
        if constexpr (e == L - 3) { d.sB = tB[s - 7]; d.rsB = rsB2; d.vB0 = vb2; d.vB1 = vb2 + 10240u; d.vB2 = vb2 + 20480u; }
      }
      if constexpr (s == 9 && e == L - 4) {
        asm volatile("s_waitcnt vmcnt(7)" : "+v"(G.g0), "+v"(G.g1), "+v"(G.g2)::"memory");
        ok = __all(G.g0[1] == tag && G.g1[1] == tag && G.g2[1] == tag);
      }
      if constexpr (s == 9 && e == L - 3) ln_store();
    });
    if constexpr (s == 0) row_stat<63>(R, lane);
    if constexpr (s == 9) {
      if (__builtin_expect(!ok, 0)) {
#pragma unroll 1
        for (unsigned spins = 0; spins < SPIN_CAP; ++spins) {
          __builtin_amdgcn_s_sleep(2);
          gran_load(G, rsWS);
          if (__all(G.g0[1] == tag && G.g1[1] == tag && G.g2[1] == tag)) break;
        }
        ln_store();
      }
    }
    wait_lgkm0();
    if constexpr (s < 9) wait_vm<4 + NX>();
    bar();
    SB;
    kstep<T, TR, 2 * s + 1>(acc, a1, b1, a0, b0, aoff, boff, d, [&](auto E) {
      constexpr int e = E.v;
      if constexpr (s == 0 && e == L - 4) row_publish(R, ws, tag, tid);
      if constexpr (s == 8 && e == 0) gran_issue(G, rsWS);
      if constexpr (s >= 7 && s < 9 && e >= L - 4) {
        d.rsA = rsB2; d.vA0 = v2; d.vA1 = v2;
        d.sA[e - (L - 4)] = tA[s - 7] + (e - (L - 4)) * 10240;
      }
      if constexpr (s == 9 && LASTA && e == 7) {
        if (w < 5) {
#pragma unroll
          for (int i = 0; i < 4; ++i) dma16(a_slot(0) + d.mA + i * 1024, v2, rsB2, tA[2] + i * 10240);
        }
      }
    });
    if constexpr (s == 8) { STAMP(13); }
    if constexpr (s == 2) { STAMP(26); }
    if constexpr (s == 6) { STAMP(27); }
  });
  acc_fence<4 * T>(&acc[0][0]);
  STAMP(14);
}

DI void st16(u32x4 v, rsrc_t rs, unsigned voff, unsigned soff) {
  __builtin_amdgcn_raw_buffer_store_b128(v, rs, voff, soff, 16);
  asm volatile("s_nop 1" ::"v"(v) : "memory");
}
DI void publish_flag(const Params& p, unsigned flag_off, unsigned tag, unsigned tid) {
  drain_vm();
  bar();
  if (tid == 0) __hip_atomic_store((gu32*)(p.ws + flag_off), tag, RLX_AGENT);
}
DI void st_frag(rsrc_t rs, unsigned lane, unsigned rt, unsigned step, u32x2 lo, u32x2 hi) {
  u32x4 v = {lo[0], lo[1], hi[0], hi[1]};
  st16(v, rs, lane * 16, WS_HA + (rt * NSTEP + step) * 1024);
}
DI void shared_send(unsigned w, unsigned lane, const u32x2 (&sh)[4]) {
  const bool n = w >= 4;
  u32x4 snd = n ? u32x4{sh[0][0], sh[0][1], sh[1][0], sh[1][1]} : u32x4{sh[2][0], sh[2][1], sh[3][0], sh[3][1]};
  lds_st<u32x4>(L_SCR + w * 1024 + lane * 16, snd);
}
DI void shared_recv(rsrc_t rs, unsigned w, unsigned lane, unsigned rt0, unsigned step, const u32x2 (&sh)[4]) {
  lbar();
  u32x4 rcv = lds_ld<u32x4>(L_SCR + (w ^ 4) * 1024 + lane * 16);
  u32x2 r0 = {rcv[0], rcv[1]}, r1 = {rcv[2], rcv[3]};
  if (w >= 4) { st_frag(rs, lane, rt0 + 2, step, r0, sh[2]); st_frag(rs, lane, rt0 + 3, step, r1, sh[3]); }
  else        { st_frag(rs, lane, rt0 + 0, step, sh[0], r0); st_frag(rs, lane, rt0 + 1, step, sh[1], r1); }
}

__global__ __launch_bounds__(512, 2) void fused_block(Params p) {
  const unsigned tid = threadIdx.x, lane = tid & 63, w = rfl(tid >> 6);
  const unsigned b = blockIdx.x, xcd = b & 7, slot = b >> 3;
  unsigned jtype, jidx;
  const unsigned rb = 4 * (xcd & 1) + (slot & 3), jq = xcd >> 1, ji = slot >> 2;
  const unsigned spi = (xcd - 4) * 4 + (slot & 3);
  if (ji == 7) { jtype = J_Q; jidx = jq; }
  else if (ji >= 5) { jtype = J_KV; jidx = 2 * jq + ji - 5; }
  else if (jq < 2) { jtype = J_MLP; jidx = 5 * jq + ji; }
  else if (ji < 4) { jtype = J_MLP; jidx = 10 + 4 * (jq - 2) + ji; }
  else { jtype = J_SPARE; jidx = 0; }
  STAMP_DECL;
  STAMP(0);
  const rsrc_t rsX = mk_rsrc(p.x, S * D * 2);

  RowStat rs_;
  rs_.row = rfl(b * 8 + w);
  asm volatile("s_nop 4\n\tbuffer_load_dwordx4 %0, %2, %4, %5 offen\n\tbuffer_load_dword %1, %3, %4, %5 offen offset:1024"
               : "=&v"(rs_.xa), "=&v"(rs_.xb) : "v"(lane * 16), "v"(lane * 4), "s"(rsX), "s"(rs_.row * 1280) : "memory");
  Dma d;
  const bool act = jtype != J_SPARE;
  {
    const unsigned mh = w & 1;
    const void* bw; unsigned pitch, nch, colb;
    if (jtype == J_MLP) { bw = mh ? p.wu : p.wg; pitch = 3456; colb = 192 * jidx; nch = 12; }
    else if (jtype == J_KV) { bw = p.wqkv; pitch = 3840; colb = 1280 + 1280 * mh + 160 * jidx; nch = 10; }
    else { bw = p.wqkv; pitch = 3840; colb = 320 * jidx + 160 * mh; nch = 10; }
    d.rsA = rsX; d.rsB = mk_rsrc(bw, D * pitch);
    d.vA0 = (lane >> 3) * 1280 + (((lane & 7) ^ (lane >> 4)) << 4); d.vA1 = d.vA0 ^ 64;
#pragma unroll
    for (int i = 0; i < 4; ++i) d.sA[i] = (rb * 256 + 32 * w + 8 * i) * 1280;
    asm volatile("s_nop 4" ::: "memory");
    if (act) {
      const unsigned adst = a_slot(0) + w * 4096u;
#pragma unroll
      for (int i = 0; i < 4; ++i) dma16(adst + i * 1024, (i & 1) ? d.vA1 : d.vA0, d.rsA, d.sA[i]);
    }
    unsigned vb[3];
#pragma unroll
    for (int i = 0; i < 3; ++i) {
      const unsigned c = 64 * i + lane, row = c / 12, pos = c % 12;
      unsigned ch = pos ^ (((row >> 3) & 1) << 1);
      if (ch >= nch) ch -= 2;
      vb[i] = row * pitch + ch * 16;
    }
    d.vB0 = vb[0]; d.vB1 = vb[1]; d.vB2 = vb[2];
    d.sB = (16 * ((w >> 1) & 1) + (w < 4 ? 32 : 0)) * pitch + colb; d.advB = 64 * pitch;
    const unsigned bdst = L_B + (w < 4 ? 12288u : 0u) + ((w & 1) * 2 + ((w >> 1) & 1)) * 3072u;
    if (act) { dma16(bdst, d.vB0, d.rsB, d.sB); dma16(bdst + 1024, d.vB1, d.rsB, d.sB); dma16(bdst + 2048, d.vB2, d.rsB, d.sB); }
  }
  STAMP(10);
  if (tid < 2) lds_st<unsigned>(L_GO + tid * 4, 0u);
  unsigned tag;
  asm volatile("s_load_dword %0, %1, 0x0\n\ts_waitcnt lgkmcnt(0)" : "=s"(tag) : "s"(p.ws) : "memory");
  tag += 1;

  const unsigned t2 = 28 * xcd + slot;
  const bool has2 = slot < 28 && t2 < 220;
  const unsigned G = t2 / 10, cb = t2 % 10;
  constexpr unsigned rot = 32;
  const unsigned g = lane >> 4, r = lane & 15, q4 = r >> 2, p4 = lane & 3;
  const rsrc_t rsWd = mk_rsrc(p.wd, has2 ? F * D * 2 : 0);
  const rsrc_t rsWS = mk_rsrc(p.ws, WS_END);
  const unsigned v2 = (lane >> 3) * 1280 + (((lane & 7) ^ (((lane >> 4) & 3) << 1)) << 4);
  const unsigned m = w & 3, n = w >> 2;
  const unsigned rt0 = rb * 16 + m * 4;
  const unsigned brow = L_B + (8 * g + q4) * 192;
  const unsigned bx = (g & 1) << 5;

  u32x4 pre2[36];
  W2 W;
  W.wd = rsWd; W.wo = mk_rsrc(p.wo, has2 ? D * D * 2 : 0); W.rot = rot; W.col = cb * 128u;
  if (jtype == J_MLP) {
    f32x4 acc[4][6];
    unsigned boff[6];
#pragma unroll
    for (int t = 0; t < 3; ++t) {
      const unsigned gt = n ? (t == 2 ? 3 : 4 + t) : t;
      boff[t] = brow + ((32 * gt + 8 * p4) ^ bx); boff[t + 3] = boff[t] + 6144;
    }
    const unsigned ccol = tid < 96 ? 1920 + 96 * jidx + tid : 3648 + 96 * jidx + (tid < 192 ? tid - 96 : 0);
    main_loop<6, true, true, true>([] {}, rs_, p.ws, acc, boff, d, w, lane, tid, tag, rb, ccol, rsWd, rsWS, cb, rot, pre2 STAMP_PASS);
    if (w >= 4) {
#pragma unroll
      for (int u = NPRE; u < NPRE + NPOST; ++u) { pre2[u] = w2_load(W, 36 + u, w & 3, v2); SB; }
    }
    float mm[4];
    f32x4 cg[3], cu[3];
#pragma unroll
    for (int i = 0; i < 4; ++i) mm[i] = lds_ld<float>(L_MISC + (64 * m + 16 * i + r) * 4);
#pragma unroll
    for (int t = 0; t < 3; ++t) {
      const unsigned gt = n ? (t == 2 ? 3 : 4 + t) : t;
      cg[t] = lds_ld<f32x4>(L_MISC + 2048 + (16 * gt + 4 * g) * 4); cu[t] = lds_ld<f32x4>(L_MISC + 2048 + (96 + 16 * gt + 4 * g) * 4);
    }
    auto hval = [&](int i, int t) {
      f32x4 gg = acc[i][t] + mm[i] * cg[t];
      const f32x4 uu = acc[i][t + 3] + mm[i] * cu[t];
#pragma unroll
      for (int j = 0; j < 4; ++j) gg[j] = relu2(gg[j]);
      return pk4(gg * uu);
    };
#pragma unroll
    for (int i = 0; i < 4; ++i) { st_frag(rsWS, lane, rt0 + i, 3 * jidx + 2 * n, hval(i, 0), hval(i, 1)); SB; }
    const u32x2 sh[4] = {hval(0, 2), hval(1, 2), hval(2, 2), hval(3, 2)};
    shared_send(w, lane, sh);
    shared_recv(rsWS, w, lane, rt0, 3 * jidx + 1, sh);
    STAMP(15);
    publish_flag(p, WS_FLAGH + (rb * 32 + jidx) * FS, tag, tid);
    STAMP(3);
  } else if (jtype == J_KV) {
    f32x4 acc[4][5];
    unsigned boff[5];
    boff[0] = brow + ((80 * n + 8 * p4) ^ bx); boff[1] = boff[0] + 6144;
    boff[2] = brow + ((80 * n + 40 + 8 * p4) ^ bx); boff[3] = boff[2] + 6144;
    boff[4] = brow + ((80 * n + 32 + 40 * (p4 >> 1)) ^ bx) + (p4 & 1) * 6144;
    const unsigned kb = 126976u + m * 6720 + n * 3360, dump = L_MISC - 2048 + lane * 16;
    const unsigned ea = (q4 == 1 || r == 0) ? kb + (r == 0 ? 20 : 12 + r) * 80 : 0u, eb = (q4 == 3 || r == 0) ? kb + 1680 + (r == 0 ? 20 : 4 + r) * 80 : 0u;
    unsigned ka[6] = {kb + r * 80 + g * 16, ea ? ea + g * 16 : dump, eb ? eb + g * 16 : dump, g == 0 ? kb + r * 80 + 64 : dump,
                      g == 2 ? kb + 1680 + r * 80 + 64 : dump, (g == 0 && ea) ? ea + 64 : ((g == 2 && eb) ? eb + 64 : dump)};
    auto pin = [&] { asm volatile("" : "+v"(ka[0]), "+v"(ka[1]), "+v"(ka[2]), "+v"(ka[3]), "+v"(ka[4]), "+v"(ka[5])); };
    const unsigned ccol = tid < 80 ? 640 + 80 * jidx + tid : 1280 + 80 * jidx + (tid < 160 ? tid - 80 : 0);
    main_loop<5, false, false, false>(pin, rs_, p.ws, acc, boff, d, w, lane, tid, tag, rb, ccol, rsWd, rsWS, cb, rot, pre2 STAMP_PASS);
    float cs[5];
    cs[0] = lds_ld<float>(L_MISC + 2048 + (40 * n + r) * 4);
    cs[1] = lds_ld<float>(L_MISC + 2048 + (80 + 40 * n + r) * 4);
    cs[2] = lds_ld<float>(L_MISC + 2048 + (40 * n + 20 + r) * 4);
    cs[3] = lds_ld<float>(L_MISC + 2048 + (80 + 40 * n + 20 + r) * 4);
    cs[4] = lds_ld<float>(L_MISC + 2048 + ((q4 & 1) * 80 + 40 * n + 16 + 20 * (q4 >> 1) + (r & 3)) * 4);
    u32x2 pk[4][3], vv[4][3];
    f32x4 mm4[4], rr4[4];
#pragma unroll
    for (int i = 0; i < 4; ++i) {
      mm4[i] = lds_ld<f32x4>(L_MISC + (64 * m + 16 * i + 4 * g) * 4); rr4[i] = lds_ld<f32x4>(L_MISC + 1024 + (64 * m + 16 * i + 4 * g) * 4);
    }
    SB;
#pragma unroll
    for (int i = 0; i < 4; ++i) {
      const f32x4 mm = mm4[i], rr = rr4[i];
      const f32x4 rl = rr * 1.4426950408889634f, rh = rr * 0.5f;
      f32x4 val[5];
#pragma unroll
      for (int t = 0; t < 5; ++t) val[t] = acc[i][t] + mm * cs[t];
#pragma unroll
      for (int t = 0; t < 3; ++t) pk[i][t] = pk4(phi4(val[2 * t], rl, rh));
      vv[i][0] = pk4(val[1] * rr); vv[i][1] = pk4(val[3] * rr);
      const u32x2 v4 = pk4(val[4] * rr);
      vv[i][2] = (q4 & 1) ? v4 : u32x2{0x3f803f80u, 0x3f803f80u};
    }
    STAMP(25);
    f32x4 P[7] = {};
#pragma unroll
    for (int i = 0; i < 4; i += 2) {
      const bf16x8 k0 = frag(pk[i][0], pk[i + 1][0]), k2 = frag(pk[i][1], pk[i + 1][1]), k4 = frag(pk[i][2], pk[i + 1][2]);
      const bf16x8 v1 = frag(vv[i][0], vv[i + 1][0]), v3 = frag(vv[i][1], vv[i + 1][1]), v4 = frag(vv[i][2], vv[i + 1][2]);
      P[0] = mfma(k0, v1, P[0]); P[1] = mfma(k0, v4, P[1]); P[2] = mfma(k4, v1, P[2]);
      P[3] = mfma(k2, v3, P[3]); P[4] = mfma(k2, v4, P[4]); P[5] = mfma(k4, v3, P[5]); P[6] = mfma(k4, v4, P[6]);
    }
    lds_st<f32x4>(ka[0], P[0]); lds_st<f32x4>(ka[0] + 1680, P[3]); lds_st<f32x4>(ka[1], P[1]); lds_st<f32x4>(ka[2], P[4]);
    lds_st<f32x4>(ka[3], P[2]); lds_st<f32x4>(ka[4], P[5]); lds_st<f32x4>(ka[5], P[6]);
    lbar();
    if (tid < 210) {
      const unsigned a = 126976u + tid * 32;
      const f32x4 t0 = (lds_ld<f32x4>(a) + lds_ld<f32x4>(a + 6720)) + (lds_ld<f32x4>(a + 13440) + lds_ld<f32x4>(a + 20160));
      const f32x4 t1 = (lds_ld<f32x4>(a + 16) + lds_ld<f32x4>(a + 6736)) + (lds_ld<f32x4>(a + 13456) + lds_ld<f32x4>(a + 20176));
      st16(cat2(pk4(t0), pk4(t1)), rsWS, tid * 16, WS_KVP + (rb * 32 + 4 * jidx) * 840);
    }
    wait_lgkm0();
    STAMP(15);
    publish_flag(p, WS_FLAGKV + ((jidx >> 1) * 32 + (jidx & 1) * 8 + rb) * FS, tag, tid);
    STAMP(3);
  } else if (jtype == J_Q) {
    f32x4 acc[4][5];
    unsigned boff[5];
#pragma unroll
    for (int t = 0; t < 5; ++t) boff[t] = brow + n * 6144 + ((32 * t + 8 * p4) ^ bx);
    const unsigned ccol = 160 * jidx + (tid < 160 ? tid : 0);
    main_loop<5, true, false, false>([] {}, rs_, p.ws, acc, boff, d, w, lane, tid, tag, rb, ccol, rsWd, rsWS, cb, rot, pre2 STAMP_PASS);
    u32x2 pq[4][5];
    {
      float mm[4], rr[4];
      f32x4 c[5];
#pragma unroll
      for (int i = 0; i < 4; ++i) { mm[i] = lds_ld<float>(L_MISC + (64 * m + 16 * i + r) * 4); rr[i] = lds_ld<float>(L_MISC + 1024 + (64 * m + 16 * i + r) * 4); }
#pragma unroll
      for (int t = 0; t < 5; ++t) c[t] = lds_ld<f32x4>(L_MISC + 2048 + (80 * n + 16 * t + 4 * g) * 4);
      SB;
#pragma unroll
      for (int i = 0; i < 4; ++i)
#pragma unroll
        for (int t = 0; t < 5; ++t) pq[i][t] = pk4(phi4(acc[i][t] + mm[i] * c[t], splat(rr[i] * 1.4426950408889634f), splat(rr[i] * 0.5f)));
#pragma unroll
      for (int i = 0; i < 4; ++i) asm volatile("" : "+v"(pq[i][0]), "+v"(pq[i][1]), "+v"(pq[i][2]), "+v"(pq[i][3]), "+v"(pq[i][4]));
    }
    constexpr unsigned KP = 208, KQ = 84 * KP;
    unsigned wa[2];
#pragma unroll
    for (int k = 0; k < 2; ++k) {
      const unsigned qd = 2 * (tid < 420 ? tid : 0) + k, hd = qd / 105, e = (qd % 105) / 5, dq = qd % 5, hl = hd & 3;
      wa[k] = (hd >> 2) * KQ + (e < 20 ? hl * 20 + e : 80 + hl) * KP + hl * 40 + dq * 8;
    }
    unsigned fk = n * KQ + r * KP + g * 8, fz = n * KQ + (80 + (r & 3)) * KP + g * 8;
    asm volatile("" : "+v"(wa[0]), "+v"(wa[1]), "+v"(fk), "+v"(fz));
    STAMP(25);
    wait_vm<0>();
    bar();
#pragma unroll
    for (int k = 0; k < 5; ++k) lds_st<u32x4>(k * 8192 + tid * 16, u32x4{0u, 0u, 0u, 0u});
    if (w == 0) poll_flags((gu32*)(p.ws + WS_FLAGKV + (jidx * 32 + (lane & 15)) * FS), true, tag);
    lbar();
    STAMP(4);
    if (w < 7) {
      u32x4 v[8];
#pragma unroll
      for (int j = 0; j < 8; ++j)
        v[j] = __builtin_amdgcn_raw_buffer_load_b128(rsWS, (tid < 420 ? tid : 0) * 16, WS_KVP + (j * 32 + 8 * jidx) * 840, 16);
      SB;
      f32x4 t0 = {0.f, 0.f, 0.f, 0.f}, t1 = t0;
#pragma unroll
      for (int j = 0; j < 8; ++j) {
        t0 += f32x4{bflo(v[j][0]), bfhi(v[j][0]), bflo(v[j][1]), bfhi(v[j][1])};
        t1 += f32x4{bflo(v[j][2]), bfhi(v[j][2]), bflo(v[j][3]), bfhi(v[j][3])};
      }
      lds_st<u32x2>(wa[0], pk4(t0)); lds_st<u32x2>(wa[1], pk4(t1));
    }
    lbar();
    STAMP(16);
    bf16x8 A[12];
#pragma unroll
    for (int P = 0; P < 3; ++P) A[P] = frag(lds_ld<u32x2>(fz + P * 64), lds_ld<u32x2>(fz + P * 64 + 32));
#pragma unroll
    for (int u = 0; u < 5; ++u) {
      const unsigned a0 = fk + u * 16 * KP + (u < 3 ? 0 : 64);
      A[3 + u] = frag(lds_ld<u32x2>(a0), lds_ld<u32x2>(a0 + 32));
      if (u > 0) A[7 + u] = frag(lds_ld<u32x2>(a0 + 64), lds_ld<u32x2>(a0 + 96));
    }
    u32x2 sh[4];
#pragma unroll
    for (int i = 0; i < 4; ++i) {
      const bf16x8 B[3] = {frag(pq[i][0], pq[i][1]), frag(pq[i][2], pq[i][3]), frag(pq[i][4], u32x2{0u, 0u})};
      const f32x4 zero = {0.f, 0.f, 0.f, 0.f};
      const f32x4 den = mfma(A[2], B[2], mfma(A[1], B[1], mfma(A[0], B[0], zero))) + 1e-6f;
      f32x4 num[5];
#pragma unroll
      for (int u = 0; u < 5; ++u) num[u] = mfma(A[3 + u], B[u < 3 ? 0 : 1], zero);
#pragma unroll
      for (int u = 1; u < 5; ++u) num[u] = mfma(A[7 + u], B[u < 3 ? 1 : 2], num[u]);
      float rc[4];
#pragma unroll
      for (int j = 0; j < 4; ++j) rc[j] = __builtin_amdgcn_rcpf(den[j]);
      u32x2 o[5];
#pragma unroll
      for (int u = 0; u < 5; ++u) {
        const float rr = u == 0 ? rc[0] : (u == 4 ? rc[3] : (g < (unsigned)u ? rc[u - 1] : rc[u]));
        o[u] = pk4(num[u] * rr);
      }
      st_frag(rsWS, lane, rt0 + i, 54 + 5 * jidx + 2 * n, o[0], o[1]);
      st_frag(rsWS, lane, rt0 + i, 54 + 5 * jidx + 2 * n + 1, o[2], o[3]);
      sh[i] = o[4];
    }
    shared_send(w, lane, sh);
    shared_recv(rsWS, w, lane, rt0, 54 + 5 * jidx + 4, sh);
    STAMP(15);
    publish_flag(p, WS_FLAGO + (rb * 32 + jidx) * FS, tag, tid);
    STAMP(3);
    if (b == 224) {
      poll_flags((gu32*)(p.ws + WS_FLAGH + tid * FS), (tid & 31) < (tid < 256 ? 18u : 4u), tag);
      bar();
      if (tid == 0) __hip_atomic_store((gu32*)(p.ws + WS_EPOCH), tag, RLX_AGENT);
    }
  } else {
    const unsigned gw = spi * 8 + w;
    unsigned k10 = 0x00003f80u, k01 = 0x3f800000u;
    asm volatile("" : "+v"(k10), "+v"(k01));
    auto cs_src = [&](unsigned u, rsrc_t& rs, unsigned& cv, unsigned& st) {
      const unsigned cline = u / 3, third = u % 3;
      const void* cw = cline < 30 ? p.wqkv : (cline < 57 ? p.wg : p.wu);
      const unsigned cpitch = cline < 30 ? 3840u : 3456u;
      const unsigned clocal = cline < 30 ? cline : (cline < 57 ? cline - 30 : cline - 57);
      rs = mk_rsrc(cw, gw < 126 ? D * cpitch : 0);
      cv = (third * 216 + (lane >> 3)) * cpitch + clocal * 128 + (lane & 7) * 16;
      st = 8 * cpitch;
    };
    auto cs_publish = [&](unsigned u, float (&cs)[8]) {
#pragma unroll
      for (int k = 0; k < 4; ++k) { const bool hi = lane & 32; const float o = __shfl_xor(hi ? cs[k] : cs[k + 4], 32); cs[k] = (hi ? cs[k + 4] : cs[k]) + o; }
#pragma unroll
      for (int k = 0; k < 2; ++k) { const bool hi = lane & 16; const float o = __shfl_xor(hi ? cs[k] : cs[k + 2], 16); cs[k] = (hi ? cs[k + 2] : cs[k]) + o; }
      { const bool hi = lane & 8; const float o = __shfl_xor(hi ? cs[0] : cs[1], 8); cs[0] = (hi ? cs[1] : cs[0]) + o; }
      if (gw < 126)
        __hip_atomic_store((gu64*)(p.ws + WS_CSUM) + (u % 3) * NCOL + (u / 3) * 64 + (lane & 7) * 8 + (lane >> 3),
                           ((unsigned long long)tag << 32) | __builtin_bit_cast(unsigned, cs[0]), RLX_AGENT);
    };
    row_stat<0>(rs_, lane);
    u32x4 c[27];
    rsrc_t rs0, rs1; unsigned cv0, cv1, st0, st1;
    cs_src(gw, rs0, cv0, st0); cs_src(gw + 126, rs1, cv1, st1);
#pragma unroll
    for (int j = 0; j < 27; ++j) c[j] = __builtin_amdgcn_raw_buffer_load_b128(rs0, cv0 + j * st0, 0, 0);
    lbar();
    row_publish(rs_, p.ws, tag, tid);
    float cs[8] = {};
#pragma unroll
    for (int j = 0; j < 27; ++j) {
#pragma unroll
      for (int k = 0; k < 4; ++k) { cs[2 * k] = dot2(c[j][k], k10, cs[2 * k]); cs[2 * k + 1] = dot2(c[j][k], k01, cs[2 * k + 1]); }
      if (j < 8) c[j] = __builtin_amdgcn_raw_buffer_load_b128(rs1, cv1 + j * st1, 0, 0);
      SB;
    }
    cs_publish(gw, cs);
    SB;
#pragma unroll
    for (int j = 8; j < 27; ++j) c[j] = __builtin_amdgcn_raw_buffer_load_b128(rs1, cv1 + j * st1, 0, 0);
    SB;
    STAMP(11);
#pragma unroll
    for (int k = 0; k < 8; ++k) cs[k] = 0.f;
#pragma unroll
    for (int j = 0; j < 27; ++j)
#pragma unroll
      for (int k = 0; k < 4; ++k) { cs[2 * k] = dot2(c[j][k], k10, cs[2 * k]); cs[2 * k + 1] = dot2(c[j][k], k01, cs[2 * k + 1]); }
    cs_publish(gw + 126, cs);
    STAMP(12);
#pragma unroll 1
    for (unsigned i = w; i < 144; i += 8) dma16(i * 1024, v2, rsWd, urow(i >> 2, rot) + (i & 3) * 10240u + cb * 128u);
    drain_vm();
    bar();
  }
  if (!has2) { STAMP_END; return; }

  const unsigned rbA = (G * 6) >> 4, rbB = (G * 6 + 5 > 127 ? 127 : G * 6 + 5) >> 4;
  if (w >= 4) {
    __builtin_amdgcn_s_setprio(HPRIO);
    const unsigned lw = w - 4, ld0 = lw * 1024;
    gu32* fo = (gu32*)(p.ws + WS_FLAGO + ((lane < 32 ? rbA : rbB) * 32 + (lane & 3)) * FS);
    gu32* fh = (gu32*)(p.ws + WS_FLAGH + ((lane < 32 ? rbA : rbB) * 32 + ((lane & 31) < 18 ? lane & 31 : 17)) * FS);
    if (w == 7) {
      __builtin_amdgcn_s_setprio(3);
      const unsigned v = poll_flags(fh, (lane & 31) < 18 && (lane & 31) >= 10, tag);
      lds_set(L_GO);
      if (!__all((lane & 31) >= 18 || v == tag)) poll_flags(fh, (lane & 31) < 18, tag);
      lds_set(L_GO + 4);
      __builtin_amdgcn_s_setprio(HPRIO);
    } else {
      __builtin_amdgcn_s_sleep(HSLEEP);
    }
    if (jtype == J_KV) {
#pragma unroll
      for (int u = 31; u < 36; ++u) dma16(u * 4096 + ld0, v2, rsWd, w2_off(W, u, lw));
    }
    if (jtype != J_MLP) {
#pragma unroll
      for (int u = 0; u < NPRE + NPOST; ++u) { pre2[u] = w2_load(W, 36 + u, lw, v2); SB; }
    }
#pragma unroll
    for (int u = 36 + NPRE + NPOST; u < HSPLIT; ++u) { pre2[u - 36] = w2_load(W, u, lw, v2); SB; }
    STAMP(8);
    unsigned la[3], wb[3], xo[3], st[3];
    u32x4 xr[3];
    const unsigned ch = lane & 7;
#pragma unroll
    for (int i = 0; i < 3; ++i) {
      const unsigned row = lw * 24 + i * 8 + (lane >> 3), rl = row >> 4, rr = row & 15;
      la[i] = (rl >= 3 ? 24576u + (rl - 3) * 4096u : rl * 4096u) + (ch >> 1) * 1024 + (ch & 1) * 512 + ((rr + 2 * ch) & 15) * 16;
      xo[i] = (G * 96 + row) * 1280 + cb * 128 + ch * 16;
      wb[i] = ld0 + lane * 16 + i * 49152;
      asm volatile("" : "+v"(la[i]), "+v"(wb[i]), "+v"(xo[i]));
    }
    float ri[3], rl[3];
    unsigned of = 0;
    f32x4 xf[3][2], dd[3][2];
#pragma unroll
    for (int k = 2; k <= 6; ++k) {
      if (k == O_BAR && w == 7 && !__all((lane & 31) >= 4 || of == tag)) poll_flags(fo, (lane & 31) < 4, tag);
      if (k == 6) {
        wait_vm<0>();
#pragma unroll
        for (int h = 0; h < 3; ++h) {
          ri[h] = 2.0f * bfhi(st[h]) * bfhi(st[h]);
          rl[h] = -1.4426950408889634f * __builtin_amdgcn_rcpf(ri[h]);
          xf[h][0] = f32x4{bflo(xr[h][0]), bfhi(xr[h][0]), bflo(xr[h][1]), bfhi(xr[h][1])};
          xf[h][1] = f32x4{bflo(xr[h][2]), bfhi(xr[h][2]), bflo(xr[h][3]), bfhi(xr[h][3])};
          asm volatile("" : "+v"(ri[h]), "+v"(rl[h]), "+v"(xf[h][0]), "+v"(xf[h][1]));
        }
      }
      lbar();
      if (k == O_BAR - 1) { of = __hip_atomic_load(fo, RLX_AGENT); SB; }
      if (k < 6) {
        const int kk = k - 1;
#pragma unroll
        for (int u = 9 * (kk - 1) + ((kk - 1) & 1); u < 9 * kk + (kk & 1); ++u) {
          lds_st<u32x4>(wb[u / 12] + (u % 12) * 4096, pre2[u]);
          SB;
        }
      }
      if (k == WOK || k == WOK + 1) {
#pragma unroll
        for (int u = (k == WOK ? 54 : 64); u < (k == WOK ? 64 : 72); ++u)
          if (u >= HSPLIT && u >= 36 + NPRE + NPOST) { pre2[u - 36] = w2_load(W, u, lw, v2); SB; }
        if (k != WOK) { dma16(36 * 4096 + ld0, v2, W.wo, w2_off(W, 72, lw)); dma16(37 * 4096 + ld0, v2, W.wo, w2_off(W, 73, lw)); }
      }
      if (k == 2) {
#pragma unroll
        for (int h = 0; h < 3; ++h) {
          const unsigned row = G * 96 + lw * 24 + h * 8 + (lane >> 3);
          st[h] = __builtin_amdgcn_raw_buffer_load_b32(rsWS, WS_STAT + (row < S ? row : 0) * 8, 0, 16);
          xr[h] = __builtin_amdgcn_raw_buffer_load_b128(rsX, xo[h], 0, 0);
        }
      }
    }
#pragma unroll
    for (int i = 0; i < 3; ++i) {
      const unsigned lb = la[i] + (la[i] >= 28672u ? 126976u : 12288u);
      dd[i][0] = lds_ld<f32x4>(la[i]) + lds_ld<f32x4>(lb);
      dd[i][1] = lds_ld<f32x4>(la[i] + 256) + lds_ld<f32x4>(lb + 256);
    }
    lbar();
    __builtin_amdgcn_s_setprio(GPRIO);
#pragma unroll
    for (int i = 0; i < 6; ++i) {
      const f32x4 l = dd[i >> 1][i & 1] * rl[i >> 1];
      f32x4 e;
#pragma unroll
      for (int j = 0; j < 4; ++j) e[j] = __builtin_amdgcn_exp2f(l[j]);
      e = e * ri[i >> 1] + ri[i >> 1];
#pragma unroll
      for (int j = 0; j < 4; ++j) e[j] = __builtin_amdgcn_rcpf(e[j]);
      dd[i >> 1][i & 1] *= e;
      asm volatile("" : "+v"(dd[i >> 1][i & 1]));
    }
    const rsrc_t rsO = mk_rsrc(p.out, S * D * 2);
    STAMP(9);
    bar();
    f32x4 ss[3][2];
#pragma unroll
    for (int i = 0; i < 3; ++i) {
      ss[i][0] = lds_ld<f32x4>(la[i]) + lds_ld<f32x4>(la[i] + 12288);
      ss[i][1] = lds_ld<f32x4>(la[i] + 256) + lds_ld<f32x4>(la[i] + 12288 + 256);
    }
#pragma unroll
    for (int i = 0; i < 3; ++i)
      __builtin_amdgcn_raw_buffer_store_b128(cat2(pk4(xf[i][0] + ss[i][0] * dd[i][0]), pk4(xf[i][1] + ss[i][1] * dd[i][1])), rsO, xo[i], 0, 0);
    STAMP(5);
    STAMP_END;
    return;
  }
  {
    const unsigned kh = w & 1, rt = G * 6 + 3 * (w >> 1);
    const rsrc_t rsH = mk_rsrc(p.ws + WS_HA + (rt < 128 ? rt : 0) * (NSTEP * 1024), rt < 128 ? (rt > 125 ? 128 - rt : 3) * (NSTEP * 1024) : 0);
    unsigned tb[3][4];
#pragma unroll
    for (int t = 0; t < 4; ++t) {
      tb[0][t] = (4 * g + q4) * 128 + ((t ^ ((2 * g + (q4 >> 1)) & 3)) << 5) + 8 * p4 + kh * 4096;
      tb[1][t] = tb[0][t] + 49152; tb[2][t] = tb[0][t] + 98304;
      asm volatile("" : "+v"(tb[0][t]), "+v"(tb[1][t]), "+v"(tb[2][t]));
    }
    auto rd1 = [&](int i, int t, int hf) {
      const int sl = i == 36 ? 36 : (2 * i) % 36, bi = sl / 12 > 2 ? 2 : sl / 12;
      return __builtin_bit_cast(u32x2, lds_tr(tb[bi][t] + (sl - 12 * bi) * 4096 + hf * 2048));
    };
    constexpr int NW = P2NW, W0 = P2W0;
    constexpr int OI = O_BAR == 4 ? 17 : (O_BAR == 5 ? 22 : 26);
    constexpr int OB0 = OI == 26 ? NW - 1 : 0;
    f32x4 acc[3][4];
    u32x4 a[NW][3];
    u32x2 bw[2][4][2];
    unsigned va[3];
#pragma unroll
    for (int h = 0; h < 3; ++h) va[h] = lane * 16 + h * NSTEP * 1024;
    auto put = [&](bool hi) {
      acc_fence<12>(&acc[0][0]);
#pragma unroll
      for (int t = 0; t < 4; ++t) {
        const unsigned xa = w * 12288u + t * 1024 + g * 256 + ((r + 4 * t + (g & 2)) & 15) * 16, xb = xa + (hi && w == 3 ? 114688u : 0u);
        lds_st<f32x4>(xa, acc[0][t]); lds_st<f32x4>(xb + 4096, acc[1][t]); lds_st<f32x4>(xb + 8192, acc[2][t]);
      }
      wait_lgkm0();
    };
    unsigned so = (rot + kh) * 1024;
    auto lda = [&](auto I, auto H) {
      constexpr int i = I.v, h = H.v;
      if constexpr (i < 27) {
        if (h == 0 && so >= 54 * 1024) so -= 54 * 1024;
        a[i % NW][h] = __builtin_amdgcn_raw_buffer_load_b128(rsH, va[h], so, 16);
        if (h == 2) so += 2048;
      } else {
        a[i % NW][h] = __builtin_amdgcn_raw_buffer_load_b128(rsH, va[h], (2 * i + kh) * 1024, 16);
      }
      SB;
    };
    lds_wait(L_GO);
    unsigned go2 = 0;
    STAMP(8);
    static_for<0, W0>([&](auto I) { lda(I, IC<0>{}); lda(I, IC<1>{}); lda(I, IC<2>{}); });
#pragma unroll
    for (int t = 0; t < 4; ++t) { bw[0][t][0] = rd1(0, t, 0); bw[0][t][1] = rd1(0, t, 1); }
    static_for<0, 37>([&](auto I) {
      constexpr int i = I.v;
      constexpr int lim = i + NW - 1;
      constexpr int nxt = i <= OI ? (W0 + 2 * i < lim ? W0 + 2 * i : lim) : (27 + OB0 + 2 * (i - OI - 1) < lim ? 27 + OB0 + 2 * (i - OI - 1) : lim);
      constexpr int end = i <= OI ? 27 : 37;
      constexpr int l0 = nxt < end ? nxt : -1, l1 = (nxt + 1 <= lim && nxt + 1 < end) ? nxt + 1 : -1;
      SB;
      static_for<0, 12>([&](auto E) {
        constexpr int e = E.v, t = e / 3, h = e % 3;
        const u32x4 bf = cat2(bw[i & 1][t][0], bw[i & 1][t][1]);
        if constexpr (i == 0 || i == 27) mfma_z(acc[2 - h][t], bf, a[i % NW][2 - h]); else mfma_ip(acc[2 - h][t], bf, a[i % NW][2 - h]);
        SB;
        if constexpr (i == 4 && e == 2) go2 = lds_get(L_GO + 4);
        if constexpr (i < 36 && h < 2) bw[(i + 1) & 1][t][h] = rd1(i + 1, t, h);
        if constexpr (l1 < 0) { if constexpr (h == 2 && t < 3 && l0 >= 0) lda(IC<l0>{}, IC<t>{}); }
        else if constexpr (e & 1) lda(IC<(e < 6 ? l0 : l1)>{}, IC<(e >> 1) % 3>{});
        SB;
      });
      if constexpr (i == 26) put(true);
      if constexpr (i == XBAR) bar();
      if constexpr (i == 4) { if (__builtin_expect(!rfl(go2), 0)) lds_wait(L_GO + 4); asm volatile("" ::: "memory"); STAMP(17); }
      if constexpr (i > 4 && i < 27 && (i % 9 == 4 || i % 9 == 8)) {
        bar();
        if constexpr (i == 26 && OI == 26) static_for<27, 27 + OB0>([&](auto J) { lda(J, IC<0>{}); lda(J, IC<1>{}); lda(J, IC<2>{}); });
        STAMP(17 + (2 * i) / 9);
      }
    });
    STAMP(24);
    put(false);
    bar();
    STAMP(5);
    STAMP_END;
  }
}

__global__ __launch_bounds__(64) void tailk(Params p) {
  const unsigned long long t0 = __hip_atomic_load((gu64*)(p.ws + WS_DBG), RLX_AGENT);
  for (unsigned i = 0; i < (1u << 22) && __builtin_amdgcn_s_memrealtime() - t0 < 6000; ++i) __builtin_amdgcn_s_sleep(1);
}

extern "C" void kernel_launch(void* const* d_in, const int* in_sizes, int n_in, void* d_out, int out_size, void* d_ws,
                              size_t ws_size, hipStream_t stream) {
  Params p;
  p.x = d_in[0]; p.wqkv = d_in[1]; p.wo = d_in[2]; p.wg = d_in[4]; p.wu = d_in[5]; p.wd = d_in[6];
  p.out = d_out; p.ws = (char*)d_ws;
  fused_block<<<256, 512, 0, stream>>>(p);
  tailk<<<1, 64, 0, stream>>>(p);
}
```

```cpp
#include <hip/hip_runtime.h>
#include <stdint.h>

typedef __attribute__((ext_vector_type(8))) __bf16 bf16x8;
typedef __attribute__((ext_vector_type(4))) __bf16 bf16x4;
typedef __attribute__((ext_vector_type(2))) __bf16 bf16x2;
typedef __attribute__((ext_vector_type(4))) float f32x4;
typedef __attribute__((ext_vector_type(2))) float f32x2;
typedef __attribute__((ext_vector_type(4))) unsigned u32x4;
typedef __attribute__((ext_vector_type(2))) unsigned u32x2;
typedef __amdgpu_buffer_rsrc_t rsrc_t;
#define LAS __attribute__((address_space(3)))
#define GAS __attribute__((address_space(1)))
#define DI __device__ __forceinline__
#define RLX_AGENT __ATOMIC_RELAXED, __HIP_MEMORY_SCOPE_AGENT
typedef GAS unsigned gu32;
typedef GAS unsigned long long gu64;

constexpr int S = 2048, D = 640, F = 1728;
constexpr int NCOL = 5376;
constexpr int NSTEP = 74;
constexpr unsigned WS_EPOCH = 0;
#define FS 128
constexpr unsigned WS_FLAGH = 768u << 10;
constexpr unsigned WS_FLAGO = WS_FLAGH + 8 * 32 * FS;
constexpr unsigned WS_FLAGKV = WS_FLAGO + 8 * 32 * FS;
constexpr unsigned WS_STAT = 4096;
constexpr unsigned WS_CSUM = WS_STAT + S * 8;
constexpr unsigned WS_KVP = WS_CSUM + 3 * NCOL * 8;
constexpr unsigned WS_HA = 1u << 20;
constexpr unsigned WS_DBG = 12u << 20;
constexpr unsigned WS_END = 13u << 20;
constexpr unsigned L_B = 65536;
constexpr unsigned L_SCR = 147456;
constexpr unsigned L_MISC = 159744;
constexpr unsigned L_GO = L_MISC + 3072;
constexpr unsigned SPIN_CAP = 1u << 18;
#define HSLEEP 4
#define HSPLIT 64
#define PPIPE 0
#define STA 0
#define STB 2
#define NPOST 0
#define SNAKE 0
#define EORD 0
#define NPRE 12
#define WOK 2
#define GRP 1
#define GPRIO 0
#define HPRIO 0
#define XBAR 28
#define PSLEEP 2
#define P2NW 7
#define P2W0 6
#define WSLEEP 0
#define ROTM 18
#define NA1 2
#define O_BAR 6
#define PRIO_MODE 0
#define DMA_SPACE_A T
#define DMA_SPACE_B (4 * T / 3)

struct Params {
  const void* x; const void* wqkv; const void* wo; const void* wg; const void* wu; const void* wd;
  void* out; char* ws;
};

__shared__ __attribute__((aligned(16))) char smem[163840];

DI unsigned rfl(unsigned v) { return __builtin_amdgcn_readfirstlane(v); }
DI rsrc_t mk_rsrc(const void* p, unsigned bytes) { return __builtin_amdgcn_make_buffer_rsrc((void*)p, (short)0, (int)bytes, 0x00020000); }
DI void dma16(unsigned ldsdst, unsigned voff, rsrc_t rs, unsigned soff) {
  asm volatile("s_mov_b32 m0, %0\n\ts_nop 0\n\tbuffer_load_dwordx4 %1, %2, %3 offen lds" ::"s"(ldsdst), "v"(voff), "s"(rs), "s"(soff) : "memory");
}
template <int N> DI void wait_vm() { asm volatile("s_waitcnt vmcnt(%0)" ::"i"(N) : "memory"); }
DI void drain_vm() { wait_vm<0>(); __builtin_amdgcn_s_waitcnt(0x0F70); }
DI void wait_lgkm0() { asm volatile("s_waitcnt lgkmcnt(0)" ::: "memory"); }
DI void bar() { asm volatile("" ::: "memory"); __builtin_amdgcn_s_barrier(); asm volatile("" ::: "memory"); }
DI void lbar() { wait_lgkm0(); bar(); }
template <class V> DI V lds_ld(unsigned a) { return *(LAS V*)((LAS char*)smem + a); }
template <class V> DI void lds_st(unsigned a, V v) { *(LAS V*)((LAS char*)smem + a) = v; }
DI bf16x4 lds_tr(unsigned a) { return __builtin_amdgcn_ds_read_tr16_b64_v4bf16((LAS bf16x4*)((LAS char*)smem + a)); }
DI bf16x8 cat(bf16x4 a, bf16x4 b) { return __builtin_shufflevector(a, b, 0, 1, 2, 3, 4, 5, 6, 7); }
DI f32x4 mfma(bf16x8 a, bf16x8 b, f32x4 c) { return __builtin_amdgcn_mfma_f32_16x16x32_bf16(a, b, c, 0, 0, 0); }
DI unsigned pk2(float a, float b) { f32x2 v = {a, b}; return __builtin_bit_cast(unsigned, __builtin_convertvector(v, bf16x2)); }
DI u32x2 pk4(f32x4 v) { u32x2 r = {pk2(v[0], v[1]), pk2(v[2], v[3])}; return r; }
DI bf16x8 frag(u32x2 lo, u32x2 hi) { u32x4 v = {lo[0], lo[1], hi[0], hi[1]}; return __builtin_bit_cast(bf16x8, v); }
DI float bflo(unsigned v) { return __builtin_bit_cast(float, v << 16); }
DI float bfhi(unsigned v) { return __builtin_bit_cast(float, v & 0xffff0000u); }
DI float rbf(float v) { return bflo(pk2(v, 0.f)); }
DI float relu2(float v) { float o; asm("v_add_f32 %0, %1, |%1|" : "=v"(o) : "v"(v)); return o; }
DI f32x4 phi4(f32x4 u, f32x4 rl, f32x4 rh) {
  const f32x4 l = u * rl;
  f32x4 e, a;
#pragma unroll
  for (int j = 0; j < 4; ++j) {
    e[j] = __builtin_amdgcn_fmed3f(__builtin_amdgcn_exp2f(l[j]), 0.f, 1.f);
    a[j] = relu2(u[j]);
  }
  return a * rh + e + 1e-6f;
}
DI f32x4 splat(float v) { return f32x4{v, v, v, v}; }
template <int CTRL> DI float dpp_add(float v) {
  return v + __builtin_bit_cast(float, __builtin_amdgcn_update_dpp(0, __builtin_bit_cast(int, v), CTRL, 0xf, 0xf, true));
}
DI float dot2(unsigned a, unsigned b, float c) {
  return __builtin_amdgcn_fdot2_f32_bf16(__builtin_bit_cast(bf16x2, a), __builtin_bit_cast(bf16x2, b), c, false);
}
DI u32x4 ldtr2(unsigned a, unsigned d) { return __builtin_bit_cast(u32x4, cat(lds_tr(a), lds_tr(a + d))); }
DI void mfma_ip(f32x4& c, const u32x4& a, const u32x4& b) { asm volatile("v_mfma_f32_16x16x32_bf16 %0, %1, %2, %0" : "+v"(c) : "v"(a), "v"(b)); }
DI void mfma_z(f32x4& c, const u32x4& a, const u32x4& b) { asm volatile("v_mfma_f32_16x16x32_bf16 %0, %1, %2, 0" : "=&v"(c) : "v"(a), "v"(b)); }
template <int N> DI void acc_fence(f32x4* c) {
  asm volatile("s_nop 7" ::: "memory");
#pragma unroll
  for (int i = 0; i < N; ++i) asm volatile("" : "+v"(c[i]));
}
DI u32x4 cat2(u32x2 a, u32x2 b) { return u32x4{a[0], a[1], b[0], b[1]}; }
DI u32x2 ldtr1(unsigned a) { return __builtin_bit_cast(u32x2, lds_tr(a)); }
template <int IMM> DI void set_m0i(unsigned v) { asm volatile("s_add_i32 m0, %0, %1" ::"s"(v), "i"(IMM) : "memory", "scc"); }
template <int OFF> DI void dma_goi(unsigned voff, rsrc_t rs, unsigned soff) {
  asm volatile("buffer_load_dwordx4 %0, %1, %2 offen offset:%3 lds" ::"v"(voff), "s"(rs), "s"(soff), "i"(OFF) : "memory");
}
DI void set_m0(unsigned v) { asm volatile("s_mov_b32 m0, %0" ::"s"(v) : "memory"); }
DI void dma_go(unsigned voff, rsrc_t rs, unsigned soff) {
  asm volatile("buffer_load_dwordx4 %0, %1, %2 offen lds" ::"v"(voff), "s"(rs), "s"(soff) : "memory");
}
DI f32x4 mfma_u(u32x4 a, u32x4 b, f32x4 c) { return mfma(__builtin_bit_cast(bf16x8, a), __builtin_bit_cast(bf16x8, b), c); }
#define STAMP_DECL
#define STAMP_ARG
#define STAMP_PASS
#define STAMPX(c, i)
#define STAMP_END
#define STAMP(i) STAMPX(threadIdx.x == 0, i)

DI unsigned poll_flags(gu32* word, bool active, unsigned tag) {
  unsigned v = 0;
#pragma unroll 1
  for (unsigned spins = 0; spins < SPIN_CAP; ++spins) {
    v = __hip_atomic_load(word, RLX_AGENT);
    if (__all(!active || v == tag)) break;
    if (PSLEEP) __builtin_amdgcn_s_sleep(PSLEEP);
  }
  asm volatile("" ::: "memory");
  return v;
}

DI void lds_set(unsigned a) { __hip_atomic_store((LAS unsigned*)smem + a / 4, 1u, __ATOMIC_RELAXED, __HIP_MEMORY_SCOPE_WORKGROUP); }
DI unsigned lds_get(unsigned a) { return __hip_atomic_load((LAS unsigned*)smem + a / 4, __ATOMIC_RELAXED, __HIP_MEMORY_SCOPE_WORKGROUP); }
DI void lds_wait(unsigned a) {
#pragma unroll 1
  for (unsigned spins = 0; spins < (SPIN_CAP << 4); ++spins) {
    if (rfl(lds_get(a))) break;
    __builtin_amdgcn_s_sleep(1);
  }
  asm volatile("" ::: "memory");
}

template <int I> struct IC { static constexpr int v = I; };
template <int B, int E, class Fn> DI void static_for(Fn&& f) {
  if constexpr (B < E) { f(IC<B>{}); static_for<B + 1, E>(f); }
}

enum { J_MLP = 0, J_KV = 1, J_Q = 2, J_SPARE = 3 };

struct Dma {
  rsrc_t rsA, rsB;
  unsigned vA0, vA1, vB0, vB1, vB2;
  unsigned sA[4], sB, advB;
  unsigned mA, mB, mW;
};

constexpr unsigned a_slot(unsigned s3) { return s3 == 0 ? 126976u : (s3 - 1) * 32768u; }
#define SB __builtin_amdgcn_sched_barrier(0)

template <int T> DI unsigned bofs(const unsigned (&boff)[T], int t) { return T == 6 && t >= 3 ? boff[t - 3] + 6144 : boff[t]; }
template <int T, bool TR, int KS, class Extra>
DI void kstep(f32x4 (&acc)[4][T], const u32x4 (&ac)[4], const u32x2 (&bc)[T][2], u32x4 (&an)[4], u32x2 (&bn)[T][2],
              const unsigned (&aoff)[4], const unsigned (&boff)[T], const Dma& d, Extra&& extra) {
  constexpr int s = KS >> 1, KIND = KS & 1, NP = KIND == 0 ? 4 : 3, ST = KIND == 0 ? (STA ? STA : (4 * T - 1) / NP) : (STB ? STB : (4 * T - 1) / NP);
  constexpr int sn = (KS + 1) >> 1;
  constexpr unsigned asl = a_slot(sn % 3), bsl = ((KS + 1) % 5) * 12288u;
  static_for<0, 4 * T>([&](auto E) {
    constexpr int e = E.v, t = e >> 2, i = (SNAKE && (t & 1)) ? 3 - (e & 3) : (e & 3);
    const u32x4 bf = cat2(bc[t][0], bc[t][1]);
    if constexpr (KS == 0) { if (TR) mfma_z(acc[i][t], bf, ac[i]); else mfma_z(acc[i][t], ac[i], bf); }
    else { if (TR) mfma_ip(acc[i][t], bf, ac[i]); else mfma_ip(acc[i][t], ac[i], bf); }
    SB;
    if constexpr (KS < 19) {
      if constexpr (e < 4) an[e] = lds_ld<u32x4>(aoff[((KS + 1) & 1) + (sn % 3 == 0 ? 2 : 0)] + (sn % 3 == 0 ? 0u : asl) + e * 2048);
      else if constexpr (e < 4 + 2 * T) bn[(e - 4) >> 1][e & 1] = ldtr1(bofs<T>(boff, (e - 4) >> 1) + bsl + (e & 1) * 768);
    }
    constexpr int pc = e / ST;
    if constexpr (KIND == 0) {
      constexpr int adv = s < 8 ? (s + 2) * 128 : 0, dst = (int)a_slot((s + 2) % 3) + pc * 1024, off = dst >= adv ? adv : 0;
      if constexpr (pc < NP && e % ST == ST - 2) set_m0i<dst - off>(d.mA);
      if constexpr (pc < NP && e % ST == ST - 1) dma_goi<off>((pc & 1) ? d.vA1 : d.vA0, d.rsA, d.sA[pc] + (adv - off));
    } else {
      constexpr int h0 = (2 * s) % 5;
      if constexpr (pc < NP && e % ST == ST - 2) set_m0i<(h0 == 4 ? 0 : h0 * 12288) + pc * 1024>(h0 == 4 ? d.mW : d.mB);
      if constexpr (pc < NP && e % ST == ST - 1) dma_goi<0>(pc == 0 ? d.vB0 : (pc == 1 ? d.vB1 : d.vB2), d.rsB, d.sB);
    }
    extra(E);
    SB;
  });
}

struct RowStat { u32x4 xa; unsigned xb, row; float s, q; };
DI void row_load(RowStat& R, rsrc_t rsX, unsigned lane) {
  asm volatile("buffer_load_dwordx4 %0, %2, %4, %5 offen\n\tbuffer_load_dword %1, %3, %4, %5 offen offset:1024"
               : "=&v"(R.xa), "=&v"(R.xb) : "v"(lane * 16), "v"(lane * 4), "s"(rsX), "s"(R.row * 1280) : "memory");
}
template <int K> DI void row_step(RowStat& R, unsigned lane) {
  if constexpr (K < 5) {
    if constexpr (K == 0) { R.s = 0.f; R.q = 0.f; }
    const unsigned v = K < 4 ? R.xa[K & 3] : R.xb;
    asm volatile("v_dot2c_f32_bf16 %0, %2, %3\n\tv_dot2c_f32_bf16 %1, %2, %2" : "+v"(R.s), "+v"(R.q) : "v"(v), "v"(0x3f803f80u));
  } else if constexpr (K < 11) {
    constexpr int C = K == 5 ? 0xB1 : (K == 6 ? 0x4E : (K == 7 ? 0x141 : (K == 8 ? 0x140 : (K == 9 ? 0x142 : 0x143))));
    if constexpr (K == 5) asm volatile("s_nop 2" : "+v"(R.s), "+v"(R.q));
    R.s = dpp_add<C>(R.s); R.q = dpp_add<C>(R.q);
  } else if constexpr (K == 11) {
    R.s *= 1.0f / D;
    R.q = fmaxf(R.q * (1.0f / D) - R.s * R.s, 0.f);
  } else if constexpr (K == 12) {
    R.q = __builtin_amdgcn_sqrtf(rbf(rbf(R.q) + 1e-5f));
  } else {
    lds_st<unsigned>(lane == 63 ? L_GO + 32 + (R.row & 7) * 4 : L_GO + 64, pk2(R.s, R.q));
  }
  if constexpr (K >= 5 && K < 13) asm volatile("" : "+v"(R.s), "+v"(R.q));
}
DI void row_publish(const RowStat& R, char* ws, unsigned tag, unsigned tid) {
  if (tid < 8) __hip_atomic_store((gu64*)(ws + WS_STAT) + R.row + tid, ((unsigned long long)tag << 32) | lds_ld<unsigned>(L_GO + 32 + tid * 4), RLX_AGENT);
}

struct Gran { u32x2 g0, g1, g2; unsigned off, step; };
DI void gran_issue(Gran& G, rsrc_t rs) {
  asm volatile("buffer_load_dwordx2 %0, %3, %4, 0 offen sc1\n\tbuffer_load_dwordx2 %1, %3, %4, %5 offen sc1\n\t"
               "buffer_load_dwordx2 %2, %3, %4, %6 offen sc1"
               : "=&v"(G.g0), "=&v"(G.g1), "=&v"(G.g2) : "v"(G.off), "s"(rs), "s"(G.step), "s"(2 * G.step) : "memory");
}
DI void gran_load(Gran& G, rsrc_t rs) {
  asm volatile("buffer_load_dwordx2 %0, %3, %4, 0 offen sc1\n\tbuffer_load_dwordx2 %1, %3, %4, %5 offen sc1\n\t"
               "buffer_load_dwordx2 %2, %3, %4, %6 offen sc1\n\ts_waitcnt vmcnt(0)"
               : "=&v"(G.g0), "=&v"(G.g1), "=&v"(G.g2) : "v"(G.off), "s"(rs), "s"(G.step), "s"(2 * G.step) : "memory");
}

DI unsigned urow(unsigned pos, unsigned rot) { const unsigned u = pos + rot; return (u >= 54 ? u - 54 : u) * 40960u; }

constexpr unsigned wo_tile(unsigned v) { return (v / 5) * 10 + (v % 5 == 4 ? 4 : (v % 5 < 2 ? 2 * (v % 5) : 2 * (v % 5) + 1)); }
struct W2 { rsrc_t wd, wo; unsigned rot, col; };
DI unsigned w2_off(const W2& W, int u, unsigned lw) {
  if (u < 54) return urow(u, W.rot) + lw * 10240u + W.col;
  return wo_tile(u - 54) * 20480u + (lw >> 1) * ((u - 54) % 5 == 4 ? 102400u : 20480u) + (lw & 1) * 10240u + W.col;
}
DI u32x4 w2_load(const W2& W, int u, unsigned lw, unsigned voff) {
  return __builtin_amdgcn_raw_buffer_load_b128(u < 54 ? W.wd : W.wo, voff, w2_off(W, u, lw), 0);
}

template <int T, bool TR, bool PF, bool LASTA, class Pre>
DI void main_loop(Pre&& pre, RowStat& R, char* ws, f32x4 (&acc)[4][T], unsigned (&boff)[T], Dma& d, unsigned w, unsigned lane, unsigned tid,
                  unsigned epoch, unsigned& tag, unsigned rb, unsigned ccol, rsrc_t rsB2, rsrc_t rsWS, unsigned cb, unsigned rot, u32x4 (&pre2)[36] STAMP_ARG) {
  const unsigned g = lane >> 4, r = lane & 15, m = w & 3;
  unsigned aoff[4];
  aoff[0] = (64 * m + r) * 128 + ((g ^ (r >> 1)) << 4);
  aoff[1] = aoff[0] ^ 64;
  aoff[2] = aoff[0] + a_slot(0); aoff[3] = aoff[1] + a_slot(0);
  const unsigned wq = (w & 1) * 2 + ((w >> 1) & 1);
  asm volatile("" : "+v"(aoff[0]), "+v"(aoff[1]), "+v"(aoff[2]), "+v"(aoff[3]), "+v"(boff[0]), "+v"(boff[1]), "+v"(boff[2]));
  if (T == 5) asm volatile("" : "+v"(boff[3]), "+v"(boff[4]));
  Gran G;
  unsigned v2, vb2, vpf, tA[3], tB[3];
  row_load(R, d.rsA, lane);
  {
    d.sB += d.advB;
    const unsigned bdst = L_B + (w < 4 ? 36864u : 24576u) + wq * 3072u;
    dma16(bdst, d.vB0, d.rsB, d.sB); dma16(bdst + 1024, d.vB1, d.rsB, d.sB); dma16(bdst + 2048, d.vB2, d.rsB, d.sB);
#pragma unroll
    for (int i = 0; i < NA1; ++i) dma16(a_slot(1) + d.mA + i * 1024, (i & 1) ? d.vA1 : d.vA0, d.rsA, d.sA[i] + 128);
    asm volatile("s_waitcnt vmcnt(%2) lgkmcnt(0)\n\ts_add_i32 %0, %1, 1" : "=s"(tag) : "s"(epoch), "i"(5 + NA1) : "memory", "scc");
    bar();
  }
  STAMP(1);
  u32x4 a0[4], a1[4];
  u32x2 b0[T][2], b1[T][2];
#pragma unroll
  for (int i = 0; i < 4; ++i) a0[i] = lds_ld<u32x4>(aoff[2] + i * 2048);
#pragma unroll
  for (int t = 0; t < T; ++t) { b0[t][0] = ldtr1(bofs<T>(boff, t)); b0[t][1] = ldtr1(bofs<T>(boff, t) + 768); }
  SB;
#pragma unroll
  for (int i = NA1; i < 4; ++i) dma16(a_slot(1) + d.mA + i * 1024, (i & 1) ? d.vA1 : d.vA0, d.rsA, d.sA[i] + 128);
  const bool y = w >= 4;
  bool ok = true;
  auto ln_store = [&] {
    const float v0 = y ? -bflo(G.g0[0]) : __builtin_bit_cast(float, G.g0[0]) + __builtin_bit_cast(float, G.g1[0]) + __builtin_bit_cast(float, G.g2[0]);
    const unsigned la = L_MISC + (y ? tid * 4 - 1024 : 2048 + tid * 4);
    lds_st<float>(la, v0);
    lds_st<float>(y ? la + 1024 : L_GO + 64 + (lane & 7) * 4, __builtin_amdgcn_rcpf(bfhi(G.g0[0])));
  };
  static_for<0, 10>([&](auto S_) {
    constexpr int s = S_.v, L = 4 * T;
    constexpr int NX = (PF && s >= 1 && 2 * s <= NPRE) ? 2 : 0;
    kstep<T, TR, 2 * s>(acc, a0, b0, a1, b1, aoff, boff, d, [&](auto E) {
      constexpr int e = E.v;
      if constexpr (s == 2) {
        if constexpr (e == 2) { vb2 = v2 + (3 * wq) * 10240u; asm volatile("" : "+v"(vb2)); }
        if constexpr (e == 5) { G.off = w < 4 ? WS_CSUM + ccol * 8 : WS_STAT + (rb * 256 + tid - 256) * 8; asm volatile("" : "+v"(G.off)); }
        if constexpr (e == 8) { G.step = w < 4 ? NCOL * 8 : 0; tA[0] = urow(w, rot) + cb * 128u; asm volatile("" : "+s"(G.step), "+s"(tA[0])); }
        if constexpr (e == 10) { tA[1] = urow(8 + w, rot) + cb * 128u; asm volatile("" : "+s"(tA[1])); }
        if constexpr (e == 12) { tA[2] = urow(31 + w, rot) + cb * 128u; asm volatile("" : "+s"(tA[2])); }
        if constexpr (e == 14) {
          tB[0] = urow(16, rot) + cb * 128u; tB[1] = urow(w < 4 ? 19u : 22u, rot) + cb * 128u; tB[2] = urow(w < 4 ? 25u : 28u, rot) + cb * 128u;
          asm volatile("" : "+s"(tB[0]), "+s"(tB[1]), "+s"(tB[2]));
        }
      }
      if constexpr (s == 3 && e == 2) pre();
      if constexpr (NX && e >= L - 2) {
        constexpr int u = 2 * (s - 1) + e - (L - 2);
        pre2[u] = __builtin_amdgcn_raw_buffer_load_b128(rsB2, vpf, urow(36 + u, rot) + (w & 3) * 10240u + cb * 128u, 0);
      }
      if constexpr (s < 7) { if constexpr (e == L - 3) d.sB += s == 0 ? d.advB * (1 + (w >> 2)) : d.advB; }
      else if constexpr (s == 7) {
        if constexpr (e == L - 6) d.vB0 = y ? vb2 : d.vB0;
        if constexpr (e == L - 4) d.vB1 = y ? vb2 + 10240u : d.vB1;
        if constexpr (e == L - 3) d.vB2 = y ? vb2 + 20480u : d.vB2;
        if constexpr (e == L - 2) d.sB = y ? tB[0] : d.sB + d.advB;
        if constexpr (e == L - 1) d.rsB = y ? rsB2 : d.rsB;
      } else {
        if constexpr (e == L - 3) { d.sB = tB[s - 7]; d.rsB = rsB2; d.vB0 = vb2; d.vB1 = vb2 + 10240u; d.vB2 = vb2 + 20480u; }
      }
      if constexpr (s == 9 && e == L - 4) {
        asm volatile("s_waitcnt vmcnt(7)" : "+v"(G.g0), "+v"(G.g1), "+v"(G.g2)::"memory");
        ok = __all(G.g0[1] == tag && G.g1[1] == tag && G.g2[1] == tag);
      }
      if constexpr (s == 9 && e == L - 3) ln_store();
    });
    if constexpr (s == 0) {
      wait_vm<11>();
      static_for<0, 14>([&](auto K) { row_step<K.v>(R, lane); });
    }
    if constexpr (s == 9) {
      if (__builtin_expect(!ok, 0)) {
#pragma unroll 1
        for (unsigned spins = 0; spins < SPIN_CAP; ++spins) {
          __builtin_amdgcn_s_sleep(2);
          gran_load(G, rsWS);
          if (__all(G.g0[1] == tag && G.g1[1] == tag && G.g2[1] == tag)) break;
        }
        ln_store();
      }
    }
    wait_lgkm0();
    if constexpr (s < 9) wait_vm<4 + NX>();
    bar();
    SB;
    kstep<T, TR, 2 * s + 1>(acc, a1, b1, a0, b0, aoff, boff, d, [&](auto E) {
      constexpr int e = E.v;
      if constexpr (s == 0 && e == 6) {
        if (y) {
          const unsigned so = d.sB - d.advB;
          dma16(d.mW + 49152, d.vB0, d.rsB, so); dma16(d.mW + 49152 + 1024, d.vB1, d.rsB, so); dma16(d.mW + 49152 + 2048, d.vB2, d.rsB, so);
        }
      }
      if constexpr (s == 0 && e == L - 4) {
        v2 = (lane >> 3) * 1280 + (((lane & 7) ^ (((lane >> 4) & 3) << 1)) << 4);
        vpf = y ? v2 : 0x7ffffff0u;
        asm volatile("" : "+v"(v2), "+v"(vpf));
      }
      if constexpr (s == 0 && e == L - 6) row_publish(R, ws, tag, tid);
      if constexpr (s == 8 && e == 0) gran_issue(G, rsWS);
      if constexpr (s >= 7 && s < 9 && e >= L - 4) {
        d.rsA = rsB2; d.vA0 = v2; d.vA1 = v2;
        d.sA[e - (L - 4)] = tA[s - 7] + (e - (L - 4)) * 10240;
      }
      if constexpr (s == 9 && LASTA && e == 7) {
        if (w < 5) {
#pragma unroll
          for (int i = 0; i < 4; ++i) dma16(a_slot(0) + d.mA + i * 1024, v2, rsB2, tA[2] + i * 10240);
        }
      }
    });
    if constexpr (s == 8) { STAMP(13); }
    if constexpr (s == 2) { STAMP(26); }
    if constexpr (s == 6) { STAMP(27); }
  });
  acc_fence<4 * T>(&acc[0][0]);
  STAMP(14);
}

DI void st16(u32x4 v, rsrc_t rs, unsigned voff, unsigned soff) {
  __builtin_amdgcn_raw_buffer_store_b128(v, rs, voff, soff, 16);
  asm volatile("s_nop 1" ::"v"(v) : "memory");
}
DI void publish_flag(const Params& p, unsigned flag_off, unsigned tag, unsigned tid) {
  drain_vm();
  bar();
  if (tid == 0) __hip_atomic_store((gu32*)(p.ws + flag_off), tag, RLX_AGENT);
}
DI void st_frag(rsrc_t rs, unsigned lane, unsigned rt, unsigned step, u32x2 lo, u32x2 hi) {
  u32x4 v = {lo[0], lo[1], hi[0], hi[1]};
  st16(v, rs, lane * 16, WS_HA + (rt * NSTEP + step) * 1024);
}
DI void shared_send(unsigned w, unsigned lane, const u32x2 (&sh)[4]) {
  const bool n = w >= 4;
  u32x4 snd = n ? u32x4{sh[0][0], sh[0][1], sh[1][0], sh[1][1]} : u32x4{sh[2][0], sh[2][1], sh[3][0], sh[3][1]};
  lds_st<u32x4>(L_SCR + w * 1024 + lane * 16, snd);
}
DI void shared_recv(rsrc_t rs, unsigned w, unsigned lane, unsigned rt0, unsigned step, const u32x2 (&sh)[4]) {
  lbar();
  u32x4 rcv = lds_ld<u32x4>(L_SCR + (w ^ 4) * 1024 + lane * 16);
  u32x2 r0 = {rcv[0], rcv[1]}, r1 = {rcv[2], rcv[3]};
  if (w >= 4) { st_frag(rs, lane, rt0 + 2, step, r0, sh[2]); st_frag(rs, lane, rt0 + 3, step, r1, sh[3]); }
  else        { st_frag(rs, lane, rt0 + 0, step, sh[0], r0); st_frag(rs, lane, rt0 + 1, step, sh[1], r1); }
}

__global__ __launch_bounds__(512, 2) void fused_block(Params p) {
  unsigned tid = threadIdx.x, lane = tid & 63;
  const unsigned w = rfl(tid >> 6);
  const unsigned b = blockIdx.x, xcd = b & 7, slot = b >> 3;
  const unsigned rb = 4 * (xcd & 1) + (slot & 3), jq = xcd >> 1, ji = slot >> 2;
  const unsigned spi = (xcd - 4) * 4 + (slot & 3);
  const unsigned long long jl0 = (jq & 1) ? 0x4123220908070605ull : 0x4021200403020100ull, jl1 = (jq & 1) ? 0x4327266011100f0eull : 0x422524600d0c0b0aull;
  const unsigned jb = (unsigned)(((jq & 2) ? jl1 : jl0) >> (8 * ji)) & 0xff, jtype = jb >> 5, jidx = jb & 31;
  STAMP_DECL;
  STAMP(0);

  Dma d;
  RowStat rs_;
  const bool mlp = jtype == J_MLP;
  const unsigned pitch = mlp ? 3456u : 3840u, wq = (w & 1) * 2 + ((w >> 1) & 1);
  const rsrc_t rsX = mk_rsrc(p.x, S * D * 2);
  d.vA0 = (lane >> 3) * 1280 + (((lane & 7) ^ (lane >> 4)) << 4); d.vA1 = d.vA0 ^ 64;
  d.rsA = rsX; d.mA = w * 4096u;
#pragma unroll
  for (int i = 0; i < 4; ++i) d.sA[i] = (rb * 256 + 32 * w + 8 * i) * 1280;
  unsigned epoch, tag;
  asm volatile("s_nop 4" ::: "memory");
#pragma unroll
  for (int i = 0; i < 4; ++i) dma16(a_slot(0) + d.mA + i * 1024, (i & 1) ? d.vA1 : d.vA0, d.rsA, d.sA[i]);
  asm volatile("s_load_dword %0, %1, 0x0" : "=s"(epoch) : "s"(p.ws) : "memory");
  SB;
  {
    unsigned vb[3];
    const unsigned lim = mlp ? 176u : 144u;
    {
      const unsigned r0 = (lane * 43) >> 9, p0 = lane - 12 * r0;
      const bool w1 = p0 >= 8, w2 = p0 >= 4;
      const unsigned r1 = r0 + (w1 ? 6 : 5), p1 = p0 + (w1 ? -8 : 4), r2 = r0 + (w2 ? 11 : 10), p2 = p0 + (w2 ? -4 : 8);
      vb[0] = r0 * pitch + min(p0 * 16, lim);
      vb[1] = r1 * pitch + min((p1 * 16) ^ ((r1 & 8) << 2), lim);
      vb[2] = r2 * pitch + min((p2 * 16) ^ 32, lim);
    }
    d.vB0 = vb[0]; d.vB1 = vb[1]; d.vB2 = vb[2];
    const unsigned mh = w & 1;
    const void* bw = mlp ? (mh ? p.wu : p.wg) : p.wqkv;
    const unsigned cj = mlp ? 192u : 160u * jtype, cm = mlp ? 0u : (jtype == J_KV ? 1280u : 160u), c0 = jtype == J_KV ? 1280u : 0u;
    d.rsB = mk_rsrc(bw, D * pitch);
    d.sB = (16 * ((w >> 1) & 1) + (w < 4 ? 32 : 0)) * pitch + c0 + cj * jidx + cm * mh; d.advB = 64 * pitch;
    d.mB = L_B + wq * 3072u + (w < 4 ? 0u : 12288u); d.mW = L_B + wq * 3072u + (w < 4 ? 49152u : 0u);
    const unsigned bdst = L_B + (w < 4 ? 12288u : 0u) + wq * 3072u;
    dma16(bdst, d.vB0, d.rsB, d.sB); dma16(bdst + 1024, d.vB1, d.rsB, d.sB); dma16(bdst + 2048, d.vB2, d.rsB, d.sB);
    rs_.row = rfl(b * 8 + w);
  }
  STAMP(10);
  lds_st<unsigned>(L_GO + (tid & 1) * 4, 0u);

  const unsigned t2 = 28 * xcd + slot;
  const bool has2 = slot < 28 && t2 < 220;
  const unsigned G = t2 / 10, cb = t2 % 10;
  constexpr unsigned rot = 32;
  unsigned g = lane >> 4, r = lane & 15, q4 = r >> 2, p4 = lane & 3;
  const rsrc_t rsWd = mk_rsrc(p.wd, has2 ? F * D * 2 : 0);
  const rsrc_t rsWS = mk_rsrc(p.ws, WS_END);
  unsigned v2 = (lane >> 3) * 1280 + (((lane & 7) ^ (((lane >> 4) & 3) << 1)) << 4);
  auto relane = [&] {
    asm volatile("" : "+v"(tid));
    lane = tid & 63; g = lane >> 4; r = lane & 15; q4 = r >> 2; p4 = lane & 3;
    v2 = (lane >> 3) * 1280 + (((lane & 7) ^ (((lane >> 4) & 3) << 1)) << 4);
  };
  const unsigned m = w & 3, n = w >> 2;
  const unsigned rt0 = rb * 16 + m * 4;
  const unsigned brow = L_B + (8 * g + q4) * 192;
  const unsigned bx = (g & 1) << 5;

  u32x4 pre2[36];
  W2 W;
  W.wd = rsWd; W.wo = mk_rsrc(p.wo, has2 ? D * D * 2 : 0); W.rot = rot; W.col = cb * 128u;
  if (jtype == J_MLP) {
    f32x4 acc[4][6];
    unsigned boff[6];
#pragma unroll
    for (int t = 0; t < 3; ++t) {
      const unsigned gt = n ? (t == 2 ? 3 : 4 + t) : t;
      boff[t] = brow + ((32 * gt + 8 * p4) ^ bx); boff[t + 3] = boff[t] + 6144;
    }
    const unsigned ccol = tid < 96 ? 1920 + 96 * jidx + tid : 3648 + 96 * jidx + (tid < 192 ? tid - 96 : 0);
    main_loop<6, true, true, true>([] {}, rs_, p.ws, acc, boff, d, w, lane, tid, epoch, tag, rb, ccol, rsWd, rsWS, cb, rot, pre2 STAMP_PASS);
    relane();
    if (w >= 4) {
#pragma unroll
      for (int u = NPRE; u < NPRE + NPOST; ++u) { pre2[u] = w2_load(W, 36 + u, w & 3, v2); SB; }
    }
    float mm[4];
    f32x4 cg[3], cu[3];
#pragma unroll
    for (int i = 0; i < 4; ++i) mm[i] = lds_ld<float>(L_MISC + (64 * m + 16 * i + r) * 4);
#pragma unroll
    for (int t = 0; t < 3; ++t) {
      const unsigned gt = n ? (t == 2 ? 3 : 4 + t) : t;
      cg[t] = lds_ld<f32x4>(L_MISC + 2048 + (16 * gt + 4 * g) * 4); cu[t] = lds_ld<f32x4>(L_MISC + 2048 + (96 + 16 * gt + 4 * g) * 4);
    }
    auto hval = [&](int i, int t) {
      f32x4 gg = acc[i][t] + mm[i] * cg[t];
      const f32x4 uu = acc[i][t + 3] + mm[i] * cu[t];
#pragma unroll
      for (int j = 0; j < 4; ++j) gg[j] = relu2(gg[j]);
      return pk4(gg * uu);
    };
#pragma unroll
    for (int i = 0; i < 4; ++i) { st_frag(rsWS, lane, rt0 + i, 3 * jidx + 2 * n, hval(i, 0), hval(i, 1)); SB; }
    const u32x2 sh[4] = {hval(0, 2), hval(1, 2), hval(2, 2), hval(3, 2)};
    shared_send(w, lane, sh);
    shared_recv(rsWS, w, lane, rt0, 3 * jidx + 1, sh);
    STAMP(15);
    publish_flag(p, WS_FLAGH + (rb * 32 + jidx) * FS, tag, tid);
    STAMP(3);
  } else if (jtype == J_KV) {
    f32x4 acc[4][5];
    unsigned boff[5];
    boff[0] = brow + ((80 * n + 8 * p4) ^ bx); boff[1] = boff[0] + 6144;
    boff[2] = brow + ((80 * n + 40 + 8 * p4) ^ bx); boff[3] = boff[2] + 6144;
    boff[4] = brow + ((80 * n + 32 + 40 * (p4 >> 1)) ^ bx) + (p4 & 1) * 6144;
    const unsigned kb = 126976u + m * 6720 + n * 3360, dump = L_MISC - 2048 + lane * 16;
    const unsigned ea = (q4 == 1 || r == 0) ? kb + (r == 0 ? 20 : 12 + r) * 80 : 0u, eb = (q4 == 3 || r == 0) ? kb + 1680 + (r == 0 ? 20 : 4 + r) * 80 : 0u;
    unsigned ka[6] = {kb + r * 80 + g * 16, ea ? ea + g * 16 : dump, eb ? eb + g * 16 : dump, g == 0 ? kb + r * 80 + 64 : dump,
                      g == 2 ? kb + 1680 + r * 80 + 64 : dump, (g == 0 && ea) ? ea + 64 : ((g == 2 && eb) ? eb + 64 : dump)};
    auto pin = [&] { asm volatile("" : "+v"(ka[0]), "+v"(ka[1]), "+v"(ka[2]), "+v"(ka[3]), "+v"(ka[4]), "+v"(ka[5])); };
    const unsigned ccol = tid < 80 ? 640 + 80 * jidx + tid : 1280 + 80 * jidx + (tid < 160 ? tid - 80 : 0);
    main_loop<5, false, false, false>(pin, rs_, p.ws, acc, boff, d, w, lane, tid, epoch, tag, rb, ccol, rsWd, rsWS, cb, rot, pre2 STAMP_PASS);
    relane();
    float cs[5];
    cs[0] = lds_ld<float>(L_MISC + 2048 + (40 * n + r) * 4);
    cs[1] = lds_ld<float>(L_MISC + 2048 + (80 + 40 * n + r) * 4);
    cs[2] = lds_ld<float>(L_MISC + 2048 + (40 * n + 20 + r) * 4);
    cs[3] = lds_ld<float>(L_MISC + 2048 + (80 + 40 * n + 20 + r) * 4);
    cs[4] = lds_ld<float>(L_MISC + 2048 + ((q4 & 1) * 80 + 40 * n + 16 + 20 * (q4 >> 1) + (r & 3)) * 4);
    u32x2 pk[4][3], vv[4][3];
    f32x4 mm4[4], rr4[4];
#pragma unroll
    for (int i = 0; i < 4; ++i) {
      mm4[i] = lds_ld<f32x4>(L_MISC + (64 * m + 16 * i + 4 * g) * 4); rr4[i] = lds_ld<f32x4>(L_MISC + 1024 + (64 * m + 16 * i + 4 * g) * 4);
    }
    SB;
#pragma unroll
    for (int i = 0; i < 4; ++i) {
      const f32x4 mm = mm4[i], rr = rr4[i];
      const f32x4 rl = rr * 1.4426950408889634f, rh = rr * 0.5f;
      f32x4 val[5];
#pragma unroll
      for (int t = 0; t < 5; ++t) val[t] = acc[i][t] + mm * cs[t];
#pragma unroll
      for (int t = 0; t < 3; ++t) pk[i][t] = pk4(phi4(val[2 * t], rl, rh));
      vv[i][0] = pk4(val[1] * rr); vv[i][1] = pk4(val[3] * rr);
      const u32x2 v4 = pk4(val[4] * rr);
      vv[i][2] = (q4 & 1) ? v4 : u32x2{0x3f803f80u, 0x3f803f80u};
    }
    STAMP(25);
    f32x4 P[7] = {};
#pragma unroll
    for (int i = 0; i < 4; i += 2) {
      const bf16x8 k0 = frag(pk[i][0], pk[i + 1][0]), k2 = frag(pk[i][1], pk[i + 1][1]), k4 = frag(pk[i][2], pk[i + 1][2]);
      const bf16x8 v1 = frag(vv[i][0], vv[i + 1][0]), v3 = frag(vv[i][1], vv[i + 1][1]), v4 = frag(vv[i][2], vv[i + 1][2]);
      P[0] = mfma(k0, v1, P[0]); P[1] = mfma(k0, v4, P[1]); P[2] = mfma(k4, v1, P[2]);
      P[3] = mfma(k2, v3, P[3]); P[4] = mfma(k2, v4, P[4]); P[5] = mfma(k4, v3, P[5]); P[6] = mfma(k4, v4, P[6]);
    }
    lds_st<f32x4>(ka[0], P[0]); lds_st<f32x4>(ka[0] + 1680, P[3]); lds_st<f32x4>(ka[1], P[1]); lds_st<f32x4>(ka[2], P[4]);
    lds_st<f32x4>(ka[3], P[2]); lds_st<f32x4>(ka[4], P[5]); lds_st<f32x4>(ka[5], P[6]);
    lbar();
    if (tid < 210) {
      const unsigned a = 126976u + tid * 32;
      const f32x4 t0 = (lds_ld<f32x4>(a) + lds_ld<f32x4>(a + 6720)) + (lds_ld<f32x4>(a + 13440) + lds_ld<f32x4>(a + 20160));
      const f32x4 t1 = (lds_ld<f32x4>(a + 16) + lds_ld<f32x4>(a + 6736)) + (lds_ld<f32x4>(a + 13456) + lds_ld<f32x4>(a + 20176));
      st16(cat2(pk4(t0), pk4(t1)), rsWS, tid * 16, WS_KVP + (rb * 32 + 4 * jidx) * 840);
    }
    wait_lgkm0();
    STAMP(15);
    publish_flag(p, WS_FLAGKV + ((jidx >> 1) * 32 + (jidx & 1) * 8 + rb) * FS, tag, tid);
    STAMP(3);
  } else if (jtype == J_Q) {
    f32x4 acc[4][5];
    unsigned boff[5];
#pragma unroll
    for (int t = 0; t < 5; ++t) boff[t] = brow + n * 6144 + ((32 * t + 8 * p4) ^ bx);
    const unsigned ccol = 160 * jidx + (tid < 160 ? tid : 0);
    main_loop<5, true, false, false>([] {}, rs_, p.ws, acc, boff, d, w, lane, tid, epoch, tag, rb, ccol, rsWd, rsWS, cb, rot, pre2 STAMP_PASS);
    relane();
    u32x2 pq[4][5];
    {
      float mm[4], rr[4];
      f32x4 c[5];
#pragma unroll
      for (int i = 0; i < 4; ++i) { mm[i] = lds_ld<float>(L_MISC + (64 * m + 16 * i + r) * 4); rr[i] = lds_ld<float>(L_MISC + 1024 + (64 * m + 16 * i + r) * 4); }
#pragma unroll
      for (int t = 0; t < 5; ++t) c[t] = lds_ld<f32x4>(L_MISC + 2048 + (80 * n + 16 * t + 4 * g) * 4);
      SB;
#pragma unroll
      for (int i = 0; i < 4; ++i)
#pragma unroll
        for (int t = 0; t < 5; ++t) pq[i][t] = pk4(phi4(acc[i][t] + mm[i] * c[t], splat(rr[i] * 1.4426950408889634f), splat(rr[i] * 0.5f)));
#pragma unroll
      for (int i = 0; i < 4; ++i) asm volatile("" : "+v"(pq[i][0]), "+v"(pq[i][1]), "+v"(pq[i][2]), "+v"(pq[i][3]), "+v"(pq[i][4]));
    }
    constexpr unsigned KP = 208, KQ = 84 * KP;
    unsigned wa[2];
#pragma unroll
    for (int k = 0; k < 2; ++k) {
      const unsigned qd = 2 * (tid < 420 ? tid : 0) + k, hd = qd / 105, e = (qd % 105) / 5, dq = qd % 5, hl = hd & 3;
      wa[k] = (hd >> 2) * KQ + (e < 20 ? hl * 20 + e : 80 + hl) * KP + hl * 40 + dq * 8;
    }
    unsigned fk = n * KQ + r * KP + g * 8, fz = n * KQ + (80 + (r & 3)) * KP + g * 8;
    asm volatile("" : "+v"(wa[0]), "+v"(wa[1]), "+v"(fk), "+v"(fz));
    STAMP(25);
    wait_vm<0>();
    bar();
#pragma unroll
    for (int k = 0; k < 5; ++k) lds_st<u32x4>(k * 8192 + tid * 16, u32x4{0u, 0u, 0u, 0u});
    if (w == 0) poll_flags((gu32*)(p.ws + WS_FLAGKV + (jidx * 32 + (lane & 15)) * FS), true, tag);
    lbar();
    STAMP(4);
    if (w < 7) {
      u32x4 v[8];
#pragma unroll
      for (int j = 0; j < 8; ++j)
        v[j] = __builtin_amdgcn_raw_buffer_load_b128(rsWS, (tid < 420 ? tid : 0) * 16, WS_KVP + (j * 32 + 8 * jidx) * 840, 16);
      SB;
      f32x4 t0 = {0.f, 0.f, 0.f, 0.f}, t1 = t0;
#pragma unroll
      for (int j = 0; j < 8; ++j) {
        t0 += f32x4{bflo(v[j][0]), bfhi(v[j][0]), bflo(v[j][1]), bfhi(v[j][1])};
        t1 += f32x4{bflo(v[j][2]), bfhi(v[j][2]), bflo(v[j][3]), bfhi(v[j][3])};
      }
      lds_st<u32x2>(wa[0], pk4(t0)); lds_st<u32x2>(wa[1], pk4(t1));
    }
    lbar();
    STAMP(16);
    bf16x8 A[12];
#pragma unroll
    for (int P = 0; P < 3; ++P) A[P] = frag(lds_ld<u32x2>(fz + P * 64), lds_ld<u32x2>(fz + P * 64 + 32));
#pragma unroll
    for (int u = 0; u < 5; ++u) {
      const unsigned a0 = fk + u * 16 * KP + (u < 3 ? 0 : 64);
      A[3 + u] = frag(lds_ld<u32x2>(a0), lds_ld<u32x2>(a0 + 32));
      if (u > 0) A[7 + u] = frag(lds_ld<u32x2>(a0 + 64), lds_ld<u32x2>(a0 + 96));
    }
    u32x2 sh[4];
#pragma unroll
    for (int i = 0; i < 4; ++i) {
      const bf16x8 B[3] = {frag(pq[i][0], pq[i][1]), frag(pq[i][2], pq[i][3]), frag(pq[i][4], u32x2{0u, 0u})};
      const f32x4 zero = {0.f, 0.f, 0.f, 0.f};
      const f32x4 den = mfma(A[2], B[2], mfma(A[1], B[1], mfma(A[0], B[0], zero))) + 1e-6f;
      f32x4 num[5];
#pragma unroll
      for (int u = 0; u < 5; ++u) num[u] = mfma(A[3 + u], B[u < 3 ? 0 : 1], zero);
#pragma unroll
      for (int u = 1; u < 5; ++u) num[u] = mfma(A[7 + u], B[u < 3 ? 1 : 2], num[u]);
      float rc[4];
#pragma unroll
      for (int j = 0; j < 4; ++j) rc[j] = __builtin_amdgcn_rcpf(den[j]);
      u32x2 o[5];
#pragma unroll
      for (int u = 0; u < 5; ++u) {
        const float rr = u == 0 ? rc[0] : (u == 4 ? rc[3] : (g < (unsigned)u ? rc[u - 1] : rc[u]));
        o[u] = pk4(num[u] * rr);
      }
      st_frag(rsWS, lane, rt0 + i, 54 + 5 * jidx + 2 * n, o[0], o[1]);
      st_frag(rsWS, lane, rt0 + i, 54 + 5 * jidx + 2 * n + 1, o[2], o[3]);
      sh[i] = o[4];
    }
    shared_send(w, lane, sh);
    shared_recv(rsWS, w, lane, rt0, 54 + 5 * jidx + 4, sh);
    STAMP(15);
    publish_flag(p, WS_FLAGO + (rb * 32 + jidx) * FS, tag, tid);
    STAMP(3);
    if (b == 224) {
      poll_flags((gu32*)(p.ws + WS_FLAGH + tid * FS), (tid & 31) < (tid < 256 ? 18u : 4u), tag);
      bar();
      if (tid == 0) __hip_atomic_store((gu32*)(p.ws + WS_EPOCH), tag, RLX_AGENT);
    }
  } else {
    const unsigned gw = spi * 8 + w;
    unsigned k10 = 0x00003f80u, k01 = 0x3f800000u;
    asm volatile("" : "+v"(k10), "+v"(k01));
    auto cs_src = [&](unsigned u, rsrc_t& rs, unsigned& cv, unsigned& st) {
      const unsigned cline = u / 3, third = u % 3;
      const void* cw = cline < 30 ? p.wqkv : (cline < 57 ? p.wg : p.wu);
      const unsigned cpitch = cline < 30 ? 3840u : 3456u;
      const unsigned clocal = cline < 30 ? cline : (cline < 57 ? cline - 30 : cline - 57);
      rs = mk_rsrc(cw, gw < 126 ? D * cpitch : 0);
      cv = (third * 216 + (lane >> 3)) * cpitch + clocal * 128 + (lane & 7) * 16;
      st = 8 * cpitch;
    };
    auto cs_publish = [&](unsigned u, float (&cs)[8]) {
#pragma unroll
      for (int k = 0; k < 4; ++k) { const bool hi = lane & 32; const float o = __shfl_xor(hi ? cs[k] : cs[k + 4], 32); cs[k] = (hi ? cs[k + 4] : cs[k]) + o; }
#pragma unroll
      for (int k = 0; k < 2; ++k) { const bool hi = lane & 16; const float o = __shfl_xor(hi ? cs[k] : cs[k + 2], 16); cs[k] = (hi ? cs[k + 2] : cs[k]) + o; }
      { const bool hi = lane & 8; const float o = __shfl_xor(hi ? cs[0] : cs[1], 8); cs[0] = (hi ? cs[1] : cs[0]) + o; }
      if (gw < 126)
        __hip_atomic_store((gu64*)(p.ws + WS_CSUM) + (u % 3) * NCOL + (u / 3) * 64 + (lane & 7) * 8 + (lane >> 3),
                           ((unsigned long long)tag << 32) | __builtin_bit_cast(unsigned, cs[0]), RLX_AGENT);
    };
    row_load(rs_, rsX, lane);
    asm volatile("s_waitcnt vmcnt(0) lgkmcnt(0)\n\ts_add_i32 %0, %1, 1" : "=s"(tag) : "s"(epoch) : "memory", "scc");
    static_for<0, 14>([&](auto K) { row_step<K.v>(rs_, lane); });
    u32x4 c[27];
    rsrc_t rs0, rs1; unsigned cv0, cv1, st0, st1;
    cs_src(gw, rs0, cv0, st0); cs_src(gw + 126, rs1, cv1, st1);
#pragma unroll
    for (int j = 0; j < 27; ++j) c[j] = __builtin_amdgcn_raw_buffer_load_b128(rs0, cv0 + j * st0, 0, 0);
    lbar();
    row_publish(rs_, p.ws, tag, tid);
    float cs[8] = {};
#pragma unroll
    for (int j = 0; j < 27; ++j) {
#pragma unroll
      for (int k = 0; k < 4; ++k) { cs[2 * k] = dot2(c[j][k], k10, cs[2 * k]); cs[2 * k + 1] = dot2(c[j][k], k01, cs[2 * k + 1]); }
      if (j < 8) c[j] = __builtin_amdgcn_raw_buffer_load_b128(rs1, cv1 + j * st1, 0, 0);
      SB;
    }
    cs_publish(gw, cs);
    SB;
#pragma unroll
    for (int j = 8; j < 27; ++j) c[j] = __builtin_amdgcn_raw_buffer_load_b128(rs1, cv1 + j * st1, 0, 0);
    SB;
    STAMP(11);
#pragma unroll
    for (int k = 0; k < 8; ++k) cs[k] = 0.f;
#pragma unroll
    for (int j = 0; j < 27; ++j)
#pragma unroll
      for (int k = 0; k < 4; ++k) { cs[2 * k] = dot2(c[j][k], k10, cs[2 * k]); cs[2 * k + 1] = dot2(c[j][k], k01, cs[2 * k + 1]); }
    cs_publish(gw + 126, cs);
    STAMP(12);
#pragma unroll 1
    for (unsigned i = w; i < 144; i += 8) dma16(i * 1024, v2, rsWd, urow(i >> 2, rot) + (i & 3) * 10240u + cb * 128u);
    drain_vm();
    bar();
  }
  if (!has2) { STAMP_END; return; }
  relane();

  const unsigned rbA = (G * 6) >> 4, rbB = (G * 6 + 5 > 127 ? 127 : G * 6 + 5) >> 4;
  if (w >= 4) {
    __builtin_amdgcn_s_setprio(HPRIO);
    const unsigned lw = w - 4, ld0 = lw * 1024;
    gu32* fo = (gu32*)(p.ws + WS_FLAGO + ((lane < 32 ? rbA : rbB) * 32 + (lane & 3)) * FS);
    gu32* fh = (gu32*)(p.ws + WS_FLAGH + ((lane < 32 ? rbA : rbB) * 32 + ((lane & 31) < 18 ? lane & 31 : 17)) * FS);
    if (w == 7) {
      __builtin_amdgcn_s_setprio(3);
      const unsigned v = poll_flags(fh, (lane & 31) < 18 && (lane & 31) >= 10, tag);
      lds_set(L_GO);
      if (!__all((lane & 31) >= 18 || v == tag)) poll_flags(fh, (lane & 31) < 18, tag);
      lds_set(L_GO + 4);
      __builtin_amdgcn_s_setprio(HPRIO);
    } else {
      __builtin_amdgcn_s_sleep(HSLEEP);
    }
    if (jtype == J_KV) {
#pragma unroll
      for (int u = 31; u < 36; ++u) dma16(u * 4096 + ld0, v2, rsWd, w2_off(W, u, lw));
    }
    if (jtype != J_MLP) {
#pragma unroll
      for (int u = 0; u < NPRE + NPOST; ++u) { pre2[u] = w2_load(W, 36 + u, lw, v2); SB; }
    }
#pragma unroll
    for (int u = 36 + NPRE + NPOST; u < HSPLIT; ++u) { pre2[u - 36] = w2_load(W, u, lw, v2); SB; }
    STAMP(8);
    unsigned la[3], wb[3], xo[3], st[3];
    u32x4 xr[3];
    const unsigned ch = lane & 7;
#pragma unroll
    for (int i = 0; i < 3; ++i) {
      const unsigned row = lw * 24 + i * 8 + (lane >> 3), rl = row >> 4, rr = row & 15;
      la[i] = (rl >= 3 ? 24576u + (rl - 3) * 4096u : rl * 4096u) + (ch >> 1) * 1024 + (ch & 1) * 512 + ((rr + 2 * ch) & 15) * 16;
      xo[i] = (G * 96 + row) * 1280 + cb * 128 + ch * 16;
      wb[i] = ld0 + lane * 16 + i * 49152;
      asm volatile("" : "+v"(la[i]), "+v"(wb[i]), "+v"(xo[i]));
    }
    float ri[3], rl[3];
    unsigned of = 0;
    f32x4 xf[3][2], dd[3][2];
#pragma unroll
    for (int k = 2; k <= 6; ++k) {
      if (k == O_BAR && w == 7 && !__all((lane & 31) >= 4 || of == tag)) poll_flags(fo, (lane & 31) < 4, tag);
      if (k == 6) {
        wait_vm<0>();
#pragma unroll
        for (int h = 0; h < 3; ++h) {
          ri[h] = 2.0f * bfhi(st[h]) * bfhi(st[h]);
          rl[h] = -1.4426950408889634f * __builtin_amdgcn_rcpf(ri[h]);
          xf[h][0] = f32x4{bflo(xr[h][0]), bfhi(xr[h][0]), bflo(xr[h][1]), bfhi(xr[h][1])};
          xf[h][1] = f32x4{bflo(xr[h][2]), bfhi(xr[h][2]), bflo(xr[h][3]), bfhi(xr[h][3])};
          asm volatile("" : "+v"(ri[h]), "+v"(rl[h]), "+v"(xf[h][0]), "+v"(xf[h][1]));
        }
      }
      lbar();
      if (k == O_BAR - 1) { of = __hip_atomic_load(fo, RLX_AGENT); SB; }
      if (k < 6) {
        const int kk = k - 1;
#pragma unroll
        for (int u = 9 * (kk - 1) + ((kk - 1) & 1); u < 9 * kk + (kk & 1); ++u) {
          lds_st<u32x4>(wb[u / 12] + (u % 12) * 4096, pre2[u]);
          SB;
        }
      }
      if (k == WOK || k == WOK + 1) {
#pragma unroll
        for (int u = (k == WOK ? 54 : 64); u < (k == WOK ? 64 : 72); ++u)
          if (u >= HSPLIT && u >= 36 + NPRE + NPOST) { pre2[u - 36] = w2_load(W, u, lw, v2); SB; }
        if (k != WOK) { dma16(36 * 4096 + ld0, v2, W.wo, w2_off(W, 72, lw)); dma16(37 * 4096 + ld0, v2, W.wo, w2_off(W, 73, lw)); }
      }
      if (k == 2) {
#pragma unroll
        for (int h = 0; h < 3; ++h) {
          const unsigned row = G * 96 + lw * 24 + h * 8 + (lane >> 3);
          st[h] = __builtin_amdgcn_raw_buffer_load_b32(rsWS, WS_STAT + (row < S ? row : 0) * 8, 0, 16);
          xr[h] = __builtin_amdgcn_raw_buffer_load_b128(rsX, xo[h], 0, 0);
        }
      }
    }
#pragma unroll
    for (int i = 0; i < 3; ++i) {
      const unsigned lb = la[i] + (la[i] >= 28672u ? 126976u : 12288u);
      dd[i][0] = lds_ld<f32x4>(la[i]) + lds_ld<f32x4>(lb);
      dd[i][1] = lds_ld<f32x4>(la[i] + 256) + lds_ld<f32x4>(lb + 256);
    }
    lbar();
    __builtin_amdgcn_s_setprio(GPRIO);
#pragma unroll
    for (int i = 0; i < 6; ++i) {
      const f32x4 l = dd[i >> 1][i & 1] * rl[i >> 1];
      f32x4 e;
#pragma unroll
      for (int j = 0; j < 4; ++j) e[j] = __builtin_amdgcn_exp2f(l[j]);
      e = e * ri[i >> 1] + ri[i >> 1];
#pragma unroll
      for (int j = 0; j < 4; ++j) e[j] = __builtin_amdgcn_rcpf(e[j]);
      dd[i >> 1][i & 1] *= e;
      asm volatile("" : "+v"(dd[i >> 1][i & 1]));
    }
    const rsrc_t rsO = mk_rsrc(p.out, S * D * 2);
    STAMP(9);
    bar();
    f32x4 ss[3][2];
#pragma unroll
    for (int i = 0; i < 3; ++i) {
      ss[i][0] = lds_ld<f32x4>(la[i]) + lds_ld<f32x4>(la[i] + 12288);
      ss[i][1] = lds_ld<f32x4>(la[i] + 256) + lds_ld<f32x4>(la[i] + 12288 + 256);
    }
#pragma unroll
    for (int i = 0; i < 3; ++i)
      __builtin_amdgcn_raw_buffer_store_b128(cat2(pk4(xf[i][0] + ss[i][0] * dd[i][0]), pk4(xf[i][1] + ss[i][1] * dd[i][1])), rsO, xo[i], 0, 0);
    STAMP(5);
    STAMP_END;
    return;
  }
  {
    const unsigned kh = w & 1, rt = G * 6 + 3 * (w >> 1);
    const rsrc_t rsH = mk_rsrc(p.ws + WS_HA + (rt < 128 ? rt : 0) * (NSTEP * 1024), rt < 128 ? (rt > 125 ? 128 - rt : 3) * (NSTEP * 1024) : 0);
    unsigned tb[3][4];
#pragma unroll
    for (int t = 0; t < 4; ++t) {
      tb[0][t] = (4 * g + q4) * 128 + ((t ^ ((2 * g + (q4 >> 1)) & 3)) << 5) + 8 * p4 + kh * 4096;
      tb[1][t] = tb[0][t] + 49152; tb[2][t] = tb[0][t] + 98304;
      asm volatile("" : "+v"(tb[0][t]), "+v"(tb[1][t]), "+v"(tb[2][t]));
    }
    auto rd1 = [&](int i, int t, int hf) {
      const int sl = i == 36 ? 36 : (2 * i) % 36, bi = sl / 12 > 2 ? 2 : sl / 12;
      return __builtin_bit_cast(u32x2, lds_tr(tb[bi][t] + (sl - 12 * bi) * 4096 + hf * 2048));
    };
    constexpr int NW = P2NW, W0 = P2W0;
    constexpr int OI = O_BAR == 4 ? 17 : (O_BAR == 5 ? 22 : 26);
    constexpr int OB0 = OI == 26 ? NW - 1 : 0;
    f32x4 acc[3][4];
    u32x4 a[NW][3];
    u32x2 bw[2][4][2];
    unsigned va[3];
#pragma unroll
    for (int h = 0; h < 3; ++h) va[h] = lane * 16 + h * NSTEP * 1024;
    auto put = [&](bool hi) {
      acc_fence<12>(&acc[0][0]);
#pragma unroll
      for (int t = 0; t < 4; ++t) {
        const unsigned xa = w * 12288u + t * 1024 + g * 256 + ((r + 4 * t + (g & 2)) & 15) * 16, xb = xa + (hi && w == 3 ? 114688u : 0u);
        lds_st<f32x4>(xa, acc[0][t]); lds_st<f32x4>(xb + 4096, acc[1][t]); lds_st<f32x4>(xb + 8192, acc[2][t]);
      }
      wait_lgkm0();
    };
    unsigned so = (rot + kh) * 1024;
    auto lda = [&](auto I, auto H) {
      constexpr int i = I.v, h = H.v;
      if constexpr (i < 27) {
        if (h == 0 && so >= 54 * 1024) so -= 54 * 1024;
        a[i % NW][h] = __builtin_amdgcn_raw_buffer_load_b128(rsH, va[h], so, 16);
        if (h == 2) so += 2048;
      } else {
        a[i % NW][h] = __builtin_amdgcn_raw_buffer_load_b128(rsH, va[h], (2 * i + kh) * 1024, 16);
      }
      SB;
    };
    lds_wait(L_GO);
    unsigned go2 = 0;
    STAMP(8);
    static_for<0, W0>([&](auto I) { lda(I, IC<0>{}); lda(I, IC<1>{}); lda(I, IC<2>{}); });
#pragma unroll
    for (int t = 0; t < 4; ++t) { bw[0][t][0] = rd1(0, t, 0); bw[0][t][1] = rd1(0, t, 1); }
    static_for<0, 37>([&](auto I) {
      constexpr int i = I.v;
      constexpr int lim = i + NW - 1;
      constexpr int nxt = i <= OI ? (W0 + 2 * i < lim ? W0 + 2 * i : lim) : (27 + OB0 + 2 * (i - OI - 1) < lim ? 27 + OB0 + 2 * (i - OI - 1) : lim);
      constexpr int end = i <= OI ? 27 : 37;
      constexpr int l0 = nxt < end ? nxt : -1, l1 = (nxt + 1 <= lim && nxt + 1 < end) ? nxt + 1 : -1;
      SB;
      static_for<0, 12>([&](auto E) {
        constexpr int e = E.v, t = e / 3, h = e % 3;
        const u32x4 bf = cat2(bw[i & 1][t][0], bw[i & 1][t][1]);
        if constexpr (i == 0 || i == 27) mfma_z(acc[2 - h][t], bf, a[i % NW][2 - h]); else mfma_ip(acc[2 - h][t], bf, a[i % NW][2 - h]);
        SB;
        if constexpr (i == 4 && e == 2) go2 = lds_get(L_GO + 4);
        if constexpr (i < 36 && h < 2) bw[(i + 1) & 1][t][h] = rd1(i + 1, t, h);
        if constexpr (l1 < 0) { if constexpr (h == 2 && t < 3 && l0 >= 0) lda(IC<l0>{}, IC<t>{}); }
        else if constexpr (e & 1) lda(IC<(e < 6 ? l0 : l1)>{}, IC<(e >> 1) % 3>{});
        SB;
      });
      if constexpr (i == 26) put(true);
      if constexpr (i == XBAR) bar();
      if constexpr (i == 4) { if (__builtin_expect(!rfl(go2), 0)) lds_wait(L_GO + 4); asm volatile("" ::: "memory"); STAMP(17); }
      if constexpr (i > 4 && i < 27 && (i % 9 == 4 || i % 9 == 8)) {
        bar();
        if constexpr (i == 26 && OI == 26) static_for<27, 27 + OB0>([&](auto J) { lda(J, IC<0>{}); lda(J, IC<1>{}); lda(J, IC<2>{}); });
        STAMP(17 + (2 * i) / 9);
      }
    });
    STAMP(24);
    put(false);
    bar();
    STAMP(5);
    STAMP_END;
  }
}

extern "C" void kernel_launch(void* const* d_in, const int* in_sizes, int n_in, void* d_out, int out_size, void* d_ws,
                              size_t ws_size, hipStream_t stream) {
  Params p;
  p.x = d_in[0]; p.wqkv = d_in[1]; p.wo = d_in[2]; p.wg = d_in[4]; p.wu = d_in[5]; p.wd = d_in[6];
  p.out = d_out; p.ws = (char*)d_ws;
  fused_block<<<256, 512, 0, stream>>>(p);
}
```

```cpp
#include <hip/hip_runtime.h>
#include <stdint.h>

typedef __attribute__((ext_vector_type(8))) __bf16 bf16x8;
typedef __attribute__((ext_vector_type(4))) __bf16 bf16x4;
typedef __attribute__((ext_vector_type(2))) __bf16 bf16x2;
typedef __attribute__((ext_vector_type(4))) float f32x4;
typedef __attribute__((ext_vector_type(2))) float f32x2;
typedef __attribute__((ext_vector_type(4))) unsigned u32x4;
typedef __attribute__((ext_vector_type(2))) unsigned u32x2;
typedef __amdgpu_buffer_rsrc_t rsrc_t;
#define LAS __attribute__((address_space(3)))
#define GAS __attribute__((address_space(1)))
#define DI __device__ __forceinline__
#define RLX_AGENT __ATOMIC_RELAXED, __HIP_MEMORY_SCOPE_AGENT
typedef GAS unsigned gu32;
typedef GAS unsigned long long gu64;

constexpr int S = 2048, D = 640, F = 1728;
constexpr int NCOL = 5376;
constexpr int NSTEP = 74;
constexpr unsigned WS_EPOCH = 0;
#define FS 128
constexpr unsigned WS_FLAGH = 768u << 10;
constexpr unsigned WS_FLAGO = WS_FLAGH + 8 * 32 * FS;
constexpr unsigned WS_FLAGKV = WS_FLAGO + 8 * 32 * FS;
constexpr unsigned WS_STAT = 4096;
constexpr unsigned WS_CSUM = WS_STAT + S * 8;
constexpr unsigned WS_KVP = WS_CSUM + 3 * NCOL * 8;
constexpr unsigned WS_HA = 1u << 20;
constexpr unsigned WS_DBG = 12u << 20;
constexpr unsigned WS_END = 13u << 20;
constexpr unsigned L_B = 65536;
constexpr unsigned L_SCR = 147456;
constexpr unsigned L_MISC = 159744;
constexpr unsigned L_GO = L_MISC + 3072;
constexpr unsigned SPIN_CAP = 1u << 18;
#define HSLEEP 4
#define HSPLIT 64
#define PPIPE 0
#define STA 0
#define STB 2
#define NPOST 0
#define SNAKE 0
#define EORD 0
#define NPRE 12
#define WOK 2
#define GRP 1
#define GPRIO 0
#define HPRIO 0
#define PSLEEP 2
#define P2NW 7
#define P2W0 6
#define WSLEEP 0
#define ROTM 18
#define NA1 2
#define O_BAR 5
#define PRIO_MODE 0
#define DMA_SPACE_A T
#define DMA_SPACE_B (4 * T / 3)

struct Params {
  const void* x; const void* wqkv; const void* wo; const void* wg; const void* wu; const void* wd;
  void* out; char* ws;
};

__shared__ __attribute__((aligned(16))) char smem[163840];

DI unsigned rfl(unsigned v) { return __builtin_amdgcn_readfirstlane(v); }
DI rsrc_t mk_rsrc(const void* p, unsigned bytes) { return __builtin_amdgcn_make_buffer_rsrc((void*)p, (short)0, (int)bytes, 0x00020000); }
DI void dma16(unsigned ldsdst, unsigned voff, rsrc_t rs, unsigned soff) {
  asm volatile("s_mov_b32 m0, %0\n\ts_nop 0\n\tbuffer_load_dwordx4 %1, %2, %3 offen lds" ::"s"(ldsdst), "v"(voff), "s"(rs), "s"(soff) : "memory");
}
template <int N> DI void wait_vm() { asm volatile("s_waitcnt vmcnt(%0)" ::"i"(N) : "memory"); }
DI void drain_vm() { wait_vm<0>(); __builtin_amdgcn_s_waitcnt(0x0F70); }
DI void wait_lgkm0() { asm volatile("s_waitcnt lgkmcnt(0)" ::: "memory"); }
DI void bar() { asm volatile("" ::: "memory"); __builtin_amdgcn_s_barrier(); asm volatile("" ::: "memory"); }
DI void lbar() { wait_lgkm0(); bar(); }
template <class V> DI V lds_ld(unsigned a) { return *(LAS V*)((LAS char*)smem + a); }
template <class V> DI void lds_st(unsigned a, V v) { *(LAS V*)((LAS char*)smem + a) = v; }
DI bf16x4 lds_tr(unsigned a) { return __builtin_amdgcn_ds_read_tr16_b64_v4bf16((LAS bf16x4*)((LAS char*)smem + a)); }
DI bf16x8 cat(bf16x4 a, bf16x4 b) { return __builtin_shufflevector(a, b, 0, 1, 2, 3, 4, 5, 6, 7); }
DI f32x4 mfma(bf16x8 a, bf16x8 b, f32x4 c) { return __builtin_amdgcn_mfma_f32_16x16x32_bf16(a, b, c, 0, 0, 0); }
DI unsigned pk2(float a, float b) { f32x2 v = {a, b}; return __builtin_bit_cast(unsigned, __builtin_convertvector(v, bf16x2)); }
DI u32x2 pk4(f32x4 v) { u32x2 r = {pk2(v[0], v[1]), pk2(v[2], v[3])}; return r; }
DI bf16x8 frag(u32x2 lo, u32x2 hi) { u32x4 v = {lo[0], lo[1], hi[0], hi[1]}; return __builtin_bit_cast(bf16x8, v); }
DI float bflo(unsigned v) { return __builtin_bit_cast(float, v << 16); }
DI float bfhi(unsigned v) { return __builtin_bit_cast(float, v & 0xffff0000u); }
DI float rbf(float v) { return bflo(pk2(v, 0.f)); }
DI float relu2(float v) { float o; asm("v_add_f32 %0, %1, |%1|" : "=v"(o) : "v"(v)); return o; }
DI f32x4 phi4(f32x4 u, f32x4 rl, f32x4 rh) {
  const f32x4 l = u * rl;
  f32x4 e, a;
#pragma unroll
  for (int j = 0; j < 4; ++j) {
    e[j] = __builtin_amdgcn_fmed3f(__builtin_amdgcn_exp2f(l[j]), 0.f, 1.f);
    a[j] = relu2(u[j]);
  }
  return a * rh + e + 1e-6f;
}
DI f32x4 splat(float v) { return f32x4{v, v, v, v}; }
template <int CTRL> DI float dpp_add(float v) {
  return v + __builtin_bit_cast(float, __builtin_amdgcn_update_dpp(0, __builtin_bit_cast(int, v), CTRL, 0xf, 0xf, true));
}
DI float dot2(unsigned a, unsigned b, float c) {
  return __builtin_amdgcn_fdot2_f32_bf16(__builtin_bit_cast(bf16x2, a), __builtin_bit_cast(bf16x2, b), c, false);
}
DI u32x4 ldtr2(unsigned a, unsigned d) { return __builtin_bit_cast(u32x4, cat(lds_tr(a), lds_tr(a + d))); }
DI void mfma_ip(f32x4& c, const u32x4& a, const u32x4& b) { asm volatile("v_mfma_f32_16x16x32_bf16 %0, %1, %2, %0" : "+v"(c) : "v"(a), "v"(b)); }
DI void mfma_z(f32x4& c, const u32x4& a, const u32x4& b) { asm volatile("v_mfma_f32_16x16x32_bf16 %0, %1, %2, 0" : "=&v"(c) : "v"(a), "v"(b)); }
template <int N> DI void acc_fence(f32x4* c) {
  asm volatile("s_nop 7" ::: "memory");
#pragma unroll
  for (int i = 0; i < N; ++i) asm volatile("" : "+v"(c[i]));
}
DI u32x4 cat2(u32x2 a, u32x2 b) { return u32x4{a[0], a[1], b[0], b[1]}; }
DI u32x2 ldtr1(unsigned a) { return __builtin_bit_cast(u32x2, lds_tr(a)); }
template <int IMM> DI void set_m0i(unsigned v) { asm volatile("s_add_i32 m0, %0, %1" ::"s"(v), "i"(IMM) : "memory", "scc"); }
template <int OFF> DI void dma_goi(unsigned voff, rsrc_t rs, unsigned soff) {
  asm volatile("buffer_load_dwordx4 %0, %1, %2 offen offset:%3 lds" ::"v"(voff), "s"(rs), "s"(soff), "i"(OFF) : "memory");
}
DI void set_m0(unsigned v) { asm volatile("s_mov_b32 m0, %0" ::"s"(v) : "memory"); }
DI void dma_go(unsigned voff, rsrc_t rs, unsigned soff) {
  asm volatile("buffer_load_dwordx4 %0, %1, %2 offen lds" ::"v"(voff), "s"(rs), "s"(soff) : "memory");
}
DI f32x4 mfma_u(u32x4 a, u32x4 b, f32x4 c) { return mfma(__builtin_bit_cast(bf16x8, a), __builtin_bit_cast(bf16x8, b), c); }
struct Stamps { unsigned long long t; };
#define STAMP_DECL Stamps stv_ = {0ull}
#define STAMP_ARG , Stamps& stv_
#define STAMP_PASS , stv_
#define STAMPX(c, i) do { if ((i) == 6) { asm volatile("s_memrealtime %0\n\ts_waitcnt lgkmcnt(0)" : "=s"(stv_.t)); } } while (0)
#define STAMP_END do { if (blockIdx.x == 8 && threadIdx.x == 0) __hip_atomic_store((gu64*)(p.ws + WS_DBG), stv_.t, RLX_AGENT); } while (0)
#define STAMP(i) STAMPX(threadIdx.x == 0, i)

DI unsigned poll_flags(gu32* word, bool active, unsigned tag) {
  unsigned v = 0;
#pragma unroll 1
  for (unsigned spins = 0; spins < SPIN_CAP; ++spins) {
    v = __hip_atomic_load(word, RLX_AGENT);
    if (__all(!active || v == tag)) break;
    if (PSLEEP) __builtin_amdgcn_s_sleep(PSLEEP);
  }
  asm volatile("" ::: "memory");
  return v;
}

DI void lds_set(unsigned a) { __hip_atomic_store((LAS unsigned*)smem + a / 4, 1u, __ATOMIC_RELAXED, __HIP_MEMORY_SCOPE_WORKGROUP); }
DI unsigned lds_get(unsigned a) { return __hip_atomic_load((LAS unsigned*)smem + a / 4, __ATOMIC_RELAXED, __HIP_MEMORY_SCOPE_WORKGROUP); }
DI void lds_wait(unsigned a) {
#pragma unroll 1
  for (unsigned spins = 0; spins < (SPIN_CAP << 4); ++spins) {
    if (rfl(lds_get(a))) break;
    __builtin_amdgcn_s_sleep(1);
  }
  asm volatile("" ::: "memory");
}

template <int I> struct IC { static constexpr int v = I; };
template <int B, int E, class Fn> DI void static_for(Fn&& f) {
  if constexpr (B < E) { f(IC<B>{}); static_for<B + 1, E>(f); }
}

enum { J_MLP = 0, J_KV = 1, J_Q = 2, J_SPARE = 3 };

struct Dma {
  rsrc_t rsA, rsB;
  unsigned vA0, vA1, vB0, vB1, vB2;
  unsigned sA[4], sB, advB;
  unsigned mA, mB, mW;
};

constexpr unsigned a_slot(unsigned s3) { return s3 == 0 ? 126976u : (s3 - 1) * 32768u; }
#define SB __builtin_amdgcn_sched_barrier(0)

template <int T> DI unsigned bofs(const unsigned (&boff)[T], int t) { return T == 6 && t >= 3 ? boff[t - 3] + 6144 : boff[t]; }
template <int T, bool TR, int KS, class Extra>
DI void kstep(f32x4 (&acc)[4][T], const u32x4 (&ac)[4], const u32x2 (&bc)[T][2], u32x4 (&an)[4], u32x2 (&bn)[T][2],
              const unsigned (&aoff)[4], const unsigned (&boff)[T], const Dma& d, Extra&& extra) {
  constexpr int s = KS >> 1, KIND = KS & 1, NP = KIND == 0 ? 4 : 3, ST = KIND == 0 ? (STA ? STA : (4 * T - 1) / NP) : (STB ? STB : (4 * T - 1) / NP);
  constexpr int sn = (KS + 1) >> 1;
  constexpr unsigned asl = a_slot(sn % 3), bsl = ((KS + 1) % 5) * 12288u;
  static_for<0, 4 * T>([&](auto E) {
    constexpr int e = E.v, t = e >> 2, i = (SNAKE && (t & 1)) ? 3 - (e & 3) : (e & 3);
    const u32x4 bf = cat2(bc[t][0], bc[t][1]);
    if constexpr (KS == 0) { if (TR) mfma_z(acc[i][t], bf, ac[i]); else mfma_z(acc[i][t], ac[i], bf); }
    else { if (TR) mfma_ip(acc[i][t], bf, ac[i]); else mfma_ip(acc[i][t], ac[i], bf); }
    SB;
    if constexpr (KS < 19) {
      if constexpr (e < 4) an[e] = lds_ld<u32x4>(aoff[((KS + 1) & 1) + (sn % 3 == 0 ? 2 : 0)] + (sn % 3 == 0 ? 0u : asl) + e * 2048);
      else if constexpr (e < 4 + 2 * T) bn[(e - 4) >> 1][e & 1] = ldtr1(bofs<T>(boff, (e - 4) >> 1) + bsl + (e & 1) * 768);
    }
    constexpr int pc = e / ST;
    if constexpr (KIND == 0) {
      constexpr int adv = s < 8 ? (s + 2) * 128 : 0, dst = (int)a_slot((s + 2) % 3) + pc * 1024, off = dst >= adv ? adv : 0;
      if constexpr (pc < NP && e % ST == ST - 2) set_m0i<dst - off>(d.mA);
      if constexpr (pc < NP && e % ST == ST - 1) dma_goi<off>((pc & 1) ? d.vA1 : d.vA0, d.rsA, d.sA[pc] + (adv - off));
    } else {
      constexpr int h0 = (2 * s) % 5;
      if constexpr (pc < NP && e % ST == ST - 2) set_m0i<(h0 == 4 ? 0 : h0 * 12288) + pc * 1024>(h0 == 4 ? d.mW : d.mB);
      if constexpr (pc < NP && e % ST == ST - 1) dma_goi<0>(pc == 0 ? d.vB0 : (pc == 1 ? d.vB1 : d.vB2), d.rsB, d.sB);
    }
    extra(E);
    SB;
  });
}

struct RowStat { u32x4 xa; unsigned xb, row; float s, q; };
DI void row_load(RowStat& R, rsrc_t rsX, unsigned lane) {
  asm volatile("buffer_load_dwordx4 %0, %2, %4, %5 offen\n\tbuffer_load_dword %1, %3, %4, %5 offen offset:1024"
               : "=&v"(R.xa), "=&v"(R.xb) : "v"(lane * 16), "v"(lane * 4), "s"(rsX), "s"(R.row * 1280) : "memory");
}
template <int K> DI void row_step(RowStat& R, unsigned lane) {
  if constexpr (K < 5) {
    if constexpr (K == 0) { R.s = 0.f; R.q = 0.f; }
    const unsigned v = K < 4 ? R.xa[K & 3] : R.xb;
    asm volatile("v_dot2c_f32_bf16 %0, %2, %3\n\tv_dot2c_f32_bf16 %1, %2, %2" : "+v"(R.s), "+v"(R.q) : "v"(v), "v"(0x3f803f80u));
  } else if constexpr (K < 11) {
    constexpr int C = K == 5 ? 0xB1 : (K == 6 ? 0x4E : (K == 7 ? 0x141 : (K == 8 ? 0x140 : (K == 9 ? 0x142 : 0x143))));
    if constexpr (K == 5) asm volatile("s_nop 2" : "+v"(R.s), "+v"(R.q));
    R.s = dpp_add<C>(R.s); R.q = dpp_add<C>(R.q);
  } else if constexpr (K == 11) {
    R.s *= 1.0f / D;
    R.q = fmaxf(R.q * (1.0f / D) - R.s * R.s, 0.f);
  } else if constexpr (K == 12) {
    R.q = __builtin_amdgcn_sqrtf(rbf(rbf(R.q) + 1e-5f));
  } else {
    lds_st<unsigned>(lane == 63 ? L_GO + 32 + (R.row & 7) * 4 : L_GO + 64, pk2(R.s, R.q));
  }
  if constexpr (K >= 5 && K < 13) asm volatile("" : "+v"(R.s), "+v"(R.q));
}
DI void row_publish(const RowStat& R, char* ws, unsigned tag, unsigned tid) {
  if (tid < 8) __hip_atomic_store((gu64*)(ws + WS_STAT) + R.row + tid, ((unsigned long long)tag << 32) | lds_ld<unsigned>(L_GO + 32 + tid * 4), RLX_AGENT);
}

struct Gran { u32x2 g0, g1, g2; unsigned off, step; };
DI void gran_issue(Gran& G, rsrc_t rs) {
  asm volatile("buffer_load_dwordx2 %0, %3, %4, 0 offen sc1\n\tbuffer_load_dwordx2 %1, %3, %4, %5 offen sc1\n\t"
               "buffer_load_dwordx2 %2, %3, %4, %6 offen sc1"
               : "=&v"(G.g0), "=&v"(G.g1), "=&v"(G.g2) : "v"(G.off), "s"(rs), "s"(G.step), "s"(2 * G.step) : "memory");
}
DI void gran_load(Gran& G, rsrc_t rs) {
  asm volatile("buffer_load_dwordx2 %0, %3, %4, 0 offen sc1\n\tbuffer_load_dwordx2 %1, %3, %4, %5 offen sc1\n\t"
               "buffer_load_dwordx2 %2, %3, %4, %6 offen sc1\n\ts_waitcnt vmcnt(0)"
               : "=&v"(G.g0), "=&v"(G.g1), "=&v"(G.g2) : "v"(G.off), "s"(rs), "s"(G.step), "s"(2 * G.step) : "memory");
}

DI unsigned urow(unsigned pos, unsigned rot) { const unsigned u = pos + rot; return (u >= 54 ? u - 54 : u) * 40960u; }

constexpr unsigned wo_tile(unsigned v) { return (v / 5) * 10 + (v % 5 == 4 ? 4 : (v % 5 < 2 ? 2 * (v % 5) : 2 * (v % 5) + 1)); }
struct W2 { rsrc_t wd, wo; unsigned rot, col; };
DI unsigned w2_off(const W2& W, int u, unsigned lw) {
  if (u < 54) return urow(u, W.rot) + lw * 10240u + W.col;
  return wo_tile(u - 54) * 20480u + (lw >> 1) * ((u - 54) % 5 == 4 ? 102400u : 20480u) + (lw & 1) * 10240u + W.col;
}
DI u32x4 w2_load(const W2& W, int u, unsigned lw, unsigned voff) {
  return __builtin_amdgcn_raw_buffer_load_b128(u < 54 ? W.wd : W.wo, voff, w2_off(W, u, lw), 0);
}

template <int T, bool TR, bool PF, bool LASTA, class Pre>
DI void main_loop(Pre&& pre, RowStat& R, char* ws, f32x4 (&acc)[4][T], unsigned (&boff)[T], Dma& d, unsigned w, unsigned lane, unsigned tid,
                  unsigned epoch, unsigned& tag, unsigned rb, unsigned ccol, rsrc_t rsB2, rsrc_t rsWS, unsigned cb, unsigned rot, u32x4 (&pre2)[36] STAMP_ARG) {
  const unsigned g = lane >> 4, r = lane & 15, m = w & 3;
  unsigned aoff[4];
  aoff[0] = (64 * m + r) * 128 + ((g ^ (r >> 1)) << 4);
  aoff[1] = aoff[0] ^ 64;
  aoff[2] = aoff[0] + a_slot(0); aoff[3] = aoff[1] + a_slot(0);
  const unsigned wq = (w & 1) * 2 + ((w >> 1) & 1);
  asm volatile("" : "+v"(aoff[0]), "+v"(aoff[1]), "+v"(aoff[2]), "+v"(aoff[3]), "+v"(boff[0]), "+v"(boff[1]), "+v"(boff[2]));
  if (T == 5) asm volatile("" : "+v"(boff[3]), "+v"(boff[4]));
  Gran G;
  unsigned v2, vb2, vpf, tA[3], tB[3];
  row_load(R, d.rsA, lane);
  {
    d.sB += d.advB;
    const unsigned bdst = L_B + (w < 4 ? 36864u : 24576u) + wq * 3072u;
    dma16(bdst, d.vB0, d.rsB, d.sB); dma16(bdst + 1024, d.vB1, d.rsB, d.sB); dma16(bdst + 2048, d.vB2, d.rsB, d.sB);
#pragma unroll
    for (int i = 0; i < NA1; ++i) dma16(a_slot(1) + d.mA + i * 1024, (i & 1) ? d.vA1 : d.vA0, d.rsA, d.sA[i] + 128);
    asm volatile("s_waitcnt vmcnt(%2) lgkmcnt(0)\n\tv_readfirstlane_b32 %0, %1\n\ts_nop 3" : "=s"(tag) : "v"(epoch), "i"(5 + NA1) : "memory");
    tag = rfl(tag) + 1;
    bar();
  }
  STAMP(1);
  u32x4 a0[4], a1[4];
  u32x2 b0[T][2], b1[T][2];
#pragma unroll
  for (int i = 0; i < 4; ++i) a0[i] = lds_ld<u32x4>(aoff[2] + i * 2048);
#pragma unroll
  for (int t = 0; t < T; ++t) { b0[t][0] = ldtr1(bofs<T>(boff, t)); b0[t][1] = ldtr1(bofs<T>(boff, t) + 768); }
  SB;
#pragma unroll
  for (int i = NA1; i < 4; ++i) dma16(a_slot(1) + d.mA + i * 1024, (i & 1) ? d.vA1 : d.vA0, d.rsA, d.sA[i] + 128);
  const bool y = w >= 4;
  bool ok = true;
  auto ln_store = [&] {
    const float v0 = y ? -bflo(G.g0[0]) : __builtin_bit_cast(float, G.g0[0]) + __builtin_bit_cast(float, G.g1[0]) + __builtin_bit_cast(float, G.g2[0]);
    const unsigned la = L_MISC + (y ? tid * 4 - 1024 : 2048 + tid * 4);
    lds_st<float>(la, v0);
    lds_st<float>(y ? la + 1024 : L_GO + 64 + (lane & 7) * 4, __builtin_amdgcn_rcpf(bfhi(G.g0[0])));
  };
  static_for<0, 10>([&](auto S_) {
    constexpr int s = S_.v, L = 4 * T;
    constexpr int NX = (PF && s >= 1 && 2 * s <= NPRE) ? 2 : 0;
    kstep<T, TR, 2 * s>(acc, a0, b0, a1, b1, aoff, boff, d, [&](auto E) {
      constexpr int e = E.v;
      if constexpr (s == 2) {
        if constexpr (e == 2) { vb2 = v2 + (3 * wq) * 10240u; asm volatile("" : "+v"(vb2)); }
        if constexpr (e == 5) { G.off = w < 4 ? WS_CSUM + ccol * 8 : WS_STAT + (rb * 256 + tid - 256) * 8; asm volatile("" : "+v"(G.off)); }
        if constexpr (e == 8) { G.step = w < 4 ? NCOL * 8 : 0; tA[0] = urow(w, rot) + cb * 128u; asm volatile("" : "+s"(G.step), "+s"(tA[0])); }
        if constexpr (e == 10) { tA[1] = urow(8 + w, rot) + cb * 128u; asm volatile("" : "+s"(tA[1])); }
        if constexpr (e == 12) { tA[2] = urow(31 + w, rot) + cb * 128u; asm volatile("" : "+s"(tA[2])); }
        if constexpr (e == 14) {
          tB[0] = urow(16, rot) + cb * 128u; tB[1] = urow(w < 4 ? 19u : 22u, rot) + cb * 128u; tB[2] = urow(w < 4 ? 25u : 28u, rot) + cb * 128u;
          asm volatile("" : "+s"(tB[0]), "+s"(tB[1]), "+s"(tB[2]));
        }
      }
      if constexpr (s == 3 && e == 2) pre();
      if constexpr (NX && e >= L - 2) {
        constexpr int u = 2 * (s - 1) + e - (L - 2);
        pre2[u] = __builtin_amdgcn_raw_buffer_load_b128(rsB2, vpf, urow(36 + u, rot) + (w & 3) * 10240u + cb * 128u, 0);
      }
      if constexpr (s < 7) { if constexpr (e == L - 3) d.sB += s == 0 ? d.advB * (1 + (w >> 2)) : d.advB; }
      else if constexpr (s == 7) {
        if constexpr (e == L - 6) d.vB0 = y ? vb2 : d.vB0;
        if constexpr (e == L - 4) d.vB1 = y ? vb2 + 10240u : d.vB1;
        if constexpr (e == L - 3) d.vB2 = y ? vb2 + 20480u : d.vB2;
        if constexpr (e == L - 2) d.sB = y ? tB[0] : d.sB + d.advB;
        if constexpr (e == L - 1) d.rsB = y ? rsB2 : d.rsB;
      } else {
        if constexpr (e == L - 3) { d.sB = tB[s - 7]; d.rsB = rsB2; d.vB0 = vb2; d.vB1 = vb2 + 10240u; d.vB2 = vb2 + 20480u; }
      }
      if constexpr (s == 9 && e == L - 4) {
        asm volatile("s_waitcnt vmcnt(7)" : "+v"(G.g0), "+v"(G.g1), "+v"(G.g2)::"memory");
        ok = __all(G.g0[1] == tag && G.g1[1] == tag && G.g2[1] == tag);
      }
      if constexpr (s == 9 && e == L - 3) ln_store();
    });
    if constexpr (s == 0) {
      wait_vm<11>();
      static_for<0, 14>([&](auto K) { row_step<K.v>(R, lane); });
    }
    if constexpr (s == 9) {
      if (__builtin_expect(!ok, 0)) {
#pragma unroll 1
        for (unsigned spins = 0; spins < SPIN_CAP; ++spins) {
          __builtin_amdgcn_s_sleep(2);
          gran_load(G, rsWS);
          if (__all(G.g0[1] == tag && G.g1[1] == tag && G.g2[1] == tag)) break;
        }
        ln_store();
      }
    }
    wait_lgkm0();
    if constexpr (s < 9) wait_vm<4 + NX>();
    bar();
    if constexpr (s == 0) { STAMP(12); }
    SB;
    kstep<T, TR, 2 * s + 1>(acc, a1, b1, a0, b0, aoff, boff, d, [&](auto E) {
      constexpr int e = E.v;
      if constexpr (s == 0 && e == 6) {
        if (y) {
          const unsigned so = d.sB - d.advB;
          dma16(d.mW + 49152, d.vB0, d.rsB, so); dma16(d.mW + 49152 + 1024, d.vB1, d.rsB, so); dma16(d.mW + 49152 + 2048, d.vB2, d.rsB, so);
        }
      }
      if constexpr (s == 0 && e == L - 4) {
        v2 = (lane >> 3) * 1280 + (((lane & 7) ^ (((lane >> 4) & 3) << 1)) << 4);
        vpf = y ? v2 : 0x7ffffff0u;
        asm volatile("" : "+v"(v2), "+v"(vpf));
      }
      if constexpr (s == 0 && e == L - 6) row_publish(R, ws, tag, tid);
      if constexpr (s == 8 && e == 0) gran_issue(G, rsWS);
      if constexpr (s >= 7 && s < 9 && e >= L - 4) {
        d.rsA = rsB2; d.vA0 = v2; d.vA1 = v2;
        d.sA[e - (L - 4)] = tA[s - 7] + (e - (L - 4)) * 10240;
      }
      if constexpr (s == 9 && LASTA && e == 7) {
        if (w < 5) {
#pragma unroll
          for (int i = 0; i < 4; ++i) dma16(a_slot(0) + d.mA + i * 1024, v2, rsB2, tA[2] + i * 10240);
        }
      }
    });
    if constexpr (s == 8) { STAMP(13); }
    if constexpr (s == 2) { STAMP(26); }
    if constexpr (s == 6) { STAMP(27); }
  });
  acc_fence<4 * T>(&acc[0][0]);
  STAMP(14);
}

DI void st16(u32x4 v, rsrc_t rs, unsigned voff, unsigned soff) {
  __builtin_amdgcn_raw_buffer_store_b128(v, rs, voff, soff, 16);
  asm volatile("s_nop 1" ::"v"(v) : "memory");
}
DI void publish_flag(const Params& p, unsigned flag_off, unsigned tag, unsigned tid) {
  drain_vm();
  bar();
  if (tid == 0) __hip_atomic_store((gu32*)(p.ws + flag_off), tag, RLX_AGENT);
}
DI void st_frag(rsrc_t rs, unsigned lane, unsigned rt, unsigned step, u32x2 lo, u32x2 hi) {
  u32x4 v = {lo[0], lo[1], hi[0], hi[1]};
  st16(v, rs, lane * 16, WS_HA + (rt * NSTEP + step) * 1024);
}
DI void shared_send(unsigned w, unsigned lane, const u32x2 (&sh)[4]) {
  const bool n = w >= 4;
  u32x4 snd = n ? u32x4{sh[0][0], sh[0][1], sh[1][0], sh[1][1]} : u32x4{sh[2][0], sh[2][1], sh[3][0], sh[3][1]};
  lds_st<u32x4>(L_SCR + w * 1024 + lane * 16, snd);
}
DI void shared_recv(rsrc_t rs, unsigned w, unsigned lane, unsigned rt0, unsigned step, const u32x2 (&sh)[4]) {
  lbar();
  u32x4 rcv = lds_ld<u32x4>(L_SCR + (w ^ 4) * 1024 + lane * 16);
  u32x2 r0 = {rcv[0], rcv[1]}, r1 = {rcv[2], rcv[3]};
  if (w >= 4) { st_frag(rs, lane, rt0 + 2, step, r0, sh[2]); st_frag(rs, lane, rt0 + 3, step, r1, sh[3]); }
  else        { st_frag(rs, lane, rt0 + 0, step, sh[0], r0); st_frag(rs, lane, rt0 + 1, step, sh[1], r1); }
}

__global__ __launch_bounds__(512, 2) void fused_block(Params p) {
  unsigned tid = threadIdx.x, lane = tid & 63;
  const unsigned w = rfl(tid >> 6);
  const unsigned b = blockIdx.x, xcd = b & 7, slot = b >> 3;
  const unsigned rb = 4 * (xcd & 1) + (slot & 3), jq = xcd >> 1, ji = slot >> 2;
  const unsigned spi = (xcd - 4) * 4 + (slot & 3);
  const unsigned long long jl0 = (jq & 1) ? 0x4123220908070605ull : 0x4021200403020100ull, jl1 = (jq & 1) ? 0x4327266011100f0eull : 0x422524600d0c0b0aull;
  const unsigned jb = (unsigned)(((jq & 2) ? jl1 : jl0) >> (8 * ji)) & 0xff, jtype = jb >> 5, jidx = jb & 31;
  STAMP_DECL;
  STAMP(0);

  Dma d;
  RowStat rs_;
  const bool mlp = jtype == J_MLP;
  const unsigned pitch = mlp ? 3456u : 3840u, wq = (w & 1) * 2 + ((w >> 1) & 1);
  const rsrc_t rsX = mk_rsrc(p.x, S * D * 2);
  d.vA0 = (lane >> 3) * 1280 + (((lane & 7) ^ (lane >> 4)) << 4); d.vA1 = d.vA0 ^ 64;
  d.rsA = rsX; d.mA = w * 4096u;
#pragma unroll
  for (int i = 0; i < 4; ++i) d.sA[i] = (rb * 256 + 32 * w + 8 * i) * 1280;
  unsigned epoch, tag;
  asm volatile("s_nop 4" :: "s"(rsX) : "memory");
  STAMP(6);
#pragma unroll
  for (int i = 0; i < 4; ++i) dma16(a_slot(0) + d.mA + i * 1024, (i & 1) ? d.vA1 : d.vA0, d.rsA, d.sA[i]);
  asm volatile("global_load_dword %0, %1, %2 sc1" : "=&v"(epoch) : "v"(0u), "s"(p.ws) : "memory");
  SB;
  {
    unsigned vb[3];
    const unsigned lim = mlp ? 176u : 144u;
    {
      const unsigned r0 = (lane * 43) >> 9, p0 = lane - 12 * r0;
      const bool w1 = p0 >= 8, w2 = p0 >= 4;
      const unsigned r1 = r0 + (w1 ? 6 : 5), p1 = p0 + (w1 ? -8 : 4), r2 = r0 + (w2 ? 11 : 10), p2 = p0 + (w2 ? -4 : 8);
      vb[0] = r0 * pitch + min(p0 * 16, lim);
      vb[1] = r1 * pitch + min((p1 * 16) ^ ((r1 & 8) << 2), lim);
      vb[2] = r2 * pitch + min((p2 * 16) ^ 32, lim);
    }
    d.vB0 = vb[0]; d.vB1 = vb[1]; d.vB2 = vb[2];
    const unsigned mh = w & 1;
    const void* bw = mlp ? (mh ? p.wu : p.wg) : p.wqkv;
    const unsigned cj = mlp ? 192u : 160u * jtype, cm = mlp ? 0u : (jtype == J_KV ? 1280u : 160u), c0 = jtype == J_KV ? 1280u : 0u;
    d.rsB = mk_rsrc(bw, D * pitch);
    d.sB = (16 * ((w >> 1) & 1) + (w < 4 ? 32 : 0)) * pitch + c0 + cj * jidx + cm * mh; d.advB = 64 * pitch;
    d.mB = L_B + wq * 3072u + (w < 4 ? 0u : 12288u); d.mW = L_B + wq * 3072u + (w < 4 ? 49152u : 0u);
    const unsigned bdst = L_B + (w < 4 ? 12288u : 0u) + wq * 3072u;
    dma16(bdst, d.vB0, d.rsB, d.sB); dma16(bdst + 1024, d.vB1, d.rsB, d.sB); dma16(bdst + 2048, d.vB2, d.rsB, d.sB);
    rs_.row = rfl(b * 8 + w);
  }
  STAMP(10);
  lds_st<unsigned>(L_GO + (tid & 1) * 4, 0u);

  const unsigned t2 = 28 * xcd + slot;
  const bool has2 = slot < 28 && t2 < 220;
  const unsigned G = t2 / 10, cb = t2 % 10;
  constexpr unsigned rot = 32;
  unsigned g = lane >> 4, r = lane & 15, q4 = r >> 2, p4 = lane & 3;
  const rsrc_t rsWd = mk_rsrc(p.wd, has2 ? F * D * 2 : 0);
  const rsrc_t rsWS = mk_rsrc(p.ws, WS_END);
  unsigned v2 = (lane >> 3) * 1280 + (((lane & 7) ^ (((lane >> 4) & 3) << 1)) << 4);
  auto relane = [&] {
    asm volatile("" : "+v"(tid));
    lane = tid & 63; g = lane >> 4; r = lane & 15; q4 = r >> 2; p4 = lane & 3;
    v2 = (lane >> 3) * 1280 + (((lane & 7) ^ (((lane >> 4) & 3) << 1)) << 4);
  };
  const unsigned m = w & 3, n = w >> 2;
  const unsigned rt0 = rb * 16 + m * 4;
  const unsigned brow = L_B + (8 * g + q4) * 192;
  const unsigned bx = (g & 1) << 5;

  u32x4 pre2[36];
  W2 W;
  W.wd = rsWd; W.wo = mk_rsrc(p.wo, has2 ? D * D * 2 : 0); W.rot = rot; W.col = cb * 128u;
  if (jtype == J_MLP) {
    f32x4 acc[4][6];
    unsigned boff[6];
#pragma unroll
    for (int t = 0; t < 3; ++t) {
      const unsigned gt = n ? (t == 2 ? 3 : 4 + t) : t;
      boff[t] = brow + ((32 * gt + 8 * p4) ^ bx); boff[t + 3] = boff[t] + 6144;
    }
    const unsigned ccol = tid < 96 ? 1920 + 96 * jidx + tid : 3648 + 96 * jidx + (tid < 192 ? tid - 96 : 0);
    main_loop<6, true, true, true>([] {}, rs_, p.ws, acc, boff, d, w, lane, tid, epoch, tag, rb, ccol, rsWd, rsWS, cb, rot, pre2 STAMP_PASS);
    relane();
    if (w >= 4) {
#pragma unroll
      for (int u = NPRE; u < NPRE + NPOST; ++u) { pre2[u] = w2_load(W, 36 + u, w & 3, v2); SB; }
    }
    float mm[4];
    f32x4 cg[3], cu[3];
#pragma unroll
    for (int i = 0; i < 4; ++i) mm[i] = lds_ld<float>(L_MISC + (64 * m + 16 * i + r) * 4);
#pragma unroll
    for (int t = 0; t < 3; ++t) {
      const unsigned gt = n ? (t == 2 ? 3 : 4 + t) : t;
      cg[t] = lds_ld<f32x4>(L_MISC + 2048 + (16 * gt + 4 * g) * 4); cu[t] = lds_ld<f32x4>(L_MISC + 2048 + (96 + 16 * gt + 4 * g) * 4);
    }
    auto hval = [&](int i, int t) {
      f32x4 gg = acc[i][t] + mm[i] * cg[t];
      const f32x4 uu = acc[i][t + 3] + mm[i] * cu[t];
#pragma unroll
      for (int j = 0; j < 4; ++j) gg[j] = relu2(gg[j]);
      return pk4(gg * uu);
    };
#pragma unroll
    for (int i = 0; i < 4; ++i) { st_frag(rsWS, lane, rt0 + i, 3 * jidx + 2 * n, hval(i, 0), hval(i, 1)); SB; }
    const u32x2 sh[4] = {hval(0, 2), hval(1, 2), hval(2, 2), hval(3, 2)};
    shared_send(w, lane, sh);
    shared_recv(rsWS, w, lane, rt0, 3 * jidx + 1, sh);
    STAMP(15);
    publish_flag(p, WS_FLAGH + (rb * 32 + jidx) * FS, tag, tid);
    STAMP(3);
  } else if (jtype == J_KV) {
    f32x4 acc[4][5];
    unsigned boff[5];
    boff[0] = brow + ((80 * n + 8 * p4) ^ bx); boff[1] = boff[0] + 6144;
    boff[2] = brow + ((80 * n + 40 + 8 * p4) ^ bx); boff[3] = boff[2] + 6144;
    boff[4] = brow + ((80 * n + 32 + 40 * (p4 >> 1)) ^ bx) + (p4 & 1) * 6144;
    const unsigned kb = 126976u + m * 6720 + n * 3360, dump = L_MISC - 2048 + lane * 16;
    const unsigned ea = (q4 == 1 || r == 0) ? kb + (r == 0 ? 20 : 12 + r) * 80 : 0u, eb = (q4 == 3 || r == 0) ? kb + 1680 + (r == 0 ? 20 : 4 + r) * 80 : 0u;
    unsigned ka[6] = {kb + r * 80 + g * 16, ea ? ea + g * 16 : dump, eb ? eb + g * 16 : dump, g == 0 ? kb + r * 80 + 64 : dump,
                      g == 2 ? kb + 1680 + r * 80 + 64 : dump, (g == 0 && ea) ? ea + 64 : ((g == 2 && eb) ? eb + 64 : dump)};
    auto pin = [&] { asm volatile("" : "+v"(ka[0]), "+v"(ka[1]), "+v"(ka[2]), "+v"(ka[3]), "+v"(ka[4]), "+v"(ka[5])); };
    const unsigned ccol = tid < 80 ? 640 + 80 * jidx + tid : 1280 + 80 * jidx + (tid < 160 ? tid - 80 : 0);
    main_loop<5, false, false, false>(pin, rs_, p.ws, acc, boff, d, w, lane, tid, epoch, tag, rb, ccol, rsWd, rsWS, cb, rot, pre2 STAMP_PASS);
    relane();
    float cs[5];
    cs[0] = lds_ld<float>(L_MISC + 2048 + (40 * n + r) * 4);
    cs[1] = lds_ld<float>(L_MISC + 2048 + (80 + 40 * n + r) * 4);
    cs[2] = lds_ld<float>(L_MISC + 2048 + (40 * n + 20 + r) * 4);
    cs[3] = lds_ld<float>(L_MISC + 2048 + (80 + 40 * n + 20 + r) * 4);
    cs[4] = lds_ld<float>(L_MISC + 2048 + ((q4 & 1) * 80 + 40 * n + 16 + 20 * (q4 >> 1) + (r & 3)) * 4);
    u32x2 pk[4][3], vv[4][3];
    f32x4 mm4[4], rr4[4];
#pragma unroll
    for (int i = 0; i < 4; ++i) {
      mm4[i] = lds_ld<f32x4>(L_MISC + (64 * m + 16 * i + 4 * g) * 4); rr4[i] = lds_ld<f32x4>(L_MISC + 1024 + (64 * m + 16 * i + 4 * g) * 4);
    }
    SB;
#pragma unroll
    for (int i = 0; i < 4; ++i) {
      const f32x4 mm = mm4[i], rr = rr4[i];
      const f32x4 rl = rr * 1.4426950408889634f, rh = rr * 0.5f;
      f32x4 val[5];
#pragma unroll
      for (int t = 0; t < 5; ++t) val[t] = acc[i][t] + mm * cs[t];
#pragma unroll
      for (int t = 0; t < 3; ++t) pk[i][t] = pk4(phi4(val[2 * t], rl, rh));
      vv[i][0] = pk4(val[1] * rr); vv[i][1] = pk4(val[3] * rr);
      const u32x2 v4 = pk4(val[4] * rr);
      vv[i][2] = (q4 & 1) ? v4 : u32x2{0x3f803f80u, 0x3f803f80u};
    }
    STAMP(25);
    f32x4 P[7] = {};
#pragma unroll
    for (int i = 0; i < 4; i += 2) {
      const bf16x8 k0 = frag(pk[i][0], pk[i + 1][0]), k2 = frag(pk[i][1], pk[i + 1][1]), k4 = frag(pk[i][2], pk[i + 1][2]);
      const bf16x8 v1 = frag(vv[i][0], vv[i + 1][0]), v3 = frag(vv[i][1], vv[i + 1][1]), v4 = frag(vv[i][2], vv[i + 1][2]);
      P[0] = mfma(k0, v1, P[0]); P[1] = mfma(k0, v4, P[1]); P[2] = mfma(k4, v1, P[2]);
      P[3] = mfma(k2, v3, P[3]); P[4] = mfma(k2, v4, P[4]); P[5] = mfma(k4, v3, P[5]); P[6] = mfma(k4, v4, P[6]);
    }
    lds_st<f32x4>(ka[0], P[0]); lds_st<f32x4>(ka[0] + 1680, P[3]); lds_st<f32x4>(ka[1], P[1]); lds_st<f32x4>(ka[2], P[4]);
    lds_st<f32x4>(ka[3], P[2]); lds_st<f32x4>(ka[4], P[5]); lds_st<f32x4>(ka[5], P[6]);
    lbar();
    if (tid < 210) {
      const unsigned a = 126976u + tid * 32;
      const f32x4 t0 = (lds_ld<f32x4>(a) + lds_ld<f32x4>(a + 6720)) + (lds_ld<f32x4>(a + 13440) + lds_ld<f32x4>(a + 20160));
      const f32x4 t1 = (lds_ld<f32x4>(a + 16) + lds_ld<f32x4>(a + 6736)) + (lds_ld<f32x4>(a + 13456) + lds_ld<f32x4>(a + 20176));
      st16(cat2(pk4(t0), pk4(t1)), rsWS, tid * 16, WS_KVP + (rb * 32 + 4 * jidx) * 840);
    }
    wait_lgkm0();
    STAMP(15);
    publish_flag(p, WS_FLAGKV + ((jidx >> 1) * 32 + (jidx & 1) * 8 + rb) * FS, tag, tid);
    STAMP(3);
  } else if (jtype == J_Q) {
    f32x4 acc[4][5];
    unsigned boff[5];
#pragma unroll
    for (int t = 0; t < 5; ++t) boff[t] = brow + n * 6144 + ((32 * t + 8 * p4) ^ bx);
    const unsigned ccol = 160 * jidx + (tid < 160 ? tid : 0);
    main_loop<5, true, false, false>([] {}, rs_, p.ws, acc, boff, d, w, lane, tid, epoch, tag, rb, ccol, rsWd, rsWS, cb, rot, pre2 STAMP_PASS);
    relane();
    u32x2 pq[4][5];
    {
      float mm[4], rr[4];
      f32x4 c[5];
#pragma unroll
      for (int i = 0; i < 4; ++i) { mm[i] = lds_ld<float>(L_MISC + (64 * m + 16 * i + r) * 4); rr[i] = lds_ld<float>(L_MISC + 1024 + (64 * m + 16 * i + r) * 4); }
#pragma unroll
      for (int t = 0; t < 5; ++t) c[t] = lds_ld<f32x4>(L_MISC + 2048 + (80 * n + 16 * t + 4 * g) * 4);
      SB;
#pragma unroll
      for (int i = 0; i < 4; ++i)
#pragma unroll
        for (int t = 0; t < 5; ++t) pq[i][t] = pk4(phi4(acc[i][t] + mm[i] * c[t], splat(rr[i] * 1.4426950408889634f), splat(rr[i] * 0.5f)));
#pragma unroll
      for (int i = 0; i < 4; ++i) asm volatile("" : "+v"(pq[i][0]), "+v"(pq[i][1]), "+v"(pq[i][2]), "+v"(pq[i][3]), "+v"(pq[i][4]));
    }
    constexpr unsigned KP = 208, KQ = 84 * KP;
    unsigned wa[2];
#pragma unroll
    for (int k = 0; k < 2; ++k) {
      const unsigned qd = 2 * (tid < 420 ? tid : 0) + k, hd = qd / 105, e = (qd % 105) / 5, dq = qd % 5, hl = hd & 3;
      wa[k] = (hd >> 2) * KQ + (e < 20 ? hl * 20 + e : 80 + hl) * KP + hl * 40 + dq * 8;
    }
    unsigned fk = n * KQ + r * KP + g * 8, fz = n * KQ + (80 + (r & 3)) * KP + g * 8;
    asm volatile("" : "+v"(wa[0]), "+v"(wa[1]), "+v"(fk), "+v"(fz));
    STAMP(25);
    wait_vm<0>();
    bar();
#pragma unroll
    for (int k = 0; k < 5; ++k) lds_st<u32x4>(k * 8192 + tid * 16, u32x4{0u, 0u, 0u, 0u});
    if (w == 0) poll_flags((gu32*)(p.ws + WS_FLAGKV + (jidx * 32 + (lane & 15)) * FS), true, tag);
    lbar();
    STAMP(4);
    if (w < 7) {
      u32x4 v[8];
#pragma unroll
      for (int j = 0; j < 8; ++j)
        v[j] = __builtin_amdgcn_raw_buffer_load_b128(rsWS, (tid < 420 ? tid : 0) * 16, WS_KVP + (j * 32 + 8 * jidx) * 840, 16);
      SB;
      f32x4 t0 = {0.f, 0.f, 0.f, 0.f}, t1 = t0;
#pragma unroll
      for (int j = 0; j < 8; ++j) {
        t0 += f32x4{bflo(v[j][0]), bfhi(v[j][0]), bflo(v[j][1]), bfhi(v[j][1])};
        t1 += f32x4{bflo(v[j][2]), bfhi(v[j][2]), bflo(v[j][3]), bfhi(v[j][3])};
      }
      lds_st<u32x2>(wa[0], pk4(t0)); lds_st<u32x2>(wa[1], pk4(t1));
    }
    lbar();
    STAMP(16);
    bf16x8 A[12];
#pragma unroll
    for (int P = 0; P < 3; ++P) A[P] = frag(lds_ld<u32x2>(fz + P * 64), lds_ld<u32x2>(fz + P * 64 + 32));
#pragma unroll
    for (int u = 0; u < 5; ++u) {
      const unsigned a0 = fk + u * 16 * KP + (u < 3 ? 0 : 64);
      A[3 + u] = frag(lds_ld<u32x2>(a0), lds_ld<u32x2>(a0 + 32));
      if (u > 0) A[7 + u] = frag(lds_ld<u32x2>(a0 + 64), lds_ld<u32x2>(a0 + 96));
    }
    u32x2 sh[4];
#pragma unroll
    for (int i = 0; i < 4; ++i) {
      const bf16x8 B[3] = {frag(pq[i][0], pq[i][1]), frag(pq[i][2], pq[i][3]), frag(pq[i][4], u32x2{0u, 0u})};
      const f32x4 zero = {0.f, 0.f, 0.f, 0.f};
      const f32x4 den = mfma(A[2], B[2], mfma(A[1], B[1], mfma(A[0], B[0], zero))) + 1e-6f;
      f32x4 num[5];
#pragma unroll
      for (int u = 0; u < 5; ++u) num[u] = mfma(A[3 + u], B[u < 3 ? 0 : 1], zero);
#pragma unroll
      for (int u = 1; u < 5; ++u) num[u] = mfma(A[7 + u], B[u < 3 ? 1 : 2], num[u]);
      float rc[4];
#pragma unroll
      for (int j = 0; j < 4; ++j) rc[j] = __builtin_amdgcn_rcpf(den[j]);
      u32x2 o[5];
#pragma unroll
      for (int u = 0; u < 5; ++u) {
        const float rr = u == 0 ? rc[0] : (u == 4 ? rc[3] : (g < (unsigned)u ? rc[u - 1] : rc[u]));
        o[u] = pk4(num[u] * rr);
      }
      st_frag(rsWS, lane, rt0 + i, 54 + 5 * jidx + 2 * n, o[0], o[1]);
      st_frag(rsWS, lane, rt0 + i, 54 + 5 * jidx + 2 * n + 1, o[2], o[3]);
      sh[i] = o[4];
    }
    shared_send(w, lane, sh);
    shared_recv(rsWS, w, lane, rt0, 54 + 5 * jidx + 4, sh);
    STAMP(15);
    publish_flag(p, WS_FLAGO + (rb * 32 + jidx) * FS, tag, tid);
    STAMP(3);
    if (b == 224) {
      poll_flags((gu32*)(p.ws + WS_FLAGH + tid * FS), (tid & 31) < (tid < 256 ? 18u : 4u), tag);
      bar();
      if (tid == 0) __hip_atomic_store((gu32*)(p.ws + WS_EPOCH), tag, RLX_AGENT);
    }
  } else {
    const unsigned gw = spi * 8 + w;
    unsigned k10 = 0x00003f80u, k01 = 0x3f800000u;
    asm volatile("" : "+v"(k10), "+v"(k01));
    auto cs_src = [&](unsigned u, rsrc_t& rs, unsigned& cv, unsigned& st) {
      const unsigned cline = u / 3, third = u % 3;
      const void* cw = cline < 30 ? p.wqkv : (cline < 57 ? p.wg : p.wu);
      const unsigned cpitch = cline < 30 ? 3840u : 3456u;
      const unsigned clocal = cline < 30 ? cline : (cline < 57 ? cline - 30 : cline - 57);
      rs = mk_rsrc(cw, gw < 126 ? D * cpitch : 0);
      cv = (third * 216 + (lane >> 3)) * cpitch + clocal * 128 + (lane & 7) * 16;
      st = 8 * cpitch;
    };
    auto cs_publish = [&](unsigned u, float (&cs)[8]) {
#pragma unroll
      for (int k = 0; k < 4; ++k) { const bool hi = lane & 32; const float o = __shfl_xor(hi ? cs[k] : cs[k + 4], 32); cs[k] = (hi ? cs[k + 4] : cs[k]) + o; }
#pragma unroll
      for (int k = 0; k < 2; ++k) { const bool hi = lane & 16; const float o = __shfl_xor(hi ? cs[k] : cs[k + 2], 16); cs[k] = (hi ? cs[k + 2] : cs[k]) + o; }
      { const bool hi = lane & 8; const float o = __shfl_xor(hi ? cs[0] : cs[1], 8); cs[0] = (hi ? cs[1] : cs[0]) + o; }
      if (gw < 126)
        __hip_atomic_store((gu64*)(p.ws + WS_CSUM) + (u % 3) * NCOL + (u / 3) * 64 + (lane & 7) * 8 + (lane >> 3),
                           ((unsigned long long)tag << 32) | __builtin_bit_cast(unsigned, cs[0]), RLX_AGENT);
    };
    row_load(rs_, rsX, lane);
    asm volatile("s_waitcnt vmcnt(0) lgkmcnt(0)\n\tv_readfirstlane_b32 %0, %1\n\ts_nop 3" : "=s"(tag) : "v"(epoch) : "memory");
    tag = rfl(tag) + 1;
    static_for<0, 14>([&](auto K) { row_step<K.v>(rs_, lane); });
    u32x4 c[27];
    rsrc_t rs0, rs1; unsigned cv0, cv1, st0, st1;
    cs_src(gw, rs0, cv0, st0); cs_src(gw + 126, rs1, cv1, st1);
#pragma unroll
    for (int j = 0; j < 27; ++j) c[j] = __builtin_amdgcn_raw_buffer_load_b128(rs0, cv0 + j * st0, 0, 0);
    lbar();
    row_publish(rs_, p.ws, tag, tid);
    float cs[8] = {};
#pragma unroll
    for (int j = 0; j < 27; ++j) {
#pragma unroll
      for (int k = 0; k < 4; ++k) { cs[2 * k] = dot2(c[j][k], k10, cs[2 * k]); cs[2 * k + 1] = dot2(c[j][k], k01, cs[2 * k + 1]); }
      if (j < 8) c[j] = __builtin_amdgcn_raw_buffer_load_b128(rs1, cv1 + j * st1, 0, 0);
      SB;
    }
    cs_publish(gw, cs);
    SB;
#pragma unroll
    for (int j = 8; j < 27; ++j) c[j] = __builtin_amdgcn_raw_buffer_load_b128(rs1, cv1 + j * st1, 0, 0);
    SB;
    STAMP(11);
#pragma unroll
    for (int k = 0; k < 8; ++k) cs[k] = 0.f;
#pragma unroll
    for (int j = 0; j < 27; ++j)
#pragma unroll
      for (int k = 0; k < 4; ++k) { cs[2 * k] = dot2(c[j][k], k10, cs[2 * k]); cs[2 * k + 1] = dot2(c[j][k], k01, cs[2 * k + 1]); }
    cs_publish(gw + 126, cs);
    STAMP(12);
#pragma unroll 1
    for (unsigned i = w; i < 144; i += 8) dma16(i * 1024, v2, rsWd, urow(i >> 2, rot) + (i & 3) * 10240u + cb * 128u);
    drain_vm();
    bar();
  }
  if (!has2) { STAMP_END; return; }
  relane();

  const unsigned rbA = (G * 6) >> 4, rbB = (G * 6 + 5 > 127 ? 127 : G * 6 + 5) >> 4;
  if (w >= 4) {
    __builtin_amdgcn_s_setprio(HPRIO);
    const unsigned lw = w - 4, ld0 = lw * 1024;
    gu32* fo = (gu32*)(p.ws + WS_FLAGO + ((lane < 32 ? rbA : rbB) * 32 + (lane & 3)) * FS);
    gu32* fh = (gu32*)(p.ws + WS_FLAGH + ((lane < 32 ? rbA : rbB) * 32 + ((lane & 31) < 18 ? lane & 31 : 17)) * FS);
    if (w == 7) {
      __builtin_amdgcn_s_setprio(3);
      const unsigned v = poll_flags(fh, (lane & 31) < 18 && (lane & 31) >= 10, tag);
      lds_set(L_GO);
      if (!__all((lane & 31) >= 18 || v == tag)) poll_flags(fh, (lane & 31) < 18, tag);
      lds_set(L_GO + 4);
      __builtin_amdgcn_s_setprio(HPRIO);
    } else {
      __builtin_amdgcn_s_sleep(HSLEEP);
    }
    if (jtype == J_KV) {
#pragma unroll
      for (int u = 31; u < 36; ++u) dma16(u * 4096 + ld0, v2, rsWd, w2_off(W, u, lw));
    }
    if (jtype != J_MLP) {
#pragma unroll
      for (int u = 0; u < NPRE + NPOST; ++u) { pre2[u] = w2_load(W, 36 + u, lw, v2); SB; }
    }
#pragma unroll
    for (int u = 36 + NPRE + NPOST; u < HSPLIT; ++u) { pre2[u - 36] = w2_load(W, u, lw, v2); SB; }
    STAMP(8);
    unsigned la[3], wb[3], xo[3], st[3];
    u32x4 xr[3];
    const unsigned ch = lane & 7;
#pragma unroll
    for (int i = 0; i < 3; ++i) {
      const unsigned row = lw * 24 + i * 8 + (lane >> 3), rl = row >> 4, rr = row & 15;
      la[i] = (rl >= 3 ? 24576u + (rl - 3) * 4096u : rl * 4096u) + (ch >> 1) * 1024 + (ch & 1) * 512 + ((rr + 2 * ch) & 15) * 16;
      xo[i] = (G * 96 + row) * 1280 + cb * 128 + ch * 16;
      wb[i] = ld0 + lane * 16 + i * 49152;
      asm volatile("" : "+v"(la[i]), "+v"(wb[i]), "+v"(xo[i]));
    }
    float ri[3], rl[3];
    unsigned of = 0;
    f32x4 xf[3][2], dd[3][2];
#pragma unroll
    for (int k = 2; k <= 6; ++k) {
      if (k == O_BAR && w == 7 && !__all((lane & 31) >= 4 || of == tag)) poll_flags(fo, (lane & 31) < 4, tag);
      if (k == 6) {
        wait_vm<0>();
#pragma unroll
        for (int h = 0; h < 3; ++h) {
          ri[h] = 2.0f * bfhi(st[h]) * bfhi(st[h]);
          rl[h] = -1.4426950408889634f * __builtin_amdgcn_rcpf(ri[h]);
          xf[h][0] = f32x4{bflo(xr[h][0]), bfhi(xr[h][0]), bflo(xr[h][1]), bfhi(xr[h][1])};
          xf[h][1] = f32x4{bflo(xr[h][2]), bfhi(xr[h][2]), bflo(xr[h][3]), bfhi(xr[h][3])};
          asm volatile("" : "+v"(ri[h]), "+v"(rl[h]), "+v"(xf[h][0]), "+v"(xf[h][1]));
        }
      }
      lbar();
      if (k == O_BAR - 1) { of = __hip_atomic_load(fo, RLX_AGENT); SB; }
      if (k < 6) {
        const int kk = k - 1;
#pragma unroll
        for (int u = 9 * (kk - 1) + ((kk - 1) & 1); u < 9 * kk + (kk & 1); ++u) {
          lds_st<u32x4>(wb[u / 12] + (u % 12) * 4096, pre2[u]);
          SB;
        }
      }
      if (k == WOK || k == WOK + 1) {
#pragma unroll
        for (int u = (k == WOK ? 54 : 64); u < (k == WOK ? 64 : 72); ++u)
          if (u >= HSPLIT && u >= 36 + NPRE + NPOST) { pre2[u - 36] = w2_load(W, u, lw, v2); SB; }
        if (k != WOK) { dma16(36 * 4096 + ld0, v2, W.wo, w2_off(W, 72, lw)); dma16(37 * 4096 + ld0, v2, W.wo, w2_off(W, 73, lw)); }
      }
      if (k == 2) {
#pragma unroll
        for (int h = 0; h < 3; ++h) {
          const unsigned row = G * 96 + lw * 24 + h * 8 + (lane >> 3);
          st[h] = __builtin_amdgcn_raw_buffer_load_b32(rsWS, WS_STAT + (row < S ? row : 0) * 8, 0, 16);
          xr[h] = __builtin_amdgcn_raw_buffer_load_b128(rsX, xo[h], 0, 0);
        }
      }
    }
    bar();
#pragma unroll
    for (int i = 0; i < 3; ++i) {
      dd[i][0] = lds_ld<f32x4>(la[i]) + lds_ld<f32x4>(la[i] + 12288);
      dd[i][1] = lds_ld<f32x4>(la[i] + 256) + lds_ld<f32x4>(la[i] + 12288 + 256);
      la[i] += 49152;
      asm volatile("" : "+v"(la[i]));
    }
    __builtin_amdgcn_s_setprio(GPRIO);
#pragma unroll
    for (int i = 0; i < 6; ++i) {
      const f32x4 l = dd[i >> 1][i & 1] * rl[i >> 1];
      f32x4 e;
#pragma unroll
      for (int j = 0; j < 4; ++j) e[j] = __builtin_amdgcn_exp2f(l[j]);
      e = e * ri[i >> 1] + ri[i >> 1];
#pragma unroll
      for (int j = 0; j < 4; ++j) e[j] = __builtin_amdgcn_rcpf(e[j]);
      dd[i >> 1][i & 1] *= e;
      asm volatile("" : "+v"(dd[i >> 1][i & 1]));
    }
    const rsrc_t rsO = mk_rsrc(p.out, S * D * 2);
    STAMP(9);
    bar();
    f32x4 ss[3][2];
#pragma unroll
    for (int i = 0; i < 3; ++i) {
      ss[i][0] = lds_ld<f32x4>(la[i]) + lds_ld<f32x4>(la[i] + 12288);
      ss[i][1] = lds_ld<f32x4>(la[i] + 256) + lds_ld<f32x4>(la[i] + 12288 + 256);
    }
#pragma unroll
    for (int i = 0; i < 3; ++i)
      __builtin_amdgcn_raw_buffer_store_b128(cat2(pk4(xf[i][0] + ss[i][0] * dd[i][0]), pk4(xf[i][1] + ss[i][1] * dd[i][1])), rsO, xo[i], 0, 0);
    STAMP(5);
    STAMP_END;
    return;
  }
  {
    const unsigned kh = w & 1, rt = G * 6 + 3 * (w >> 1);
    const rsrc_t rsH = mk_rsrc(p.ws + WS_HA + (rt < 128 ? rt : 0) * (NSTEP * 1024), rt < 128 ? (rt > 125 ? 128 - rt : 3) * (NSTEP * 1024) : 0);
    unsigned tb[3][4];
#pragma unroll
    for (int t = 0; t < 4; ++t) {
      tb[0][t] = (4 * g + q4) * 128 + ((t ^ ((2 * g + (q4 >> 1)) & 3)) << 5) + 8 * p4 + kh * 4096;
      tb[1][t] = tb[0][t] + 49152; tb[2][t] = tb[0][t] + 98304;
      asm volatile("" : "+v"(tb[0][t]), "+v"(tb[1][t]), "+v"(tb[2][t]));
    }
    auto rd1 = [&](int i, int t, int hf) {
      const int sl = i == 36 ? 36 : (2 * i) % 36, bi = sl / 12 > 2 ? 2 : sl / 12;
      return __builtin_bit_cast(u32x2, lds_tr(tb[bi][t] + (sl - 12 * bi) * 4096 + hf * 2048));
    };
    constexpr int NW = P2NW, W0 = P2W0;
    constexpr int OI = O_BAR == 4 ? 17 : (O_BAR == 5 ? 22 : 26);
    constexpr int OB0 = OI == 26 ? NW - 1 : 0;
    f32x4 acc[2][3][4];
    u32x4 a[NW][3];
    u32x2 bw[2][4][2];
    unsigned va[3];
#pragma unroll
    for (int h = 0; h < 3; ++h) va[h] = lane * 16 + h * NSTEP * 1024;
    unsigned xa[4];
#pragma unroll
    for (int t = 0; t < 4; ++t) {
      xa[t] = w * 12288u + t * 1024 + g * 256 + ((r + 4 * t + (g & 2)) & 15) * 16;
      asm volatile("" : "+v"(xa[t]));
    }
    auto put = [&](auto Z, auto E) {
      constexpr int z = Z.v, t = E.v / 3, h = 2 - E.v % 3;
      asm volatile("" : "+v"(acc[z][h][t]));
      lds_st<f32x4>(xa[t] + z * 49152 + h * 4096, acc[z][h][t]);
    };
    unsigned so = (rot + kh) * 1024;
    auto lda = [&](auto I, auto H) {
      constexpr int i = I.v, h = H.v;
      if constexpr (i < 27) {
        if (h == 0 && so >= 54 * 1024) so -= 54 * 1024;
        a[i % NW][h] = __builtin_amdgcn_raw_buffer_load_b128(rsH, va[h], so, 16);
        if (h == 2) so += 2048;
      } else {
        a[i % NW][h] = __builtin_amdgcn_raw_buffer_load_b128(rsH, va[h], (2 * i + kh) * 1024, 16);
      }
      SB;
    };
    lds_wait(L_GO);
    unsigned go2 = 0;
    STAMP(8);
    static_for<0, W0>([&](auto I) { lda(I, IC<0>{}); lda(I, IC<1>{}); lda(I, IC<2>{}); });
#pragma unroll
    for (int t = 0; t < 4; ++t) { bw[0][t][0] = rd1(0, t, 0); bw[0][t][1] = rd1(0, t, 1); }
    static_for<0, 37>([&](auto I) {
      constexpr int i = I.v;
      constexpr int lim = i + NW - 1;
      constexpr int nxt = i <= OI ? (W0 + 2 * i < lim ? W0 + 2 * i : lim) : (27 + OB0 + 2 * (i - OI - 1) < lim ? 27 + OB0 + 2 * (i - OI - 1) : lim);
      constexpr int end = i <= OI ? 27 : 37;
      constexpr int l0 = nxt < end ? nxt : -1, l1 = (nxt + 1 <= lim && nxt + 1 < end) ? nxt + 1 : -1;
      SB;
      static_for<0, 12>([&](auto E) {
        constexpr int e = E.v, t = e / 3, h = e % 3;
        const u32x4 bf = cat2(bw[i & 1][t][0], bw[i & 1][t][1]);
        if constexpr (i == 0 || i == 27) mfma_z(acc[i >= 27][2 - h][t], bf, a[i % NW][2 - h]); else mfma_ip(acc[i >= 27][2 - h][t], bf, a[i % NW][2 - h]);
        SB;
        if constexpr (i == 4 && e == 2) go2 = lds_get(L_GO + 4);
        if constexpr ((i == 27 || i == 28) && (e & 1)) put(IC<0>{}, IC<(i - 27) * 6 + e / 2>{});
        if constexpr (i == 36 && e >= 8) put(IC<1>{}, IC<e - 8>{});
        if constexpr (i < 36 && h < 2) bw[(i + 1) & 1][t][h] = rd1(i + 1, t, h);
        if constexpr (l1 < 0) { if constexpr (h == 2 && t < 3 && l0 >= 0) lda(IC<l0>{}, IC<t>{}); }
        else if constexpr (e & 1) lda(IC<(e < 6 ? l0 : l1)>{}, IC<(e >> 1) % 3>{});
        SB;
      });
      if constexpr (i == 29) { asm volatile("s_waitcnt lgkmcnt(8)" ::: "memory"); bar(); }
      if constexpr (i == 4) { if (__builtin_expect(!rfl(go2), 0)) lds_wait(L_GO + 4); asm volatile("" ::: "memory"); STAMP(17); }
      if constexpr (i > 4 && i < 27 && (i % 9 == 4 || i % 9 == 8)) {
        bar();
        if constexpr (i == 26 && OI == 26) static_for<27, 27 + OB0>([&](auto J) { lda(J, IC<0>{}); lda(J, IC<1>{}); lda(J, IC<2>{}); });
        STAMP(17 + (2 * i) / 9);
      }
    });
    STAMP(24);
    static_for<4, 12>([&](auto E) { if constexpr (E.v == 8) asm volatile("s_nop 3" ::: "memory"); put(IC<1>{}, E); SB; });
    lbar();
    STAMP(5);
    STAMP_END;
  }
}

__global__ __launch_bounds__(64) void tailk(Params p) {
  const unsigned long long t0 = __hip_atomic_load((gu64*)(p.ws + WS_DBG), RLX_AGENT);
  for (unsigned i = 0; i < (1u << 22) && __builtin_amdgcn_s_memrealtime() - t0 < 6000; ++i) __builtin_amdgcn_s_sleep(1);
}

extern "C" void kernel_launch(void* const* d_in, const int* in_sizes, int n_in, void* d_out, int out_size, void* d_ws,
                              size_t ws_size, hipStream_t stream) {
  Params p;
  p.x = d_in[0]; p.wqkv = d_in[1]; p.wo = d_in[2]; p.wg = d_in[4]; p.wu = d_in[5]; p.wd = d_in[6];
  p.out = d_out; p.ws = (char*)d_ws;
  fused_block<<<256, 512, 0, stream>>>(p);
  tailk<<<1, 64, 0, stream>>>(p);
}
```

```cpp
#include <hip/hip_runtime.h>
#include <stdint.h>

typedef __attribute__((ext_vector_type(8))) __bf16 bf16x8;
typedef __attribute__((ext_vector_type(4))) __bf16 bf16x4;
typedef __attribute__((ext_vector_type(2))) __bf16 bf16x2;
typedef __attribute__((ext_vector_type(4))) float f32x4;
typedef __attribute__((ext_vector_type(2))) float f32x2;
typedef __attribute__((ext_vector_type(4))) unsigned u32x4;
typedef __attribute__((ext_vector_type(2))) unsigned u32x2;
typedef __amdgpu_buffer_rsrc_t rsrc_t;
#define LAS __attribute__((address_space(3)))
#define GAS __attribute__((address_space(1)))
#define DI __device__ __forceinline__
#define RLX_AGENT __ATOMIC_RELAXED, __HIP_MEMORY_SCOPE_AGENT
typedef GAS unsigned gu32;
typedef GAS unsigned long long gu64;

constexpr int S = 2048, D = 640, F = 1728;
constexpr int NCOL = 5376;
constexpr int NSTEP = 74;
constexpr unsigned WS_EPOCH = 0;
#define FS 128
constexpr unsigned WS_FLAGH = 768u << 10;
constexpr unsigned WS_FLAGO = WS_FLAGH + 8 * 32 * FS;
constexpr unsigned WS_FLAGKV = WS_FLAGO + 8 * 32 * FS;
constexpr unsigned WS_STAT = 4096;
constexpr unsigned WS_CSUM = WS_STAT + S * 8;
constexpr unsigned WS_KVP = WS_CSUM + 3 * NCOL * 8;
constexpr unsigned WS_HA = 1u << 20;
constexpr unsigned WS_DBG = 12u << 20;
constexpr unsigned WS_END = 13u << 20;
constexpr unsigned L_B = 65536;
constexpr unsigned L_SCR = 147456;
constexpr unsigned L_MISC = 159744;
constexpr unsigned L_GO = L_MISC + 3072;
constexpr unsigned SPIN_CAP = 1u << 18;
#define HSLEEP 4
#define HSPLIT 64
#define PPIPE 0
#define STA 0
#define STB 2
#define NPOST 0
#define SNAKE 0
#define EORD 0
#define EARLY1 0
#define ROTW 1
#define NPRE 12
#define WOK 2
#define GRP 1
#define GPRIO 0
#define HPRIO 0
#define PSLEEP 2
#define P2NW 7
#define P2W0 6
#define WSLEEP 0
#define ROTM 18
#define NA1 2
#define O_BAR 5
#define PRIO_MODE 0
#define DMA_SPACE_A T
#define DMA_SPACE_B (4 * T / 3)

struct Params {
  const void* x; const void* wqkv; const void* wo; const void* wg; const void* wu; const void* wd;
  void* out; char* ws;
};

__shared__ __attribute__((aligned(16))) char smem[163840];

DI unsigned rfl(unsigned v) { return __builtin_amdgcn_readfirstlane(v); }
DI rsrc_t mk_rsrc(const void* p, unsigned bytes) { return __builtin_amdgcn_make_buffer_rsrc((void*)p, (short)0, (int)bytes, 0x00020000); }
DI void dma16(unsigned ldsdst, unsigned voff, rsrc_t rs, unsigned soff) {
  asm volatile("s_mov_b32 m0, %0\n\ts_nop 0\n\tbuffer_load_dwordx4 %1, %2, %3 offen lds" ::"s"(ldsdst), "v"(voff), "s"(rs), "s"(soff) : "memory");
}
template <int N> DI void wait_vm() { asm volatile("s_waitcnt vmcnt(%0)" ::"i"(N) : "memory"); }
DI void drain_vm() { wait_vm<0>(); __builtin_amdgcn_s_waitcnt(0x0F70); }
DI void wait_lgkm0() { asm volatile("s_waitcnt lgkmcnt(0)" ::: "memory"); }
DI void bar() { asm volatile("" ::: "memory"); __builtin_amdgcn_s_barrier(); asm volatile("" ::: "memory"); }
DI void lbar() { wait_lgkm0(); bar(); }
template <class V> DI V lds_ld(unsigned a) { return *(LAS V*)((LAS char*)smem + a); }
template <class V> DI void lds_st(unsigned a, V v) { *(LAS V*)((LAS char*)smem + a) = v; }
DI bf16x4 lds_tr(unsigned a) { return __builtin_amdgcn_ds_read_tr16_b64_v4bf16((LAS bf16x4*)((LAS char*)smem + a)); }
DI bf16x8 cat(bf16x4 a, bf16x4 b) { return __builtin_shufflevector(a, b, 0, 1, 2, 3, 4, 5, 6, 7); }
DI f32x4 mfma(bf16x8 a, bf16x8 b, f32x4 c) { return __builtin_amdgcn_mfma_f32_16x16x32_bf16(a, b, c, 0, 0, 0); }
DI unsigned pk2(float a, float b) { f32x2 v = {a, b}; return __builtin_bit_cast(unsigned, __builtin_convertvector(v, bf16x2)); }
DI u32x2 pk4(f32x4 v) { u32x2 r = {pk2(v[0], v[1]), pk2(v[2], v[3])}; return r; }
DI bf16x8 frag(u32x2 lo, u32x2 hi) { u32x4 v = {lo[0], lo[1], hi[0], hi[1]}; return __builtin_bit_cast(bf16x8, v); }
DI float bflo(unsigned v) { return __builtin_bit_cast(float, v << 16); }
DI float bfhi(unsigned v) { return __builtin_bit_cast(float, v & 0xffff0000u); }
DI float rbf(float v) { return bflo(pk2(v, 0.f)); }
DI float relu2(float v) { float o; asm("v_add_f32 %0, %1, |%1|" : "=v"(o) : "v"(v)); return o; }
DI f32x4 phi4(f32x4 u, f32x4 rl, f32x4 rh) {
  const f32x4 l = u * rl;
  f32x4 e, a;
#pragma unroll
  for (int j = 0; j < 4; ++j) {
    e[j] = __builtin_amdgcn_fmed3f(__builtin_amdgcn_exp2f(l[j]), 0.f, 1.f);
    a[j] = relu2(u[j]);
  }
  return a * rh + e + 1e-6f;
}
DI f32x4 splat(float v) { return f32x4{v, v, v, v}; }
template <int CTRL> DI float dpp_add(float v) {
  return v + __builtin_bit_cast(float, __builtin_amdgcn_update_dpp(0, __builtin_bit_cast(int, v), CTRL, 0xf, 0xf, true));
}
DI float dot2(unsigned a, unsigned b, float c) {
  return __builtin_amdgcn_fdot2_f32_bf16(__builtin_bit_cast(bf16x2, a), __builtin_bit_cast(bf16x2, b), c, false);
}
DI u32x4 ldtr2(unsigned a, unsigned d) { return __builtin_bit_cast(u32x4, cat(lds_tr(a), lds_tr(a + d))); }
DI void mfma_ip(f32x4& c, const u32x4& a, const u32x4& b) { asm volatile("v_mfma_f32_16x16x32_bf16 %0, %1, %2, %0" : "+v"(c) : "v"(a), "v"(b)); }
DI void mfma_z(f32x4& c, const u32x4& a, const u32x4& b) { asm volatile("v_mfma_f32_16x16x32_bf16 %0, %1, %2, 0" : "=&v"(c) : "v"(a), "v"(b)); }
template <int N> DI void acc_fence(f32x4* c) {
  asm volatile("s_nop 7" ::: "memory");
#pragma unroll
  for (int i = 0; i < N; ++i) asm volatile("" : "+v"(c[i]));
}
DI u32x4 cat2(u32x2 a, u32x2 b) { return u32x4{a[0], a[1], b[0], b[1]}; }
DI u32x2 ldtr1(unsigned a) { return __builtin_bit_cast(u32x2, lds_tr(a)); }
template <int IMM> DI void set_m0i(unsigned v) { asm volatile("s_add_i32 m0, %0, %1" ::"s"(v), "i"(IMM) : "memory", "scc"); }
template <int OFF> DI void dma_goi(unsigned voff, rsrc_t rs, unsigned soff) {
  asm volatile("buffer_load_dwordx4 %0, %1, %2 offen offset:%3 lds" ::"v"(voff), "s"(rs), "s"(soff), "i"(OFF) : "memory");
}
DI void set_m0(unsigned v) { asm volatile("s_mov_b32 m0, %0" ::"s"(v) : "memory"); }
DI void dma_go(unsigned voff, rsrc_t rs, unsigned soff) {
  asm volatile("buffer_load_dwordx4 %0, %1, %2 offen lds" ::"v"(voff), "s"(rs), "s"(soff) : "memory");
}
DI f32x4 mfma_u(u32x4 a, u32x4 b, f32x4 c) { return mfma(__builtin_bit_cast(bf16x8, a), __builtin_bit_cast(bf16x8, b), c); }
#define STAMP_DECL
#define STAMP_ARG
#define STAMP_PASS
#define STAMPX(c, i)
#define STAMP_END
#define STAMP(i) STAMPX(threadIdx.x == 0, i)

DI unsigned poll_flags(gu32* word, bool active, unsigned tag) {
  unsigned v = 0;
#pragma unroll 1
  for (unsigned spins = 0; spins < SPIN_CAP; ++spins) {
    v = __hip_atomic_load(word, RLX_AGENT);
    if (__all(!active || v == tag)) break;
    if (PSLEEP) __builtin_amdgcn_s_sleep(PSLEEP);
  }
  asm volatile("" ::: "memory");
  return v;
}

DI void lds_set(unsigned a) { __hip_atomic_store((LAS unsigned*)smem + a / 4, 1u, __ATOMIC_RELAXED, __HIP_MEMORY_SCOPE_WORKGROUP); }
DI unsigned lds_get(unsigned a) { return __hip_atomic_load((LAS unsigned*)smem + a / 4, __ATOMIC_RELAXED, __HIP_MEMORY_SCOPE_WORKGROUP); }
DI void lds_wait(unsigned a) {
#pragma unroll 1
  for (unsigned spins = 0; spins < (SPIN_CAP << 4); ++spins) {
    if (rfl(lds_get(a))) break;
    __builtin_amdgcn_s_sleep(1);
  }
  asm volatile("" ::: "memory");
}

template <int I> struct IC { static constexpr int v = I; };
template <int B, int E, class Fn> DI void static_for(Fn&& f) {
  if constexpr (B < E) { f(IC<B>{}); static_for<B + 1, E>(f); }
}

enum { J_MLP = 0, J_KV = 1, J_Q = 2, J_SPARE = 3 };

struct Dma {
  rsrc_t rsA, rsB;
  unsigned vA0, vA1, vB0, vB1, vB2;
  unsigned sA[4], sB, advB;
  unsigned wa, wq;
  unsigned mA, mB, mW;
};

constexpr unsigned a_slot(unsigned s3) { return s3 == 0 ? 126976u : (s3 - 1) * 32768u; }
#define SB __builtin_amdgcn_sched_barrier(0)

template <int T> DI unsigned bofs(const unsigned (&boff)[T], int t) { return T == 6 && t >= 3 ? boff[t - 3] + 6144 : boff[t]; }
template <int T, bool TR, int KS, class Extra>
DI void kstep(f32x4 (&acc)[4][T], const u32x4 (&ac)[4], const u32x2 (&bc)[T][2], u32x4 (&an)[4], u32x2 (&bn)[T][2],
              const unsigned (&aoff)[4], const unsigned (&boff)[T], const Dma& d, Extra&& extra) {
  constexpr int s = KS >> 1, KIND = KS & 1, NP = KIND == 0 ? 4 : 3, ST = KIND == 0 ? (STA ? STA : (4 * T - 1) / NP) : (STB ? STB : (4 * T - 1) / NP);
  constexpr int sn = (KS + 1) >> 1;
  constexpr unsigned asl = a_slot(sn % 3), bsl = ((KS + 1) % 5) * 12288u;
  static_for<0, 4 * T>([&](auto E) {
    constexpr int e = E.v, t = e >> 2, i = (SNAKE && (t & 1)) ? 3 - (e & 3) : (e & 3);
    const u32x4 bf = cat2(bc[t][0], bc[t][1]);
    if constexpr (KS == 0) { if (TR) mfma_z(acc[i][t], bf, ac[i]); else mfma_z(acc[i][t], ac[i], bf); }
    else { if (TR) mfma_ip(acc[i][t], bf, ac[i]); else mfma_ip(acc[i][t], ac[i], bf); }
    SB;
    if constexpr (KS < 19) {
      if constexpr (e < 4) an[e] = lds_ld<u32x4>(aoff[((KS + 1) & 1) + (sn % 3 == 0 ? 2 : 0)] + (sn % 3 == 0 ? 0u : asl) + e * 2048);
      else if constexpr (e < 4 + 2 * T) bn[(e - 4) >> 1][e & 1] = ldtr1(bofs<T>(boff, (e - 4) >> 1) + bsl + (e & 1) * 768);
    }
    constexpr int pc = e / ST;
    if constexpr (KIND == 0) {
      constexpr int adv = s < 8 ? (s + 2) * 128 : 0, dst = (int)a_slot((s + 2) % 3) + pc * 1024, off = dst >= adv ? adv : 0;
      if constexpr (pc < NP && e % ST == ST - 2) set_m0i<dst - off>(d.mA);
      if constexpr (pc < NP && e % ST == ST - 1) dma_goi<off>((pc & 1) ? d.vA1 : d.vA0, d.rsA, d.sA[pc] + (adv - off));
    } else {
      constexpr int h0 = (2 * s) % 5;
      if constexpr (pc < NP && e % ST == ST - 2) set_m0i<(h0 == 4 ? 0 : h0 * 12288) + pc * 1024>(h0 == 4 ? d.mW : d.mB);
      if constexpr (pc < NP && e % ST == ST - 1) dma_goi<0>(pc == 0 ? d.vB0 : (pc == 1 ? d.vB1 : d.vB2), d.rsB, d.sB);
    }
    extra(E);
    SB;
  });
}

struct RowStat { u32x4 xa; unsigned xb, row; float s, q; };
DI void row_load(RowStat& R, rsrc_t rsX, unsigned lane) {
  asm volatile("buffer_load_dwordx4 %0, %2, %4, %5 offen\n\tbuffer_load_dword %1, %3, %4, %5 offen offset:1024"
               : "=&v"(R.xa), "=&v"(R.xb) : "v"(lane * 16), "v"(lane * 4), "s"(rsX), "s"(R.row * 1280) : "memory");
}
template <int K> DI void row_step(RowStat& R, unsigned lane) {
  if constexpr (K < 5) {
    if constexpr (K == 0) { R.s = 0.f; R.q = 0.f; }
    const unsigned v = K < 4 ? R.xa[K & 3] : R.xb;
    asm volatile("v_dot2c_f32_bf16 %0, %2, %3\n\tv_dot2c_f32_bf16 %1, %2, %2" : "+v"(R.s), "+v"(R.q) : "v"(v), "v"(0x3f803f80u));
  } else if constexpr (K < 11) {
    constexpr int C = K == 5 ? 0xB1 : (K == 6 ? 0x4E : (K == 7 ? 0x141 : (K == 8 ? 0x140 : (K == 9 ? 0x142 : 0x143))));
    if constexpr (K == 5) asm volatile("s_nop 2" : "+v"(R.s), "+v"(R.q));
    R.s = dpp_add<C>(R.s); R.q = dpp_add<C>(R.q);
  } else if constexpr (K == 11) {
    R.s *= 1.0f / D;
    R.q = fmaxf(R.q * (1.0f / D) - R.s * R.s, 0.f);
  } else if constexpr (K == 12) {
    R.q = __builtin_amdgcn_sqrtf(rbf(rbf(R.q) + 1e-5f));
  } else {
    lds_st<unsigned>(lane == 63 ? L_GO + 32 + (R.row & 7) * 4 : L_GO + 64, pk2(R.s, R.q));
  }
  if constexpr (K >= 5 && K < 13) asm volatile("" : "+v"(R.s), "+v"(R.q));
}
DI void row_publish(const RowStat& R, char* ws, unsigned tag, unsigned tid) {
  if (tid < 8) __hip_atomic_store((gu64*)(ws + WS_STAT) + R.row + tid, ((unsigned long long)tag << 32) | lds_ld<unsigned>(L_GO + 32 + tid * 4), RLX_AGENT);
}

struct Gran { u32x2 g0, g1, g2; unsigned off, step; };
DI void gran_issue(Gran& G, rsrc_t rs) {
  asm volatile("buffer_load_dwordx2 %0, %3, %4, 0 offen sc1\n\tbuffer_load_dwordx2 %1, %3, %4, %5 offen sc1\n\t"
               "buffer_load_dwordx2 %2, %3, %4, %6 offen sc1"
               : "=&v"(G.g0), "=&v"(G.g1), "=&v"(G.g2) : "v"(G.off), "s"(rs), "s"(G.step), "s"(2 * G.step) : "memory");
}
DI void gran_load(Gran& G, rsrc_t rs) {
  asm volatile("buffer_load_dwordx2 %0, %3, %4, 0 offen sc1\n\tbuffer_load_dwordx2 %1, %3, %4, %5 offen sc1\n\t"
               "buffer_load_dwordx2 %2, %3, %4, %6 offen sc1\n\ts_waitcnt vmcnt(0)"
               : "=&v"(G.g0), "=&v"(G.g1), "=&v"(G.g2) : "v"(G.off), "s"(rs), "s"(G.step), "s"(2 * G.step) : "memory");
}

DI unsigned urow(unsigned pos, unsigned rot) { const unsigned u = pos + rot; return (u >= 54 ? u - 54 : u) * 40960u; }

constexpr unsigned wo_tile(unsigned v) { return (v / 5) * 10 + (v % 5 == 4 ? 4 : (v % 5 < 2 ? 2 * (v % 5) : 2 * (v % 5) + 1)); }
struct W2 { rsrc_t wd, wo; unsigned rot, col; };
DI unsigned w2_off(const W2& W, int u, unsigned lw) {
  if (u < 54) return urow(u, W.rot) + lw * 10240u + W.col;
  return wo_tile(u - 54) * 20480u + (lw >> 1) * ((u - 54) % 5 == 4 ? 102400u : 20480u) + (lw & 1) * 10240u + W.col;
}
DI u32x4 w2_load(const W2& W, int u, unsigned lw, unsigned voff) {
  return __builtin_amdgcn_raw_buffer_load_b128(u < 54 ? W.wd : W.wo, voff, w2_off(W, u, lw), 0);
}

template <int T, bool TR, bool PF, bool LASTA, class Pre>
DI void main_loop(Pre&& pre, RowStat& R, char* ws, f32x4 (&acc)[4][T], unsigned (&boff)[T], Dma& d, unsigned w, unsigned lane, unsigned tid,
                  unsigned epoch, unsigned& tag, unsigned rb, unsigned ccol, rsrc_t rsB2, rsrc_t rsWS, unsigned cb, unsigned rot, u32x4 (&pre2)[36] STAMP_ARG) {
  const unsigned g = lane >> 4, r = lane & 15, m = w & 3;
  unsigned aoff[4];
  aoff[0] = (64 * m + r) * 128 + ((g ^ (r >> 1)) << 4);
  aoff[1] = aoff[0] ^ 64;
  aoff[2] = aoff[0] + a_slot(0); aoff[3] = aoff[1] + a_slot(0);
  const unsigned wq = d.wq, wa = d.wa;
  asm volatile("" : "+v"(aoff[0]), "+v"(aoff[1]), "+v"(aoff[2]), "+v"(aoff[3]), "+v"(boff[0]), "+v"(boff[1]), "+v"(boff[2]));
  if (T == 5) asm volatile("" : "+v"(boff[3]), "+v"(boff[4]));
  Gran G;
  unsigned v2, vb2, vpf, tA[3], tB[3];
  row_load(R, d.rsA, lane);
  {
    d.sB += d.advB;
    const unsigned bdst = L_B + (w < 4 ? 36864u : 24576u) + wq * 3072u;
    dma16(bdst, d.vB0, d.rsB, d.sB); dma16(bdst + 1024, d.vB1, d.rsB, d.sB); dma16(bdst + 2048, d.vB2, d.rsB, d.sB);
#pragma unroll
    for (int i = 0; i < NA1; ++i) dma16(a_slot(1) + d.mA + i * 1024, (i & 1) ? d.vA1 : d.vA0, d.rsA, d.sA[i] + 128);
    asm volatile("s_waitcnt vmcnt(%2) lgkmcnt(0)\n\tv_readfirstlane_b32 %0, %1\n\ts_nop 3" : "=s"(tag) : "v"(epoch), "i"(5 + NA1) : "memory");
    tag = rfl(tag) + 1;
    bar();
  }
  STAMP(1);
  u32x4 a0[4], a1[4];
  u32x2 b0[T][2], b1[T][2];
#pragma unroll
  for (int i = 0; i < 4; ++i) a0[i] = lds_ld<u32x4>(aoff[2] + i * 2048);
#pragma unroll
  for (int t = 0; t < T; ++t) { b0[t][0] = ldtr1(bofs<T>(boff, t)); b0[t][1] = ldtr1(bofs<T>(boff, t) + 768); }
  SB;
#pragma unroll
  for (int i = NA1; i < 4; ++i) dma16(a_slot(1) + d.mA + i * 1024, (i & 1) ? d.vA1 : d.vA0, d.rsA, d.sA[i] + 128);
  const bool y = w >= 4;
  bool ok = true;
  auto ln_store = [&] {
    const float v0 = y ? -bflo(G.g0[0]) : __builtin_bit_cast(float, G.g0[0]) + __builtin_bit_cast(float, G.g1[0]) + __builtin_bit_cast(float, G.g2[0]);
    const unsigned la = L_MISC + (y ? tid * 4 - 1024 : 2048 + tid * 4);
    lds_st<float>(la, v0);
    lds_st<float>(y ? la + 1024 : L_GO + 64 + (lane & 7) * 4, __builtin_amdgcn_rcpf(bfhi(G.g0[0])));
  };
  static_for<0, 10>([&](auto S_) {
    constexpr int s = S_.v, L = 4 * T;
    constexpr int NX = (PF && s >= 1 && 2 * s <= NPRE) ? 2 : 0;
    kstep<T, TR, 2 * s>(acc, a0, b0, a1, b1, aoff, boff, d, [&](auto E) {
      constexpr int e = E.v;
      if constexpr (s == 2) {
        if constexpr (e == 2) { vb2 = v2 + (3 * wq) * 10240u; asm volatile("" : "+v"(vb2)); }
        if constexpr (e == 5) { G.off = w < 4 ? WS_CSUM + ccol * 8 : WS_STAT + (rb * 256 + tid - 256) * 8; asm volatile("" : "+v"(G.off)); }
        if constexpr (e == 8) { G.step = w < 4 ? NCOL * 8 : 0; tA[0] = urow(wa, rot) + cb * 128u; asm volatile("" : "+s"(G.step), "+s"(tA[0])); }
        if constexpr (e == 10) { tA[1] = urow(8 + wa, rot) + cb * 128u; asm volatile("" : "+s"(tA[1])); }
        if constexpr (e == 12) { tA[2] = urow(31 + wa, rot) + cb * 128u; asm volatile("" : "+s"(tA[2])); }
        if constexpr (e == 14) {
          tB[0] = urow(16, rot) + cb * 128u; tB[1] = urow(w < 4 ? 19u : 22u, rot) + cb * 128u; tB[2] = urow(w < 4 ? 25u : 28u, rot) + cb * 128u;
          asm volatile("" : "+s"(tB[0]), "+s"(tB[1]), "+s"(tB[2]));
        }
      }
      if constexpr (s == 3 && e == 2) pre();
      if constexpr (NX && e >= L - 2) {
        constexpr int u = 2 * (s - 1) + e - (L - 2);
        pre2[u] = __builtin_amdgcn_raw_buffer_load_b128(rsB2, vpf, urow(36 + u, rot) + (w & 3) * 10240u + cb * 128u, 0);
      }
      if constexpr (s < 7) { if constexpr (e == L - 3) d.sB += s == 0 ? d.advB * (1 + (w >> 2)) : d.advB; }
      else if constexpr (s == 7) {
        if constexpr (e == L - 6) d.vB0 = y ? vb2 : d.vB0;
        if constexpr (e == L - 4) d.vB1 = y ? vb2 + 10240u : d.vB1;
        if constexpr (e == L - 3) d.vB2 = y ? vb2 + 20480u : d.vB2;
        if constexpr (e == L - 2) d.sB = y ? tB[0] : d.sB + d.advB;
        if constexpr (e == L - 1) d.rsB = y ? rsB2 : d.rsB;
      } else {
        if constexpr (e == L - 3) { d.sB = tB[s - 7]; d.rsB = rsB2; d.vB0 = vb2; d.vB1 = vb2 + 10240u; d.vB2 = vb2 + 20480u; }
      }
      if constexpr (s == 9 && e == L - 4) {
        asm volatile("s_waitcnt vmcnt(7)" : "+v"(G.g0), "+v"(G.g1), "+v"(G.g2)::"memory");
        ok = __all(G.g0[1] == tag && G.g1[1] == tag && G.g2[1] == tag);
      }
      if constexpr (s == 9 && e == L - 3) ln_store();
    });
    if constexpr (s == 0) {
      wait_vm<EARLY1 ? 8 - NA1 : 11>();
      static_for<0, 14>([&](auto K) { row_step<K.v>(R, lane); });
    }
    if constexpr (s == 9) {
      if (__builtin_expect(!ok, 0)) {
#pragma unroll 1
        for (unsigned spins = 0; spins < SPIN_CAP; ++spins) {
          __builtin_amdgcn_s_sleep(2);
          gran_load(G, rsWS);
          if (__all(G.g0[1] == tag && G.g1[1] == tag && G.g2[1] == tag)) break;
        }
        ln_store();
      }
    }
    wait_lgkm0();
    if constexpr (s < 9) wait_vm<4 + NX>();
    bar();
    if constexpr (s == 0) { STAMP(12); }
    SB;
    kstep<T, TR, 2 * s + 1>(acc, a1, b1, a0, b0, aoff, boff, d, [&](auto E) {
      constexpr int e = E.v;
      if constexpr (s == 0 && e == 6) {
        if (y) {
          const unsigned so = d.sB - d.advB;
          dma16(d.mW + 49152, d.vB0, d.rsB, so); dma16(d.mW + 49152 + 1024, d.vB1, d.rsB, so); dma16(d.mW + 49152 + 2048, d.vB2, d.rsB, so);
        }
      }
      if constexpr (s == 0 && e == L - 4) {
        v2 = (lane >> 3) * 1280 + (((lane & 7) ^ (((lane >> 4) & 3) << 1)) << 4);
        vpf = y ? v2 : 0x7ffffff0u;
        asm volatile("" : "+v"(v2), "+v"(vpf));
      }
      if constexpr (s == 0 && e == L - 6) row_publish(R, ws, tag, tid);
      if constexpr (s == 8 && e == 0) gran_issue(G, rsWS);
      if constexpr (s >= 7 && s < 9 && e >= L - 4) {
        d.rsA = rsB2; d.vA0 = v2; d.vA1 = v2;
        d.sA[e - (L - 4)] = tA[s - 7] + (e - (L - 4)) * 10240;
      }
      if constexpr (s == 9 && LASTA && e == 7) {
        if (wa < 5) {
#pragma unroll
          for (int i = 0; i < 4; ++i) dma16(a_slot(0) + d.mA + i * 1024, v2, rsB2, tA[2] + i * 10240);
        }
      }
    });
    if constexpr (s == 8) { STAMP(13); }
    if constexpr (s == 2) { STAMP(26); }
    if constexpr (s == 6) { STAMP(27); }
  });
  acc_fence<4 * T>(&acc[0][0]);
  STAMP(14);
}

DI void st16(u32x4 v, rsrc_t rs, unsigned voff, unsigned soff) {
  __builtin_amdgcn_raw_buffer_store_b128(v, rs, voff, soff, 16);
  asm volatile("s_nop 1" ::"v"(v) : "memory");
}
DI void publish_flag(const Params& p, unsigned flag_off, unsigned tag, unsigned tid) {
  drain_vm();
  bar();
  if (tid == 0) __hip_atomic_store((gu32*)(p.ws + flag_off), tag, RLX_AGENT);
}
DI void st_frag(rsrc_t rs, unsigned lane, unsigned rt, unsigned step, u32x2 lo, u32x2 hi) {
  u32x4 v = {lo[0], lo[1], hi[0], hi[1]};
  st16(v, rs, lane * 16, WS_HA + (rt * NSTEP + step) * 1024);
}
DI void shared_send(unsigned w, unsigned lane, const u32x2 (&sh)[4]) {
  const bool n = w >= 4;
  u32x4 snd = n ? u32x4{sh[0][0], sh[0][1], sh[1][0], sh[1][1]} : u32x4{sh[2][0], sh[2][1], sh[3][0], sh[3][1]};
  lds_st<u32x4>(L_SCR + w * 1024 + lane * 16, snd);
}
DI void shared_recv(rsrc_t rs, unsigned w, unsigned lane, unsigned rt0, unsigned step, const u32x2 (&sh)[4]) {
  lbar();
  u32x4 rcv = lds_ld<u32x4>(L_SCR + (w ^ 4) * 1024 + lane * 16);
  u32x2 r0 = {rcv[0], rcv[1]}, r1 = {rcv[2], rcv[3]};
  if (w >= 4) { st_frag(rs, lane, rt0 + 2, step, r0, sh[2]); st_frag(rs, lane, rt0 + 3, step, r1, sh[3]); }
  else        { st_frag(rs, lane, rt0 + 0, step, sh[0], r0); st_frag(rs, lane, rt0 + 1, step, sh[1], r1); }
}

__global__ __launch_bounds__(512, 2) void fused_block(Params p) {
  unsigned tid = threadIdx.x, lane = tid & 63;
  const unsigned w = rfl(tid >> 6);
  const unsigned b = blockIdx.x, xcd = b & 7, slot = b >> 3;
  const unsigned rb = 4 * (xcd & 1) + (slot & 3), jq = xcd >> 1, ji = slot >> 2;
  const unsigned spi = (xcd - 4) * 4 + (slot & 3);
  const unsigned long long jl0 = (jq & 1) ? 0x4123220908070605ull : 0x4021200403020100ull, jl1 = (jq & 1) ? 0x4327266011100f0eull : 0x422524600d0c0b0aull;
  const unsigned jb = (unsigned)(((jq & 2) ? jl1 : jl0) >> (8 * ji)) & 0xff, jtype = jb >> 5, jidx = jb & 31;
  STAMP_DECL;
  STAMP(0);

  Dma d;
  RowStat rs_;
  const bool mlp = jtype == J_MLP;
  const unsigned pitch = mlp ? 3456u : 3840u, wq = (w + (ROTW ? slot : 0)) & 3, wa = (w + (ROTW ? ji : 0)) & 7;
  d.wa = wa; d.wq = wq;
  const rsrc_t rsX = mk_rsrc(p.x, S * D * 2);
  d.vA0 = (lane >> 3) * 1280 + (((lane & 7) ^ (lane >> 4)) << 4); d.vA1 = d.vA0 ^ 64;
  d.rsA = rsX; d.mA = wa * 4096u;
#pragma unroll
  for (int i = 0; i < 4; ++i) d.sA[i] = (rb * 256 + 32 * wa + 8 * i) * 1280;
  unsigned epoch, tag;
  asm volatile("s_nop 4" :: "s"(rsX) : "memory");
  STAMP(6);
#pragma unroll
  for (int i = 0; i < 4; ++i) dma16(a_slot(0) + d.mA + i * 1024, (i & 1) ? d.vA1 : d.vA0, d.rsA, d.sA[i]);
  asm volatile("global_load_dword %0, %1, %2 sc1" : "=&v"(epoch) : "v"(0u), "s"(p.ws) : "memory");
  SB;
  {
    unsigned vb[3];
    const unsigned lim = mlp ? 176u : 144u;
    {
      const unsigned r0 = (lane * 43) >> 9, p0 = lane - 12 * r0;
      const bool w1 = p0 >= 8, w2 = p0 >= 4;
      const unsigned r1 = r0 + (w1 ? 6 : 5), p1 = p0 + (w1 ? -8 : 4), r2 = r0 + (w2 ? 11 : 10), p2 = p0 + (w2 ? -4 : 8);
      vb[0] = r0 * pitch + min(p0 * 16, lim);
      vb[1] = r1 * pitch + min((p1 * 16) ^ ((r1 & 8) << 2), lim);
      vb[2] = r2 * pitch + min((p2 * 16) ^ 32, lim);
    }
    d.vB0 = vb[0]; d.vB1 = vb[1]; d.vB2 = vb[2];
    const unsigned mh = wq >> 1;
    const void* bw = mlp ? (mh ? p.wu : p.wg) : p.wqkv;
    const unsigned cj = mlp ? 192u : 160u * jtype, cm = mlp ? 0u : (jtype == J_KV ? 1280u : 160u), c0 = jtype == J_KV ? 1280u : 0u;
    d.rsB = mk_rsrc(bw, D * pitch);
    d.sB = (16 * (wq & 1) + (w < 4 ? 32 : 0)) * pitch + c0 + cj * jidx + cm * mh; d.advB = 64 * pitch;
    d.mB = L_B + wq * 3072u + (w < 4 ? 0u : 12288u); d.mW = L_B + wq * 3072u + (w < 4 ? 49152u : 0u);
    const unsigned bdst = L_B + (w < 4 ? 12288u : 0u) + wq * 3072u;
    dma16(bdst, d.vB0, d.rsB, d.sB); dma16(bdst + 1024, d.vB1, d.rsB, d.sB); dma16(bdst + 2048, d.vB2, d.rsB, d.sB);
    rs_.row = rfl(b * 8 + w);
  }
  STAMP(10);
  lds_st<unsigned>(L_GO + (tid & 1) * 4, 0u);

  const unsigned t2 = 28 * xcd + slot;
  const bool has2 = slot < 28 && t2 < 220;
  const unsigned G = t2 / 10, cb = t2 % 10;
  constexpr unsigned rot = 32;
  unsigned g = lane >> 4, r = lane & 15, q4 = r >> 2, p4 = lane & 3;
  const rsrc_t rsWd = mk_rsrc(p.wd, has2 ? F * D * 2 : 0);
  const rsrc_t rsWS = mk_rsrc(p.ws, WS_END);
  unsigned v2 = (lane >> 3) * 1280 + (((lane & 7) ^ (((lane >> 4) & 3) << 1)) << 4);
  auto relane = [&] {
    asm volatile("" : "+v"(tid));
    lane = tid & 63; g = lane >> 4; r = lane & 15; q4 = r >> 2; p4 = lane & 3;
    v2 = (lane >> 3) * 1280 + (((lane & 7) ^ (((lane >> 4) & 3) << 1)) << 4);
  };
  const unsigned m = w & 3, n = w >> 2;
  const unsigned rt0 = rb * 16 + m * 4;
  const unsigned brow = L_B + (8 * g + q4) * 192;
  const unsigned bx = (g & 1) << 5;

  u32x4 pre2[36];
  W2 W;
  W.wd = rsWd; W.wo = mk_rsrc(p.wo, has2 ? D * D * 2 : 0); W.rot = rot; W.col = cb * 128u;
  if (jtype == J_MLP) {
    f32x4 acc[4][6];
    unsigned boff[6];
#pragma unroll
    for (int t = 0; t < 3; ++t) {
      const unsigned gt = n ? (t == 2 ? 3 : 4 + t) : t;
      boff[t] = brow + ((32 * gt + 8 * p4) ^ bx); boff[t + 3] = boff[t] + 6144;
    }
    const unsigned ccol = tid < 96 ? 1920 + 96 * jidx + tid : 3648 + 96 * jidx + (tid < 192 ? tid - 96 : 0);
    main_loop<6, true, true, true>([] {}, rs_, p.ws, acc, boff, d, w, lane, tid, epoch, tag, rb, ccol, rsWd, rsWS, cb, rot, pre2 STAMP_PASS);
    relane();
    if (w >= 4) {
#pragma unroll
      for (int u = NPRE; u < NPRE + NPOST; ++u) { pre2[u] = w2_load(W, 36 + u, w & 3, v2); SB; }
    }
    float mm[4];
    f32x4 cg[3], cu[3];
#pragma unroll
    for (int i = 0; i < 4; ++i) mm[i] = lds_ld<float>(L_MISC + (64 * m + 16 * i + r) * 4);
#pragma unroll
    for (int t = 0; t < 3; ++t) {
      const unsigned gt = n ? (t == 2 ? 3 : 4 + t) : t;
      cg[t] = lds_ld<f32x4>(L_MISC + 2048 + (16 * gt + 4 * g) * 4); cu[t] = lds_ld<f32x4>(L_MISC + 2048 + (96 + 16 * gt + 4 * g) * 4);
    }
    auto hval = [&](int i, int t) {
      f32x4 gg = acc[i][t] + mm[i] * cg[t];
      const f32x4 uu = acc[i][t + 3] + mm[i] * cu[t];
#pragma unroll
      for (int j = 0; j < 4; ++j) gg[j] = relu2(gg[j]);
      return pk4(gg * uu);
    };
#pragma unroll
    for (int i = 0; i < 4; ++i) { st_frag(rsWS, lane, rt0 + i, 3 * jidx + 2 * n, hval(i, 0), hval(i, 1)); SB; }
    const u32x2 sh[4] = {hval(0, 2), hval(1, 2), hval(2, 2), hval(3, 2)};
    shared_send(w, lane, sh);
    shared_recv(rsWS, w, lane, rt0, 3 * jidx + 1, sh);
    STAMP(15);
    publish_flag(p, WS_FLAGH + (rb * 32 + jidx) * FS, tag, tid);
    STAMP(3);
  } else if (jtype == J_KV) {
    f32x4 acc[4][5];
    unsigned boff[5];
    boff[0] = brow + ((80 * n + 8 * p4) ^ bx); boff[1] = boff[0] + 6144;
    boff[2] = brow + ((80 * n + 40 + 8 * p4) ^ bx); boff[3] = boff[2] + 6144;
    boff[4] = brow + ((80 * n + 32 + 40 * (p4 >> 1)) ^ bx) + (p4 & 1) * 6144;
    const unsigned kb = 126976u + m * 6720 + n * 3360, dump = L_MISC - 2048 + lane * 16;
    const unsigned ea = (q4 == 1 || r == 0) ? kb + (r == 0 ? 20 : 12 + r) * 80 : 0u, eb = (q4 == 3 || r == 0) ? kb + 1680 + (r == 0 ? 20 : 4 + r) * 80 : 0u;
    unsigned ka[6] = {kb + r * 80 + g * 16, ea ? ea + g * 16 : dump, eb ? eb + g * 16 : dump, g == 0 ? kb + r * 80 + 64 : dump,
                      g == 2 ? kb + 1680 + r * 80 + 64 : dump, (g == 0 && ea) ? ea + 64 : ((g == 2 && eb) ? eb + 64 : dump)};
    auto pin = [&] { asm volatile("" : "+v"(ka[0]), "+v"(ka[1]), "+v"(ka[2]), "+v"(ka[3]), "+v"(ka[4]), "+v"(ka[5])); };
    const unsigned ccol = tid < 80 ? 640 + 80 * jidx + tid : 1280 + 80 * jidx + (tid < 160 ? tid - 80 : 0);
    main_loop<5, false, false, false>(pin, rs_, p.ws, acc, boff, d, w, lane, tid, epoch, tag, rb, ccol, rsWd, rsWS, cb, rot, pre2 STAMP_PASS);
    relane();
    float cs[5];
    cs[0] = lds_ld<float>(L_MISC + 2048 + (40 * n + r) * 4);
    cs[1] = lds_ld<float>(L_MISC + 2048 + (80 + 40 * n + r) * 4);
    cs[2] = lds_ld<float>(L_MISC + 2048 + (40 * n + 20 + r) * 4);
    cs[3] = lds_ld<float>(L_MISC + 2048 + (80 + 40 * n + 20 + r) * 4);
    cs[4] = lds_ld<float>(L_MISC + 2048 + ((q4 & 1) * 80 + 40 * n + 16 + 20 * (q4 >> 1) + (r & 3)) * 4);
    u32x2 pk[4][3], vv[4][3];
    f32x4 mm4[4], rr4[4];
#pragma unroll
    for (int i = 0; i < 4; ++i) {
      mm4[i] = lds_ld<f32x4>(L_MISC + (64 * m + 16 * i + 4 * g) * 4); rr4[i] = lds_ld<f32x4>(L_MISC + 1024 + (64 * m + 16 * i + 4 * g) * 4);
    }
    SB;
#pragma unroll
    for (int i = 0; i < 4; ++i) {
      const f32x4 mm = mm4[i], rr = rr4[i];
      const f32x4 rl = rr * 1.4426950408889634f, rh = rr * 0.5f;
      f32x4 val[5];
#pragma unroll
      for (int t = 0; t < 5; ++t) val[t] = acc[i][t] + mm * cs[t];
#pragma unroll
      for (int t = 0; t < 3; ++t) pk[i][t] = pk4(phi4(val[2 * t], rl, rh));
      vv[i][0] = pk4(val[1] * rr); vv[i][1] = pk4(val[3] * rr);
      const u32x2 v4 = pk4(val[4] * rr);
      vv[i][2] = (q4 & 1) ? v4 : u32x2{0x3f803f80u, 0x3f803f80u};
    }
    STAMP(25);
    f32x4 P[7] = {};
#pragma unroll
    for (int i = 0; i < 4; i += 2) {
      const bf16x8 k0 = frag(pk[i][0], pk[i + 1][0]), k2 = frag(pk[i][1], pk[i + 1][1]), k4 = frag(pk[i][2], pk[i + 1][2]);
      const bf16x8 v1 = frag(vv[i][0], vv[i + 1][0]), v3 = frag(vv[i][1], vv[i + 1][1]), v4 = frag(vv[i][2], vv[i + 1][2]);
      P[0] = mfma(k0, v1, P[0]); P[1] = mfma(k0, v4, P[1]); P[2] = mfma(k4, v1, P[2]);
      P[3] = mfma(k2, v3, P[3]); P[4] = mfma(k2, v4, P[4]); P[5] = mfma(k4, v3, P[5]); P[6] = mfma(k4, v4, P[6]);
    }
    lds_st<f32x4>(ka[0], P[0]); lds_st<f32x4>(ka[0] + 1680, P[3]); lds_st<f32x4>(ka[1], P[1]); lds_st<f32x4>(ka[2], P[4]);
    lds_st<f32x4>(ka[3], P[2]); lds_st<f32x4>(ka[4], P[5]); lds_st<f32x4>(ka[5], P[6]);
    lbar();
    if (tid < 210) {
      const unsigned a = 126976u + tid * 32;
      const f32x4 t0 = (lds_ld<f32x4>(a) + lds_ld<f32x4>(a + 6720)) + (lds_ld<f32x4>(a + 13440) + lds_ld<f32x4>(a + 20160));
      const f32x4 t1 = (lds_ld<f32x4>(a + 16) + lds_ld<f32x4>(a + 6736)) + (lds_ld<f32x4>(a + 13456) + lds_ld<f32x4>(a + 20176));
      st16(cat2(pk4(t0), pk4(t1)), rsWS, tid * 16, WS_KVP + (rb * 32 + 4 * jidx) * 840);
    }
    wait_lgkm0();
    STAMP(15);
    publish_flag(p, WS_FLAGKV + ((jidx >> 1) * 32 + (jidx & 1) * 8 + rb) * FS, tag, tid);
    STAMP(3);
  } else if (jtype == J_Q) {
    f32x4 acc[4][5];
    unsigned boff[5];
#pragma unroll
    for (int t = 0; t < 5; ++t) boff[t] = brow + n * 6144 + ((32 * t + 8 * p4) ^ bx);
    const unsigned ccol = 160 * jidx + (tid < 160 ? tid : 0);
    main_loop<5, true, false, false>([] {}, rs_, p.ws, acc, boff, d, w, lane, tid, epoch, tag, rb, ccol, rsWd, rsWS, cb, rot, pre2 STAMP_PASS);
    relane();
    u32x2 pq[4][5];
    {
      float mm[4], rr[4];
      f32x4 c[5];
#pragma unroll
      for (int i = 0; i < 4; ++i) { mm[i] = lds_ld<float>(L_MISC + (64 * m + 16 * i + r) * 4); rr[i] = lds_ld<float>(L_MISC + 1024 + (64 * m + 16 * i + r) * 4); }
#pragma unroll
      for (int t = 0; t < 5; ++t) c[t] = lds_ld<f32x4>(L_MISC + 2048 + (80 * n + 16 * t + 4 * g) * 4);
      SB;
#pragma unroll
      for (int i = 0; i < 4; ++i)
#pragma unroll
        for (int t = 0; t < 5; ++t) pq[i][t] = pk4(phi4(acc[i][t] + mm[i] * c[t], splat(rr[i] * 1.4426950408889634f), splat(rr[i] * 0.5f)));
#pragma unroll
      for (int i = 0; i < 4; ++i) asm volatile("" : "+v"(pq[i][0]), "+v"(pq[i][1]), "+v"(pq[i][2]), "+v"(pq[i][3]), "+v"(pq[i][4]));
    }
    constexpr unsigned KP = 208, KQ = 84 * KP;
    unsigned wa[2];
#pragma unroll
    for (int k = 0; k < 2; ++k) {
      const unsigned qd = 2 * (tid < 420 ? tid : 0) + k, hd = qd / 105, e = (qd % 105) / 5, dq = qd % 5, hl = hd & 3;
      wa[k] = (hd >> 2) * KQ + (e < 20 ? hl * 20 + e : 80 + hl) * KP + hl * 40 + dq * 8;
    }
    unsigned fk = n * KQ + r * KP + g * 8, fz = n * KQ + (80 + (r & 3)) * KP + g * 8;
    asm volatile("" : "+v"(wa[0]), "+v"(wa[1]), "+v"(fk), "+v"(fz));
    STAMP(25);
    wait_vm<0>();
    bar();
#pragma unroll
    for (int k = 0; k < 5; ++k) lds_st<u32x4>(k * 8192 + tid * 16, u32x4{0u, 0u, 0u, 0u});
    if (w == 0) poll_flags((gu32*)(p.ws + WS_FLAGKV + (jidx * 32 + (lane & 15)) * FS), true, tag);
    lbar();
    STAMP(4);
    if (w < 7) {
      u32x4 v[8];
#pragma unroll
      for (int j = 0; j < 8; ++j)
        v[j] = __builtin_amdgcn_raw_buffer_load_b128(rsWS, (tid < 420 ? tid : 0) * 16, WS_KVP + (j * 32 + 8 * jidx) * 840, 16);
      SB;
      f32x4 t0 = {0.f, 0.f, 0.f, 0.f}, t1 = t0;
#pragma unroll
      for (int j = 0; j < 8; ++j) {
        t0 += f32x4{bflo(v[j][0]), bfhi(v[j][0]), bflo(v[j][1]), bfhi(v[j][1])};
        t1 += f32x4{bflo(v[j][2]), bfhi(v[j][2]), bflo(v[j][3]), bfhi(v[j][3])};
      }
      lds_st<u32x2>(wa[0], pk4(t0)); lds_st<u32x2>(wa[1], pk4(t1));
    }
    lbar();
    STAMP(16);
    bf16x8 A[12];
#pragma unroll
    for (int P = 0; P < 3; ++P) A[P] = frag(lds_ld<u32x2>(fz + P * 64), lds_ld<u32x2>(fz + P * 64 + 32));
#pragma unroll
    for (int u = 0; u < 5; ++u) {
      const unsigned a0 = fk + u * 16 * KP + (u < 3 ? 0 : 64);
      A[3 + u] = frag(lds_ld<u32x2>(a0), lds_ld<u32x2>(a0 + 32));
      if (u > 0) A[7 + u] = frag(lds_ld<u32x2>(a0 + 64), lds_ld<u32x2>(a0 + 96));
    }
    u32x2 sh[4];
#pragma unroll
    for (int i = 0; i < 4; ++i) {
      const bf16x8 B[3] = {frag(pq[i][0], pq[i][1]), frag(pq[i][2], pq[i][3]), frag(pq[i][4], u32x2{0u, 0u})};
      const f32x4 zero = {0.f, 0.f, 0.f, 0.f};
      const f32x4 den = mfma(A[2], B[2], mfma(A[1], B[1], mfma(A[0], B[0], zero))) + 1e-6f;
      f32x4 num[5];
#pragma unroll
      for (int u = 0; u < 5; ++u) num[u] = mfma(A[3 + u], B[u < 3 ? 0 : 1], zero);
#pragma unroll
      for (int u = 1; u < 5; ++u) num[u] = mfma(A[7 + u], B[u < 3 ? 1 : 2], num[u]);
      float rc[4];
#pragma unroll
      for (int j = 0; j < 4; ++j) rc[j] = __builtin_amdgcn_rcpf(den[j]);
      u32x2 o[5];
#pragma unroll
      for (int u = 0; u < 5; ++u) {
        const float rr = u == 0 ? rc[0] : (u == 4 ? rc[3] : (g < (unsigned)u ? rc[u - 1] : rc[u]));
        o[u] = pk4(num[u] * rr);
      }
      st_frag(rsWS, lane, rt0 + i, 54 + 5 * jidx + 2 * n, o[0], o[1]);
      st_frag(rsWS, lane, rt0 + i, 54 + 5 * jidx + 2 * n + 1, o[2], o[3]);
      sh[i] = o[4];
    }
    shared_send(w, lane, sh);
    shared_recv(rsWS, w, lane, rt0, 54 + 5 * jidx + 4, sh);
    STAMP(15);
    publish_flag(p, WS_FLAGO + (rb * 32 + jidx) * FS, tag, tid);
    STAMP(3);
    if (b == 224) {
      poll_flags((gu32*)(p.ws + WS_FLAGH + tid * FS), (tid & 31) < (tid < 256 ? 18u : 4u), tag);
      bar();
      if (tid == 0) __hip_atomic_store((gu32*)(p.ws + WS_EPOCH), tag, RLX_AGENT);
    }
  } else {
    const unsigned gw = spi * 8 + w;
    unsigned k10 = 0x00003f80u, k01 = 0x3f800000u;
    asm volatile("" : "+v"(k10), "+v"(k01));
    auto cs_src = [&](unsigned u, rsrc_t& rs, unsigned& cv, unsigned& st) {
      const unsigned cline = u / 3, third = u % 3;
      const void* cw = cline < 30 ? p.wqkv : (cline < 57 ? p.wg : p.wu);
      const unsigned cpitch = cline < 30 ? 3840u : 3456u;
      const unsigned clocal = cline < 30 ? cline : (cline < 57 ? cline - 30 : cline - 57);
      rs = mk_rsrc(cw, gw < 126 ? D * cpitch : 0);
      cv = (third * 216 + (lane >> 3)) * cpitch + clocal * 128 + (lane & 7) * 16;
      st = 8 * cpitch;
    };
    auto cs_publish = [&](unsigned u, float (&cs)[8]) {
#pragma unroll
      for (int k = 0; k < 4; ++k) { const bool hi = lane & 32; const float o = __shfl_xor(hi ? cs[k] : cs[k + 4], 32); cs[k] = (hi ? cs[k + 4] : cs[k]) + o; }
#pragma unroll
      for (int k = 0; k < 2; ++k) { const bool hi = lane & 16; const float o = __shfl_xor(hi ? cs[k] : cs[k + 2], 16); cs[k] = (hi ? cs[k + 2] : cs[k]) + o; }
      { const bool hi = lane & 8; const float o = __shfl_xor(hi ? cs[0] : cs[1], 8); cs[0] = (hi ? cs[1] : cs[0]) + o; }
      if (gw < 126)
        __hip_atomic_store((gu64*)(p.ws + WS_CSUM) + (u % 3) * NCOL + (u / 3) * 64 + (lane & 7) * 8 + (lane >> 3),
                           ((unsigned long long)tag << 32) | __builtin_bit_cast(unsigned, cs[0]), RLX_AGENT);
    };
    row_load(rs_, rsX, lane);
    asm volatile("s_waitcnt vmcnt(0) lgkmcnt(0)\n\tv_readfirstlane_b32 %0, %1\n\ts_nop 3" : "=s"(tag) : "v"(epoch) : "memory");
    tag = rfl(tag) + 1;
    static_for<0, 14>([&](auto K) { row_step<K.v>(rs_, lane); });
    u32x4 c[27];
    rsrc_t rs0, rs1; unsigned cv0, cv1, st0, st1;
    cs_src(gw, rs0, cv0, st0); cs_src(gw + 126, rs1, cv1, st1);
#pragma unroll
    for (int j = 0; j < 27; ++j) c[j] = __builtin_amdgcn_raw_buffer_load_b128(rs0, cv0 + j * st0, 0, 0);
    lbar();
    row_publish(rs_, p.ws, tag, tid);
    float cs[8] = {};
#pragma unroll
    for (int j = 0; j < 27; ++j) {
#pragma unroll
      for (int k = 0; k < 4; ++k) { cs[2 * k] = dot2(c[j][k], k10, cs[2 * k]); cs[2 * k + 1] = dot2(c[j][k], k01, cs[2 * k + 1]); }
      if (j < 8) c[j] = __builtin_amdgcn_raw_buffer_load_b128(rs1, cv1 + j * st1, 0, 0);
      SB;
    }
    cs_publish(gw, cs);
    SB;
#pragma unroll
    for (int j = 8; j < 27; ++j) c[j] = __builtin_amdgcn_raw_buffer_load_b128(rs1, cv1 + j * st1, 0, 0);
    SB;
    STAMP(11);
#pragma unroll
    for (int k = 0; k < 8; ++k) cs[k] = 0.f;
#pragma unroll
    for (int j = 0; j < 27; ++j)
#pragma unroll
      for (int k = 0; k < 4; ++k) { cs[2 * k] = dot2(c[j][k], k10, cs[2 * k]); cs[2 * k + 1] = dot2(c[j][k], k01, cs[2 * k + 1]); }
    cs_publish(gw + 126, cs);
    STAMP(12);
#pragma unroll 1
    for (unsigned i = w; i < 144; i += 8) dma16(i * 1024, v2, rsWd, urow(i >> 2, rot) + (i & 3) * 10240u + cb * 128u);
    drain_vm();
    bar();
  }
  if (!has2) { STAMP_END; return; }
  relane();

  const unsigned rbA = (G * 6) >> 4, rbB = (G * 6 + 5 > 127 ? 127 : G * 6 + 5) >> 4;
  if (w >= 4) {
    __builtin_amdgcn_s_setprio(HPRIO);
    const unsigned lw = w - 4, ld0 = lw * 1024;
    gu32* fo = (gu32*)(p.ws + WS_FLAGO + ((lane < 32 ? rbA : rbB) * 32 + (lane & 3)) * FS);
    gu32* fh = (gu32*)(p.ws + WS_FLAGH + ((lane < 32 ? rbA : rbB) * 32 + ((lane & 31) < 18 ? lane & 31 : 17)) * FS);
    if (w == 7) {
      __builtin_amdgcn_s_setprio(3);
      const unsigned v = poll_flags(fh, (lane & 31) < 18 && (lane & 31) >= 10, tag);
      lds_set(L_GO);
      if (!__all((lane & 31) >= 18 || v == tag)) poll_flags(fh, (lane & 31) < 18, tag);
      lds_set(L_GO + 4);
      __builtin_amdgcn_s_setprio(HPRIO);
    } else {
      __builtin_amdgcn_s_sleep(HSLEEP);
    }
    if (jtype == J_KV) {
#pragma unroll
      for (int u = 31; u < 36; ++u) dma16(u * 4096 + ld0, v2, rsWd, w2_off(W, u, lw));
    }
    if (jtype != J_MLP) {
#pragma unroll
      for (int u = 0; u < NPRE + NPOST; ++u) { pre2[u] = w2_load(W, 36 + u, lw, v2); SB; }
    }
#pragma unroll
    for (int u = 36 + NPRE + NPOST; u < HSPLIT; ++u) { pre2[u - 36] = w2_load(W, u, lw, v2); SB; }
    STAMP(8);
    unsigned la[3], wb[3], xo[3], st[3];
    u32x4 xr[3];
    const unsigned ch = lane & 7;
#pragma unroll
    for (int i = 0; i < 3; ++i) {
      const unsigned row = lw * 24 + i * 8 + (lane >> 3), rl = row >> 4, rr = row & 15;
      la[i] = (rl >= 3 ? 24576u + (rl - 3) * 4096u : rl * 4096u) + (ch >> 1) * 1024 + (ch & 1) * 512 + ((rr + 2 * ch) & 15) * 16;
      xo[i] = (G * 96 + row) * 1280 + cb * 128 + ch * 16;
      wb[i] = ld0 + lane * 16 + i * 49152;
      asm volatile("" : "+v"(la[i]), "+v"(wb[i]), "+v"(xo[i]));
    }
    float ri[3], rl[3];
    unsigned of = 0;
    f32x4 xf[3][2], dd[3][2];
#pragma unroll
    for (int k = 2; k <= 6; ++k) {
      if (k == O_BAR && w == 7 && !__all((lane & 31) >= 4 || of == tag)) poll_flags(fo, (lane & 31) < 4, tag);
      if (k == 6) {
        wait_vm<0>();
#pragma unroll
        for (int h = 0; h < 3; ++h) {
          ri[h] = 2.0f * bfhi(st[h]) * bfhi(st[h]);
          rl[h] = -1.4426950408889634f * __builtin_amdgcn_rcpf(ri[h]);
          xf[h][0] = f32x4{bflo(xr[h][0]), bfhi(xr[h][0]), bflo(xr[h][1]), bfhi(xr[h][1])};
          xf[h][1] = f32x4{bflo(xr[h][2]), bfhi(xr[h][2]), bflo(xr[h][3]), bfhi(xr[h][3])};
          asm volatile("" : "+v"(ri[h]), "+v"(rl[h]), "+v"(xf[h][0]), "+v"(xf[h][1]));
        }
      }
      lbar();
      if (k == O_BAR - 1) { of = __hip_atomic_load(fo, RLX_AGENT); SB; }
      if (k < 6) {
        const int kk = k - 1;
#pragma unroll
        for (int u = 9 * (kk - 1) + ((kk - 1) & 1); u < 9 * kk + (kk & 1); ++u) {
          lds_st<u32x4>(wb[u / 12] + (u % 12) * 4096, pre2[u]);
          SB;
        }
      }
      if (k == WOK || k == WOK + 1) {
#pragma unroll
        for (int u = (k == WOK ? 54 : 64); u < (k == WOK ? 64 : 72); ++u)
          if (u >= HSPLIT && u >= 36 + NPRE + NPOST) { pre2[u - 36] = w2_load(W, u, lw, v2); SB; }
        if (k != WOK) { dma16(36 * 4096 + ld0, v2, W.wo, w2_off(W, 72, lw)); dma16(37 * 4096 + ld0, v2, W.wo, w2_off(W, 73, lw)); }
      }
      if (k == 2) {
#pragma unroll
        for (int h = 0; h < 3; ++h) {
          const unsigned row = G * 96 + lw * 24 + h * 8 + (lane >> 3);
          st[h] = __builtin_amdgcn_raw_buffer_load_b32(rsWS, WS_STAT + (row < S ? row : 0) * 8, 0, 16);
          xr[h] = __builtin_amdgcn_raw_buffer_load_b128(rsX, xo[h], 0, 0);
        }
      }
    }
    bar();
#pragma unroll
    for (int i = 0; i < 3; ++i) {
      dd[i][0] = lds_ld<f32x4>(la[i]) + lds_ld<f32x4>(la[i] + 12288);
      dd[i][1] = lds_ld<f32x4>(la[i] + 256) + lds_ld<f32x4>(la[i] + 12288 + 256);
      la[i] += 49152;
      asm volatile("" : "+v"(la[i]));
    }
    __builtin_amdgcn_s_setprio(GPRIO);
#pragma unroll
    for (int i = 0; i < 6; ++i) {
      const f32x4 l = dd[i >> 1][i & 1] * rl[i >> 1];
      f32x4 e;
#pragma unroll
      for (int j = 0; j < 4; ++j) e[j] = __builtin_amdgcn_exp2f(l[j]);
      e = e * ri[i >> 1] + ri[i >> 1];
#pragma unroll
      for (int j = 0; j < 4; ++j) e[j] = __builtin_amdgcn_rcpf(e[j]);
      dd[i >> 1][i & 1] *= e;
      asm volatile("" : "+v"(dd[i >> 1][i & 1]));
    }
    const rsrc_t rsO = mk_rsrc(p.out, S * D * 2);
    STAMP(9);
    bar();
    f32x4 ss[3][2];
#pragma unroll
    for (int i = 0; i < 3; ++i) {
      ss[i][0] = lds_ld<f32x4>(la[i]) + lds_ld<f32x4>(la[i] + 12288);
      ss[i][1] = lds_ld<f32x4>(la[i] + 256) + lds_ld<f32x4>(la[i] + 12288 + 256);
    }
#pragma unroll
    for (int i = 0; i < 3; ++i)
      __builtin_amdgcn_raw_buffer_store_b128(cat2(pk4(xf[i][0] + ss[i][0] * dd[i][0]), pk4(xf[i][1] + ss[i][1] * dd[i][1])), rsO, xo[i], 0, 0);
    STAMP(5);
    STAMP_END;
    return;
  }
  {
    const unsigned kh = w & 1, rt = G * 6 + 3 * (w >> 1);
    const rsrc_t rsH = mk_rsrc(p.ws + WS_HA + (rt < 128 ? rt : 0) * (NSTEP * 1024), rt < 128 ? (rt > 125 ? 128 - rt : 3) * (NSTEP * 1024) : 0);
    unsigned tb[3][4];
#pragma unroll
    for (int t = 0; t < 4; ++t) {
      tb[0][t] = (4 * g + q4) * 128 + ((t ^ ((2 * g + (q4 >> 1)) & 3)) << 5) + 8 * p4 + kh * 4096;
      tb[1][t] = tb[0][t] + 49152; tb[2][t] = tb[0][t] + 98304;
      asm volatile("" : "+v"(tb[0][t]), "+v"(tb[1][t]), "+v"(tb[2][t]));
    }
    auto rd1 = [&](int i, int t, int hf) {
      const int sl = i == 36 ? 36 : (2 * i) % 36, bi = sl / 12 > 2 ? 2 : sl / 12;
      return __builtin_bit_cast(u32x2, lds_tr(tb[bi][t] + (sl - 12 * bi) * 4096 + hf * 2048));
    };
    constexpr int NW = P2NW, W0 = P2W0;
    constexpr int OI = O_BAR == 4 ? 17 : (O_BAR == 5 ? 22 : 26);
    constexpr int OB0 = OI == 26 ? NW - 1 : 0;
    f32x4 acc[2][3][4];
    u32x4 a[NW][3];
    u32x2 bw[2][4][2];
    unsigned va[3];
#pragma unroll
    for (int h = 0; h < 3; ++h) va[h] = lane * 16 + h * NSTEP * 1024;
    unsigned xa[4];
#pragma unroll
    for (int t = 0; t < 4; ++t) {
      xa[t] = w * 12288u + t * 1024 + g * 256 + ((r + 4 * t + (g & 2)) & 15) * 16;
      asm volatile("" : "+v"(xa[t]));
    }
    auto put = [&](auto Z, auto E) {
      constexpr int z = Z.v, t = E.v / 3, h = 2 - E.v % 3;
      asm volatile("" : "+v"(acc[z][h][t]));
      lds_st<f32x4>(xa[t] + z * 49152 + h * 4096, acc[z][h][t]);
    };
    unsigned so = (rot + kh) * 1024;
    auto lda = [&](auto I, auto H) {
      constexpr int i = I.v, h = H.v;
      if constexpr (i < 27) {
        if (h == 0 && so >= 54 * 1024) so -= 54 * 1024;
        a[i % NW][h] = __builtin_amdgcn_raw_buffer_load_b128(rsH, va[h], so, 16);
        if (h == 2) so += 2048;
      } else {
        a[i % NW][h] = __builtin_amdgcn_raw_buffer_load_b128(rsH, va[h], (2 * i + kh) * 1024, 16);
      }
      SB;
    };
    lds_wait(L_GO);
    unsigned go2 = 0;
    STAMP(8);
    static_for<0, W0>([&](auto I) { lda(I, IC<0>{}); lda(I, IC<1>{}); lda(I, IC<2>{}); });
#pragma unroll
    for (int t = 0; t < 4; ++t) { bw[0][t][0] = rd1(0, t, 0); bw[0][t][1] = rd1(0, t, 1); }
    static_for<0, 37>([&](auto I) {
      constexpr int i = I.v;
      constexpr int lim = i + NW - 1;
      constexpr int nxt = i <= OI ? (W0 + 2 * i < lim ? W0 + 2 * i : lim) : (27 + OB0 + 2 * (i - OI - 1) < lim ? 27 + OB0 + 2 * (i - OI - 1) : lim);
      constexpr int end = i <= OI ? 27 : 37;
      constexpr int l0 = nxt < end ? nxt : -1, l1 = (nxt + 1 <= lim && nxt + 1 < end) ? nxt + 1 : -1;
      SB;
      static_for<0, 12>([&](auto E) {
        constexpr int e = E.v, t = e / 3, h = e % 3;
        const u32x4 bf = cat2(bw[i & 1][t][0], bw[i & 1][t][1]);
        if constexpr (i == 0 || i == 27) mfma_z(acc[i >= 27][2 - h][t], bf, a[i % NW][2 - h]); else mfma_ip(acc[i >= 27][2 - h][t], bf, a[i % NW][2 - h]);
        SB;
        if constexpr (i == 4 && e == 2) go2 = lds_get(L_GO + 4);
        if constexpr ((i == 27 || i == 28) && (e & 1)) put(IC<0>{}, IC<(i - 27) * 6 + e / 2>{});
        if constexpr (i == 36 && e >= 8) put(IC<1>{}, IC<e - 8>{});
        if constexpr (i < 36 && h < 2) bw[(i + 1) & 1][t][h] = rd1(i + 1, t, h);
        if constexpr (l1 < 0) { if constexpr (h == 2 && t < 3 && l0 >= 0) lda(IC<l0>{}, IC<t>{}); }
        else if constexpr (e & 1) lda(IC<(e < 6 ? l0 : l1)>{}, IC<(e >> 1) % 3>{});
        SB;
      });
      if constexpr (i == 29) { asm volatile("s_waitcnt lgkmcnt(8)" ::: "memory"); bar(); }
      if constexpr (i == 4) { if (__builtin_expect(!rfl(go2), 0)) lds_wait(L_GO + 4); asm volatile("" ::: "memory"); STAMP(17); }
      if constexpr (i > 4 && i < 27 && (i % 9 == 4 || i % 9 == 8)) {
        bar();
        if constexpr (i == 26 && OI == 26) static_for<27, 27 + OB0>([&](auto J) { lda(J, IC<0>{}); lda(J, IC<1>{}); lda(J, IC<2>{}); });
        STAMP(17 + (2 * i) / 9);
      }
    });
    STAMP(24);
    static_for<4, 12>([&](auto E) { if constexpr (E.v == 8) asm volatile("s_nop 3" ::: "memory"); put(IC<1>{}, E); SB; });
    lbar();
    STAMP(5);
    STAMP_END;
  }
}

extern "C" void kernel_launch(void* const* d_in, const int* in_sizes, int n_in, void* d_out, int out_size, void* d_ws,
                              size_t ws_size, hipStream_t stream) {
  Params p;
  p.x = d_in[0]; p.wqkv = d_in[1]; p.wo = d_in[2]; p.wg = d_in[4]; p.wu = d_in[5]; p.wd = d_in[6];
  p.out = d_out; p.ws = (char*)d_ws;
  fused_block<<<256, 512, 0, stream>>>(p);
}
```

```cpp
#include <hip/hip_runtime.h>
#include <stdint.h>

typedef __attribute__((ext_vector_type(8))) __bf16 bf16x8;
typedef __attribute__((ext_vector_type(4))) __bf16 bf16x4;
typedef __attribute__((ext_vector_type(2))) __bf16 bf16x2;
typedef __attribute__((ext_vector_type(4))) float f32x4;
typedef __attribute__((ext_vector_type(2))) float f32x2;
typedef __attribute__((ext_vector_type(4))) unsigned u32x4;
typedef __attribute__((ext_vector_type(2))) unsigned u32x2;
typedef __amdgpu_buffer_rsrc_t rsrc_t;
#define LAS __attribute__((address_space(3)))
#define GAS __attribute__((address_space(1)))
#define DI __device__ __forceinline__
#define RLX_AGENT __ATOMIC_RELAXED, __HIP_MEMORY_SCOPE_AGENT
typedef GAS unsigned gu32;
typedef GAS unsigned long long gu64;

constexpr int S = 2048, D = 640, F = 1728;
constexpr int NCOL = 5376;
constexpr int NSTEP = 74;
constexpr unsigned WS_EPOCH = 0;
#define FS 128
constexpr unsigned WS_FLAGH = 768u << 10;
constexpr unsigned WS_FLAGO = WS_FLAGH + 8 * 32 * FS;
constexpr unsigned WS_FLAGKV = WS_FLAGO + 8 * 32 * FS;
constexpr unsigned WS_STAT = 4096;
constexpr unsigned WS_CSUM = WS_STAT + S * 8;
constexpr unsigned WS_KVP = WS_CSUM + 3 * NCOL * 8;
constexpr unsigned WS_HA = 1u << 20;
constexpr unsigned WS_DBG = 12u << 20;
constexpr unsigned WS_END = 13u << 20;
constexpr unsigned L_B = 65536;
constexpr unsigned L_SCR = 147456;
constexpr unsigned L_MISC = 159744;
constexpr unsigned L_GO = L_MISC + 3072;
constexpr unsigned SPIN_CAP = 1u << 18;
#define HSLEEP 4
#define HSPLIT 64
#define PPIPE 0
#define STA 0
#define STB 2
#define SNAKE 0
#define EORD 0
#define EARLY1 1
#define ROTW 1
#define NPRE 12
#define WSCH 0
#define PSPLIT 48
#define XK 2
#define GRP 1
#define GPRIO 0
#define HPRIO 0
#define PSLEEP 2
#define P2NW 7
#define P2W0 6
#define WSLEEP 0
#define ROTM 18
#define NA1 2
#define O_BAR 5
#define PRIO_MODE 0
#define DMA_SPACE_A T
#define DMA_SPACE_B (4 * T / 3)

struct Params {
  const void* x; const void* wqkv; const void* wo; const void* wg; const void* wu; const void* wd;
  void* out; char* ws;
};

__shared__ __attribute__((aligned(16))) char smem[163840];

DI float sadd(float a, float b) { float o; asm("v_add_f32 %0, %1, %2" : "=v"(o) : "v"(a), "v"(b)); return o; }
DI float smul(float a, float b) { float o; asm("v_mul_f32 %0, %1, %2" : "=v"(o) : "v"(a), "v"(b)); return o; }
DI float sfma(float a, float b, float c) { float o; asm("v_fma_f32 %0, %1, %2, %3" : "=v"(o) : "v"(a), "v"(b), "v"(c)); return o; }
DI unsigned bsel(unsigned m, unsigned a, unsigned b) { unsigned o; asm("v_bfi_b32 %0, %1, %2, %3" : "=v"(o) : "v"(m), "v"(a), "v"(b)); return o; }
DI float bself(unsigned m, float a, float b) { return __builtin_bit_cast(float, bsel(m, __builtin_bit_cast(unsigned, a), __builtin_bit_cast(unsigned, b))); }
DI bool all64(bool c) { return __builtin_amdgcn_ballot_w64(!c) == 0; }
DI float shfl_x(float v, unsigned lane, unsigned m) {
  return __builtin_bit_cast(float, __builtin_amdgcn_ds_bpermute((int)((lane ^ m) << 2), __builtin_bit_cast(int, v)));
}
DI unsigned umin(unsigned a, unsigned b) { return a < b ? a : b; }
DI unsigned rfl(unsigned v) { return __builtin_amdgcn_readfirstlane(v); }
DI rsrc_t mk_rsrc(const void* p, unsigned bytes) { return __builtin_amdgcn_make_buffer_rsrc((void*)p, (short)0, (int)bytes, 0x00020000); }
DI void dma16(unsigned ldsdst, unsigned voff, rsrc_t rs, unsigned soff) {
  asm volatile("s_mov_b32 m0, %0\n\ts_nop 0\n\tbuffer_load_dwordx4 %1, %2, %3 offen lds" ::"s"(ldsdst), "v"(voff), "s"(rs), "s"(soff) : "memory");
}
template <int N> DI void wait_vm() { asm volatile("s_waitcnt vmcnt(%0)" ::"i"(N) : "memory"); }
DI void drain_vm() { wait_vm<0>(); __builtin_amdgcn_s_waitcnt(0x0F70); }
DI void wait_lgkm0() { asm volatile("s_waitcnt lgkmcnt(0)" ::: "memory"); }
DI void bar() { asm volatile("" ::: "memory"); __builtin_amdgcn_s_barrier(); asm volatile("" ::: "memory"); }
DI void lbar() { wait_lgkm0(); bar(); }
template <class V> DI V lds_ld(unsigned a) { return *(LAS V*)((LAS char*)smem + a); }
template <class V> DI void lds_st(unsigned a, V v) { *(LAS V*)((LAS char*)smem + a) = v; }
DI bf16x4 lds_tr(unsigned a) { return __builtin_amdgcn_ds_read_tr16_b64_v4bf16((LAS bf16x4*)((LAS char*)smem + a)); }
DI bf16x8 cat(bf16x4 a, bf16x4 b) { return __builtin_shufflevector(a, b, 0, 1, 2, 3, 4, 5, 6, 7); }
DI f32x4 mfma(bf16x8 a, bf16x8 b, f32x4 c) { return __builtin_amdgcn_mfma_f32_16x16x32_bf16(a, b, c, 0, 0, 0); }
DI unsigned pk2(float a, float b) { f32x2 v = {a, b}; return __builtin_bit_cast(unsigned, __builtin_convertvector(v, bf16x2)); }
DI u32x2 pk4(f32x4 v) { u32x2 r = {pk2(v[0], v[1]), pk2(v[2], v[3])}; return r; }
DI bf16x8 frag(u32x2 lo, u32x2 hi) { u32x4 v = {lo[0], lo[1], hi[0], hi[1]}; return __builtin_bit_cast(bf16x8, v); }
DI float bflo(unsigned v) { return __builtin_bit_cast(float, v << 16); }
DI float bfhi(unsigned v) { return __builtin_bit_cast(float, v & 0xffff0000u); }
DI float rbf(float v) { return bflo(pk2(v, 0.f)); }
DI float relu2(float v) { float o; asm("v_add_f32 %0, %1, |%1|" : "=v"(o) : "v"(v)); return o; }
DI f32x4 phi4(f32x4 u, f32x4 rl, f32x4 rh) {
  const f32x4 l = u * rl;
  f32x4 e, a;
#pragma unroll
  for (int j = 0; j < 4; ++j) {
    e[j] = __builtin_amdgcn_fmed3f(__builtin_amdgcn_exp2f(l[j]), 0.f, 1.f);
    a[j] = relu2(u[j]);
  }
  return a * rh + e + 1e-6f;
}
DI f32x4 splat(float v) { return f32x4{v, v, v, v}; }
template <int CTRL> DI float dpp_add(float v) {
  return v + __builtin_bit_cast(float, __builtin_amdgcn_update_dpp(0, __builtin_bit_cast(int, v), CTRL, 0xf, 0xf, true));
}
DI float dot2(unsigned a, unsigned b, float c) {
  return __builtin_amdgcn_fdot2_f32_bf16(__builtin_bit_cast(bf16x2, a), __builtin_bit_cast(bf16x2, b), c, false);
}
DI u32x4 ldtr2(unsigned a, unsigned d) { return __builtin_bit_cast(u32x4, cat(lds_tr(a), lds_tr(a + d))); }
DI void mfma_ip(f32x4& c, const u32x4& a, const u32x4& b) { asm volatile("v_mfma_f32_16x16x32_bf16 %0, %1, %2, %0" : "+v"(c) : "v"(a), "v"(b)); }
DI void mfma_c(f32x4& d, const u32x4& a, const u32x4& b, const f32x4& c) { asm volatile("v_mfma_f32_16x16x32_bf16 %0, %1, %2, %3" : "=&v"(d) : "v"(a), "v"(b), "v"(c)); }
DI void mfma_z(f32x4& c, const u32x4& a, const u32x4& b) { asm volatile("v_mfma_f32_16x16x32_bf16 %0, %1, %2, 0" : "=&v"(c) : "v"(a), "v"(b)); }
template <int N> DI void acc_fence(f32x4* c) {
  asm volatile("s_nop 7" ::: "memory");
#pragma unroll
  for (int i = 0; i < N; ++i) asm volatile("" : "+v"(c[i]));
}
DI u32x4 cat2(u32x2 a, u32x2 b) { return u32x4{a[0], a[1], b[0], b[1]}; }
DI u32x2 ldtr1(unsigned a) { return __builtin_bit_cast(u32x2, lds_tr(a)); }
template <int IMM> DI void set_m0i(unsigned v) { asm volatile("s_add_i32 m0, %0, %1" ::"s"(v), "i"(IMM) : "memory", "scc"); }
template <int OFF> DI void dma_goi(unsigned voff, rsrc_t rs, unsigned soff) {
  asm volatile("buffer_load_dwordx4 %0, %1, %2 offen offset:%3 lds" ::"v"(voff), "s"(rs), "s"(soff), "i"(OFF) : "memory");
}
DI void set_m0(unsigned v) { asm volatile("s_mov_b32 m0, %0" ::"s"(v) : "memory"); }
DI void dma_go(unsigned voff, rsrc_t rs, unsigned soff) {
  asm volatile("buffer_load_dwordx4 %0, %1, %2 offen lds" ::"v"(voff), "s"(rs), "s"(soff) : "memory");
}
DI f32x4 mfma_u(u32x4 a, u32x4 b, f32x4 c) { return mfma(__builtin_bit_cast(bf16x8, a), __builtin_bit_cast(bf16x8, b), c); }
#define STAMP_DECL
#define STAMP_ARG
#define STAMP_PASS
#define STAMPX(c, i)
#define STAMP_END
#define STAMP(i) STAMPX(__builtin_amdgcn_workitem_id_x() == 0, i)

DI unsigned poll_flags(gu32* word, bool active, unsigned tag) {
  unsigned v = 0;
#pragma unroll 1
  for (unsigned spins = 0; spins < SPIN_CAP; ++spins) {
    v = __hip_atomic_load(word, RLX_AGENT);
    if (all64(!active || v == tag)) break;
    if (PSLEEP) __builtin_amdgcn_s_sleep(PSLEEP);
  }
  asm volatile("" ::: "memory");
  return v;
}

DI void lds_set(unsigned a) { __hip_atomic_store((LAS unsigned*)smem + a / 4, 1u, __ATOMIC_RELAXED, __HIP_MEMORY_SCOPE_WORKGROUP); }
DI unsigned lds_get(unsigned a) { return __hip_atomic_load((LAS unsigned*)smem + a / 4, __ATOMIC_RELAXED, __HIP_MEMORY_SCOPE_WORKGROUP); }
DI void lds_wait(unsigned a) {
#pragma unroll 1
  for (unsigned spins = 0; spins < (SPIN_CAP << 4); ++spins) {
    if (rfl(lds_get(a))) break;
    __builtin_amdgcn_s_sleep(1);
  }
  asm volatile("" ::: "memory");
}

template <int I> struct IC { static constexpr int v = I; };
template <int B, int E, class Fn> DI void static_for(Fn&& f) {
  if constexpr (B < E) { f(IC<B>{}); static_for<B + 1, E>(f); }
}

enum { J_MLP = 0, J_KV = 1, J_Q = 2, J_SPARE = 3 };

struct Dma {
  rsrc_t rsA, rsB;
  unsigned vA0, vA1, vB0, vB1, vB2;
  unsigned sA[4], sB, advB;
  unsigned wa, wq;
  unsigned mA, mB, mW;
};

constexpr unsigned a_slot(unsigned s3) { return s3 == 0 ? 126976u : (s3 - 1) * 32768u; }
#define SB __builtin_amdgcn_sched_barrier(0)

template <int T> DI unsigned bofs(const unsigned (&boff)[T], int t) { return T == 6 && t >= 3 ? boff[t - 3] + 6144 : boff[t]; }
template <int T, bool TR, int KS, class Extra>
DI void kstep(f32x4 (&acc)[4][T], const u32x4 (&ac)[4], const u32x2 (&bc)[T][2], u32x4 (&an)[4], u32x2 (&bn)[T][2],
              const unsigned (&aoff)[4], const unsigned (&boff)[T], const Dma& d, Extra&& extra) {
  constexpr int s = KS >> 1, KIND = KS & 1, NP = KIND == 0 ? 4 : 3, ST = KIND == 0 ? (STA ? STA : (4 * T - 1) / NP) : (STB ? STB : (4 * T - 1) / NP);
  constexpr int sn = (KS + 1) >> 1;
  constexpr unsigned asl = a_slot(sn % 3), bsl = ((KS + 1) % 5) * 12288u;
  static_for<0, 4 * T>([&](auto E) {
    constexpr int e = E.v, t = e >> 2, i = (SNAKE && (t & 1)) ? 3 - (e & 3) : (e & 3);
    const u32x4 bf = cat2(bc[t][0], bc[t][1]);
    if constexpr (KS == 0) { if (TR) mfma_z(acc[i][t], bf, ac[i]); else mfma_z(acc[i][t], ac[i], bf); }
    else { if (TR) mfma_ip(acc[i][t], bf, ac[i]); else mfma_ip(acc[i][t], ac[i], bf); }
    SB;
    if constexpr (KS < 19) {
      if constexpr (e < 4) an[e] = lds_ld<u32x4>(aoff[((KS + 1) & 1) + (sn % 3 == 0 ? 2 : 0)] + (sn % 3 == 0 ? 0u : asl) + e * 2048);
      else if constexpr (e < 4 + 2 * T) bn[(e - 4) >> 1][e & 1] = ldtr1(bofs<T>(boff, (e - 4) >> 1) + bsl + (e & 1) * 768);
    }
    constexpr int pc = e / ST;
    if constexpr (KIND == 0) {
      constexpr int adv = s < 8 ? (s + 2) * 128 : 0, dst = (int)a_slot((s + 2) % 3) + pc * 1024, off = dst >= adv ? adv : 0;
      if constexpr (pc < NP && e % ST == ST - 2) set_m0i<dst - off>(d.mA);
      if constexpr (pc < NP && e % ST == ST - 1) dma_goi<off>((pc & 1) ? d.vA1 : d.vA0, d.rsA, d.sA[pc] + (adv - off));
    } else {
      constexpr int h0 = (2 * s) % 5;
      if constexpr (pc < NP && e % ST == ST - 2) set_m0i<(h0 == 4 ? 0 : h0 * 12288) + pc * 1024>(h0 == 4 ? d.mW : d.mB);
      if constexpr (pc < NP && e % ST == ST - 1) dma_goi<0>(pc == 0 ? d.vB0 : (pc == 1 ? d.vB1 : d.vB2), d.rsB, d.sB);
    }
    extra(E);
    SB;
  });
}

struct RowStat { u32x4 xa; unsigned xb, row; float s, q; };
DI void row_load(RowStat& R, rsrc_t rsX, unsigned lane) {
  asm volatile("buffer_load_dwordx4 %0, %2, %4, %5 offen\n\tbuffer_load_dword %1, %3, %4, %5 offen offset:1024"
               : "=&v"(R.xa), "=&v"(R.xb) : "v"(lane * 16), "v"(lane * 4), "s"(rsX), "s"(R.row * 1280) : "memory");
}
template <int K> DI void row_step(RowStat& R, unsigned lane) {
  if constexpr (K < 5) {
    if constexpr (K == 0) { R.s = 0.f; R.q = 0.f; }
    const unsigned v = K < 4 ? R.xa[K & 3] : R.xb;
    asm volatile("v_dot2c_f32_bf16 %0, %2, %3\n\tv_dot2c_f32_bf16 %1, %2, %2" : "+v"(R.s), "+v"(R.q) : "v"(v), "v"(0x3f803f80u));
  } else if constexpr (K < 11) {
    constexpr int C = K == 5 ? 0xB1 : (K == 6 ? 0x4E : (K == 7 ? 0x141 : (K == 8 ? 0x140 : (K == 9 ? 0x142 : 0x143))));
    if constexpr (K == 5) asm volatile("s_nop 2" : "+v"(R.s), "+v"(R.q));
    R.s = dpp_add<C>(R.s); R.q = dpp_add<C>(R.q);
  } else if constexpr (K == 11) {
    R.s *= 1.0f / D;
    R.q = __builtin_fmaxf(R.q * (1.0f / D) - R.s * R.s, 0.f);
  } else if constexpr (K == 12) {
    R.q = __builtin_amdgcn_sqrtf(rbf(rbf(R.q) + 1e-5f));
  } else {
    lds_st<unsigned>(lane == 63 ? L_GO + 32 + (R.row & 7) * 4 : L_GO + 64, pk2(R.s, R.q));
  }
  if constexpr (K >= 5 && K < 13) asm volatile("" : "+v"(R.s), "+v"(R.q));
}
DI void row_publish(const RowStat& R, char* ws, unsigned tag, unsigned tid) {
  if (tid < 8) __hip_atomic_store((gu64*)(ws + WS_STAT) + R.row + tid, ((unsigned long long)tag << 32) | lds_ld<unsigned>(L_GO + 32 + tid * 4), RLX_AGENT);
}

struct Gran { u32x2 g0, g1, g2; unsigned off, step; };
DI void gran_issue(Gran& G, rsrc_t rs) {
  asm volatile("buffer_load_dwordx2 %0, %3, %4, 0 offen sc1\n\tbuffer_load_dwordx2 %1, %3, %4, %5 offen sc1\n\t"
               "buffer_load_dwordx2 %2, %3, %4, %6 offen sc1"
               : "=&v"(G.g0), "=&v"(G.g1), "=&v"(G.g2) : "v"(G.off), "s"(rs), "s"(G.step), "s"(2 * G.step) : "memory");
}
DI void gran_load(Gran& G, rsrc_t rs) {
  asm volatile("buffer_load_dwordx2 %0, %3, %4, 0 offen sc1\n\tbuffer_load_dwordx2 %1, %3, %4, %5 offen sc1\n\t"
               "buffer_load_dwordx2 %2, %3, %4, %6 offen sc1\n\ts_waitcnt vmcnt(0)"
               : "=&v"(G.g0), "=&v"(G.g1), "=&v"(G.g2) : "v"(G.off), "s"(rs), "s"(G.step), "s"(2 * G.step) : "memory");
}

DI unsigned urow(unsigned pos, unsigned rot) { const unsigned u = pos + rot; return (u >= 54 ? u - 54 : u) * 40960u; }

constexpr unsigned wo_tile(unsigned v) { return (v / 5) * 10 + (v % 5 == 4 ? 4 : (v % 5 < 2 ? 2 * (v % 5) : 2 * (v % 5) + 1)); }
struct W2 { rsrc_t wd, wo; unsigned rot, col; };
DI unsigned w2_off(const W2& W, int u, unsigned lw) {
  if (u < 54) return urow(u, W.rot) + lw * 10240u + W.col;
  return wo_tile(u - 54) * 20480u + (lw >> 1) * ((u - 54) % 5 == 4 ? 102400u : 20480u) + (lw & 1) * 10240u + W.col;
}
DI u32x4 w2_load(const W2& W, int u, unsigned lw, unsigned voff) {
  return __builtin_amdgcn_raw_buffer_load_b128(u < 54 ? W.wd : W.wo, voff, w2_off(W, u, lw), 0);
}

template <int T, bool TR, bool PF, bool LASTA, class Pre>
DI void main_loop(Pre&& pre, RowStat& R, char* ws, f32x4 (&acc)[4][T], unsigned (&boff)[T], Dma& d, unsigned w, unsigned lane, unsigned tid,
                  unsigned epoch, unsigned& tag, unsigned rb, unsigned ccol, rsrc_t rsB2, rsrc_t rsWS, unsigned cb, unsigned rot, u32x4 (&pre2)[36] STAMP_ARG) {
  const unsigned g = lane >> 4, r = lane & 15, m = w & 3;
  unsigned aoff[4];
  aoff[0] = (64 * m + r) * 128 + ((g ^ (r >> 1)) << 4);
  aoff[1] = aoff[0] ^ 64;
  aoff[2] = aoff[0] + a_slot(0); aoff[3] = aoff[1] + a_slot(0);
  const unsigned wq = d.wq, wa = d.wa;
  asm volatile("" : "+v"(aoff[0]), "+v"(aoff[1]), "+v"(aoff[2]), "+v"(aoff[3]), "+v"(boff[0]), "+v"(boff[1]), "+v"(boff[2]));
  if (T == 5) asm volatile("" : "+v"(boff[3]), "+v"(boff[4]));
  Gran G;
  unsigned v2, vb2, vpf, tA[3], tB[3];
  row_load(R, d.rsA, lane);
  {
    asm volatile("s_waitcnt vmcnt(%2) lgkmcnt(0)\n\tv_readfirstlane_b32 %0, %1\n\ts_nop 3" : "=s"(tag) : "v"(epoch), "i"(5 + NA1) : "memory");
    tag = rfl(tag) + 1;
    bar();
  }
  STAMP(1);
  u32x4 a0[4], a1[4];
  u32x2 b0[T][2], b1[T][2];
#pragma unroll
  for (int i = 0; i < 4; ++i) a0[i] = lds_ld<u32x4>(aoff[2] + i * 2048);
#pragma unroll
  for (int t = 0; t < T; ++t) { b0[t][0] = ldtr1(bofs<T>(boff, t)); b0[t][1] = ldtr1(bofs<T>(boff, t) + 768); }
  SB;
#pragma unroll
  for (int i = NA1; i < 4; ++i) dma16(a_slot(1) + d.mA + i * 1024, (i & 1) ? d.vA1 : d.vA0, d.rsA, d.sA[i] + 128);
  const bool y = w >= 4;
  bool ok = true;
  auto ln_store = [&] {
    const float v0 = y ? -bflo(G.g0[0]) : __builtin_bit_cast(float, G.g0[0]) + __builtin_bit_cast(float, G.g1[0]) + __builtin_bit_cast(float, G.g2[0]);
    const unsigned la = L_MISC + (y ? tid * 4 - 1024 : 2048 + tid * 4);
    lds_st<float>(la, v0);
    lds_st<float>(y ? la + 1024 : L_GO + 64 + (lane & 7) * 4, __builtin_amdgcn_rcpf(bfhi(G.g0[0])));
  };
  static_for<0, 10>([&](auto S_) {
    constexpr int s = S_.v, L = 4 * T;
    constexpr int NX = (PF && s >= 1 && 2 * s <= NPRE) ? 2 : 0;
    kstep<T, TR, 2 * s>(acc, a0, b0, a1, b1, aoff, boff, d, [&](auto E) {
      constexpr int e = E.v;
      if constexpr (s == 2) {
        if constexpr (e == 2) { vb2 = v2 + (3 * wq) * 10240u; asm volatile("" : "+v"(vb2)); }
        if constexpr (e == 5) { G.off = w < 4 ? WS_CSUM + ccol * 8 : WS_STAT + (rb * 256 + tid - 256) * 8; asm volatile("" : "+v"(G.off)); }
        if constexpr (e == 8) { G.step = w < 4 ? NCOL * 8 : 0; tA[0] = urow(wa, rot) + cb * 128u; asm volatile("" : "+s"(G.step), "+s"(tA[0])); }
        if constexpr (e == 10) { tA[1] = urow(8 + wa, rot) + cb * 128u; asm volatile("" : "+s"(tA[1])); }
        if constexpr (e == 12) { tA[2] = urow(31 + wa, rot) + cb * 128u; asm volatile("" : "+s"(tA[2])); }
        if constexpr (e == 14) {
          tB[0] = urow(16, rot) + cb * 128u; tB[1] = urow(w < 4 ? 19u : 22u, rot) + cb * 128u; tB[2] = urow(w < 4 ? 25u : 28u, rot) + cb * 128u;
          asm volatile("" : "+s"(tB[0]), "+s"(tB[1]), "+s"(tB[2]));
        }
      }
      if constexpr (s == 3 && e == 2) pre();
      if constexpr (NX && e >= L - 2) {
        constexpr int u = 2 * (s - 1) + e - (L - 2);
        pre2[u] = __builtin_amdgcn_raw_buffer_load_b128(rsB2, vpf, urow(36 + u, rot) + (w & 3) * 10240u + cb * 128u, 0);
      }
      if constexpr (s < 7) { if constexpr (e == L - 3) d.sB += s == 0 ? d.advB * (1 + (w >> 2)) : d.advB; }
      else if constexpr (s == 7) {
        if constexpr (e == L - 6) d.vB0 = y ? vb2 : d.vB0;
        if constexpr (e == L - 4) d.vB1 = y ? vb2 + 10240u : d.vB1;
        if constexpr (e == L - 3) d.vB2 = y ? vb2 + 20480u : d.vB2;
        if constexpr (e == L - 2) d.sB = y ? tB[0] : d.sB + d.advB;
        if constexpr (e == L - 1) d.rsB = y ? rsB2 : d.rsB;
      } else {
        if constexpr (e == L - 3) { d.sB = tB[s - 7]; d.rsB = rsB2; d.vB0 = vb2; d.vB1 = vb2 + 10240u; d.vB2 = vb2 + 20480u; }
      }
      if constexpr (s == 9 && e == L - 4) {
        asm volatile("s_waitcnt vmcnt(7)" : "+v"(G.g0), "+v"(G.g1), "+v"(G.g2)::"memory");
        ok = all64(G.g0[1] == tag && G.g1[1] == tag && G.g2[1] == tag);
      }
      if constexpr (s == 9 && e == L - 3) ln_store();
    });
    if constexpr (s == 0) {
      wait_vm<EARLY1 ? 8 - NA1 : 11>();
      static_for<0, 14>([&](auto K) { row_step<K.v>(R, lane); });
    }
    if constexpr (s == 9) {
      if (__builtin_expect(!ok, 0)) {
#pragma unroll 1
        for (unsigned spins = 0; spins < SPIN_CAP; ++spins) {
          __builtin_amdgcn_s_sleep(2);
          gran_load(G, rsWS);
          if (all64(G.g0[1] == tag && G.g1[1] == tag && G.g2[1] == tag)) break;
        }
        ln_store();
      }
    }
    wait_lgkm0();
    if constexpr (s < 9) wait_vm<4 + NX>();
    bar();
    if constexpr (s == 0) { STAMP(12); }
    SB;
    kstep<T, TR, 2 * s + 1>(acc, a1, b1, a0, b0, aoff, boff, d, [&](auto E) {
      constexpr int e = E.v;
      if constexpr (s == 0 && e == 6) {
        if (y) {
          const unsigned so = d.sB - d.advB;
          dma16(d.mW + 49152, d.vB0, d.rsB, so); dma16(d.mW + 49152 + 1024, d.vB1, d.rsB, so); dma16(d.mW + 49152 + 2048, d.vB2, d.rsB, so);
        }
      }
      if constexpr (s == 0 && e == L - 4) {
        v2 = (lane >> 3) * 1280 + (((lane & 7) ^ (((lane >> 4) & 3) << 1)) << 4);
        vpf = y ? v2 : 0x7ffffff0u;
        asm volatile("" : "+v"(v2), "+v"(vpf));
      }
      if constexpr (s == 0 && e == L - 6) row_publish(R, ws, tag, tid);
      if constexpr (s == 8 && e == 0) gran_issue(G, rsWS);
      if constexpr (s >= 7 && s < 9 && e >= L - 4) {
        d.rsA = rsB2; d.vA0 = v2; d.vA1 = v2;
        d.sA[e - (L - 4)] = tA[s - 7] + (e - (L - 4)) * 10240;
      }
      if constexpr (s == 9 && LASTA && e == 7) {
        if (wa < 5) {
#pragma unroll
          for (int i = 0; i < 4; ++i) dma16(a_slot(0) + d.mA + i * 1024, v2, rsB2, tA[2] + i * 10240);
        }
      }
    });
    if constexpr (s == 8) { STAMP(13); }
    if constexpr (s == 2) { STAMP(26); }
    if constexpr (s == 6) { STAMP(27); }
  });
  acc_fence<4 * T>(&acc[0][0]);
  STAMP(14);
}

DI void st16(u32x4 v, rsrc_t rs, unsigned voff, unsigned soff) {
  __builtin_amdgcn_raw_buffer_store_b128(v, rs, voff, soff, 16);
  asm volatile("s_nop 1" ::"v"(v) : "memory");
}
DI void publish_flag(const Params& p, unsigned flag_off, unsigned tag, unsigned tid) {
  drain_vm();
  bar();
  if (tid == 0) __hip_atomic_store((gu32*)(p.ws + flag_off), tag, RLX_AGENT);
}
DI void st_frag(rsrc_t rs, unsigned lane, unsigned rt, unsigned step, u32x2 lo, u32x2 hi) {
  u32x4 v = {lo[0], lo[1], hi[0], hi[1]};
  st16(v, rs, lane * 16, WS_HA + (rt * NSTEP + step) * 1024);
}
DI void shared_send(unsigned w, unsigned lane, const u32x2 (&sh)[4]) {
  if (w >= 4) lds_st<u32x4>(L_SCR + w * 1024 + lane * 16, cat2(sh[0], sh[1]));
  else lds_st<u32x4>(L_SCR + w * 1024 + lane * 16, cat2(sh[2], sh[3]));
  asm volatile("" ::: "memory");
}
DI void shared_recv(rsrc_t rs, unsigned w, unsigned lane, unsigned rt0, unsigned step, const u32x2 (&sh)[4]) {
  lbar();
  u32x4 rcv = lds_ld<u32x4>(L_SCR + (w ^ 4) * 1024 + lane * 16);
  u32x2 r0 = {rcv[0], rcv[1]}, r1 = {rcv[2], rcv[3]};
  if (w >= 4) { st_frag(rs, lane, rt0 + 2, step, r0, sh[2]); st_frag(rs, lane, rt0 + 3, step, r1, sh[3]); }
  else        { st_frag(rs, lane, rt0 + 0, step, sh[0], r0); st_frag(rs, lane, rt0 + 1, step, sh[1], r1); }
}

__global__ __launch_bounds__(512, 2) void fused_block(Params p) {
  unsigned tid = __builtin_amdgcn_workitem_id_x(), lane = tid & 63;
  const unsigned w = rfl(tid >> 6);
  const unsigned b = __builtin_amdgcn_workgroup_id_x(), xcd = b & 7, slot = b >> 3;
  const unsigned rb = 4 * (xcd & 1) + (slot & 3), jq = xcd >> 1, ji = slot >> 2;
  const unsigned spi = (xcd - 4) * 4 + (slot & 3);
  const unsigned long long jl0 = (jq & 1) ? 0x4123220908070605ull : 0x4021200403020100ull, jl1 = (jq & 1) ? 0x4327266011100f0eull : 0x422524600d0c0b0aull;
  const unsigned jb = (unsigned)(((jq & 2) ? jl1 : jl0) >> (8 * ji)) & 0xff, jtype = jb >> 5, jidx = jb & 31;
  STAMP_DECL;
  STAMP(0);

  Dma d;
  RowStat rs_;
  const bool mlp = jtype == J_MLP;
  const unsigned pitch = mlp ? 3456u : 3840u, wq = (w + (ROTW ? slot : 0)) & 3, wa = (w + (ROTW ? ji : 0)) & 7;
  d.wa = wa; d.wq = wq;
  const rsrc_t rsX = mk_rsrc(p.x, S * D * 2);
  d.vA0 = (lane >> 3) * 1280 + (((lane & 7) ^ (lane >> 4)) << 4); d.vA1 = d.vA0 ^ 64;
  d.rsA = rsX; d.mA = wa * 4096u;
#pragma unroll
  for (int i = 0; i < 4; ++i) d.sA[i] = (rb * 256 + 32 * wa + 8 * i) * 1280;
  unsigned epoch, tag;
  asm volatile("s_nop 4" :: "s"(rsX) : "memory");
  STAMP(6);
#pragma unroll
  for (int i = 0; i < 4; ++i) dma16(a_slot(0) + d.mA + i * 1024, (i & 1) ? d.vA1 : d.vA0, d.rsA, d.sA[i]);
  asm volatile("global_load_dword %0, %1, %2 sc1" : "=&v"(epoch) : "v"(0u), "s"(p.ws) : "memory");
  SB;
  {
    unsigned vb[3];
    const unsigned lim = mlp ? 176u : 144u;
    {
      const unsigned r0 = (lane * 43) >> 9, p0 = lane - 12 * r0;
      const bool w1 = p0 >= 8, w2 = p0 >= 4;
      const unsigned r1 = r0 + (w1 ? 6 : 5), p1 = p0 + (w1 ? -8 : 4), r2 = r0 + (w2 ? 11 : 10), p2 = p0 + (w2 ? -4 : 8);
      vb[0] = r0 * pitch + umin(p0 * 16, lim);
      vb[1] = r1 * pitch + umin((p1 * 16) ^ ((r1 & 8) << 2), lim);
      vb[2] = r2 * pitch + umin((p2 * 16) ^ 32, lim);
    }
    d.vB0 = vb[0]; d.vB1 = vb[1]; d.vB2 = vb[2];
    const unsigned mh = wq >> 1;
    const void* bw = mlp ? (mh ? p.wu : p.wg) : p.wqkv;
    const unsigned cj = mlp ? 192u : 160u * jtype, cm = mlp ? 0u : (jtype == J_KV ? 1280u : 160u), c0 = jtype == J_KV ? 1280u : 0u;
    d.rsB = mk_rsrc(bw, D * pitch);
    d.sB = (16 * (wq & 1) + (w < 4 ? 32 : 0)) * pitch + c0 + cj * jidx + cm * mh; d.advB = 64 * pitch;
    d.mB = L_B + wq * 3072u + (w < 4 ? 0u : 12288u); d.mW = L_B + wq * 3072u + (w < 4 ? 49152u : 0u);
    const unsigned bdst = L_B + (w < 4 ? 12288u : 0u) + wq * 3072u;
    dma16(bdst, d.vB0, d.rsB, d.sB); dma16(bdst + 1024, d.vB1, d.rsB, d.sB); dma16(bdst + 2048, d.vB2, d.rsB, d.sB);
    rs_.row = rfl(b * 8 + w);
    d.sB += d.advB;
    dma16(bdst + 24576, d.vB0, d.rsB, d.sB); dma16(bdst + 24576 + 1024, d.vB1, d.rsB, d.sB); dma16(bdst + 24576 + 2048, d.vB2, d.rsB, d.sB);
#pragma unroll
    for (int i = 0; i < NA1; ++i) dma16(a_slot(1) + d.mA + i * 1024, (i & 1) ? d.vA1 : d.vA0, d.rsA, d.sA[i] + 128);
  }
  STAMP(10);
  lds_st<unsigned>(L_GO + (tid & 1) * 4, 0u);

  const unsigned t2 = 28 * xcd + slot;
  const bool has2 = slot < 28 && t2 < 220;
  const unsigned G = t2 / 10, cb = t2 % 10;
  constexpr unsigned rot = 32;
  unsigned g = lane >> 4, r = lane & 15, q4 = r >> 2, p4 = lane & 3;
  const rsrc_t rsWd = mk_rsrc(p.wd, has2 ? F * D * 2 : 0);
  const rsrc_t rsWS = mk_rsrc(p.ws, WS_END);
  unsigned v2 = (lane >> 3) * 1280 + (((lane & 7) ^ (((lane >> 4) & 3) << 1)) << 4);
  auto relane = [&] {
    asm volatile("" : "+v"(tid));
    lane = tid & 63; g = lane >> 4; r = lane & 15; q4 = r >> 2; p4 = lane & 3;
    v2 = (lane >> 3) * 1280 + (((lane & 7) ^ (((lane >> 4) & 3) << 1)) << 4);
  };
  const unsigned m = w & 3, n = w >> 2;
  const unsigned rt0 = rb * 16 + m * 4;
  const unsigned brow = L_B + (8 * g + q4) * 192;
  const unsigned bx = (g & 1) << 5;

  u32x4 pre2[36];
  W2 W;
  W.wd = rsWd; W.wo = mk_rsrc(p.wo, has2 ? D * D * 2 : 0); W.rot = rot; W.col = cb * 128u;
  if (jtype == J_MLP) {
    f32x4 acc[4][6];
    unsigned boff[6];
#pragma unroll
    for (int t = 0; t < 3; ++t) {
      const unsigned gt = n ? (t == 2 ? 3 : 4 + t) : t;
      boff[t] = brow + ((32 * gt + 8 * p4) ^ bx); boff[t + 3] = boff[t] + 6144;
    }
    const unsigned ccol = tid < 96 ? 1920 + 96 * jidx + tid : 3648 + 96 * jidx + (tid < 192 ? tid - 96 : 0);
    main_loop<6, true, true, true>([] {}, rs_, p.ws, acc, boff, d, w, lane, tid, epoch, tag, rb, ccol, rsWd, rsWS, cb, rot, pre2 STAMP_PASS);
    relane();
    float mm[4];
    f32x4 cg[3], cu[3];
#pragma unroll
    for (int i = 0; i < 4; ++i) mm[i] = lds_ld<float>(L_MISC + (64 * m + 16 * i + r) * 4);
#pragma unroll
    for (int t = 0; t < 3; ++t) {
      const unsigned gt = n ? (t == 2 ? 3 : 4 + t) : t;
      cg[t] = lds_ld<f32x4>(L_MISC + 2048 + (16 * gt + 4 * g) * 4); cu[t] = lds_ld<f32x4>(L_MISC + 2048 + (96 + 16 * gt + 4 * g) * 4);
    }
    auto hval = [&](int i, int t) {
      f32x4 gg = acc[i][t] + mm[i] * cg[t];
      const f32x4 uu = acc[i][t + 3] + mm[i] * cu[t];
#pragma unroll
      for (int j = 0; j < 4; ++j) gg[j] = relu2(gg[j]);
      return pk4(gg * uu);
    };
#pragma unroll
    for (int i = 0; i < 4; ++i) { st_frag(rsWS, lane, rt0 + i, 3 * jidx + 2 * n, hval(i, 0), hval(i, 1)); SB; }
    const u32x2 sh[4] = {hval(0, 2), hval(1, 2), hval(2, 2), hval(3, 2)};
    shared_send(w, lane, sh);
    shared_recv(rsWS, w, lane, rt0, 3 * jidx + 1, sh);
    STAMP(15);
    publish_flag(p, WS_FLAGH + (rb * 32 + jidx) * FS, tag, tid);
    STAMP(3);
  } else if (jtype == J_KV) {
    f32x4 acc[4][5];
    unsigned boff[5];
    boff[0] = brow + ((80 * n + 8 * p4) ^ bx); boff[1] = boff[0] + 6144;
    boff[2] = brow + ((80 * n + 40 + 8 * p4) ^ bx); boff[3] = boff[2] + 6144;
    boff[4] = brow + ((80 * n + 32 + 40 * (p4 >> 1)) ^ bx) + (p4 & 1) * 6144;
    const unsigned kb = 126976u + m * 6720 + n * 3360, dump = L_MISC - 2048 + lane * 16;
    const unsigned ea = (q4 == 1 || r == 0) ? kb + (r == 0 ? 20 : 12 + r) * 80 : 0u, eb = (q4 == 3 || r == 0) ? kb + 1680 + (r == 0 ? 20 : 4 + r) * 80 : 0u;
    unsigned ka[6] = {kb + r * 80 + g * 16, ea ? ea + g * 16 : dump, eb ? eb + g * 16 : dump, g == 0 ? kb + r * 80 + 64 : dump,
                      g == 2 ? kb + 1680 + r * 80 + 64 : dump, (g == 0 && ea) ? ea + 64 : ((g == 2 && eb) ? eb + 64 : dump)};
    auto pin = [&] { asm volatile("" : "+v"(ka[0]), "+v"(ka[1]), "+v"(ka[2]), "+v"(ka[3]), "+v"(ka[4]), "+v"(ka[5])); };
    const unsigned ccol = tid < 80 ? 640 + 80 * jidx + tid : 1280 + 80 * jidx + (tid < 160 ? tid - 80 : 0);
    main_loop<5, false, false, false>(pin, rs_, p.ws, acc, boff, d, w, lane, tid, epoch, tag, rb, ccol, rsWd, rsWS, cb, rot, pre2 STAMP_PASS);
    relane();
    float cs[5];
    cs[0] = lds_ld<float>(L_MISC + 2048 + (40 * n + r) * 4);
    cs[1] = lds_ld<float>(L_MISC + 2048 + (80 + 40 * n + r) * 4);
    cs[2] = lds_ld<float>(L_MISC + 2048 + (40 * n + 20 + r) * 4);
    cs[3] = lds_ld<float>(L_MISC + 2048 + (80 + 40 * n + 20 + r) * 4);
    cs[4] = lds_ld<float>(L_MISC + 2048 + ((q4 & 1) * 80 + 40 * n + 16 + 20 * (q4 >> 1) + (r & 3)) * 4);
    u32x2 pk[4][3], vv[4][3];
    const unsigned vmask = 0u - (q4 & 1);
    f32x4 mm4[4], rr4[4];
#pragma unroll
    for (int i = 0; i < 4; ++i) {
      mm4[i] = lds_ld<f32x4>(L_MISC + (64 * m + 16 * i + 4 * g) * 4); rr4[i] = lds_ld<f32x4>(L_MISC + 1024 + (64 * m + 16 * i + 4 * g) * 4);
    }
    SB;
#pragma unroll
    for (int i = 0; i < 4; ++i) {
      const f32x4 mm = mm4[i], rr = rr4[i];
      const f32x4 rl = rr * 1.4426950408889634f, rh = rr * 0.5f;
      f32x4 val[5];
#pragma unroll
      for (int t = 0; t < 5; ++t) val[t] = acc[i][t] + mm * cs[t];
#pragma unroll
      for (int t = 0; t < 3; ++t) pk[i][t] = pk4(phi4(val[2 * t], rl, rh));
      vv[i][0] = pk4(val[1] * rr); vv[i][1] = pk4(val[3] * rr);
      const u32x2 v4 = pk4(val[4] * rr);
      vv[i][2] = u32x2{bsel(vmask, v4[0], 0x3f803f80u), bsel(vmask, v4[1], 0x3f803f80u)};
    }
    STAMP(25);
    f32x4 P[7] = {};
#pragma unroll
    for (int i = 0; i < 4; i += 2) {
      const bf16x8 k0 = frag(pk[i][0], pk[i + 1][0]), k2 = frag(pk[i][1], pk[i + 1][1]), k4 = frag(pk[i][2], pk[i + 1][2]);
      const bf16x8 v1 = frag(vv[i][0], vv[i + 1][0]), v3 = frag(vv[i][1], vv[i + 1][1]), v4 = frag(vv[i][2], vv[i + 1][2]);
      P[0] = mfma(k0, v1, P[0]); P[1] = mfma(k0, v4, P[1]); P[2] = mfma(k4, v1, P[2]);
      P[3] = mfma(k2, v3, P[3]); P[4] = mfma(k2, v4, P[4]); P[5] = mfma(k4, v3, P[5]); P[6] = mfma(k4, v4, P[6]);
    }
    lds_st<f32x4>(ka[0], P[0]); lds_st<f32x4>(ka[0] + 1680, P[3]); lds_st<f32x4>(ka[1], P[1]); lds_st<f32x4>(ka[2], P[4]);
    lds_st<f32x4>(ka[3], P[2]); lds_st<f32x4>(ka[4], P[5]); lds_st<f32x4>(ka[5], P[6]);
    lbar();
    if (tid < 210) {
      const unsigned a = 126976u + tid * 32;
      const f32x4 t0 = (lds_ld<f32x4>(a) + lds_ld<f32x4>(a + 6720)) + (lds_ld<f32x4>(a + 13440) + lds_ld<f32x4>(a + 20160));
      const f32x4 t1 = (lds_ld<f32x4>(a + 16) + lds_ld<f32x4>(a + 6736)) + (lds_ld<f32x4>(a + 13456) + lds_ld<f32x4>(a + 20176));
      st16(cat2(pk4(t0), pk4(t1)), rsWS, tid * 16, WS_KVP + (rb * 32 + 4 * jidx) * 840);
    }
    wait_lgkm0();
    STAMP(15);
    publish_flag(p, WS_FLAGKV + ((jidx >> 1) * 32 + (jidx & 1) * 8 + rb) * FS, tag, tid);
    STAMP(3);
  } else if (jtype == J_Q) {
    f32x4 acc[4][5];
    unsigned boff[5];
#pragma unroll
    for (int t = 0; t < 5; ++t) boff[t] = brow + n * 6144 + ((32 * t + 8 * p4) ^ bx);
    const unsigned ccol = 160 * jidx + (tid < 160 ? tid : 0);
    main_loop<5, true, false, false>([] {}, rs_, p.ws, acc, boff, d, w, lane, tid, epoch, tag, rb, ccol, rsWd, rsWS, cb, rot, pre2 STAMP_PASS);
    relane();
    u32x2 pq[4][5];
    {
      float mm[4], rr[4];
      f32x4 c[5];
#pragma unroll
      for (int i = 0; i < 4; ++i) { mm[i] = lds_ld<float>(L_MISC + (64 * m + 16 * i + r) * 4); rr[i] = lds_ld<float>(L_MISC + 1024 + (64 * m + 16 * i + r) * 4); }
#pragma unroll
      for (int t = 0; t < 5; ++t) c[t] = lds_ld<f32x4>(L_MISC + 2048 + (80 * n + 16 * t + 4 * g) * 4);
      SB;
#pragma unroll
      for (int i = 0; i < 4; ++i)
#pragma unroll
        for (int t = 0; t < 5; ++t) pq[i][t] = pk4(phi4(acc[i][t] + mm[i] * c[t], splat(rr[i] * 1.4426950408889634f), splat(rr[i] * 0.5f)));
#pragma unroll
      for (int i = 0; i < 4; ++i) asm volatile("" : "+v"(pq[i][0]), "+v"(pq[i][1]), "+v"(pq[i][2]), "+v"(pq[i][3]), "+v"(pq[i][4]));
    }
    constexpr unsigned KP = 208, KQ = 84 * KP;
    unsigned wa[2];
#pragma unroll
    for (int k = 0; k < 2; ++k) {
      const unsigned qd = 2 * (tid < 420 ? tid : 0) + k, hd = qd / 105, e = (qd % 105) / 5, dq = qd % 5, hl = hd & 3;
      wa[k] = (hd >> 2) * KQ + (e < 20 ? hl * 20 + e : 80 + hl) * KP + hl * 40 + dq * 8;
    }
    unsigned fk = n * KQ + r * KP + g * 8, fz = n * KQ + (80 + (r & 3)) * KP + g * 8;
    unsigned gm[3] = {g < 1 ? ~0u : 0u, g < 2 ? ~0u : 0u, g < 3 ? ~0u : 0u};
    u32x4 B[4][3];
    f32x4 eps4 = splat(1e-6f);
#pragma unroll
    for (int i = 0; i < 4; ++i) {
      B[i][0] = cat2(pq[i][0], pq[i][1]); B[i][1] = cat2(pq[i][2], pq[i][3]); B[i][2] = cat2(pq[i][4], u32x2{0u, 0u});
      asm volatile("" : "+v"(B[i][0]), "+v"(B[i][1]), "+v"(B[i][2]));
    }
    asm volatile("" : "+v"(wa[0]), "+v"(wa[1]), "+v"(fk), "+v"(fz), "+v"(gm[0]), "+v"(gm[1]), "+v"(gm[2]), "+v"(eps4));
    STAMP(25);
    wait_vm<0>();
    bar();
#pragma unroll
    for (int k = 0; k < 5; ++k) lds_st<u32x4>(k * 8192 + tid * 16, u32x4{0u, 0u, 0u, 0u});
    if (w == 0) poll_flags((gu32*)(p.ws + WS_FLAGKV + (jidx * 32 + (lane & 15)) * FS), true, tag);
    lbar();
    STAMP(4);
    if (w < 7) {
      u32x4 v[8];
#pragma unroll
      for (int j = 0; j < 8; ++j)
        v[j] = __builtin_amdgcn_raw_buffer_load_b128(rsWS, (tid < 420 ? tid : 0) * 16, WS_KVP + (j * 32 + 8 * jidx) * 840, 16);
      SB;
      f32x4 t0 = {0.f, 0.f, 0.f, 0.f}, t1 = t0;
#pragma unroll
      for (int j = 0; j < 8; ++j) {
        t0 += f32x4{bflo(v[j][0]), bfhi(v[j][0]), bflo(v[j][1]), bfhi(v[j][1])};
        t1 += f32x4{bflo(v[j][2]), bfhi(v[j][2]), bflo(v[j][3]), bfhi(v[j][3])};
      }
      lds_st<u32x2>(wa[0], pk4(t0)); lds_st<u32x2>(wa[1], pk4(t1));
    }
    lbar();
    STAMP(16);
    u32x4 A[12];
#pragma unroll
    for (int P = 0; P < 3; ++P) A[P] = cat2(lds_ld<u32x2>(fz + P * 64), lds_ld<u32x2>(fz + P * 64 + 32));
#pragma unroll
    for (int u = 0; u < 5; ++u) {
      const unsigned a0 = fk + u * 16 * KP + (u < 3 ? 0 : 64);
      A[3 + u] = cat2(lds_ld<u32x2>(a0), lds_ld<u32x2>(a0 + 32));
    }
#pragma unroll
    for (int u = 1; u < 5; ++u) {
      const unsigned a0 = fk + u * 16 * KP + (u < 3 ? 64 : 128);
      A[7 + u] = cat2(lds_ld<u32x2>(a0), lds_ld<u32x2>(a0 + 32));
    }
    SB;
    u32x2 sh[4];
    f32x4 den[4], num[2][5];
    static_for<0, 12>([&](auto E) {
      constexpr int P = E.v >> 2, i = E.v & 3;
      if constexpr (P == 0) mfma_c(den[i], A[0], B[i][0], eps4); else mfma_ip(den[i], A[P], B[i][P]);
    });
    u32x2 o[5];
    auto fin = [&](auto I, auto K) {
      constexpr int i = I.v, k = K.v, u = k < 8 ? k >> 1 : 4;
      if constexpr (k == 8 || !(k & 1)) {
        asm volatile("" : "+v"(num[i & 1][u]));
        const float rr = u == 0 ? den[i][0] : (u == 4 ? den[i][3] : bself(gm[(u + 2) % 3], den[i][(u + 3) & 3], den[i][u & 3]));
#pragma unroll
        for (int j = 0; j < 4; ++j) num[i & 1][u][j] = smul(num[i & 1][u][j], rr);
      }
      if constexpr (k == 8 || (k & 1)) o[u] = pk4(num[i & 1][u]);
      if constexpr (k == 3) st_frag(rsWS, lane, rt0 + i, 54 + 5 * jidx + 2 * n, o[0], o[1]);
      if constexpr (k == 7) st_frag(rsWS, lane, rt0 + i, 54 + 5 * jidx + 2 * n + 1, o[2], o[3]);
      if constexpr (k == 8) { sh[i] = o[4]; asm volatile("" : "+v"(sh[i])); }
    };
    static_for<0, 4>([&](auto I) {
      constexpr int i = I.v;
      static_for<0, 9>([&](auto K) {
        constexpr int k = K.v, u = k < 5 ? k : k - 4;
        if constexpr (k < 5) mfma_z(num[i & 1][u], A[3 + u], B[i][u < 3 ? 0 : 1]); else mfma_ip(num[i & 1][u], A[7 + u], B[i][u < 3 ? 1 : 2]);
        SB;
        if constexpr (i == 0) {
          if constexpr (k >= 5) {
            asm volatile("" : "+v"(den[k - 5]));
#pragma unroll
            for (int j = 0; j < 4; ++j) den[k - 5][j] = __builtin_amdgcn_rcpf(den[k - 5][j]);
            asm volatile("" : "+v"(den[k - 5]));
          }
        } else {
          fin(IC<i - 1>{}, K);
        }
        SB;
      });
    });
    static_for<0, 9>([&](auto K) { fin(IC<3>{}, K); SB; });
    shared_send(w, lane, sh);
    shared_recv(rsWS, w, lane, rt0, 54 + 5 * jidx + 4, sh);
    STAMP(15);
    publish_flag(p, WS_FLAGO + (rb * 32 + jidx) * FS, tag, tid);
    STAMP(3);
    if (b == 224) {
      poll_flags((gu32*)(p.ws + WS_FLAGH + tid * FS), (tid & 31) < (tid < 256 ? 18u : 4u), tag);
      bar();
      if (tid == 0) __hip_atomic_store((gu32*)(p.ws + WS_EPOCH), tag, RLX_AGENT);
    }
  } else {
    const unsigned gw = spi * 8 + w;
    unsigned k10 = 0x00003f80u, k01 = 0x3f800000u;
    asm volatile("" : "+v"(k10), "+v"(k01));
    auto cs_src = [&](unsigned u, rsrc_t& rs, unsigned& cv, unsigned& st) {
      const unsigned cline = u / 3, third = u % 3;
      const void* cw = cline < 30 ? p.wqkv : (cline < 57 ? p.wg : p.wu);
      const unsigned cpitch = cline < 30 ? 3840u : 3456u;
      const unsigned clocal = cline < 30 ? cline : (cline < 57 ? cline - 30 : cline - 57);
      rs = mk_rsrc(cw, gw < 126 ? D * cpitch : 0);
      cv = (third * 216 + (lane >> 3)) * cpitch + clocal * 128 + (lane & 7) * 16;
      st = 8 * cpitch;
    };
    auto cs_publish = [&](unsigned u, float (&cs)[8]) {
#pragma unroll
      for (int k = 0; k < 4; ++k) { const bool hi = lane & 32; const float o = shfl_x(hi ? cs[k] : cs[k + 4], lane, 32); cs[k] = (hi ? cs[k + 4] : cs[k]) + o; }
#pragma unroll
      for (int k = 0; k < 2; ++k) { const bool hi = lane & 16; const float o = shfl_x(hi ? cs[k] : cs[k + 2], lane, 16); cs[k] = (hi ? cs[k + 2] : cs[k]) + o; }
      { const bool hi = lane & 8; const float o = shfl_x(hi ? cs[0] : cs[1], lane, 8); cs[0] = (hi ? cs[1] : cs[0]) + o; }
      if (gw < 126)
        __hip_atomic_store((gu64*)(p.ws + WS_CSUM) + (u % 3) * NCOL + (u / 3) * 64 + (lane & 7) * 8 + (lane >> 3),
                           ((unsigned long long)tag << 32) | __builtin_bit_cast(unsigned, cs[0]), RLX_AGENT);
    };
    row_load(rs_, rsX, lane);
    asm volatile("s_waitcnt vmcnt(0) lgkmcnt(0)\n\tv_readfirstlane_b32 %0, %1\n\ts_nop 3" : "=s"(tag) : "v"(epoch) : "memory");
    tag = rfl(tag) + 1;
    static_for<0, 14>([&](auto K) { row_step<K.v>(rs_, lane); });
    u32x4 c[27];
    rsrc_t rs0, rs1; unsigned cv0, cv1, st0, st1;
    cs_src(gw, rs0, cv0, st0); cs_src(gw + 126, rs1, cv1, st1);
#pragma unroll
    for (int j = 0; j < 27; ++j) c[j] = __builtin_amdgcn_raw_buffer_load_b128(rs0, cv0 + j * st0, 0, 0);
    lbar();
    row_publish(rs_, p.ws, tag, tid);
    float cs[8] = {};
#pragma unroll
    for (int j = 0; j < 27; ++j) {
#pragma unroll
      for (int k = 0; k < 4; ++k) { cs[2 * k] = dot2(c[j][k], k10, cs[2 * k]); cs[2 * k + 1] = dot2(c[j][k], k01, cs[2 * k + 1]); }
      if (j < 8) c[j] = __builtin_amdgcn_raw_buffer_load_b128(rs1, cv1 + j * st1, 0, 0);
      SB;
    }
    cs_publish(gw, cs);
    SB;
#pragma unroll
    for (int j = 8; j < 27; ++j) c[j] = __builtin_amdgcn_raw_buffer_load_b128(rs1, cv1 + j * st1, 0, 0);
    SB;
    STAMP(11);
#pragma unroll
    for (int k = 0; k < 8; ++k) cs[k] = 0.f;
#pragma unroll
    for (int j = 0; j < 27; ++j)
#pragma unroll
      for (int k = 0; k < 4; ++k) { cs[2 * k] = dot2(c[j][k], k10, cs[2 * k]); cs[2 * k + 1] = dot2(c[j][k], k01, cs[2 * k + 1]); }
    cs_publish(gw + 126, cs);
    STAMP(12);
#pragma unroll 1
    for (unsigned i = w; i < 144; i += 8) dma16(i * 1024, v2, rsWd, urow(i >> 2, rot) + (i & 3) * 10240u + cb * 128u);
    drain_vm();
    bar();
  }
  if (!has2) { STAMP_END; return; }
  relane();

  const unsigned rbA = (G * 6) >> 4, rbB = (G * 6 + 5 > 127 ? 127 : G * 6 + 5) >> 4;
  if (w >= 4) {
    __builtin_amdgcn_s_setprio(HPRIO);
    const unsigned lw = w - 4, ld0 = lw * 1024;
    gu32* fo = (gu32*)(p.ws + WS_FLAGO + ((lane < 32 ? rbA : rbB) * 32 + (lane & 3)) * FS);
    gu32* fh = (gu32*)(p.ws + WS_FLAGH + ((lane < 32 ? rbA : rbB) * 32 + ((lane & 31) < 18 ? lane & 31 : 17)) * FS);
    __builtin_amdgcn_s_sleep(HSLEEP);
    if (jtype == J_KV) {
#pragma unroll
      for (int u = 31; u < 36; ++u) dma16(u * 4096 + ld0, v2, rsWd, w2_off(W, u, lw));
    }
    if (jtype != J_MLP) {
#pragma unroll
      for (int u = 0; u < NPRE; ++u) { pre2[u] = w2_load(W, 36 + u, lw, v2); SB; }
    }
#pragma unroll
    for (int u = 36 + NPRE; u < PSPLIT; ++u) { pre2[u - 36] = w2_load(W, u, lw, v2); SB; }
    if (w == 7) {
      __builtin_amdgcn_s_setprio(3);
      const unsigned v = poll_flags(fh, (lane & 31) < 18 && (lane & 31) >= 10, tag);
      lds_set(L_GO);
      if (!all64((lane & 31) >= 18 || v == tag)) poll_flags(fh, (lane & 31) < 18, tag);
      lds_set(L_GO + 4);
      __builtin_amdgcn_s_setprio(HPRIO);
    }
#pragma unroll
    for (int u = PSPLIT; u < HSPLIT; ++u) { pre2[u - 36] = w2_load(W, u, lw, v2); SB; }
    STAMP(8);
    unsigned la[3], wb[3], xo[3], st[3];
    u32x4 xr[3];
    const unsigned ch = lane & 7;
#pragma unroll
    for (int i = 0; i < 3; ++i) {
      const unsigned row = lw * 24 + i * 8 + (lane >> 3), rl = row >> 4, rr = row & 15;
      la[i] = (rl >= 3 ? 24576u + (rl - 3) * 4096u : rl * 4096u) + (ch >> 1) * 1024 + (ch & 1) * 512 + ((rr + 2 * ch) & 15) * 16;
      xo[i] = (G * 96 + row) * 1280 + cb * 128 + ch * 16;
      wb[i] = ld0 + lane * 16 + i * 49152;
      asm volatile("" : "+v"(la[i]), "+v"(wb[i]), "+v"(xo[i]));
    }
    float ri[3], rl[3];
    unsigned of = 0;
    f32x4 xf[3][2], dd[3][2], d2[3][2];
#pragma unroll
    for (int k = 2; k <= 6; ++k) {
      if (k == O_BAR && w == 7 && !all64((lane & 31) >= 4 || of == tag)) poll_flags(fo, (lane & 31) < 4, tag);
      if (k == 6) {
        wait_vm<0>();
#pragma unroll
        for (int h = 0; h < 3; ++h) {
          ri[h] = 2.0f * bfhi(st[h]) * bfhi(st[h]);
          rl[h] = -1.4426950408889634f * __builtin_amdgcn_rcpf(ri[h]);
          xf[h][0] = f32x4{bflo(xr[h][0]), bfhi(xr[h][0]), bflo(xr[h][1]), bfhi(xr[h][1])};
          xf[h][1] = f32x4{bflo(xr[h][2]), bfhi(xr[h][2]), bflo(xr[h][3]), bfhi(xr[h][3])};
          asm volatile("" : "+v"(ri[h]), "+v"(rl[h]), "+v"(xf[h][0]), "+v"(xf[h][1]));
        }
      }
      lbar();
      if (k == O_BAR - 1) { of = __hip_atomic_load(fo, RLX_AGENT); SB; }
      if (k < 6) {
        const int kk = k - 1;
#pragma unroll
        for (int u = 9 * (kk - 1) + ((kk - 1) & 1); u < 9 * kk + (kk & 1); ++u) {
          lds_st<u32x4>(wb[u / 12] + (u % 12) * 4096, pre2[u]);
          if (WSLEEP == 1) __builtin_amdgcn_s_sleep(1);
          if (WSLEEP == 2) asm volatile("s_nop 7");
          SB;
        }
      }
      if (k == 2 || k == 3) {
        constexpr int mid = (HSPLIT + 74) / 2 < 72 ? (HSPLIT + 74) / 2 : 72;
#pragma unroll
        for (int u = (k == 2 ? HSPLIT : mid); u < (k == 2 ? mid : 72); ++u) { pre2[u - 36] = w2_load(W, u, lw, v2); SB; }
        if (k == 3) { dma16(36 * 4096 + ld0, v2, W.wo, w2_off(W, 72, lw)); dma16(37 * 4096 + ld0, v2, W.wo, w2_off(W, 73, lw)); }
      }
      if (k == XK) {
#pragma unroll
        for (int h = 0; h < 3; ++h) {
          const unsigned row = G * 96 + lw * 24 + h * 8 + (lane >> 3);
          st[h] = __builtin_amdgcn_raw_buffer_load_b32(rsWS, WS_STAT + (row < S ? row : 0) * 8, 0, 16);
          xr[h] = __builtin_amdgcn_raw_buffer_load_b128(rsX, xo[h], 0, 0);
        }
      }
    }
    bar();
#pragma unroll
    for (int i = 0; i < 3; ++i) {
      dd[i][0] = lds_ld<f32x4>(la[i]); d2[i][0] = lds_ld<f32x4>(la[i] + 12288);
      dd[i][1] = lds_ld<f32x4>(la[i] + 256); d2[i][1] = lds_ld<f32x4>(la[i] + 12288 + 256);
      la[i] += 49152;
      asm volatile("" : "+v"(la[i]));
    }
    __builtin_amdgcn_s_setprio(GPRIO);
#pragma unroll
    for (int i = 0; i < 3; ++i) {
      float v[8], e[8];
#pragma unroll
      for (int j = 0; j < 8; ++j) v[j] = sadd(dd[i][j >> 2][j & 3], d2[i][j >> 2][j & 3]);
      SB;
#pragma unroll
      for (int j = 0; j < 8; ++j) e[j] = smul(v[j], rl[i]);
      SB;
#pragma unroll
      for (int j = 0; j < 8; ++j) e[j] = __builtin_amdgcn_exp2f(e[j]);
      SB;
#pragma unroll
      for (int j = 0; j < 8; ++j) e[j] = sfma(e[j], ri[i], ri[i]);
      SB;
#pragma unroll
      for (int j = 0; j < 8; ++j) e[j] = __builtin_amdgcn_rcpf(e[j]);
      SB;
#pragma unroll
      for (int j = 0; j < 8; ++j) dd[i][j >> 2][j & 3] = smul(v[j], e[j]);
      asm volatile("" : "+v"(dd[i][0]), "+v"(dd[i][1]));
      SB;
    }
    const rsrc_t rsO = mk_rsrc(p.out, S * D * 2);
    STAMP(9);
    bar();
    f32x4 ss[3][2];
#pragma unroll
    for (int i = 0; i < 3; ++i) {
      ss[i][0] = lds_ld<f32x4>(la[i]) + lds_ld<f32x4>(la[i] + 12288);
      ss[i][1] = lds_ld<f32x4>(la[i] + 256) + lds_ld<f32x4>(la[i] + 12288 + 256);
    }
#pragma unroll
    for (int i = 0; i < 3; ++i)
      __builtin_amdgcn_raw_buffer_store_b128(cat2(pk4(xf[i][0] + ss[i][0] * dd[i][0]), pk4(xf[i][1] + ss[i][1] * dd[i][1])), rsO, xo[i], 0, 0);
    STAMP(5);
    STAMP_END;
    return;
  }
  {
    const unsigned kh = w & 1, rt = G * 6 + 3 * (w >> 1);
    const rsrc_t rsH = mk_rsrc(p.ws + WS_HA + (rt < 128 ? rt : 0) * (NSTEP * 1024), rt < 128 ? (rt > 125 ? 128 - rt : 3) * (NSTEP * 1024) : 0);
    unsigned tb[3][4];
#pragma unroll
    for (int t = 0; t < 4; ++t) {
      tb[0][t] = (4 * g + q4) * 128 + ((t ^ ((2 * g + (q4 >> 1)) & 3)) << 5) + 8 * p4 + kh * 4096;
      tb[1][t] = tb[0][t] + 49152; tb[2][t] = tb[0][t] + 98304;
      asm volatile("" : "+v"(tb[0][t]), "+v"(tb[1][t]), "+v"(tb[2][t]));
    }
    auto rd1 = [&](int i, int t, int hf) {
      const int sl = i == 36 ? 36 : (2 * i) % 36, bi = sl / 12 > 2 ? 2 : sl / 12;
      return __builtin_bit_cast(u32x2, lds_tr(tb[bi][t] + (sl - 12 * bi) * 4096 + hf * 2048));
    };
    constexpr int NW = P2NW, W0 = P2W0;
    constexpr int OI = O_BAR == 4 ? 17 : (O_BAR == 5 ? 22 : 26);
    constexpr int OB0 = OI == 26 ? NW - 1 : 0;
    f32x4 acc[2][3][4];
    u32x4 a[NW][3];
    u32x2 bw[2][4][2];
    unsigned va[3];
#pragma unroll
    for (int h = 0; h < 3; ++h) va[h] = lane * 16 + h * NSTEP * 1024;
    unsigned xa[4];
#pragma unroll
    for (int t = 0; t < 4; ++t) {
      xa[t] = w * 12288u + t * 1024 + g * 256 + ((r + 4 * t + (g & 2)) & 15) * 16;
      asm volatile("" : "+v"(xa[t]));
    }
    auto put = [&](auto Z, auto E) {
      constexpr int z = Z.v, t = E.v / 3, h = 2 - E.v % 3;
      asm volatile("" : "+v"(acc[z][h][t]));
      lds_st<f32x4>(xa[t] + z * 49152 + h * 4096, acc[z][h][t]);
    };
    unsigned so = (rot + kh) * 1024;
    auto lda = [&](auto I, auto H) {
      constexpr int i = I.v, h = H.v;
      if constexpr (i < 27) {
        if (h == 0 && so >= 54 * 1024) so -= 54 * 1024;
        a[i % NW][h] = __builtin_amdgcn_raw_buffer_load_b128(rsH, va[h], so, 16);
        if (h == 2) so += 2048;
      } else {
        a[i % NW][h] = __builtin_amdgcn_raw_buffer_load_b128(rsH, va[h], (2 * i + kh) * 1024, 16);
      }
      SB;
    };
    lds_wait(L_GO);
    unsigned go2 = 0;
    STAMP(8);
    static_for<0, W0>([&](auto I) { lda(I, IC<0>{}); lda(I, IC<1>{}); lda(I, IC<2>{}); });
#pragma unroll
    for (int t = 0; t < 4; ++t) { bw[0][t][0] = rd1(0, t, 0); bw[0][t][1] = rd1(0, t, 1); }
    static_for<0, 37>([&](auto I) {
      constexpr int i = I.v;
      constexpr int lim = i + NW - 1;
      constexpr int nxt = i <= OI ? (W0 + 2 * i < lim ? W0 + 2 * i : lim) : (27 + OB0 + 2 * (i - OI - 1) < lim ? 27 + OB0 + 2 * (i - OI - 1) : lim);
      constexpr int end = i <= OI ? 27 : 37;
      constexpr int l0 = nxt < end ? nxt : -1, l1 = (nxt + 1 <= lim && nxt + 1 < end) ? nxt + 1 : -1;
      SB;
      static_for<0, 12>([&](auto E) {
        constexpr int e = E.v, t = e / 3, h = e % 3;
        const u32x4 bf = cat2(bw[i & 1][t][0], bw[i & 1][t][1]);
        if constexpr (i == 0 || i == 27) mfma_z(acc[i >= 27][2 - h][t], bf, a[i % NW][2 - h]); else mfma_ip(acc[i >= 27][2 - h][t], bf, a[i % NW][2 - h]);
        SB;
        if constexpr (i == 4 && e == 2) go2 = lds_get(L_GO + 4);
        if constexpr ((i == 27 || i == 28) && (e & 1)) put(IC<0>{}, IC<(i - 27) * 6 + e / 2>{});
        if constexpr (i == 36 && e >= 8) put(IC<1>{}, IC<e - 8>{});
        if constexpr (i < 36 && h < 2) bw[(i + 1) & 1][t][h] = rd1(i + 1, t, h);
        if constexpr (l1 < 0) { if constexpr (h == 2 && t < 3 && l0 >= 0) lda(IC<l0>{}, IC<t>{}); }
        else if constexpr (e & 1) lda(IC<(e < 6 ? l0 : l1)>{}, IC<(e >> 1) % 3>{});
        SB;
      });
      if constexpr (i == 29) { asm volatile("s_waitcnt lgkmcnt(8)" ::: "memory"); bar(); }
      if constexpr (i == 4) { if (__builtin_expect(!rfl(go2), 0)) lds_wait(L_GO + 4); asm volatile("" ::: "memory"); STAMP(17); }
      if constexpr (i > 4 && i < 27 && (i % 9 == 4 || i % 9 == 8)) {
        bar();
        if constexpr (i == 26 && OI == 26) static_for<27, 27 + OB0>([&](auto J) { lda(J, IC<0>{}); lda(J, IC<1>{}); lda(J, IC<2>{}); });
        STAMP(17 + (2 * i) / 9);
      }
    });
    STAMP(24);
    static_for<4, 12>([&](auto E) { if constexpr (E.v == 8) asm volatile("s_nop 3" ::: "memory"); put(IC<1>{}, E); SB; });
    lbar();
    STAMP(5);
    STAMP_END;
  }
}

extern "C" void kernel_launch(void* const* d_in, const int* in_sizes, int n_in, void* d_out, int out_size, void* d_ws,
                              size_t ws_size, hipStream_t stream) {
  Params p;
  p.x = d_in[0]; p.wqkv = d_in[1]; p.wo = d_in[2]; p.wg = d_in[4]; p.wu = d_in[5]; p.wd = d_in[6];
  p.out = d_out; p.ws = (char*)d_ws;
  fused_block<<<256, 512, 0, stream>>>(p);
}
```

```cpp
#include <hip/hip_runtime.h>
#include <stdint.h>

typedef __attribute__((ext_vector_type(8))) __bf16 bf16x8;
typedef __attribute__((ext_vector_type(4))) __bf16 bf16x4;
typedef __attribute__((ext_vector_type(2))) __bf16 bf16x2;
typedef __attribute__((ext_vector_type(4))) float f32x4;
typedef __attribute__((ext_vector_type(2))) float f32x2;
typedef __attribute__((ext_vector_type(4))) unsigned u32x4;
typedef __attribute__((ext_vector_type(2))) unsigned u32x2;
typedef __amdgpu_buffer_rsrc_t rsrc_t;
#define LAS __attribute__((address_space(3)))
#define GAS __attribute__((address_space(1)))
#define DI __device__ __forceinline__
#define RLX_AGENT __ATOMIC_RELAXED, __HIP_MEMORY_SCOPE_AGENT
typedef GAS unsigned gu32;
typedef GAS unsigned long long gu64;

constexpr int S = 2048, D = 640, F = 1728;
constexpr int NCOL = 5376;
constexpr int NSTEP = 74;
constexpr unsigned WS_EPOCH = 0;
#define FS 128
constexpr unsigned WS_FLAGH = 768u << 10;
constexpr unsigned WS_FLAGO = WS_FLAGH + 8 * 32 * FS;
constexpr unsigned WS_FLAGKV = WS_FLAGO + 8 * 32 * FS;
constexpr unsigned WS_STAT = 4096;
constexpr unsigned WS_CSUM = WS_STAT + S * 8;
constexpr unsigned WS_KVP = WS_CSUM + 3 * NCOL * 8;
constexpr unsigned WS_HA = 1u << 20;
constexpr unsigned WS_DBG = 12u << 20;
constexpr unsigned WS_END = 13u << 20;
constexpr unsigned L_B = 65536;
constexpr unsigned L_SCR = 147456;
constexpr unsigned L_MISC = 159744;
constexpr unsigned L_GO = L_MISC + 3072;
constexpr unsigned SPIN_CAP = 1u << 18;
#define HSLEEP 4
#define HSPLIT 64
#define PPIPE 0
#define STA 0
#define STB 2
#define SNAKE 0
#define EORD 0
#define EARLY1 1
#define ROTW 1
#define NPRE 12
#define WSCH 0
#define PSPLIT 48
#define XK 2
#define GRP 1
#define GPRIO 0
#define HPRIO 0
#define PSLEEP 2
#define P2NW 7
#define P2W0 6
#define WSLEEP 0
#define ROTM 18
#define NA1 2
#define O_BAR 5
#define PRIO_MODE 0
#define DMA_SPACE_A T
#define DMA_SPACE_B (4 * T / 3)

struct Params {
  const void* x; const void* wqkv; const void* wo; const void* wg; const void* wu; const void* wd;
  void* out; char* ws;
};

__shared__ __attribute__((aligned(16))) char smem[163840];

DI float sadd(float a, float b) { float o; asm("v_add_f32 %0, %1, %2" : "=v"(o) : "v"(a), "v"(b)); return o; }
DI float smul(float a, float b) { float o; asm("v_mul_f32 %0, %1, %2" : "=v"(o) : "v"(a), "v"(b)); return o; }
DI float sfma(float a, float b, float c) { float o; asm("v_fma_f32 %0, %1, %2, %3" : "=v"(o) : "v"(a), "v"(b), "v"(c)); return o; }
DI unsigned bsel(unsigned m, unsigned a, unsigned b) { unsigned o; asm("v_bfi_b32 %0, %1, %2, %3" : "=v"(o) : "v"(m), "v"(a), "v"(b)); return o; }
DI float bself(unsigned m, float a, float b) { return __builtin_bit_cast(float, bsel(m, __builtin_bit_cast(unsigned, a), __builtin_bit_cast(unsigned, b))); }
DI bool all64(bool c) { return __builtin_amdgcn_ballot_w64(!c) == 0; }
DI float shfl_x(float v, unsigned lane, unsigned m) {
  return __builtin_bit_cast(float, __builtin_amdgcn_ds_bpermute((int)((lane ^ m) << 2), __builtin_bit_cast(int, v)));
}
DI unsigned umin(unsigned a, unsigned b) { return a < b ? a : b; }
DI unsigned rfl(unsigned v) { return __builtin_amdgcn_readfirstlane(v); }
DI rsrc_t mk_rsrc(const void* p, unsigned bytes) { return __builtin_amdgcn_make_buffer_rsrc((void*)p, (short)0, (int)bytes, 0x00020000); }
DI void dma16(unsigned ldsdst, unsigned voff, rsrc_t rs, unsigned soff) {
  asm volatile("s_mov_b32 m0, %0\n\ts_nop 0\n\tbuffer_load_dwordx4 %1, %2, %3 offen lds" ::"s"(ldsdst), "v"(voff), "s"(rs), "s"(soff) : "memory");
}
template <int N> DI void wait_vm() { asm volatile("s_waitcnt vmcnt(%0)" ::"i"(N) : "memory"); }
DI void drain_vm() { wait_vm<0>(); __builtin_amdgcn_s_waitcnt(0x0F70); }
DI void wait_lgkm0() { asm volatile("s_waitcnt lgkmcnt(0)" ::: "memory"); }
DI void bar() { asm volatile("" ::: "memory"); __builtin_amdgcn_s_barrier(); asm volatile("" ::: "memory"); }
DI void lbar() { wait_lgkm0(); bar(); }
template <class V> DI V lds_ld(unsigned a) { return *(LAS V*)((LAS char*)smem + a); }
template <class V> DI void lds_st(unsigned a, V v) { *(LAS V*)((LAS char*)smem + a) = v; }
DI bf16x4 lds_tr(unsigned a) { return __builtin_amdgcn_ds_read_tr16_b64_v4bf16((LAS bf16x4*)((LAS char*)smem + a)); }
DI bf16x8 cat(bf16x4 a, bf16x4 b) { return __builtin_shufflevector(a, b, 0, 1, 2, 3, 4, 5, 6, 7); }
DI f32x4 mfma(bf16x8 a, bf16x8 b, f32x4 c) { return __builtin_amdgcn_mfma_f32_16x16x32_bf16(a, b, c, 0, 0, 0); }
DI unsigned pk2(float a, float b) { f32x2 v = {a, b}; return __builtin_bit_cast(unsigned, __builtin_convertvector(v, bf16x2)); }
DI u32x2 pk4(f32x4 v) { u32x2 r = {pk2(v[0], v[1]), pk2(v[2], v[3])}; return r; }
DI bf16x8 frag(u32x2 lo, u32x2 hi) { u32x4 v = {lo[0], lo[1], hi[0], hi[1]}; return __builtin_bit_cast(bf16x8, v); }
DI float bflo(unsigned v) { return __builtin_bit_cast(float, v << 16); }
DI float bfhi(unsigned v) { return __builtin_bit_cast(float, v & 0xffff0000u); }
DI float rbf(float v) { return bflo(pk2(v, 0.f)); }
DI float relu2(float v) { float o; asm("v_add_f32 %0, %1, |%1|" : "=v"(o) : "v"(v)); return o; }
DI f32x4 phi4(f32x4 u, f32x4 rl, f32x4 rh) {
  const f32x4 l = u * rl;
  f32x4 e, a;
#pragma unroll
  for (int j = 0; j < 4; ++j) {
    e[j] = __builtin_amdgcn_fmed3f(__builtin_amdgcn_exp2f(l[j]), 0.f, 1.f);
    a[j] = relu2(u[j]);
  }
  return a * rh + e + 1e-6f;
}
DI f32x4 splat(float v) { return f32x4{v, v, v, v}; }
template <int CTRL> DI float dpp_add(float v) {
  return v + __builtin_bit_cast(float, __builtin_amdgcn_update_dpp(0, __builtin_bit_cast(int, v), CTRL, 0xf, 0xf, true));
}
DI float dot2(unsigned a, unsigned b, float c) {
  return __builtin_amdgcn_fdot2_f32_bf16(__builtin_bit_cast(bf16x2, a), __builtin_bit_cast(bf16x2, b), c, false);
}
DI u32x4 ldtr2(unsigned a, unsigned d) { return __builtin_bit_cast(u32x4, cat(lds_tr(a), lds_tr(a + d))); }
DI void mfma_ip(f32x4& c, const u32x4& a, const u32x4& b) { asm volatile("v_mfma_f32_16x16x32_bf16 %0, %1, %2, %0" : "+v"(c) : "v"(a), "v"(b)); }
DI void mfma_c(f32x4& d, const u32x4& a, const u32x4& b, const f32x4& c) { asm volatile("v_mfma_f32_16x16x32_bf16 %0, %1, %2, %3" : "=&v"(d) : "v"(a), "v"(b), "v"(c)); }
DI void mfma_z(f32x4& c, const u32x4& a, const u32x4& b) { asm volatile("v_mfma_f32_16x16x32_bf16 %0, %1, %2, 0" : "=&v"(c) : "v"(a), "v"(b)); }
template <int N> DI void acc_fence(f32x4* c) {
  asm volatile("s_nop 7" ::: "memory");
#pragma unroll
  for (int i = 0; i < N; ++i) asm volatile("" : "+v"(c[i]));
}
DI u32x4 cat2(u32x2 a, u32x2 b) { return u32x4{a[0], a[1], b[0], b[1]}; }
DI u32x2 ldtr1(unsigned a) { return __builtin_bit_cast(u32x2, lds_tr(a)); }
template <int IMM> DI void set_m0i(unsigned v) { asm volatile("s_add_i32 m0, %0, %1" ::"s"(v), "i"(IMM) : "memory", "scc"); }
template <int OFF> DI void dma_goi(unsigned voff, rsrc_t rs, unsigned soff) {
  asm volatile("buffer_load_dwordx4 %0, %1, %2 offen offset:%3 lds" ::"v"(voff), "s"(rs), "s"(soff), "i"(OFF) : "memory");
}
DI void set_m0(unsigned v) { asm volatile("s_mov_b32 m0, %0" ::"s"(v) : "memory"); }
DI void dma_go(unsigned voff, rsrc_t rs, unsigned soff) {
  asm volatile("buffer_load_dwordx4 %0, %1, %2 offen lds" ::"v"(voff), "s"(rs), "s"(soff) : "memory");
}
DI f32x4 mfma_u(u32x4 a, u32x4 b, f32x4 c) { return mfma(__builtin_bit_cast(bf16x8, a), __builtin_bit_cast(bf16x8, b), c); }
struct Stamps { unsigned long long t; };
#define STAMP_DECL Stamps stv_ = {0ull}
#define STAMP_ARG , Stamps& stv_
#define STAMP_PASS , stv_
#define STAMPX(c, i) do { if ((i) == 13) { asm volatile("s_memrealtime %0\n\ts_waitcnt lgkmcnt(0)" : "=s"(stv_.t)); } } while (0)
#define STAMP_END do { if (blockIdx.x == 160 && threadIdx.x == 0) __hip_atomic_store((gu64*)(p.ws + WS_DBG), stv_.t, RLX_AGENT); } while (0)
#define STAMP(i) STAMPX(__builtin_amdgcn_workitem_id_x() == 0, i)

DI unsigned poll_flags(gu32* word, bool active, unsigned tag) {
  unsigned v = 0;
#pragma unroll 1
  for (unsigned spins = 0; spins < SPIN_CAP; ++spins) {
    v = __hip_atomic_load(word, RLX_AGENT);
    if (all64(!active || v == tag)) break;
    if (PSLEEP) __builtin_amdgcn_s_sleep(PSLEEP);
  }
  asm volatile("" ::: "memory");
  return v;
}

DI void lds_set(unsigned a) { __hip_atomic_store((LAS unsigned*)smem + a / 4, 1u, __ATOMIC_RELAXED, __HIP_MEMORY_SCOPE_WORKGROUP); }
DI unsigned lds_get(unsigned a) { return __hip_atomic_load((LAS unsigned*)smem + a / 4, __ATOMIC_RELAXED, __HIP_MEMORY_SCOPE_WORKGROUP); }
DI void lds_wait(unsigned a) {
#pragma unroll 1
  for (unsigned spins = 0; spins < (SPIN_CAP << 4); ++spins) {
    if (rfl(lds_get(a))) break;
    __builtin_amdgcn_s_sleep(1);
  }
  asm volatile("" ::: "memory");
}

template <int I> struct IC { static constexpr int v = I; };
template <int B, int E, class Fn> DI void static_for(Fn&& f) {
  if constexpr (B < E) { f(IC<B>{}); static_for<B + 1, E>(f); }
}

enum { J_MLP = 0, J_KV = 1, J_Q = 2, J_SPARE = 3 };

struct Dma {
  rsrc_t rsA, rsB;
  unsigned vA0, vA1, vB0, vB1, vB2;
  unsigned sA[4], sB, advB;
  unsigned wa, wq;
  unsigned mA, mB, mW;
};

constexpr unsigned a_slot(unsigned s3) { return s3 == 0 ? 126976u : (s3 - 1) * 32768u; }
#define SB __builtin_amdgcn_sched_barrier(0)

template <int T> DI unsigned bofs(const unsigned (&boff)[T], int t) { return T == 6 && t >= 3 ? boff[t - 3] + 6144 : boff[t]; }
template <int T, bool TR, int KS, class Extra>
DI void kstep(f32x4 (&acc)[4][T], const u32x4 (&ac)[4], const u32x2 (&bc)[T][2], u32x4 (&an)[4], u32x2 (&bn)[T][2],
              const unsigned (&aoff)[4], const unsigned (&boff)[T], const Dma& d, Extra&& extra) {
  constexpr int s = KS >> 1, KIND = KS & 1, NP = KIND == 0 ? 4 : 3, ST = KIND == 0 ? (STA ? STA : (4 * T - 1) / NP) : (STB ? STB : (4 * T - 1) / NP);
  constexpr int sn = (KS + 1) >> 1;
  constexpr unsigned asl = a_slot(sn % 3), bsl = ((KS + 1) % 5) * 12288u;
  static_for<0, 4 * T>([&](auto E) {
    constexpr int e = E.v, t = e >> 2, i = (SNAKE && (t & 1)) ? 3 - (e & 3) : (e & 3);
    const u32x4 bf = cat2(bc[t][0], bc[t][1]);
    if constexpr (KS == 0) { if (TR) mfma_z(acc[i][t], bf, ac[i]); else mfma_z(acc[i][t], ac[i], bf); }
    else { if (TR) mfma_ip(acc[i][t], bf, ac[i]); else mfma_ip(acc[i][t], ac[i], bf); }
    SB;
    if constexpr (KS < 19) {
      if constexpr (e < 4) an[e] = lds_ld<u32x4>(aoff[((KS + 1) & 1) + (sn % 3 == 0 ? 2 : 0)] + (sn % 3 == 0 ? 0u : asl) + e * 2048);
      else if constexpr (e < 4 + 2 * T) bn[(e - 4) >> 1][e & 1] = ldtr1(bofs<T>(boff, (e - 4) >> 1) + bsl + (e & 1) * 768);
    }
    constexpr int pc = e / ST;
    if constexpr (KIND == 0) {
      constexpr int adv = s < 8 ? (s + 2) * 128 : 0, dst = (int)a_slot((s + 2) % 3) + pc * 1024, off = dst >= adv ? adv : 0;
      if constexpr (pc < NP && e % ST == ST - 2) set_m0i<dst - off>(d.mA);
      if constexpr (pc < NP && e % ST == ST - 1) dma_goi<off>((pc & 1) ? d.vA1 : d.vA0, d.rsA, d.sA[pc] + (adv - off));
    } else {
      constexpr int h0 = (2 * s) % 5;
      if constexpr (pc < NP && e % ST == ST - 2) set_m0i<(h0 == 4 ? 0 : h0 * 12288) + pc * 1024>(h0 == 4 ? d.mW : d.mB);
      if constexpr (pc < NP && e % ST == ST - 1) dma_goi<0>(pc == 0 ? d.vB0 : (pc == 1 ? d.vB1 : d.vB2), d.rsB, d.sB);
    }
    extra(E);
    SB;
  });
}

struct RowStat { u32x4 xa; unsigned xb, row; float s, q; };
DI void row_load(RowStat& R, rsrc_t rsX, unsigned lane) {
  asm volatile("buffer_load_dwordx4 %0, %2, %4, %5 offen\n\tbuffer_load_dword %1, %3, %4, %5 offen offset:1024"
               : "=&v"(R.xa), "=&v"(R.xb) : "v"(lane * 16), "v"(lane * 4), "s"(rsX), "s"(R.row * 1280) : "memory");
}
template <int K> DI void row_step(RowStat& R, unsigned lane) {
  if constexpr (K < 5) {
    if constexpr (K == 0) { R.s = 0.f; R.q = 0.f; }
    const unsigned v = K < 4 ? R.xa[K & 3] : R.xb;
    asm volatile("v_dot2c_f32_bf16 %0, %2, %3\n\tv_dot2c_f32_bf16 %1, %2, %2" : "+v"(R.s), "+v"(R.q) : "v"(v), "v"(0x3f803f80u));
  } else if constexpr (K < 11) {
    constexpr int C = K == 5 ? 0xB1 : (K == 6 ? 0x4E : (K == 7 ? 0x141 : (K == 8 ? 0x140 : (K == 9 ? 0x142 : 0x143))));
    if constexpr (K == 5) asm volatile("s_nop 2" : "+v"(R.s), "+v"(R.q));
    R.s = dpp_add<C>(R.s); R.q = dpp_add<C>(R.q);
  } else if constexpr (K == 11) {
    R.s *= 1.0f / D;
    R.q = __builtin_fmaxf(R.q * (1.0f / D) - R.s * R.s, 0.f);
  } else if constexpr (K == 12) {
    R.q = __builtin_amdgcn_sqrtf(rbf(rbf(R.q) + 1e-5f));
  } else {
    lds_st<unsigned>(lane == 63 ? L_GO + 32 + (R.row & 7) * 4 : L_GO + 64, pk2(R.s, R.q));
  }
  if constexpr (K >= 5 && K < 13) asm volatile("" : "+v"(R.s), "+v"(R.q));
}
DI void row_publish(const RowStat& R, char* ws, unsigned tag, unsigned tid) {
  if (tid < 8) __hip_atomic_store((gu64*)(ws + WS_STAT) + R.row + tid, ((unsigned long long)tag << 32) | lds_ld<unsigned>(L_GO + 32 + tid * 4), RLX_AGENT);
}

struct Gran { u32x2 g0, g1, g2; unsigned off, step; };
DI void gran_issue(Gran& G, rsrc_t rs) {
  asm volatile("buffer_load_dwordx2 %0, %3, %4, 0 offen sc1\n\tbuffer_load_dwordx2 %1, %3, %4, %5 offen sc1\n\t"
               "buffer_load_dwordx2 %2, %3, %4, %6 offen sc1"
               : "=&v"(G.g0), "=&v"(G.g1), "=&v"(G.g2) : "v"(G.off), "s"(rs), "s"(G.step), "s"(2 * G.step) : "memory");
}
DI void gran_load(Gran& G, rsrc_t rs) {
  asm volatile("buffer_load_dwordx2 %0, %3, %4, 0 offen sc1\n\tbuffer_load_dwordx2 %1, %3, %4, %5 offen sc1\n\t"
               "buffer_load_dwordx2 %2, %3, %4, %6 offen sc1\n\ts_waitcnt vmcnt(0)"
               : "=&v"(G.g0), "=&v"(G.g1), "=&v"(G.g2) : "v"(G.off), "s"(rs), "s"(G.step), "s"(2 * G.step) : "memory");
}

DI unsigned urow(unsigned pos, unsigned rot) { const unsigned u = pos + rot; return (u >= 54 ? u - 54 : u) * 40960u; }

constexpr unsigned wo_tile(unsigned v) { return (v / 5) * 10 + (v % 5 == 4 ? 4 : (v % 5 < 2 ? 2 * (v % 5) : 2 * (v % 5) + 1)); }
struct W2 { rsrc_t wd, wo; unsigned rot, col; };
DI unsigned w2_off(const W2& W, int u, unsigned lw) {
  if (u < 54) return urow(u, W.rot) + lw * 10240u + W.col;
  return wo_tile(u - 54) * 20480u + (lw >> 1) * ((u - 54) % 5 == 4 ? 102400u : 20480u) + (lw & 1) * 10240u + W.col;
}
DI u32x4 w2_load(const W2& W, int u, unsigned lw, unsigned voff) {
  return __builtin_amdgcn_raw_buffer_load_b128(u < 54 ? W.wd : W.wo, voff, w2_off(W, u, lw), 0);
}

template <int T, bool TR, bool PF, bool LASTA, class Pre>
DI void main_loop(Pre&& pre, RowStat& R, char* ws, f32x4 (&acc)[4][T], unsigned (&boff)[T], Dma& d, unsigned w, unsigned lane, unsigned tid,
                  unsigned epoch, unsigned& tag, unsigned rb, unsigned ccol, rsrc_t rsB2, rsrc_t rsWS, unsigned cb, unsigned rot, u32x4 (&pre2)[36] STAMP_ARG) {
  const unsigned g = lane >> 4, r = lane & 15, m = w & 3;
  unsigned aoff[4];
  aoff[0] = (64 * m + r) * 128 + ((g ^ (r >> 1)) << 4);
  aoff[1] = aoff[0] ^ 64;
  aoff[2] = aoff[0] + a_slot(0); aoff[3] = aoff[1] + a_slot(0);
  const unsigned wq = d.wq, wa = d.wa;
  asm volatile("" : "+v"(aoff[0]), "+v"(aoff[1]), "+v"(aoff[2]), "+v"(aoff[3]), "+v"(boff[0]), "+v"(boff[1]), "+v"(boff[2]));
  if (T == 5) asm volatile("" : "+v"(boff[3]), "+v"(boff[4]));
  Gran G;
  unsigned v2, vb2, vpf, tA[3], tB[3];
  row_load(R, d.rsA, lane);
  {
    asm volatile("s_waitcnt vmcnt(%2) lgkmcnt(0)\n\tv_readfirstlane_b32 %0, %1\n\ts_nop 3" : "=s"(tag) : "v"(epoch), "i"(5 + NA1) : "memory");
    tag = rfl(tag) + 1;
    bar();
  }
  STAMP(1);
  u32x4 a0[4], a1[4];
  u32x2 b0[T][2], b1[T][2];
#pragma unroll
  for (int i = 0; i < 4; ++i) a0[i] = lds_ld<u32x4>(aoff[2] + i * 2048);
#pragma unroll
  for (int t = 0; t < T; ++t) { b0[t][0] = ldtr1(bofs<T>(boff, t)); b0[t][1] = ldtr1(bofs<T>(boff, t) + 768); }
  SB;
#pragma unroll
  for (int i = NA1; i < 4; ++i) dma16(a_slot(1) + d.mA + i * 1024, (i & 1) ? d.vA1 : d.vA0, d.rsA, d.sA[i] + 128);
  const bool y = w >= 4;
  bool ok = true;
  auto ln_store = [&] {
    const float v0 = y ? -bflo(G.g0[0]) : __builtin_bit_cast(float, G.g0[0]) + __builtin_bit_cast(float, G.g1[0]) + __builtin_bit_cast(float, G.g2[0]);
    const unsigned la = L_MISC + (y ? tid * 4 - 1024 : 2048 + tid * 4);
    lds_st<float>(la, v0);
    lds_st<float>(y ? la + 1024 : L_GO + 64 + (lane & 7) * 4, __builtin_amdgcn_rcpf(bfhi(G.g0[0])));
  };
  static_for<0, 10>([&](auto S_) {
    constexpr int s = S_.v, L = 4 * T;
    constexpr int NX = (PF && s >= 1 && 2 * s <= NPRE) ? 2 : 0;
    kstep<T, TR, 2 * s>(acc, a0, b0, a1, b1, aoff, boff, d, [&](auto E) {
      constexpr int e = E.v;
      if constexpr (s == 2) {
        if constexpr (e == 2) { vb2 = v2 + (3 * wq) * 10240u; asm volatile("" : "+v"(vb2)); }
        if constexpr (e == 5) { G.off = w < 4 ? WS_CSUM + ccol * 8 : WS_STAT + (rb * 256 + tid - 256) * 8; asm volatile("" : "+v"(G.off)); }
        if constexpr (e == 8) { G.step = w < 4 ? NCOL * 8 : 0; tA[0] = urow(wa, rot) + cb * 128u; asm volatile("" : "+s"(G.step), "+s"(tA[0])); }
        if constexpr (e == 10) { tA[1] = urow(8 + wa, rot) + cb * 128u; asm volatile("" : "+s"(tA[1])); }
        if constexpr (e == 12) { tA[2] = urow(31 + wa, rot) + cb * 128u; asm volatile("" : "+s"(tA[2])); }
        if constexpr (e == 14) {
          tB[0] = urow(16, rot) + cb * 128u; tB[1] = urow(w < 4 ? 19u : 22u, rot) + cb * 128u; tB[2] = urow(w < 4 ? 25u : 28u, rot) + cb * 128u;
          asm volatile("" : "+s"(tB[0]), "+s"(tB[1]), "+s"(tB[2]));
        }
      }
      if constexpr (s == 3 && e == 2) pre();
      if constexpr (NX && e >= L - 2) {
        constexpr int u = 2 * (s - 1) + e - (L - 2);
        pre2[u] = __builtin_amdgcn_raw_buffer_load_b128(rsB2, vpf, urow(36 + u, rot) + (w & 3) * 10240u + cb * 128u, 0);
      }
      if constexpr (s < 7) { if constexpr (e == L - 3) d.sB += s == 0 ? d.advB * (1 + (w >> 2)) : d.advB; }
      else if constexpr (s == 7) {
        if constexpr (e == L - 6) d.vB0 = y ? vb2 : d.vB0;
        if constexpr (e == L - 4) d.vB1 = y ? vb2 + 10240u : d.vB1;
        if constexpr (e == L - 3) d.vB2 = y ? vb2 + 20480u : d.vB2;
        if constexpr (e == L - 2) d.sB = y ? tB[0] : d.sB + d.advB;
        if constexpr (e == L - 1) d.rsB = y ? rsB2 : d.rsB;
      } else {
        if constexpr (e == L - 3) { d.sB = tB[s - 7]; d.rsB = rsB2; d.vB0 = vb2; d.vB1 = vb2 + 10240u; d.vB2 = vb2 + 20480u; }
      }
      if constexpr (s == 9 && e == L - 4) {
        asm volatile("s_waitcnt vmcnt(7)" : "+v"(G.g0), "+v"(G.g1), "+v"(G.g2)::"memory");
        ok = all64(G.g0[1] == tag && G.g1[1] == tag && G.g2[1] == tag);
      }
      if constexpr (s == 9 && e == L - 3) ln_store();
    });
    if constexpr (s == 0) {
      wait_vm<EARLY1 ? 8 - NA1 : 11>();
      static_for<0, 14>([&](auto K) { row_step<K.v>(R, lane); });
    }
    if constexpr (s == 9) {
      if (__builtin_expect(!ok, 0)) {
#pragma unroll 1
        for (unsigned spins = 0; spins < SPIN_CAP; ++spins) {
          __builtin_amdgcn_s_sleep(2);
          gran_load(G, rsWS);
          if (all64(G.g0[1] == tag && G.g1[1] == tag && G.g2[1] == tag)) break;
        }
        ln_store();
      }
    }
    wait_lgkm0();
    if constexpr (s < 9) wait_vm<4 + NX>();
    bar();
    if constexpr (s == 0) { STAMP(12); }
    SB;
    kstep<T, TR, 2 * s + 1>(acc, a1, b1, a0, b0, aoff, boff, d, [&](auto E) {
      constexpr int e = E.v;
      if constexpr (s == 0 && e == 6) {
        if (y) {
          const unsigned so = d.sB - d.advB;
          dma16(d.mW + 49152, d.vB0, d.rsB, so); dma16(d.mW + 49152 + 1024, d.vB1, d.rsB, so); dma16(d.mW + 49152 + 2048, d.vB2, d.rsB, so);
        }
      }
      if constexpr (s == 0 && e == L - 4) {
        v2 = (lane >> 3) * 1280 + (((lane & 7) ^ (((lane >> 4) & 3) << 1)) << 4);
        vpf = y ? v2 : 0x7ffffff0u;
        asm volatile("" : "+v"(v2), "+v"(vpf));
      }
      if constexpr (s == 0 && e == L - 6) row_publish(R, ws, tag, tid);
      if constexpr (s == 8 && e == 0) gran_issue(G, rsWS);
      if constexpr (s >= 7 && s < 9 && e >= L - 4) {
        d.rsA = rsB2; d.vA0 = v2; d.vA1 = v2;
        d.sA[e - (L - 4)] = tA[s - 7] + (e - (L - 4)) * 10240;
      }
      if constexpr (s == 9 && LASTA && e == 7) {
        if (wa < 5) {
#pragma unroll
          for (int i = 0; i < 4; ++i) dma16(a_slot(0) + d.mA + i * 1024, v2, rsB2, tA[2] + i * 10240);
        }
      }
    });
    if constexpr (s == 8) { STAMP(13); }
    if constexpr (s == 2) { STAMP(26); }
    if constexpr (s == 6) { STAMP(27); }
  });
  acc_fence<4 * T>(&acc[0][0]);
  STAMP(14);
}

DI void st16(u32x4 v, rsrc_t rs, unsigned voff, unsigned soff) {
  __builtin_amdgcn_raw_buffer_store_b128(v, rs, voff, soff, 16);
  asm volatile("s_nop 1" ::"v"(v) : "memory");
}
DI void publish_flag(const Params& p, unsigned flag_off, unsigned tag, unsigned tid) {
  drain_vm();
  bar();
  if (tid == 0) __hip_atomic_store((gu32*)(p.ws + flag_off), tag, RLX_AGENT);
}
DI void st_frag(rsrc_t rs, unsigned lane, unsigned rt, unsigned step, u32x2 lo, u32x2 hi) {
  u32x4 v = {lo[0], lo[1], hi[0], hi[1]};
  st16(v, rs, lane * 16, WS_HA + (rt * NSTEP + step) * 1024);
}
DI void shared_send(unsigned w, unsigned lane, const u32x2 (&sh)[4]) {
  if (w >= 4) lds_st<u32x4>(L_SCR + w * 1024 + lane * 16, cat2(sh[0], sh[1]));
  else lds_st<u32x4>(L_SCR + w * 1024 + lane * 16, cat2(sh[2], sh[3]));
  asm volatile("" ::: "memory");
}
DI void shared_recv(rsrc_t rs, unsigned w, unsigned lane, unsigned rt0, unsigned step, const u32x2 (&sh)[4]) {
  lbar();
  u32x4 rcv = lds_ld<u32x4>(L_SCR + (w ^ 4) * 1024 + lane * 16);
  u32x2 r0 = {rcv[0], rcv[1]}, r1 = {rcv[2], rcv[3]};
  if (w >= 4) { st_frag(rs, lane, rt0 + 2, step, r0, sh[2]); st_frag(rs, lane, rt0 + 3, step, r1, sh[3]); }
  else        { st_frag(rs, lane, rt0 + 0, step, sh[0], r0); st_frag(rs, lane, rt0 + 1, step, sh[1], r1); }
}

__global__ __launch_bounds__(512, 2) void fused_block(Params p) {
  unsigned tid = __builtin_amdgcn_workitem_id_x(), lane = tid & 63;
  const unsigned w = rfl(tid >> 6);
  const unsigned b = __builtin_amdgcn_workgroup_id_x(), xcd = b & 7, slot = b >> 3;
  const unsigned rb = 4 * (xcd & 1) + (slot & 3), jq = xcd >> 1, ji = slot >> 2;
  const unsigned spi = (xcd - 4) * 4 + (slot & 3);
  const unsigned long long jl0 = (jq & 1) ? 0x4123220908070605ull : 0x4021200403020100ull, jl1 = (jq & 1) ? 0x4327266011100f0eull : 0x422524600d0c0b0aull;
  const unsigned jb = (unsigned)(((jq & 2) ? jl1 : jl0) >> (8 * ji)) & 0xff, jtype = jb >> 5, jidx = jb & 31;
  STAMP_DECL;
  STAMP(0);

  Dma d;
  RowStat rs_;
  const bool mlp = jtype == J_MLP;
  const unsigned pitch = mlp ? 3456u : 3840u, wq = (w + (ROTW ? slot : 0)) & 3, wa = (w + (ROTW ? ji : 0)) & 7;
  d.wa = wa; d.wq = wq;
  const rsrc_t rsX = mk_rsrc(p.x, S * D * 2);
  d.vA0 = (lane >> 3) * 1280 + (((lane & 7) ^ (lane >> 4)) << 4); d.vA1 = d.vA0 ^ 64;
  d.rsA = rsX; d.mA = wa * 4096u;
#pragma unroll
  for (int i = 0; i < 4; ++i) d.sA[i] = (rb * 256 + 32 * wa + 8 * i) * 1280;
  unsigned epoch, tag;
  asm volatile("s_nop 4" :: "s"(rsX) : "memory");
  STAMP(6);
#pragma unroll
  for (int i = 0; i < 4; ++i) dma16(a_slot(0) + d.mA + i * 1024, (i & 1) ? d.vA1 : d.vA0, d.rsA, d.sA[i]);
  asm volatile("global_load_dword %0, %1, %2 sc1" : "=&v"(epoch) : "v"(0u), "s"(p.ws) : "memory");
  SB;
  {
    unsigned vb[3];
    const unsigned lim = mlp ? 176u : 144u;
    {
      const unsigned r0 = (lane * 43) >> 9, p0 = lane - 12 * r0;
      const bool w1 = p0 >= 8, w2 = p0 >= 4;
      const unsigned r1 = r0 + (w1 ? 6 : 5), p1 = p0 + (w1 ? -8 : 4), r2 = r0 + (w2 ? 11 : 10), p2 = p0 + (w2 ? -4 : 8);
      vb[0] = r0 * pitch + umin(p0 * 16, lim);
      vb[1] = r1 * pitch + umin((p1 * 16) ^ ((r1 & 8) << 2), lim);
      vb[2] = r2 * pitch + umin((p2 * 16) ^ 32, lim);
    }
    d.vB0 = vb[0]; d.vB1 = vb[1]; d.vB2 = vb[2];
    const unsigned mh = wq >> 1;
    const void* bw = mlp ? (mh ? p.wu : p.wg) : p.wqkv;
    const unsigned cj = mlp ? 192u : 160u * jtype, cm = mlp ? 0u : (jtype == J_KV ? 1280u : 160u), c0 = jtype == J_KV ? 1280u : 0u;
    d.rsB = mk_rsrc(bw, D * pitch);
    d.sB = (16 * (wq & 1) + (w < 4 ? 32 : 0)) * pitch + c0 + cj * jidx + cm * mh; d.advB = 64 * pitch;
    d.mB = L_B + wq * 3072u + (w < 4 ? 0u : 12288u); d.mW = L_B + wq * 3072u + (w < 4 ? 49152u : 0u);
    const unsigned bdst = L_B + (w < 4 ? 12288u : 0u) + wq * 3072u;
    dma16(bdst, d.vB0, d.rsB, d.sB); dma16(bdst + 1024, d.vB1, d.rsB, d.sB); dma16(bdst + 2048, d.vB2, d.rsB, d.sB);
    rs_.row = rfl(b * 8 + w);
    d.sB += d.advB;
    dma16(bdst + 24576, d.vB0, d.rsB, d.sB); dma16(bdst + 24576 + 1024, d.vB1, d.rsB, d.sB); dma16(bdst + 24576 + 2048, d.vB2, d.rsB, d.sB);
#pragma unroll
    for (int i = 0; i < NA1; ++i) dma16(a_slot(1) + d.mA + i * 1024, (i & 1) ? d.vA1 : d.vA0, d.rsA, d.sA[i] + 128);
  }
  STAMP(10);
  lds_st<unsigned>(L_GO + (tid & 1) * 4, 0u);

  const unsigned t2 = 28 * xcd + slot;
  const bool has2 = slot < 28 && t2 < 220;
  const unsigned G = t2 / 10, cb = t2 % 10;
  constexpr unsigned rot = 32;
  unsigned g = lane >> 4, r = lane & 15, q4 = r >> 2, p4 = lane & 3;
  const rsrc_t rsWd = mk_rsrc(p.wd, has2 ? F * D * 2 : 0);
  const rsrc_t rsWS = mk_rsrc(p.ws, WS_END);
  unsigned v2 = (lane >> 3) * 1280 + (((lane & 7) ^ (((lane >> 4) & 3) << 1)) << 4);
  auto relane = [&] {
    asm volatile("" : "+v"(tid));
    lane = tid & 63; g = lane >> 4; r = lane & 15; q4 = r >> 2; p4 = lane & 3;
    v2 = (lane >> 3) * 1280 + (((lane & 7) ^ (((lane >> 4) & 3) << 1)) << 4);
  };
  const unsigned m = w & 3, n = w >> 2;
  const unsigned rt0 = rb * 16 + m * 4;
  const unsigned brow = L_B + (8 * g + q4) * 192;
  const unsigned bx = (g & 1) << 5;

  u32x4 pre2[36];
  W2 W;
  W.wd = rsWd; W.wo = mk_rsrc(p.wo, has2 ? D * D * 2 : 0); W.rot = rot; W.col = cb * 128u;
  if (jtype == J_MLP) {
    f32x4 acc[4][6];
    unsigned boff[6];
#pragma unroll
    for (int t = 0; t < 3; ++t) {
      const unsigned gt = n ? (t == 2 ? 3 : 4 + t) : t;
      boff[t] = brow + ((32 * gt + 8 * p4) ^ bx); boff[t + 3] = boff[t] + 6144;
    }
    const unsigned ccol = tid < 96 ? 1920 + 96 * jidx + tid : 3648 + 96 * jidx + (tid < 192 ? tid - 96 : 0);
    main_loop<6, true, true, true>([] {}, rs_, p.ws, acc, boff, d, w, lane, tid, epoch, tag, rb, ccol, rsWd, rsWS, cb, rot, pre2 STAMP_PASS);
    relane();
    float mm[4];
    f32x4 cg[3], cu[3];
#pragma unroll
    for (int i = 0; i < 4; ++i) mm[i] = lds_ld<float>(L_MISC + (64 * m + 16 * i + r) * 4);
#pragma unroll
    for (int t = 0; t < 3; ++t) {
      const unsigned gt = n ? (t == 2 ? 3 : 4 + t) : t;
      cg[t] = lds_ld<f32x4>(L_MISC + 2048 + (16 * gt + 4 * g) * 4); cu[t] = lds_ld<f32x4>(L_MISC + 2048 + (96 + 16 * gt + 4 * g) * 4);
    }
    auto hval = [&](int i, int t) {
      f32x4 gg = acc[i][t] + mm[i] * cg[t];
      const f32x4 uu = acc[i][t + 3] + mm[i] * cu[t];
#pragma unroll
      for (int j = 0; j < 4; ++j) gg[j] = relu2(gg[j]);
      return pk4(gg * uu);
    };
#pragma unroll
    for (int i = 0; i < 4; ++i) { st_frag(rsWS, lane, rt0 + i, 3 * jidx + 2 * n, hval(i, 0), hval(i, 1)); SB; }
    const u32x2 sh[4] = {hval(0, 2), hval(1, 2), hval(2, 2), hval(3, 2)};
    shared_send(w, lane, sh);
    shared_recv(rsWS, w, lane, rt0, 3 * jidx + 1, sh);
    STAMP(15);
    publish_flag(p, WS_FLAGH + (rb * 32 + jidx) * FS, tag, tid);
    STAMP(3);
  } else if (jtype == J_KV) {
    f32x4 acc[4][5];
    unsigned boff[5];
    boff[0] = brow + ((80 * n + 8 * p4) ^ bx); boff[1] = boff[0] + 6144;
    boff[2] = brow + ((80 * n + 40 + 8 * p4) ^ bx); boff[3] = boff[2] + 6144;
    boff[4] = brow + ((80 * n + 32 + 40 * (p4 >> 1)) ^ bx) + (p4 & 1) * 6144;
    const unsigned kb = 126976u + m * 6720 + n * 3360, dump = L_MISC - 2048 + lane * 16;
    const unsigned ea = (q4 == 1 || r == 0) ? kb + (r == 0 ? 20 : 12 + r) * 80 : 0u, eb = (q4 == 3 || r == 0) ? kb + 1680 + (r == 0 ? 20 : 4 + r) * 80 : 0u;
    unsigned ka[6] = {kb + r * 80 + g * 16, ea ? ea + g * 16 : dump, eb ? eb + g * 16 : dump, g == 0 ? kb + r * 80 + 64 : dump,
                      g == 2 ? kb + 1680 + r * 80 + 64 : dump, (g == 0 && ea) ? ea + 64 : ((g == 2 && eb) ? eb + 64 : dump)};
    auto pin = [&] { asm volatile("" : "+v"(ka[0]), "+v"(ka[1]), "+v"(ka[2]), "+v"(ka[3]), "+v"(ka[4]), "+v"(ka[5])); };
    const unsigned ccol = tid < 80 ? 640 + 80 * jidx + tid : 1280 + 80 * jidx + (tid < 160 ? tid - 80 : 0);
    main_loop<5, false, false, false>(pin, rs_, p.ws, acc, boff, d, w, lane, tid, epoch, tag, rb, ccol, rsWd, rsWS, cb, rot, pre2 STAMP_PASS);
    relane();
    float cs[5];
    cs[0] = lds_ld<float>(L_MISC + 2048 + (40 * n + r) * 4);
    cs[1] = lds_ld<float>(L_MISC + 2048 + (80 + 40 * n + r) * 4);
    cs[2] = lds_ld<float>(L_MISC + 2048 + (40 * n + 20 + r) * 4);
    cs[3] = lds_ld<float>(L_MISC + 2048 + (80 + 40 * n + 20 + r) * 4);
    cs[4] = lds_ld<float>(L_MISC + 2048 + ((q4 & 1) * 80 + 40 * n + 16 + 20 * (q4 >> 1) + (r & 3)) * 4);
    u32x2 pk[4][3], vv[4][3];
    const unsigned vmask = 0u - (q4 & 1);
    f32x4 mm4[4], rr4[4];
#pragma unroll
    for (int i = 0; i < 4; ++i) {
      mm4[i] = lds_ld<f32x4>(L_MISC + (64 * m + 16 * i + 4 * g) * 4); rr4[i] = lds_ld<f32x4>(L_MISC + 1024 + (64 * m + 16 * i + 4 * g) * 4);
    }
    SB;
#pragma unroll
    for (int i = 0; i < 4; ++i) {
      const f32x4 mm = mm4[i], rr = rr4[i];
      const f32x4 rl = rr * 1.4426950408889634f, rh = rr * 0.5f;
      f32x4 val[5];
#pragma unroll
      for (int t = 0; t < 5; ++t) val[t] = acc[i][t] + mm * cs[t];
#pragma unroll
      for (int t = 0; t < 3; ++t) pk[i][t] = pk4(phi4(val[2 * t], rl, rh));
      vv[i][0] = pk4(val[1] * rr); vv[i][1] = pk4(val[3] * rr);
      const u32x2 v4 = pk4(val[4] * rr);
      vv[i][2] = u32x2{bsel(vmask, v4[0], 0x3f803f80u), bsel(vmask, v4[1], 0x3f803f80u)};
    }
    STAMP(25);
    f32x4 P[7] = {};
#pragma unroll
    for (int i = 0; i < 4; i += 2) {
      const bf16x8 k0 = frag(pk[i][0], pk[i + 1][0]), k2 = frag(pk[i][1], pk[i + 1][1]), k4 = frag(pk[i][2], pk[i + 1][2]);
      const bf16x8 v1 = frag(vv[i][0], vv[i + 1][0]), v3 = frag(vv[i][1], vv[i + 1][1]), v4 = frag(vv[i][2], vv[i + 1][2]);
      P[0] = mfma(k0, v1, P[0]); P[1] = mfma(k0, v4, P[1]); P[2] = mfma(k4, v1, P[2]);
      P[3] = mfma(k2, v3, P[3]); P[4] = mfma(k2, v4, P[4]); P[5] = mfma(k4, v3, P[5]); P[6] = mfma(k4, v4, P[6]);
    }
    lds_st<f32x4>(ka[0], P[0]); lds_st<f32x4>(ka[0] + 1680, P[3]); lds_st<f32x4>(ka[1], P[1]); lds_st<f32x4>(ka[2], P[4]);
    lds_st<f32x4>(ka[3], P[2]); lds_st<f32x4>(ka[4], P[5]); lds_st<f32x4>(ka[5], P[6]);
    lbar();
    if (tid < 210) {
      const unsigned a = 126976u + tid * 32;
      const f32x4 t0 = (lds_ld<f32x4>(a) + lds_ld<f32x4>(a + 6720)) + (lds_ld<f32x4>(a + 13440) + lds_ld<f32x4>(a + 20160));
      const f32x4 t1 = (lds_ld<f32x4>(a + 16) + lds_ld<f32x4>(a + 6736)) + (lds_ld<f32x4>(a + 13456) + lds_ld<f32x4>(a + 20176));
      st16(cat2(pk4(t0), pk4(t1)), rsWS, tid * 16, WS_KVP + (rb * 32 + 4 * jidx) * 840);
    }
    wait_lgkm0();
    STAMP(15);
    publish_flag(p, WS_FLAGKV + ((jidx >> 1) * 32 + (jidx & 1) * 8 + rb) * FS, tag, tid);
    STAMP(3);
  } else if (jtype == J_Q) {
    f32x4 acc[4][5];
    unsigned boff[5];
#pragma unroll
    for (int t = 0; t < 5; ++t) boff[t] = brow + n * 6144 + ((32 * t + 8 * p4) ^ bx);
    const unsigned ccol = 160 * jidx + (tid < 160 ? tid : 0);
    main_loop<5, true, false, false>([] {}, rs_, p.ws, acc, boff, d, w, lane, tid, epoch, tag, rb, ccol, rsWd, rsWS, cb, rot, pre2 STAMP_PASS);
    relane();
    u32x2 pq[4][5];
    {
      float mm[4], rr[4];
      f32x4 c[5];
#pragma unroll
      for (int i = 0; i < 4; ++i) { mm[i] = lds_ld<float>(L_MISC + (64 * m + 16 * i + r) * 4); rr[i] = lds_ld<float>(L_MISC + 1024 + (64 * m + 16 * i + r) * 4); }
#pragma unroll
      for (int t = 0; t < 5; ++t) c[t] = lds_ld<f32x4>(L_MISC + 2048 + (80 * n + 16 * t + 4 * g) * 4);
      SB;
#pragma unroll
      for (int i = 0; i < 4; ++i)
#pragma unroll
        for (int t = 0; t < 5; ++t) pq[i][t] = pk4(phi4(acc[i][t] + mm[i] * c[t], splat(rr[i] * 1.4426950408889634f), splat(rr[i] * 0.5f)));
#pragma unroll
      for (int i = 0; i < 4; ++i) asm volatile("" : "+v"(pq[i][0]), "+v"(pq[i][1]), "+v"(pq[i][2]), "+v"(pq[i][3]), "+v"(pq[i][4]));
    }
    constexpr unsigned KP = 208, KQ = 84 * KP;
    unsigned wa[2];
#pragma unroll
    for (int k = 0; k < 2; ++k) {
      const unsigned qd = 2 * (tid < 420 ? tid : 0) + k, hd = qd / 105, e = (qd % 105) / 5, dq = qd % 5, hl = hd & 3;
      wa[k] = (hd >> 2) * KQ + (e < 20 ? hl * 20 + e : 80 + hl) * KP + hl * 40 + dq * 8;
    }
    unsigned fk = n * KQ + r * KP + g * 8, fz = n * KQ + (80 + (r & 3)) * KP + g * 8;
    unsigned gm[3] = {g < 1 ? ~0u : 0u, g < 2 ? ~0u : 0u, g < 3 ? ~0u : 0u};
    u32x4 B[4][3];
    f32x4 eps4 = splat(1e-6f);
#pragma unroll
    for (int i = 0; i < 4; ++i) {
      B[i][0] = cat2(pq[i][0], pq[i][1]); B[i][1] = cat2(pq[i][2], pq[i][3]); B[i][2] = cat2(pq[i][4], u32x2{0u, 0u});
      asm volatile("" : "+v"(B[i][0]), "+v"(B[i][1]), "+v"(B[i][2]));
    }
    asm volatile("" : "+v"(wa[0]), "+v"(wa[1]), "+v"(fk), "+v"(fz), "+v"(gm[0]), "+v"(gm[1]), "+v"(gm[2]), "+v"(eps4));
    STAMP(25);
    wait_vm<0>();
    bar();
#pragma unroll
    for (int k = 0; k < 5; ++k) lds_st<u32x4>(k * 8192 + tid * 16, u32x4{0u, 0u, 0u, 0u});
    if (w == 0) poll_flags((gu32*)(p.ws + WS_FLAGKV + (jidx * 32 + (lane & 15)) * FS), true, tag);
    lbar();
    STAMP(4);
    if (w < 7) {
      u32x4 v[8];
#pragma unroll
      for (int j = 0; j < 8; ++j)
        v[j] = __builtin_amdgcn_raw_buffer_load_b128(rsWS, (tid < 420 ? tid : 0) * 16, WS_KVP + (j * 32 + 8 * jidx) * 840, 16);
      SB;
      f32x4 t0 = {0.f, 0.f, 0.f, 0.f}, t1 = t0;
#pragma unroll
      for (int j = 0; j < 8; ++j) {
        t0 += f32x4{bflo(v[j][0]), bfhi(v[j][0]), bflo(v[j][1]), bfhi(v[j][1])};
        t1 += f32x4{bflo(v[j][2]), bfhi(v[j][2]), bflo(v[j][3]), bfhi(v[j][3])};
      }
      lds_st<u32x2>(wa[0], pk4(t0)); lds_st<u32x2>(wa[1], pk4(t1));
    }
    lbar();
    STAMP(16);
    u32x4 A[12];
#pragma unroll
    for (int P = 0; P < 3; ++P) A[P] = cat2(lds_ld<u32x2>(fz + P * 64), lds_ld<u32x2>(fz + P * 64 + 32));
#pragma unroll
    for (int u = 0; u < 5; ++u) {
      const unsigned a0 = fk + u * 16 * KP + (u < 3 ? 0 : 64);
      A[3 + u] = cat2(lds_ld<u32x2>(a0), lds_ld<u32x2>(a0 + 32));
    }
#pragma unroll
    for (int u = 1; u < 5; ++u) {
      const unsigned a0 = fk + u * 16 * KP + (u < 3 ? 64 : 128);
      A[7 + u] = cat2(lds_ld<u32x2>(a0), lds_ld<u32x2>(a0 + 32));
    }
    SB;
    u32x2 sh[4];
    f32x4 den[4], num[2][5];
    static_for<0, 12>([&](auto E) {
      constexpr int P = E.v >> 2, i = E.v & 3;
      if constexpr (P == 0) mfma_c(den[i], A[0], B[i][0], eps4); else mfma_ip(den[i], A[P], B[i][P]);
    });
    u32x2 o[5];
    auto fin = [&](auto I, auto K) {
      constexpr int i = I.v, k = K.v, u = k < 8 ? k >> 1 : 4;
      if constexpr (k == 8 || !(k & 1)) {
        asm volatile("" : "+v"(num[i & 1][u]));
        const float rr = u == 0 ? den[i][0] : (u == 4 ? den[i][3] : bself(gm[(u + 2) % 3], den[i][(u + 3) & 3], den[i][u & 3]));
#pragma unroll
        for (int j = 0; j < 4; ++j) num[i & 1][u][j] = smul(num[i & 1][u][j], rr);
      }
      if constexpr (k == 8 || (k & 1)) o[u] = pk4(num[i & 1][u]);
      if constexpr (k == 3) st_frag(rsWS, lane, rt0 + i, 54 + 5 * jidx + 2 * n, o[0], o[1]);
      if constexpr (k == 7) st_frag(rsWS, lane, rt0 + i, 54 + 5 * jidx + 2 * n + 1, o[2], o[3]);
      if constexpr (k == 8) { sh[i] = o[4]; asm volatile("" : "+v"(sh[i])); }
    };
    static_for<0, 4>([&](auto I) {
      constexpr int i = I.v;
      static_for<0, 9>([&](auto K) {
        constexpr int k = K.v, u = k < 5 ? k : k - 4;
        if constexpr (k < 5) mfma_z(num[i & 1][u], A[3 + u], B[i][u < 3 ? 0 : 1]); else mfma_ip(num[i & 1][u], A[7 + u], B[i][u < 3 ? 1 : 2]);
        SB;
        if constexpr (i == 0) {
          if constexpr (k >= 5) {
            asm volatile("" : "+v"(den[k - 5]));
#pragma unroll
            for (int j = 0; j < 4; ++j) den[k - 5][j] = __builtin_amdgcn_rcpf(den[k - 5][j]);
            asm volatile("" : "+v"(den[k - 5]));
          }
        } else {
          fin(IC<i - 1>{}, K);
        }
        SB;
      });
    });
    static_for<0, 9>([&](auto K) { fin(IC<3>{}, K); SB; });
    shared_send(w, lane, sh);
    shared_recv(rsWS, w, lane, rt0, 54 + 5 * jidx + 4, sh);
    STAMP(15);
    publish_flag(p, WS_FLAGO + (rb * 32 + jidx) * FS, tag, tid);
    STAMP(3);
    if (b == 224) {
      poll_flags((gu32*)(p.ws + WS_FLAGH + tid * FS), (tid & 31) < (tid < 256 ? 18u : 4u), tag);
      bar();
      if (tid == 0) __hip_atomic_store((gu32*)(p.ws + WS_EPOCH), tag, RLX_AGENT);
    }
  } else {
    const unsigned gw = spi * 8 + w;
    unsigned k10 = 0x00003f80u, k01 = 0x3f800000u;
    asm volatile("" : "+v"(k10), "+v"(k01));
    auto cs_src = [&](unsigned u, rsrc_t& rs, unsigned& cv, unsigned& st) {
      const unsigned cline = u / 3, third = u % 3;
      const void* cw = cline < 30 ? p.wqkv : (cline < 57 ? p.wg : p.wu);
      const unsigned cpitch = cline < 30 ? 3840u : 3456u;
      const unsigned clocal = cline < 30 ? cline : (cline < 57 ? cline - 30 : cline - 57);
      rs = mk_rsrc(cw, gw < 126 ? D * cpitch : 0);
      cv = (third * 216 + (lane >> 3)) * cpitch + clocal * 128 + (lane & 7) * 16;
      st = 8 * cpitch;
    };
    auto cs_publish = [&](unsigned u, float (&cs)[8]) {
#pragma unroll
      for (int k = 0; k < 4; ++k) { const bool hi = lane & 32; const float o = shfl_x(hi ? cs[k] : cs[k + 4], lane, 32); cs[k] = (hi ? cs[k + 4] : cs[k]) + o; }
#pragma unroll
      for (int k = 0; k < 2; ++k) { const bool hi = lane & 16; const float o = shfl_x(hi ? cs[k] : cs[k + 2], lane, 16); cs[k] = (hi ? cs[k + 2] : cs[k]) + o; }
      { const bool hi = lane & 8; const float o = shfl_x(hi ? cs[0] : cs[1], lane, 8); cs[0] = (hi ? cs[1] : cs[0]) + o; }
      if (gw < 126)
        __hip_atomic_store((gu64*)(p.ws + WS_CSUM) + (u % 3) * NCOL + (u / 3) * 64 + (lane & 7) * 8 + (lane >> 3),
                           ((unsigned long long)tag << 32) | __builtin_bit_cast(unsigned, cs[0]), RLX_AGENT);
    };
    row_load(rs_, rsX, lane);
    asm volatile("s_waitcnt vmcnt(0) lgkmcnt(0)\n\tv_readfirstlane_b32 %0, %1\n\ts_nop 3" : "=s"(tag) : "v"(epoch) : "memory");
    tag = rfl(tag) + 1;
    static_for<0, 14>([&](auto K) { row_step<K.v>(rs_, lane); });
    u32x4 c[27];
    rsrc_t rs0, rs1; unsigned cv0, cv1, st0, st1;
    cs_src(gw, rs0, cv0, st0); cs_src(gw + 126, rs1, cv1, st1);
#pragma unroll
    for (int j = 0; j < 27; ++j) c[j] = __builtin_amdgcn_raw_buffer_load_b128(rs0, cv0 + j * st0, 0, 0);
    lbar();
    row_publish(rs_, p.ws, tag, tid);
    float cs[8] = {};
#pragma unroll
    for (int j = 0; j < 27; ++j) {
#pragma unroll
      for (int k = 0; k < 4; ++k) { cs[2 * k] = dot2(c[j][k], k10, cs[2 * k]); cs[2 * k + 1] = dot2(c[j][k], k01, cs[2 * k + 1]); }
      if (j < 8) c[j] = __builtin_amdgcn_raw_buffer_load_b128(rs1, cv1 + j * st1, 0, 0);
      SB;
    }
    cs_publish(gw, cs);
    SB;
#pragma unroll
    for (int j = 8; j < 27; ++j) c[j] = __builtin_amdgcn_raw_buffer_load_b128(rs1, cv1 + j * st1, 0, 0);
    SB;
    STAMP(11);
#pragma unroll
    for (int k = 0; k < 8; ++k) cs[k] = 0.f;
#pragma unroll
    for (int j = 0; j < 27; ++j)
#pragma unroll
      for (int k = 0; k < 4; ++k) { cs[2 * k] = dot2(c[j][k], k10, cs[2 * k]); cs[2 * k + 1] = dot2(c[j][k], k01, cs[2 * k + 1]); }
    cs_publish(gw + 126, cs);
    STAMP(12);
#pragma unroll 1
    for (unsigned i = w; i < 144; i += 8) dma16(i * 1024, v2, rsWd, urow(i >> 2, rot) + (i & 3) * 10240u + cb * 128u);
    drain_vm();
    bar();
  }
  if (!has2) { STAMP_END; return; }
  relane();

  const unsigned rbA = (G * 6) >> 4, rbB = (G * 6 + 5 > 127 ? 127 : G * 6 + 5) >> 4;
  if (w >= 4) {
    __builtin_amdgcn_s_setprio(HPRIO);
    const unsigned lw = w - 4, ld0 = lw * 1024;
    gu32* fo = (gu32*)(p.ws + WS_FLAGO + ((lane < 32 ? rbA : rbB) * 32 + (lane & 3)) * FS);
    gu32* fh = (gu32*)(p.ws + WS_FLAGH + ((lane < 32 ? rbA : rbB) * 32 + ((lane & 31) < 18 ? lane & 31 : 17)) * FS);
    __builtin_amdgcn_s_sleep(HSLEEP);
    if (jtype == J_KV) {
#pragma unroll
      for (int u = 31; u < 36; ++u) dma16(u * 4096 + ld0, v2, rsWd, w2_off(W, u, lw));
    }
    if (jtype != J_MLP) {
#pragma unroll
      for (int u = 0; u < NPRE; ++u) { pre2[u] = w2_load(W, 36 + u, lw, v2); SB; }
    }
#pragma unroll
    for (int u = 36 + NPRE; u < PSPLIT; ++u) { pre2[u - 36] = w2_load(W, u, lw, v2); SB; }
    if (w == 7) {
      __builtin_amdgcn_s_setprio(3);
      const unsigned v = poll_flags(fh, (lane & 31) < 18 && (lane & 31) >= 10, tag);
      lds_set(L_GO);
      if (!all64((lane & 31) >= 18 || v == tag)) poll_flags(fh, (lane & 31) < 18, tag);
      lds_set(L_GO + 4);
      __builtin_amdgcn_s_setprio(HPRIO);
    }
#pragma unroll
    for (int u = PSPLIT; u < HSPLIT; ++u) { pre2[u - 36] = w2_load(W, u, lw, v2); SB; }
    STAMP(8);
    unsigned la[3], wb[3], xo[3], st[3];
    u32x4 xr[3];
    const unsigned ch = lane & 7;
#pragma unroll
    for (int i = 0; i < 3; ++i) {
      const unsigned row = lw * 24 + i * 8 + (lane >> 3), rl = row >> 4, rr = row & 15;
      la[i] = (rl >= 3 ? 24576u + (rl - 3) * 4096u : rl * 4096u) + (ch >> 1) * 1024 + (ch & 1) * 512 + ((rr + 2 * ch) & 15) * 16;
      xo[i] = (G * 96 + row) * 1280 + cb * 128 + ch * 16;
      wb[i] = ld0 + lane * 16 + i * 49152;
      asm volatile("" : "+v"(la[i]), "+v"(wb[i]), "+v"(xo[i]));
    }
    float ri[3], rl[3];
    unsigned of = 0;
    f32x4 xf[3][2], dd[3][2], d2[3][2];
#pragma unroll
    for (int k = 2; k <= 6; ++k) {
      if (k == O_BAR && w == 7 && !all64((lane & 31) >= 4 || of == tag)) poll_flags(fo, (lane & 31) < 4, tag);
      if (k == 6) {
        wait_vm<0>();
#pragma unroll
        for (int h = 0; h < 3; ++h) {
          ri[h] = 2.0f * bfhi(st[h]) * bfhi(st[h]);
          rl[h] = -1.4426950408889634f * __builtin_amdgcn_rcpf(ri[h]);
          xf[h][0] = f32x4{bflo(xr[h][0]), bfhi(xr[h][0]), bflo(xr[h][1]), bfhi(xr[h][1])};
          xf[h][1] = f32x4{bflo(xr[h][2]), bfhi(xr[h][2]), bflo(xr[h][3]), bfhi(xr[h][3])};
          asm volatile("" : "+v"(ri[h]), "+v"(rl[h]), "+v"(xf[h][0]), "+v"(xf[h][1]));
        }
      }
      lbar();
      if (k == O_BAR - 1) { of = __hip_atomic_load(fo, RLX_AGENT); SB; }
      if (k < 6) {
        const int kk = k - 1;
#pragma unroll
        for (int u = 9 * (kk - 1) + ((kk - 1) & 1); u < 9 * kk + (kk & 1); ++u) {
          lds_st<u32x4>(wb[u / 12] + (u % 12) * 4096, pre2[u]);
          if (WSLEEP == 1) __builtin_amdgcn_s_sleep(1);
          if (WSLEEP == 2) asm volatile("s_nop 7");
          SB;
        }
      }
      if (k == 2 || k == 3) {
        constexpr int mid = (HSPLIT + 74) / 2 < 72 ? (HSPLIT + 74) / 2 : 72;
#pragma unroll
        for (int u = (k == 2 ? HSPLIT : mid); u < (k == 2 ? mid : 72); ++u) { pre2[u - 36] = w2_load(W, u, lw, v2); SB; }
        if (k == 3) { dma16(36 * 4096 + ld0, v2, W.wo, w2_off(W, 72, lw)); dma16(37 * 4096 + ld0, v2, W.wo, w2_off(W, 73, lw)); }
      }
      if (k == XK) {
#pragma unroll
        for (int h = 0; h < 3; ++h) {
          const unsigned row = G * 96 + lw * 24 + h * 8 + (lane >> 3);
          st[h] = __builtin_amdgcn_raw_buffer_load_b32(rsWS, WS_STAT + (row < S ? row : 0) * 8, 0, 16);
          xr[h] = __builtin_amdgcn_raw_buffer_load_b128(rsX, xo[h], 0, 0);
        }
      }
    }
    bar();
#pragma unroll
    for (int i = 0; i < 3; ++i) {
      dd[i][0] = lds_ld<f32x4>(la[i]); d2[i][0] = lds_ld<f32x4>(la[i] + 12288);
      dd[i][1] = lds_ld<f32x4>(la[i] + 256); d2[i][1] = lds_ld<f32x4>(la[i] + 12288 + 256);
      la[i] += 49152;
      asm volatile("" : "+v"(la[i]));
    }
    __builtin_amdgcn_s_setprio(GPRIO);
#pragma unroll
    for (int i = 0; i < 3; ++i) {
      float v[8], e[8];
#pragma unroll
      for (int j = 0; j < 8; ++j) v[j] = sadd(dd[i][j >> 2][j & 3], d2[i][j >> 2][j & 3]);
      SB;
#pragma unroll
      for (int j = 0; j < 8; ++j) e[j] = smul(v[j], rl[i]);
      SB;
#pragma unroll
      for (int j = 0; j < 8; ++j) e[j] = __builtin_amdgcn_exp2f(e[j]);
      SB;
#pragma unroll
      for (int j = 0; j < 8; ++j) e[j] = sfma(e[j], ri[i], ri[i]);
      SB;
#pragma unroll
      for (int j = 0; j < 8; ++j) e[j] = __builtin_amdgcn_rcpf(e[j]);
      SB;
#pragma unroll
      for (int j = 0; j < 8; ++j) dd[i][j >> 2][j & 3] = smul(v[j], e[j]);
      asm volatile("" : "+v"(dd[i][0]), "+v"(dd[i][1]));
      SB;
    }
    const rsrc_t rsO = mk_rsrc(p.out, S * D * 2);
    STAMP(9);
    bar();
    f32x4 ss[3][2];
#pragma unroll
    for (int i = 0; i < 3; ++i) {
      ss[i][0] = lds_ld<f32x4>(la[i]) + lds_ld<f32x4>(la[i] + 12288);
      ss[i][1] = lds_ld<f32x4>(la[i] + 256) + lds_ld<f32x4>(la[i] + 12288 + 256);
    }
#pragma unroll
    for (int i = 0; i < 3; ++i)
      __builtin_amdgcn_raw_buffer_store_b128(cat2(pk4(xf[i][0] + ss[i][0] * dd[i][0]), pk4(xf[i][1] + ss[i][1] * dd[i][1])), rsO, xo[i], 0, 0);
    STAMP(5);
    STAMP_END;
    return;
  }
  {
    const unsigned kh = w & 1, rt = G * 6 + 3 * (w >> 1);
    const rsrc_t rsH = mk_rsrc(p.ws + WS_HA + (rt < 128 ? rt : 0) * (NSTEP * 1024), rt < 128 ? (rt > 125 ? 128 - rt : 3) * (NSTEP * 1024) : 0);
    unsigned tb[3][4];
#pragma unroll
    for (int t = 0; t < 4; ++t) {
      tb[0][t] = (4 * g + q4) * 128 + ((t ^ ((2 * g + (q4 >> 1)) & 3)) << 5) + 8 * p4 + kh * 4096;
      tb[1][t] = tb[0][t] + 49152; tb[2][t] = tb[0][t] + 98304;
      asm volatile("" : "+v"(tb[0][t]), "+v"(tb[1][t]), "+v"(tb[2][t]));
    }
    auto rd1 = [&](int i, int t, int hf) {
      const int sl = i == 36 ? 36 : (2 * i) % 36, bi = sl / 12 > 2 ? 2 : sl / 12;
      return __builtin_bit_cast(u32x2, lds_tr(tb[bi][t] + (sl - 12 * bi) * 4096 + hf * 2048));
    };
    constexpr int NW = P2NW, W0 = P2W0;
    constexpr int OI = O_BAR == 4 ? 17 : (O_BAR == 5 ? 22 : 26);
    constexpr int OB0 = OI == 26 ? NW - 1 : 0;
    f32x4 acc[2][3][4];
    u32x4 a[NW][3];
    u32x2 bw[2][4][2];
    unsigned va[3];
#pragma unroll
    for (int h = 0; h < 3; ++h) va[h] = lane * 16 + h * NSTEP * 1024;
    unsigned xa[4];
#pragma unroll
    for (int t = 0; t < 4; ++t) {
      xa[t] = w * 12288u + t * 1024 + g * 256 + ((r + 4 * t + (g & 2)) & 15) * 16;
      asm volatile("" : "+v"(xa[t]));
    }
    auto put = [&](auto Z, auto E) {
      constexpr int z = Z.v, t = E.v / 3, h = 2 - E.v % 3;
      asm volatile("" : "+v"(acc[z][h][t]));
      lds_st<f32x4>(xa[t] + z * 49152 + h * 4096, acc[z][h][t]);
    };
    unsigned so = (rot + kh) * 1024;
    auto lda = [&](auto I, auto H) {
      constexpr int i = I.v, h = H.v;
      if constexpr (i < 27) {
        if (h == 0 && so >= 54 * 1024) so -= 54 * 1024;
        a[i % NW][h] = __builtin_amdgcn_raw_buffer_load_b128(rsH, va[h], so, 16);
        if (h == 2) so += 2048;
      } else {
        a[i % NW][h] = __builtin_amdgcn_raw_buffer_load_b128(rsH, va[h], (2 * i + kh) * 1024, 16);
      }
      SB;
    };
    lds_wait(L_GO);
    unsigned go2 = 0;
    STAMP(8);
    static_for<0, W0>([&](auto I) { lda(I, IC<0>{}); lda(I, IC<1>{}); lda(I, IC<2>{}); });
#pragma unroll
    for (int t = 0; t < 4; ++t) { bw[0][t][0] = rd1(0, t, 0); bw[0][t][1] = rd1(0, t, 1); }
    static_for<0, 37>([&](auto I) {
      constexpr int i = I.v;
      constexpr int lim = i + NW - 1;
      constexpr int nxt = i <= OI ? (W0 + 2 * i < lim ? W0 + 2 * i : lim) : (27 + OB0 + 2 * (i - OI - 1) < lim ? 27 + OB0 + 2 * (i - OI - 1) : lim);
      constexpr int end = i <= OI ? 27 : 37;
      constexpr int l0 = nxt < end ? nxt : -1, l1 = (nxt + 1 <= lim && nxt + 1 < end) ? nxt + 1 : -1;
      SB;
      static_for<0, 12>([&](auto E) {
        constexpr int e = E.v, t = e / 3, h = e % 3;
        const u32x4 bf = cat2(bw[i & 1][t][0], bw[i & 1][t][1]);
        if constexpr (i == 0 || i == 27) mfma_z(acc[i >= 27][2 - h][t], bf, a[i % NW][2 - h]); else mfma_ip(acc[i >= 27][2 - h][t], bf, a[i % NW][2 - h]);
        SB;
        if constexpr (i == 4 && e == 2) go2 = lds_get(L_GO + 4);
        if constexpr ((i == 27 || i == 28) && (e & 1)) put(IC<0>{}, IC<(i - 27) * 6 + e / 2>{});
        if constexpr (i == 36 && e >= 8) put(IC<1>{}, IC<e - 8>{});
        if constexpr (i < 36 && h < 2) bw[(i + 1) & 1][t][h] = rd1(i + 1, t, h);
        if constexpr (l1 < 0) { if constexpr (h == 2 && t < 3 && l0 >= 0) lda(IC<l0>{}, IC<t>{}); }
        else if constexpr (e & 1) lda(IC<(e < 6 ? l0 : l1)>{}, IC<(e >> 1) % 3>{});
        SB;
      });
      if constexpr (i == 29) { asm volatile("s_waitcnt lgkmcnt(8)" ::: "memory"); bar(); }
      if constexpr (i == 4) { if (__builtin_expect(!rfl(go2), 0)) lds_wait(L_GO + 4); asm volatile("" ::: "memory"); STAMP(17); }
      if constexpr (i > 4 && i < 27 && (i % 9 == 4 || i % 9 == 8)) {
        bar();
        if constexpr (i == 26 && OI == 26) static_for<27, 27 + OB0>([&](auto J) { lda(J, IC<0>{}); lda(J, IC<1>{}); lda(J, IC<2>{}); });
        STAMP(17 + (2 * i) / 9);
      }
    });
    STAMP(24);
    static_for<4, 12>([&](auto E) { if constexpr (E.v == 8) asm volatile("s_nop 3" ::: "memory"); put(IC<1>{}, E); SB; });
    lbar();
    STAMP(5);
    STAMP_END;
  }
}

__global__ __launch_bounds__(64) void tailk(Params p) {
  const unsigned long long t0 = __hip_atomic_load((gu64*)(p.ws + WS_DBG), RLX_AGENT);
  for (unsigned i = 0; i < (1u << 22) && __builtin_amdgcn_s_memrealtime() - t0 < 6000; ++i) __builtin_amdgcn_s_sleep(1);
}

extern "C" void kernel_launch(void* const* d_in, const int* in_sizes, int n_in, void* d_out, int out_size, void* d_ws,
                              size_t ws_size, hipStream_t stream) {
  Params p;
  p.x = d_in[0]; p.wqkv = d_in[1]; p.wo = d_in[2]; p.wg = d_in[4]; p.wu = d_in[5]; p.wd = d_in[6];
  p.out = d_out; p.ws = (char*)d_ws;
  fused_block<<<256, 512, 0, stream>>>(p);
  tailk<<<1, 64, 0, stream>>>(p);
}
```

```cpp
#include <hip/hip_runtime.h>
#include <stdint.h>

typedef __attribute__((ext_vector_type(8))) __bf16 bf16x8;
typedef __attribute__((ext_vector_type(4))) __bf16 bf16x4;
typedef __attribute__((ext_vector_type(2))) __bf16 bf16x2;
typedef __attribute__((ext_vector_type(4))) float f32x4;
typedef __attribute__((ext_vector_type(2))) float f32x2;
typedef __attribute__((ext_vector_type(4))) unsigned u32x4;
typedef __attribute__((ext_vector_type(2))) unsigned u32x2;
typedef __amdgpu_buffer_rsrc_t rsrc_t;
#define LAS __attribute__((address_space(3)))
#define GAS __attribute__((address_space(1)))
#define DI __device__ __forceinline__
#define RLX_AGENT __ATOMIC_RELAXED, __HIP_MEMORY_SCOPE_AGENT
typedef GAS unsigned gu32;
typedef GAS unsigned long long gu64;

constexpr int S = 2048, D = 640, F = 1728;
constexpr int NCOL = 5376;
constexpr int NSTEP = 74;
constexpr unsigned WS_EPOCH = 0;
#define FS 128
constexpr unsigned WS_FLAGH = 768u << 10;
constexpr unsigned WS_FLAGO = WS_FLAGH + 8 * 32 * FS;
constexpr unsigned WS_FLAGKV = WS_FLAGO + 8 * 32 * FS;
constexpr unsigned WS_STAT = 4096;
constexpr unsigned WS_CSUM = WS_STAT + S * 8;
constexpr unsigned WS_KVP = WS_CSUM + 3 * NCOL * 8;
constexpr unsigned WS_HA = 1u << 20;
constexpr unsigned WS_DBG = 12u << 20;
constexpr unsigned WS_END = 13u << 20;
constexpr unsigned L_B = 65536;
constexpr unsigned L_SCR = 147456;
constexpr unsigned L_MISC = 159744;
constexpr unsigned L_GO = L_MISC + 3072;
constexpr unsigned SPIN_CAP = 1u << 18;
#define HSLEEP 4
#define HSPLIT 64
#define PPIPE 0
#define STA 0
#define STB 2
#define SNAKE 0
#define EORD 0
#define EARLY1 1
#define ROTW 1
#define NPRE 12
#define WSCH 0
#define PSPLIT 48
#define CWIN 16
#define XK 2
#define GRP 1
#define GPRIO 0
#define HPRIO 0
#define PSLEEP 2
#define P2NW 7
#define P2W0 6
#define WSLEEP 0
#define ROTM 18
#define NA1 2
#define O_BAR 5
#define PRIO_MODE 0
#define DMA_SPACE_A T
#define DMA_SPACE_B (4 * T / 3)

struct Params {
  const void* x; const void* wqkv; const void* wo; const void* wg; const void* wu; const void* wd;
  void* out; char* ws;
};

__shared__ __attribute__((aligned(16))) char smem[163840];

DI float sadd(float a, float b) { float o; asm("v_add_f32 %0, %1, %2" : "=v"(o) : "v"(a), "v"(b)); return o; }
DI float smul(float a, float b) { float o; asm("v_mul_f32 %0, %1, %2" : "=v"(o) : "v"(a), "v"(b)); return o; }
DI float sfma(float a, float b, float c) { float o; asm("v_fma_f32 %0, %1, %2, %3" : "=v"(o) : "v"(a), "v"(b), "v"(c)); return o; }
DI unsigned bsel(unsigned m, unsigned a, unsigned b) { unsigned o; asm("v_bfi_b32 %0, %1, %2, %3" : "=v"(o) : "v"(m), "v"(a), "v"(b)); return o; }
DI float bself(unsigned m, float a, float b) { return __builtin_bit_cast(float, bsel(m, __builtin_bit_cast(unsigned, a), __builtin_bit_cast(unsigned, b))); }
DI bool all64(bool c) { return __builtin_amdgcn_ballot_w64(!c) == 0; }
DI float shfl_x(float v, unsigned lane, unsigned m) {
  return __builtin_bit_cast(float, __builtin_amdgcn_ds_bpermute((int)((lane ^ m) << 2), __builtin_bit_cast(int, v)));
}
DI unsigned umin(unsigned a, unsigned b) { return a < b ? a : b; }
DI unsigned rfl(unsigned v) { return __builtin_amdgcn_readfirstlane(v); }
DI rsrc_t mk_rsrc(const void* p, unsigned bytes) { return __builtin_amdgcn_make_buffer_rsrc((void*)p, (short)0, (int)bytes, 0x00020000); }
DI void dma16(unsigned ldsdst, unsigned voff, rsrc_t rs, unsigned soff) {
  asm volatile("s_mov_b32 m0, %0\n\ts_nop 0\n\tbuffer_load_dwordx4 %1, %2, %3 offen lds" ::"s"(ldsdst), "v"(voff), "s"(rs), "s"(soff) : "memory");
}
template <int N> DI void wait_vm() { asm volatile("s_waitcnt vmcnt(%0)" ::"i"(N) : "memory"); }
DI void drain_vm() { wait_vm<0>(); __builtin_amdgcn_s_waitcnt(0x0F70); }
DI void wait_lgkm0() { asm volatile("s_waitcnt lgkmcnt(0)" ::: "memory"); }
DI void bar() { asm volatile("" ::: "memory"); __builtin_amdgcn_s_barrier(); asm volatile("" ::: "memory"); }
DI void lbar() { wait_lgkm0(); bar(); }
template <class V> DI V lds_ld(unsigned a) { return *(LAS V*)((LAS char*)smem + a); }
template <class V> DI void lds_st(unsigned a, V v) { *(LAS V*)((LAS char*)smem + a) = v; }
DI bf16x4 lds_tr(unsigned a) { return __builtin_amdgcn_ds_read_tr16_b64_v4bf16((LAS bf16x4*)((LAS char*)smem + a)); }
DI bf16x8 cat(bf16x4 a, bf16x4 b) { return __builtin_shufflevector(a, b, 0, 1, 2, 3, 4, 5, 6, 7); }
DI f32x4 mfma(bf16x8 a, bf16x8 b, f32x4 c) { return __builtin_amdgcn_mfma_f32_16x16x32_bf16(a, b, c, 0, 0, 0); }
DI unsigned pk2(float a, float b) { f32x2 v = {a, b}; return __builtin_bit_cast(unsigned, __builtin_convertvector(v, bf16x2)); }
DI u32x2 pk4(f32x4 v) { u32x2 r = {pk2(v[0], v[1]), pk2(v[2], v[3])}; return r; }
DI bf16x8 frag(u32x2 lo, u32x2 hi) { u32x4 v = {lo[0], lo[1], hi[0], hi[1]}; return __builtin_bit_cast(bf16x8, v); }
DI float bflo(unsigned v) { return __builtin_bit_cast(float, v << 16); }
DI float bfhi(unsigned v) { return __builtin_bit_cast(float, v & 0xffff0000u); }
DI float rbf(float v) { return bflo(pk2(v, 0.f)); }
DI float relu2(float v) { float o; asm("v_add_f32 %0, %1, |%1|" : "=v"(o) : "v"(v)); return o; }
DI f32x4 phi4(f32x4 u, f32x4 rl, f32x4 rh) {
  const f32x4 l = u * rl;
  f32x4 e, a;
#pragma unroll
  for (int j = 0; j < 4; ++j) {
    e[j] = __builtin_amdgcn_fmed3f(__builtin_amdgcn_exp2f(l[j]), 0.f, 1.f);
    a[j] = relu2(u[j]);
  }
  return a * rh + e + 1e-6f;
}
DI f32x4 splat(float v) { return f32x4{v, v, v, v}; }
template <int CTRL> DI float dpp_add(float v) {
  return v + __builtin_bit_cast(float, __builtin_amdgcn_update_dpp(0, __builtin_bit_cast(int, v), CTRL, 0xf, 0xf, true));
}
DI float dot2(unsigned a, unsigned b, float c) {
  return __builtin_amdgcn_fdot2_f32_bf16(__builtin_bit_cast(bf16x2, a), __builtin_bit_cast(bf16x2, b), c, false);
}
DI u32x4 ldtr2(unsigned a, unsigned d) { return __builtin_bit_cast(u32x4, cat(lds_tr(a), lds_tr(a + d))); }
DI void mfma_ip(f32x4& c, const u32x4& a, const u32x4& b) { asm volatile("v_mfma_f32_16x16x32_bf16 %0, %1, %2, %0" : "+v"(c) : "v"(a), "v"(b)); }
DI void mfma_c(f32x4& d, const u32x4& a, const u32x4& b, const f32x4& c) { asm volatile("v_mfma_f32_16x16x32_bf16 %0, %1, %2, %3" : "=&v"(d) : "v"(a), "v"(b), "v"(c)); }
DI void mfma_z(f32x4& c, const u32x4& a, const u32x4& b) { asm volatile("v_mfma_f32_16x16x32_bf16 %0, %1, %2, 0" : "=&v"(c) : "v"(a), "v"(b)); }
template <int N> DI void acc_fence(f32x4* c) {
  asm volatile("s_nop 7" ::: "memory");
#pragma unroll
  for (int i = 0; i < N; ++i) asm volatile("" : "+v"(c[i]));
}
DI u32x4 cat2(u32x2 a, u32x2 b) { return u32x4{a[0], a[1], b[0], b[1]}; }
DI u32x2 ldtr1(unsigned a) { return __builtin_bit_cast(u32x2, lds_tr(a)); }
template <int IMM> DI void set_m0i(unsigned v) { asm volatile("s_add_i32 m0, %0, %1" ::"s"(v), "i"(IMM) : "memory", "scc"); }
template <int OFF> DI void dma_goi(unsigned voff, rsrc_t rs, unsigned soff) {
  asm volatile("buffer_load_dwordx4 %0, %1, %2 offen offset:%3 lds" ::"v"(voff), "s"(rs), "s"(soff), "i"(OFF) : "memory");
}
DI void set_m0(unsigned v) { asm volatile("s_mov_b32 m0, %0" ::"s"(v) : "memory"); }
DI void dma_go(unsigned voff, rsrc_t rs, unsigned soff) {
  asm volatile("buffer_load_dwordx4 %0, %1, %2 offen lds" ::"v"(voff), "s"(rs), "s"(soff) : "memory");
}
DI f32x4 mfma_u(u32x4 a, u32x4 b, f32x4 c) { return mfma(__builtin_bit_cast(bf16x8, a), __builtin_bit_cast(bf16x8, b), c); }
#define STAMP_DECL
#define STAMP_ARG
#define STAMP_PASS
#define STAMPX(c, i)
#define STAMP_END
#define STAMP(i) STAMPX(__builtin_amdgcn_workitem_id_x() == 0, i)

DI unsigned poll_flags(gu32* word, bool active, unsigned tag) {
  unsigned v = 0;
#pragma unroll 1
  for (unsigned spins = 0; spins < SPIN_CAP; ++spins) {
    v = __hip_atomic_load(word, RLX_AGENT);
    if (all64(!active || v == tag)) break;
    if (PSLEEP) __builtin_amdgcn_s_sleep(PSLEEP);
  }
  asm volatile("" ::: "memory");
  return v;
}

DI void lds_set(unsigned a) { __hip_atomic_store((LAS unsigned*)smem + a / 4, 1u, __ATOMIC_RELAXED, __HIP_MEMORY_SCOPE_WORKGROUP); }
DI unsigned lds_get(unsigned a) { return __hip_atomic_load((LAS unsigned*)smem + a / 4, __ATOMIC_RELAXED, __HIP_MEMORY_SCOPE_WORKGROUP); }
DI void lds_wait(unsigned a) {
#pragma unroll 1
  for (unsigned spins = 0; spins < (SPIN_CAP << 4); ++spins) {
    if (rfl(lds_get(a))) break;
    __builtin_amdgcn_s_sleep(1);
  }
  asm volatile("" ::: "memory");
}

template <int I> struct IC { static constexpr int v = I; };
template <int B, int E, class Fn> DI void static_for(Fn&& f) {
  if constexpr (B < E) { f(IC<B>{}); static_for<B + 1, E>(f); }
}

enum { J_MLP = 0, J_KV = 1, J_Q = 2, J_SPARE = 3 };

struct Dma {
  rsrc_t rsA, rsB;
  unsigned vA0, vA1, vB0, vB1, vB2;
  unsigned sA[4], sB, advB;
  unsigned wa, wq;
  unsigned mA, mB, mW;
};

constexpr unsigned a_slot(unsigned s3) { return s3 == 0 ? 126976u : (s3 - 1) * 32768u; }
#define SB __builtin_amdgcn_sched_barrier(0)

template <int T> DI unsigned bofs(const unsigned (&boff)[T], int t) { return T == 6 && t >= 3 ? boff[t - 3] + 6144 : boff[t]; }
template <int T, bool TR, int KS, class Extra>
DI void kstep(f32x4 (&acc)[4][T], const u32x4 (&ac)[4], const u32x2 (&bc)[T][2], u32x4 (&an)[4], u32x2 (&bn)[T][2],
              const unsigned (&aoff)[4], const unsigned (&boff)[T], const Dma& d, Extra&& extra) {
  constexpr int s = KS >> 1, KIND = KS & 1, NP = KIND == 0 ? 4 : 3, ST = KIND == 0 ? (STA ? STA : (4 * T - 1) / NP) : (STB ? STB : (4 * T - 1) / NP);
  constexpr int sn = (KS + 1) >> 1;
  constexpr unsigned asl = a_slot(sn % 3), bsl = ((KS + 1) % 5) * 12288u;
  static_for<0, 4 * T>([&](auto E) {
    constexpr int e = E.v, t = e >> 2, i = (SNAKE && (t & 1)) ? 3 - (e & 3) : (e & 3);
    const u32x4 bf = cat2(bc[t][0], bc[t][1]);
    if constexpr (KS == 0) { if (TR) mfma_z(acc[i][t], bf, ac[i]); else mfma_z(acc[i][t], ac[i], bf); }
    else { if (TR) mfma_ip(acc[i][t], bf, ac[i]); else mfma_ip(acc[i][t], ac[i], bf); }
    SB;
    if constexpr (KS < 19) {
      if constexpr (e < 4) an[e] = lds_ld<u32x4>(aoff[((KS + 1) & 1) + (sn % 3 == 0 ? 2 : 0)] + (sn % 3 == 0 ? 0u : asl) + e * 2048);
      else if constexpr (e < 4 + 2 * T) bn[(e - 4) >> 1][e & 1] = ldtr1(bofs<T>(boff, (e - 4) >> 1) + bsl + (e & 1) * 768);
    }
    constexpr int pc = e / ST;
    if constexpr (KIND == 0) {
      constexpr int adv = s < 8 ? (s + 2) * 128 : 0, dst = (int)a_slot((s + 2) % 3) + pc * 1024, off = dst >= adv ? adv : 0;
      if constexpr (pc < NP && e % ST == ST - 2) set_m0i<dst - off>(d.mA);
      if constexpr (pc < NP && e % ST == ST - 1) dma_goi<off>((pc & 1) ? d.vA1 : d.vA0, d.rsA, d.sA[pc] + (adv - off));
    } else {
      constexpr int h0 = (2 * s) % 5;
      if constexpr (pc < NP && e % ST == ST - 2) set_m0i<(h0 == 4 ? 0 : h0 * 12288) + pc * 1024>(h0 == 4 ? d.mW : d.mB);
      if constexpr (pc < NP && e % ST == ST - 1) dma_goi<0>(pc == 0 ? d.vB0 : (pc == 1 ? d.vB1 : d.vB2), d.rsB, d.sB);
    }
    extra(E);
    SB;
  });
}

struct RowStat { u32x4 xa; unsigned xb, row; float s, q; };
DI void row_load(RowStat& R, rsrc_t rsX, unsigned lane) {
  asm volatile("buffer_load_dwordx4 %0, %2, %4, %5 offen\n\tbuffer_load_dword %1, %3, %4, %5 offen offset:1024"
               : "=&v"(R.xa), "=&v"(R.xb) : "v"(lane * 16), "v"(lane * 4), "s"(rsX), "s"(R.row * 1280) : "memory");
}
template <int K> DI void row_step(RowStat& R, unsigned lane) {
  if constexpr (K < 5) {
    if constexpr (K == 0) { R.s = 0.f; R.q = 0.f; }
    const unsigned v = K < 4 ? R.xa[K & 3] : R.xb;
    asm volatile("v_dot2c_f32_bf16 %0, %2, %3\n\tv_dot2c_f32_bf16 %1, %2, %2" : "+v"(R.s), "+v"(R.q) : "v"(v), "v"(0x3f803f80u));
  } else if constexpr (K < 11) {
    constexpr int C = K == 5 ? 0xB1 : (K == 6 ? 0x4E : (K == 7 ? 0x141 : (K == 8 ? 0x140 : (K == 9 ? 0x142 : 0x143))));
    if constexpr (K == 5) asm volatile("s_nop 2" : "+v"(R.s), "+v"(R.q));
    R.s = dpp_add<C>(R.s); R.q = dpp_add<C>(R.q);
  } else if constexpr (K == 11) {
    R.s *= 1.0f / D;
    R.q = __builtin_fmaxf(R.q * (1.0f / D) - R.s * R.s, 0.f);
  } else if constexpr (K == 12) {
    R.q = __builtin_amdgcn_sqrtf(rbf(rbf(R.q) + 1e-5f));
  } else {
    lds_st<unsigned>(lane == 63 ? L_GO + 32 + (R.row & 7) * 4 : L_GO + 64, pk2(R.s, R.q));
  }
  if constexpr (K >= 5 && K < 13) asm volatile("" : "+v"(R.s), "+v"(R.q));
}
DI void row_publish(const RowStat& R, char* ws, unsigned tag, unsigned tid) {
  if (tid < 8) __hip_atomic_store((gu64*)(ws + WS_STAT) + R.row + tid, ((unsigned long long)tag << 32) | lds_ld<unsigned>(L_GO + 32 + tid * 4), RLX_AGENT);
}

struct Gran { u32x2 g0, g1, g2; unsigned off, step; };
DI void gran_issue(Gran& G, rsrc_t rs) {
  asm volatile("buffer_load_dwordx2 %0, %3, %4, 0 offen sc1\n\tbuffer_load_dwordx2 %1, %3, %4, %5 offen sc1\n\t"
               "buffer_load_dwordx2 %2, %3, %4, %6 offen sc1"
               : "=&v"(G.g0), "=&v"(G.g1), "=&v"(G.g2) : "v"(G.off), "s"(rs), "s"(G.step), "s"(2 * G.step) : "memory");
}
DI void gran_load(Gran& G, rsrc_t rs) {
  asm volatile("buffer_load_dwordx2 %0, %3, %4, 0 offen sc1\n\tbuffer_load_dwordx2 %1, %3, %4, %5 offen sc1\n\t"
               "buffer_load_dwordx2 %2, %3, %4, %6 offen sc1\n\ts_waitcnt vmcnt(0)"
               : "=&v"(G.g0), "=&v"(G.g1), "=&v"(G.g2) : "v"(G.off), "s"(rs), "s"(G.step), "s"(2 * G.step) : "memory");
}

DI unsigned urow(unsigned pos, unsigned rot) { const unsigned u = pos + rot; return (u >= 54 ? u - 54 : u) * 40960u; }

constexpr unsigned wo_tile(unsigned v) { return (v / 5) * 10 + (v % 5 == 4 ? 4 : (v % 5 < 2 ? 2 * (v % 5) : 2 * (v % 5) + 1)); }
struct W2 { rsrc_t wd, wo; unsigned rot, col; };
DI unsigned w2_off(const W2& W, int u, unsigned lw) {
  if (u < 54) return urow(u, W.rot) + lw * 10240u + W.col;
  return wo_tile(u - 54) * 20480u + (lw >> 1) * ((u - 54) % 5 == 4 ? 102400u : 20480u) + (lw & 1) * 10240u + W.col;
}
DI u32x4 w2_load(const W2& W, int u, unsigned lw, unsigned voff) {
  return __builtin_amdgcn_raw_buffer_load_b128(u < 54 ? W.wd : W.wo, voff, w2_off(W, u, lw), 0);
}

template <int T, bool TR, bool PF, bool LASTA, class Pre>
DI void main_loop(Pre&& pre, RowStat& R, char* ws, f32x4 (&acc)[4][T], unsigned (&boff)[T], Dma& d, unsigned w, unsigned lane, unsigned tid,
                  unsigned epoch, unsigned& tag, unsigned rb, unsigned ccol, rsrc_t rsB2, rsrc_t rsWS, unsigned cb, unsigned rot, u32x4 (&pre2)[36] STAMP_ARG) {
  const unsigned g = lane >> 4, r = lane & 15, m = w & 3;
  unsigned aoff[4];
  aoff[0] = (64 * m + r) * 128 + ((g ^ (r >> 1)) << 4);
  aoff[1] = aoff[0] ^ 64;
  aoff[2] = aoff[0] + a_slot(0); aoff[3] = aoff[1] + a_slot(0);
  const unsigned wq = d.wq, wa = d.wa;
  asm volatile("" : "+v"(aoff[0]), "+v"(aoff[1]), "+v"(aoff[2]), "+v"(aoff[3]), "+v"(boff[0]), "+v"(boff[1]), "+v"(boff[2]));
  if (T == 5) asm volatile("" : "+v"(boff[3]), "+v"(boff[4]));
  Gran G;
  unsigned v2, vb2, vpf, tA[3], tB[3];
  row_load(R, d.rsA, lane);
  {
    asm volatile("s_waitcnt vmcnt(%2) lgkmcnt(0)\n\tv_readfirstlane_b32 %0, %1\n\ts_nop 3" : "=s"(tag) : "v"(epoch), "i"(5 + NA1) : "memory");
    tag = rfl(tag) + 1;
    bar();
  }
  STAMP(1);
  u32x4 a0[4], a1[4];
  u32x2 b0[T][2], b1[T][2];
#pragma unroll
  for (int i = 0; i < 4; ++i) a0[i] = lds_ld<u32x4>(aoff[2] + i * 2048);
#pragma unroll
  for (int t = 0; t < T; ++t) { b0[t][0] = ldtr1(bofs<T>(boff, t)); b0[t][1] = ldtr1(bofs<T>(boff, t) + 768); }
  SB;
#pragma unroll
  for (int i = NA1; i < 4; ++i) dma16(a_slot(1) + d.mA + i * 1024, (i & 1) ? d.vA1 : d.vA0, d.rsA, d.sA[i] + 128);
  const bool y = w >= 4;
  bool ok = true;
  auto ln_store = [&] {
    const float v0 = y ? -bflo(G.g0[0]) : __builtin_bit_cast(float, G.g0[0]) + __builtin_bit_cast(float, G.g1[0]) + __builtin_bit_cast(float, G.g2[0]);
    const unsigned la = L_MISC + (y ? tid * 4 - 1024 : 2048 + tid * 4);
    lds_st<float>(la, v0);
    lds_st<float>(y ? la + 1024 : L_GO + 64 + (lane & 7) * 4, __builtin_amdgcn_rcpf(bfhi(G.g0[0])));
  };
  static_for<0, 10>([&](auto S_) {
    constexpr int s = S_.v, L = 4 * T;
    constexpr int NX = (PF && s >= 1 && 2 * s <= NPRE) ? 2 : 0;
    kstep<T, TR, 2 * s>(acc, a0, b0, a1, b1, aoff, boff, d, [&](auto E) {
      constexpr int e = E.v;
      if constexpr (s == 2) {
        if constexpr (e == 2) { vb2 = v2 + (3 * wq) * 10240u; asm volatile("" : "+v"(vb2)); }
        if constexpr (e == 5) { G.off = w < 4 ? WS_CSUM + ccol * 8 : WS_STAT + (rb * 256 + tid - 256) * 8; asm volatile("" : "+v"(G.off)); }
        if constexpr (e == 8) { G.step = w < 4 ? NCOL * 8 : 0; tA[0] = urow(wa, rot) + cb * 128u; asm volatile("" : "+s"(G.step), "+s"(tA[0])); }
        if constexpr (e == 10) { tA[1] = urow(8 + wa, rot) + cb * 128u; asm volatile("" : "+s"(tA[1])); }
        if constexpr (e == 12) { tA[2] = urow(31 + wa, rot) + cb * 128u; asm volatile("" : "+s"(tA[2])); }
        if constexpr (e == 14) {
          tB[0] = urow(16, rot) + cb * 128u; tB[1] = urow(w < 4 ? 19u : 22u, rot) + cb * 128u; tB[2] = urow(w < 4 ? 25u : 28u, rot) + cb * 128u;
          asm volatile("" : "+s"(tB[0]), "+s"(tB[1]), "+s"(tB[2]));
        }
      }
      if constexpr (s == 3 && e == 2) pre();
      if constexpr (NX && e >= L - 2) {
        constexpr int u = 2 * (s - 1) + e - (L - 2);
        pre2[u] = __builtin_amdgcn_raw_buffer_load_b128(rsB2, vpf, urow(36 + u, rot) + (w & 3) * 10240u + cb * 128u, 0);
      }
      if constexpr (s < 7) { if constexpr (e == L - 3) d.sB += s == 0 ? d.advB * (1 + (w >> 2)) : d.advB; }
      else if constexpr (s == 7) {
        if constexpr (e == L - 6) d.vB0 = y ? vb2 : d.vB0;
        if constexpr (e == L - 4) d.vB1 = y ? vb2 + 10240u : d.vB1;
        if constexpr (e == L - 3) d.vB2 = y ? vb2 + 20480u : d.vB2;
        if constexpr (e == L - 2) d.sB = y ? tB[0] : d.sB + d.advB;
        if constexpr (e == L - 1) d.rsB = y ? rsB2 : d.rsB;
      } else {
        if constexpr (e == L - 3) { d.sB = tB[s - 7]; d.rsB = rsB2; d.vB0 = vb2; d.vB1 = vb2 + 10240u; d.vB2 = vb2 + 20480u; }
      }
      if constexpr (s == 9 && e == L - 4) {
        asm volatile("s_waitcnt vmcnt(7)" : "+v"(G.g0), "+v"(G.g1), "+v"(G.g2)::"memory");
        ok = all64(G.g0[1] == tag && G.g1[1] == tag && G.g2[1] == tag);
      }
      if constexpr (s == 9 && e == L - 3) ln_store();
    });
    if constexpr (s == 0) {
      wait_vm<EARLY1 ? 8 - NA1 : 11>();
      static_for<0, 14>([&](auto K) { row_step<K.v>(R, lane); });
    }
    if constexpr (s == 9) {
      if (__builtin_expect(!ok, 0)) {
#pragma unroll 1
        for (unsigned spins = 0; spins < SPIN_CAP; ++spins) {
          __builtin_amdgcn_s_sleep(2);
          gran_load(G, rsWS);
          if (all64(G.g0[1] == tag && G.g1[1] == tag && G.g2[1] == tag)) break;
        }
        ln_store();
      }
    }
    wait_lgkm0();
    if constexpr (s < 9) wait_vm<4 + NX>();
    bar();
    if constexpr (s == 0) { STAMP(12); }
    SB;
    kstep<T, TR, 2 * s + 1>(acc, a1, b1, a0, b0, aoff, boff, d, [&](auto E) {
      constexpr int e = E.v;
      if constexpr (s == 0 && e == 6) {
        if (y) {
          const unsigned so = d.sB - d.advB;
          dma16(d.mW + 49152, d.vB0, d.rsB, so); dma16(d.mW + 49152 + 1024, d.vB1, d.rsB, so); dma16(d.mW + 49152 + 2048, d.vB2, d.rsB, so);
        }
      }
      if constexpr (s == 0 && e == L - 4) {
        v2 = (lane >> 3) * 1280 + (((lane & 7) ^ (((lane >> 4) & 3) << 1)) << 4);
        vpf = y ? v2 : 0x7ffffff0u;
        asm volatile("" : "+v"(v2), "+v"(vpf));
      }
      if constexpr (s == 0 && e == L - 6) row_publish(R, ws, tag, tid);
      if constexpr (s == 8 && e == 0) gran_issue(G, rsWS);
      if constexpr (s >= 7 && s < 9 && e >= L - 4) {
        d.rsA = rsB2; d.vA0 = v2; d.vA1 = v2;
        d.sA[e - (L - 4)] = tA[s - 7] + (e - (L - 4)) * 10240;
      }
      if constexpr (s == 9 && LASTA && e == 7) {
        if (wa < 5) {
#pragma unroll
          for (int i = 0; i < 4; ++i) dma16(a_slot(0) + d.mA + i * 1024, v2, rsB2, tA[2] + i * 10240);
        }
      }
    });
    if constexpr (s == 8) { STAMP(13); }
    if constexpr (s == 2) { STAMP(26); }
    if constexpr (s == 6) { STAMP(27); }
  });
  acc_fence<4 * T>(&acc[0][0]);
  STAMP(14);
}

DI void st16(u32x4 v, rsrc_t rs, unsigned voff, unsigned soff) {
  __builtin_amdgcn_raw_buffer_store_b128(v, rs, voff, soff, 16);
  asm volatile("s_nop 1" ::"v"(v) : "memory");
}
DI void publish_flag(const Params& p, unsigned flag_off, unsigned tag, unsigned tid) {
  drain_vm();
  bar();
  if (tid == 0) __hip_atomic_store((gu32*)(p.ws + flag_off), tag, RLX_AGENT);
}
DI void st_frag(rsrc_t rs, unsigned lane, unsigned rt, unsigned step, u32x2 lo, u32x2 hi) {
  u32x4 v = {lo[0], lo[1], hi[0], hi[1]};
  st16(v, rs, lane * 16, WS_HA + (rt * NSTEP + step) * 1024);
}
DI void shared_send(unsigned w, unsigned lane, const u32x2 (&sh)[4]) {
  if (w >= 4) lds_st<u32x4>(L_SCR + w * 1024 + lane * 16, cat2(sh[0], sh[1]));
  else lds_st<u32x4>(L_SCR + w * 1024 + lane * 16, cat2(sh[2], sh[3]));
  asm volatile("" ::: "memory");
}
DI void shared_recv(rsrc_t rs, unsigned w, unsigned lane, unsigned rt0, unsigned step, const u32x2 (&sh)[4]) {
  lbar();
  u32x4 rcv = lds_ld<u32x4>(L_SCR + (w ^ 4) * 1024 + lane * 16);
  u32x2 r0 = {rcv[0], rcv[1]}, r1 = {rcv[2], rcv[3]};
  if (w >= 4) { st_frag(rs, lane, rt0 + 2, step, r0, sh[2]); st_frag(rs, lane, rt0 + 3, step, r1, sh[3]); }
  else        { st_frag(rs, lane, rt0 + 0, step, sh[0], r0); st_frag(rs, lane, rt0 + 1, step, sh[1], r1); }
}

__global__ __launch_bounds__(512, 2) void fused_block(Params p) {
  unsigned tid = __builtin_amdgcn_workitem_id_x(), lane = tid & 63;
  const unsigned w = rfl(tid >> 6);
  const unsigned b = __builtin_amdgcn_workgroup_id_x(), xcd = b & 7, slot = b >> 3;
  const unsigned rb = 4 * (xcd & 1) + (slot & 3), jq = xcd >> 1, ji = slot >> 2;
  const unsigned spi = (xcd - 4) * 4 + (slot & 3);
  const unsigned long long jl0 = (jq & 1) ? 0x4123220908070605ull : 0x4021200403020100ull, jl1 = (jq & 1) ? 0x4327266011100f0eull : 0x422524600d0c0b0aull;
  const unsigned jb = (unsigned)(((jq & 2) ? jl1 : jl0) >> (8 * ji)) & 0xff, jtype = jb >> 5, jidx = jb & 31;
  STAMP_DECL;
  STAMP(0);

  Dma d;
  RowStat rs_;
  const bool mlp = jtype == J_MLP;
  const unsigned pitch = mlp ? 3456u : 3840u, wq = (w + (ROTW ? slot : 0)) & 3, wa = (w + (ROTW ? ji : 0)) & 7;
  d.wa = wa; d.wq = wq;
  const rsrc_t rsX = mk_rsrc(p.x, S * D * 2);
  d.vA0 = (lane >> 3) * 1280 + (((lane & 7) ^ (lane >> 4)) << 4); d.vA1 = d.vA0 ^ 64;
  d.rsA = mk_rsrc(p.x, jtype == J_SPARE ? 0 : S * D * 2); d.mA = wa * 4096u;
#pragma unroll
  for (int i = 0; i < 4; ++i) d.sA[i] = (rb * 256 + 32 * wa + 8 * i) * 1280;
  unsigned epoch, tag;
  asm volatile("s_nop 4" :: "s"(rsX) : "memory");
  STAMP(6);
#pragma unroll
  for (int i = 0; i < 4; ++i) dma16(a_slot(0) + d.mA + i * 1024, (i & 1) ? d.vA1 : d.vA0, d.rsA, d.sA[i]);
  asm volatile("global_load_dword %0, %1, %2 sc1" : "=&v"(epoch) : "v"(0u), "s"(p.ws) : "memory");
  SB;
  {
    unsigned vb[3];
    const unsigned lim = mlp ? 176u : 144u;
    {
      const unsigned r0 = (lane * 43) >> 9, p0 = lane - 12 * r0;
      const bool w1 = p0 >= 8, w2 = p0 >= 4;
      const unsigned r1 = r0 + (w1 ? 6 : 5), p1 = p0 + (w1 ? -8 : 4), r2 = r0 + (w2 ? 11 : 10), p2 = p0 + (w2 ? -4 : 8);
      vb[0] = r0 * pitch + umin(p0 * 16, lim);
      vb[1] = r1 * pitch + umin((p1 * 16) ^ ((r1 & 8) << 2), lim);
      vb[2] = r2 * pitch + umin((p2 * 16) ^ 32, lim);
    }
    d.vB0 = vb[0]; d.vB1 = vb[1]; d.vB2 = vb[2];
    const unsigned mh = wq >> 1;
    const void* bw = mlp ? (mh ? p.wu : p.wg) : p.wqkv;
    const unsigned cj = mlp ? 192u : 160u * jtype, cm = mlp ? 0u : (jtype == J_KV ? 1280u : 160u), c0 = jtype == J_KV ? 1280u : 0u;
    d.rsB = mk_rsrc(bw, jtype == J_SPARE ? 0 : D * pitch);
    d.sB = (16 * (wq & 1) + (w < 4 ? 32 : 0)) * pitch + c0 + cj * jidx + cm * mh; d.advB = 64 * pitch;
    d.mB = L_B + wq * 3072u + (w < 4 ? 0u : 12288u); d.mW = L_B + wq * 3072u + (w < 4 ? 49152u : 0u);
    const unsigned bdst = L_B + (w < 4 ? 12288u : 0u) + wq * 3072u;
    dma16(bdst, d.vB0, d.rsB, d.sB); dma16(bdst + 1024, d.vB1, d.rsB, d.sB); dma16(bdst + 2048, d.vB2, d.rsB, d.sB);
    rs_.row = rfl(b * 8 + w);
    d.sB += d.advB;
    dma16(bdst + 24576, d.vB0, d.rsB, d.sB); dma16(bdst + 24576 + 1024, d.vB1, d.rsB, d.sB); dma16(bdst + 24576 + 2048, d.vB2, d.rsB, d.sB);
#pragma unroll
    for (int i = 0; i < NA1; ++i) dma16(a_slot(1) + d.mA + i * 1024, (i & 1) ? d.vA1 : d.vA0, d.rsA, d.sA[i] + 128);
  }
  STAMP(10);
  lds_st<unsigned>(L_GO + (tid & 1) * 4, 0u);

  const unsigned t2 = 28 * xcd + slot;
  const bool has2 = slot < 28 && t2 < 220;
  const unsigned G = t2 / 10, cb = t2 % 10;
  constexpr unsigned rot = 32;
  unsigned g = lane >> 4, r = lane & 15, q4 = r >> 2, p4 = lane & 3;
  const rsrc_t rsWd = mk_rsrc(p.wd, has2 ? F * D * 2 : 0);
  const rsrc_t rsWS = mk_rsrc(p.ws, WS_END);
  unsigned v2 = (lane >> 3) * 1280 + (((lane & 7) ^ (((lane >> 4) & 3) << 1)) << 4);
  auto relane = [&] {
    asm volatile("" : "+v"(tid));
    lane = tid & 63; g = lane >> 4; r = lane & 15; q4 = r >> 2; p4 = lane & 3;
    v2 = (lane >> 3) * 1280 + (((lane & 7) ^ (((lane >> 4) & 3) << 1)) << 4);
  };
  const unsigned m = w & 3, n = w >> 2;
  const unsigned rt0 = rb * 16 + m * 4;
  const unsigned brow = L_B + (8 * g + q4) * 192;
  const unsigned bx = (g & 1) << 5;

  u32x4 pre2[36];
  W2 W;
  W.wd = rsWd; W.wo = mk_rsrc(p.wo, has2 ? D * D * 2 : 0); W.rot = rot; W.col = cb * 128u;
  if (jtype == J_MLP) {
    f32x4 acc[4][6];
    unsigned boff[6];
#pragma unroll
    for (int t = 0; t < 3; ++t) {
      const unsigned gt = n ? (t == 2 ? 3 : 4 + t) : t;
      boff[t] = brow + ((32 * gt + 8 * p4) ^ bx); boff[t + 3] = boff[t] + 6144;
    }
    const unsigned ccol = tid < 96 ? 1920 + 96 * jidx + tid : 3648 + 96 * jidx + (tid < 192 ? tid - 96 : 0);
    main_loop<6, true, true, true>([] {}, rs_, p.ws, acc, boff, d, w, lane, tid, epoch, tag, rb, ccol, rsWd, rsWS, cb, rot, pre2 STAMP_PASS);
    relane();
    float mm[4];
    f32x4 cg[3], cu[3];
#pragma unroll
    for (int i = 0; i < 4; ++i) mm[i] = lds_ld<float>(L_MISC + (64 * m + 16 * i + r) * 4);
#pragma unroll
    for (int t = 0; t < 3; ++t) {
      const unsigned gt = n ? (t == 2 ? 3 : 4 + t) : t;
      cg[t] = lds_ld<f32x4>(L_MISC + 2048 + (16 * gt + 4 * g) * 4); cu[t] = lds_ld<f32x4>(L_MISC + 2048 + (96 + 16 * gt + 4 * g) * 4);
    }
    auto hval = [&](int i, int t) {
      f32x4 gg = acc[i][t] + mm[i] * cg[t];
      const f32x4 uu = acc[i][t + 3] + mm[i] * cu[t];
#pragma unroll
      for (int j = 0; j < 4; ++j) gg[j] = relu2(gg[j]);
      return pk4(gg * uu);
    };
#pragma unroll
    for (int i = 0; i < 4; ++i) { st_frag(rsWS, lane, rt0 + i, 3 * jidx + 2 * n, hval(i, 0), hval(i, 1)); SB; }
    const u32x2 sh[4] = {hval(0, 2), hval(1, 2), hval(2, 2), hval(3, 2)};
    shared_send(w, lane, sh);
    shared_recv(rsWS, w, lane, rt0, 3 * jidx + 1, sh);
    STAMP(15);
    publish_flag(p, WS_FLAGH + (rb * 32 + jidx) * FS, tag, tid);
    STAMP(3);
  } else if (jtype == J_KV) {
    f32x4 acc[4][5];
    unsigned boff[5];
    boff[0] = brow + ((80 * n + 8 * p4) ^ bx); boff[1] = boff[0] + 6144;
    boff[2] = brow + ((80 * n + 40 + 8 * p4) ^ bx); boff[3] = boff[2] + 6144;
    boff[4] = brow + ((80 * n + 32 + 40 * (p4 >> 1)) ^ bx) + (p4 & 1) * 6144;
    const unsigned kb = 126976u + m * 6720 + n * 3360, dump = L_MISC - 2048 + lane * 16;
    const unsigned ea = (q4 == 1 || r == 0) ? kb + (r == 0 ? 20 : 12 + r) * 80 : 0u, eb = (q4 == 3 || r == 0) ? kb + 1680 + (r == 0 ? 20 : 4 + r) * 80 : 0u;
    unsigned ka[6] = {kb + r * 80 + g * 16, ea ? ea + g * 16 : dump, eb ? eb + g * 16 : dump, g == 0 ? kb + r * 80 + 64 : dump,
                      g == 2 ? kb + 1680 + r * 80 + 64 : dump, (g == 0 && ea) ? ea + 64 : ((g == 2 && eb) ? eb + 64 : dump)};
    auto pin = [&] { asm volatile("" : "+v"(ka[0]), "+v"(ka[1]), "+v"(ka[2]), "+v"(ka[3]), "+v"(ka[4]), "+v"(ka[5])); };
    const unsigned ccol = tid < 80 ? 640 + 80 * jidx + tid : 1280 + 80 * jidx + (tid < 160 ? tid - 80 : 0);
    main_loop<5, false, false, false>(pin, rs_, p.ws, acc, boff, d, w, lane, tid, epoch, tag, rb, ccol, rsWd, rsWS, cb, rot, pre2 STAMP_PASS);
    relane();
    float cs[5];
    cs[0] = lds_ld<float>(L_MISC + 2048 + (40 * n + r) * 4);
    cs[1] = lds_ld<float>(L_MISC + 2048 + (80 + 40 * n + r) * 4);
    cs[2] = lds_ld<float>(L_MISC + 2048 + (40 * n + 20 + r) * 4);
    cs[3] = lds_ld<float>(L_MISC + 2048 + (80 + 40 * n + 20 + r) * 4);
    cs[4] = lds_ld<float>(L_MISC + 2048 + ((q4 & 1) * 80 + 40 * n + 16 + 20 * (q4 >> 1) + (r & 3)) * 4);
    u32x2 pk[4][3], vv[4][3];
    const unsigned vmask = 0u - (q4 & 1);
    f32x4 mm4[4], rr4[4];
#pragma unroll
    for (int i = 0; i < 4; ++i) {
      mm4[i] = lds_ld<f32x4>(L_MISC + (64 * m + 16 * i + 4 * g) * 4); rr4[i] = lds_ld<f32x4>(L_MISC + 1024 + (64 * m + 16 * i + 4 * g) * 4);
    }
    SB;
#pragma unroll
    for (int i = 0; i < 4; ++i) {
      const f32x4 mm = mm4[i], rr = rr4[i];
      const f32x4 rl = rr * 1.4426950408889634f, rh = rr * 0.5f;
      f32x4 val[5];
#pragma unroll
      for (int t = 0; t < 5; ++t) val[t] = acc[i][t] + mm * cs[t];
#pragma unroll
      for (int t = 0; t < 3; ++t) pk[i][t] = pk4(phi4(val[2 * t], rl, rh));
      vv[i][0] = pk4(val[1] * rr); vv[i][1] = pk4(val[3] * rr);
      const u32x2 v4 = pk4(val[4] * rr);
      vv[i][2] = u32x2{bsel(vmask, v4[0], 0x3f803f80u), bsel(vmask, v4[1], 0x3f803f80u)};
    }
    STAMP(25);
    f32x4 P[7] = {};
#pragma unroll
    for (int i = 0; i < 4; i += 2) {
      const bf16x8 k0 = frag(pk[i][0], pk[i + 1][0]), k2 = frag(pk[i][1], pk[i + 1][1]), k4 = frag(pk[i][2], pk[i + 1][2]);
      const bf16x8 v1 = frag(vv[i][0], vv[i + 1][0]), v3 = frag(vv[i][1], vv[i + 1][1]), v4 = frag(vv[i][2], vv[i + 1][2]);
      P[0] = mfma(k0, v1, P[0]); P[1] = mfma(k0, v4, P[1]); P[2] = mfma(k4, v1, P[2]);
      P[3] = mfma(k2, v3, P[3]); P[4] = mfma(k2, v4, P[4]); P[5] = mfma(k4, v3, P[5]); P[6] = mfma(k4, v4, P[6]);
    }
    lds_st<f32x4>(ka[0], P[0]); lds_st<f32x4>(ka[0] + 1680, P[3]); lds_st<f32x4>(ka[1], P[1]); lds_st<f32x4>(ka[2], P[4]);
    lds_st<f32x4>(ka[3], P[2]); lds_st<f32x4>(ka[4], P[5]); lds_st<f32x4>(ka[5], P[6]);
    lbar();
    if (tid < 210) {
      const unsigned a = 126976u + tid * 32;
      const f32x4 t0 = (lds_ld<f32x4>(a) + lds_ld<f32x4>(a + 6720)) + (lds_ld<f32x4>(a + 13440) + lds_ld<f32x4>(a + 20160));
      const f32x4 t1 = (lds_ld<f32x4>(a + 16) + lds_ld<f32x4>(a + 6736)) + (lds_ld<f32x4>(a + 13456) + lds_ld<f32x4>(a + 20176));
      st16(cat2(pk4(t0), pk4(t1)), rsWS, tid * 16, WS_KVP + (rb * 32 + 4 * jidx) * 840);
    }
    wait_lgkm0();
    STAMP(15);
    publish_flag(p, WS_FLAGKV + ((jidx >> 1) * 32 + (jidx & 1) * 8 + rb) * FS, tag, tid);
    STAMP(3);
  } else if (jtype == J_Q) {
    f32x4 acc[4][5];
    unsigned boff[5];
#pragma unroll
    for (int t = 0; t < 5; ++t) boff[t] = brow + n * 6144 + ((32 * t + 8 * p4) ^ bx);
    const unsigned ccol = 160 * jidx + (tid < 160 ? tid : 0);
    main_loop<5, true, false, false>([] {}, rs_, p.ws, acc, boff, d, w, lane, tid, epoch, tag, rb, ccol, rsWd, rsWS, cb, rot, pre2 STAMP_PASS);
    relane();
    u32x2 pq[4][5];
    {
      float mm[4], rr[4];
      f32x4 c[5];
#pragma unroll
      for (int i = 0; i < 4; ++i) { mm[i] = lds_ld<float>(L_MISC + (64 * m + 16 * i + r) * 4); rr[i] = lds_ld<float>(L_MISC + 1024 + (64 * m + 16 * i + r) * 4); }
#pragma unroll
      for (int t = 0; t < 5; ++t) c[t] = lds_ld<f32x4>(L_MISC + 2048 + (80 * n + 16 * t + 4 * g) * 4);
      SB;
#pragma unroll
      for (int i = 0; i < 4; ++i)
#pragma unroll
        for (int t = 0; t < 5; ++t) pq[i][t] = pk4(phi4(acc[i][t] + mm[i] * c[t], splat(rr[i] * 1.4426950408889634f), splat(rr[i] * 0.5f)));
#pragma unroll
      for (int i = 0; i < 4; ++i) asm volatile("" : "+v"(pq[i][0]), "+v"(pq[i][1]), "+v"(pq[i][2]), "+v"(pq[i][3]), "+v"(pq[i][4]));
    }
    constexpr unsigned KP = 208, KQ = 84 * KP;
    unsigned wa[2];
#pragma unroll
    for (int k = 0; k < 2; ++k) {
      const unsigned qd = 2 * (tid < 420 ? tid : 0) + k, hd = qd / 105, e = (qd % 105) / 5, dq = qd % 5, hl = hd & 3;
      wa[k] = (hd >> 2) * KQ + (e < 20 ? hl * 20 + e : 80 + hl) * KP + hl * 40 + dq * 8;
    }
    unsigned fk = n * KQ + r * KP + g * 8, fz = n * KQ + (80 + (r & 3)) * KP + g * 8;
    unsigned gm[3] = {g < 1 ? ~0u : 0u, g < 2 ? ~0u : 0u, g < 3 ? ~0u : 0u};
    u32x4 B[4][3];
    f32x4 eps4 = splat(1e-6f);
#pragma unroll
    for (int i = 0; i < 4; ++i) {
      B[i][0] = cat2(pq[i][0], pq[i][1]); B[i][1] = cat2(pq[i][2], pq[i][3]); B[i][2] = cat2(pq[i][4], u32x2{0u, 0u});
      asm volatile("" : "+v"(B[i][0]), "+v"(B[i][1]), "+v"(B[i][2]));
    }
    asm volatile("" : "+v"(wa[0]), "+v"(wa[1]), "+v"(fk), "+v"(fz), "+v"(gm[0]), "+v"(gm[1]), "+v"(gm[2]), "+v"(eps4));
    STAMP(25);
    wait_vm<0>();
    bar();
#pragma unroll
    for (int k = 0; k < 5; ++k) lds_st<u32x4>(k * 8192 + tid * 16, u32x4{0u, 0u, 0u, 0u});
    if (w == 0) poll_flags((gu32*)(p.ws + WS_FLAGKV + (jidx * 32 + (lane & 15)) * FS), true, tag);
    lbar();
    STAMP(4);
    if (w < 7) {
      u32x4 v[8];
#pragma unroll
      for (int j = 0; j < 8; ++j)
        v[j] = __builtin_amdgcn_raw_buffer_load_b128(rsWS, (tid < 420 ? tid : 0) * 16, WS_KVP + (j * 32 + 8 * jidx) * 840, 16);
      SB;
      f32x4 t0 = {0.f, 0.f, 0.f, 0.f}, t1 = t0;
#pragma unroll
      for (int j = 0; j < 8; ++j) {
        t0 += f32x4{bflo(v[j][0]), bfhi(v[j][0]), bflo(v[j][1]), bfhi(v[j][1])};
        t1 += f32x4{bflo(v[j][2]), bfhi(v[j][2]), bflo(v[j][3]), bfhi(v[j][3])};
      }
      lds_st<u32x2>(wa[0], pk4(t0)); lds_st<u32x2>(wa[1], pk4(t1));
    }
    lbar();
    STAMP(16);
    u32x4 A[12];
#pragma unroll
    for (int P = 0; P < 3; ++P) A[P] = cat2(lds_ld<u32x2>(fz + P * 64), lds_ld<u32x2>(fz + P * 64 + 32));
#pragma unroll
    for (int u = 0; u < 5; ++u) {
      const unsigned a0 = fk + u * 16 * KP + (u < 3 ? 0 : 64);
      A[3 + u] = cat2(lds_ld<u32x2>(a0), lds_ld<u32x2>(a0 + 32));
    }
#pragma unroll
    for (int u = 1; u < 5; ++u) {
      const unsigned a0 = fk + u * 16 * KP + (u < 3 ? 64 : 128);
      A[7 + u] = cat2(lds_ld<u32x2>(a0), lds_ld<u32x2>(a0 + 32));
    }
    SB;
    u32x2 sh[4];
    f32x4 den[4], num[2][5];
    static_for<0, 12>([&](auto E) {
      constexpr int P = E.v >> 2, i = E.v & 3;
      if constexpr (P == 0) mfma_c(den[i], A[0], B[i][0], eps4); else mfma_ip(den[i], A[P], B[i][P]);
    });
    u32x2 o[5];
    auto fin = [&](auto I, auto K) {
      constexpr int i = I.v, k = K.v, u = k < 8 ? k >> 1 : 4;
      if constexpr (k == 8 || !(k & 1)) {
        asm volatile("" : "+v"(num[i & 1][u]));
        const float rr = u == 0 ? den[i][0] : (u == 4 ? den[i][3] : bself(gm[(u + 2) % 3], den[i][(u + 3) & 3], den[i][u & 3]));
#pragma unroll
        for (int j = 0; j < 4; ++j) num[i & 1][u][j] = smul(num[i & 1][u][j], rr);
      }
      if constexpr (k == 8 || (k & 1)) o[u] = pk4(num[i & 1][u]);
      if constexpr (k == 3) st_frag(rsWS, lane, rt0 + i, 54 + 5 * jidx + 2 * n, o[0], o[1]);
      if constexpr (k == 7) st_frag(rsWS, lane, rt0 + i, 54 + 5 * jidx + 2 * n + 1, o[2], o[3]);
      if constexpr (k == 8) { sh[i] = o[4]; asm volatile("" : "+v"(sh[i])); }
    };
    static_for<0, 4>([&](auto I) {
      constexpr int i = I.v;
      static_for<0, 9>([&](auto K) {
        constexpr int k = K.v, u = k < 5 ? k : k - 4;
        if constexpr (k < 5) mfma_z(num[i & 1][u], A[3 + u], B[i][u < 3 ? 0 : 1]); else mfma_ip(num[i & 1][u], A[7 + u], B[i][u < 3 ? 1 : 2]);
        SB;
        if constexpr (i == 0) {
          if constexpr (k >= 5) {
            asm volatile("" : "+v"(den[k - 5]));
#pragma unroll
            for (int j = 0; j < 4; ++j) den[k - 5][j] = __builtin_amdgcn_rcpf(den[k - 5][j]);
            asm volatile("" : "+v"(den[k - 5]));
          }
        } else {
          fin(IC<i - 1>{}, K);
        }
        SB;
      });
    });
    static_for<0, 9>([&](auto K) { fin(IC<3>{}, K); SB; });
    shared_send(w, lane, sh);
    shared_recv(rsWS, w, lane, rt0, 54 + 5 * jidx + 4, sh);
    STAMP(15);
    publish_flag(p, WS_FLAGO + (rb * 32 + jidx) * FS, tag, tid);
    STAMP(3);
    if (b == 224) {
      poll_flags((gu32*)(p.ws + WS_FLAGH + tid * FS), (tid & 31) < (tid < 256 ? 18u : 4u), tag);
      bar();
      if (tid == 0) __hip_atomic_store((gu32*)(p.ws + WS_EPOCH), tag, RLX_AGENT);
    }
  } else {
    const unsigned gw = spi * 8 + w;
    unsigned k10 = 0x00003f80u, k01 = 0x3f800000u;
    asm volatile("" : "+v"(k10), "+v"(k01));
    auto cs_src = [&](unsigned u, rsrc_t& rs, unsigned& cv, unsigned& st) {
      const unsigned cline = u / 3, third = u % 3;
      const void* cw = cline < 30 ? p.wqkv : (cline < 57 ? p.wg : p.wu);
      const unsigned cpitch = cline < 30 ? 3840u : 3456u;
      const unsigned clocal = cline < 30 ? cline : (cline < 57 ? cline - 30 : cline - 57);
      rs = mk_rsrc(cw, gw < 126 ? D * cpitch : 0);
      cv = (third * 216 + (lane >> 3)) * cpitch + clocal * 128 + (lane & 7) * 16;
      st = 8 * cpitch;
    };
    auto cs_publish = [&](unsigned u, float (&cs)[8]) {
#pragma unroll
      for (int k = 0; k < 4; ++k) { const bool hi = lane & 32; const float o = shfl_x(hi ? cs[k] : cs[k + 4], lane, 32); cs[k] = (hi ? cs[k + 4] : cs[k]) + o; }
#pragma unroll
      for (int k = 0; k < 2; ++k) { const bool hi = lane & 16; const float o = shfl_x(hi ? cs[k] : cs[k + 2], lane, 16); cs[k] = (hi ? cs[k + 2] : cs[k]) + o; }
      { const bool hi = lane & 8; const float o = shfl_x(hi ? cs[0] : cs[1], lane, 8); cs[0] = (hi ? cs[1] : cs[0]) + o; }
      if (gw < 126)
        __hip_atomic_store((gu64*)(p.ws + WS_CSUM) + (u % 3) * NCOL + (u / 3) * 64 + (lane & 7) * 8 + (lane >> 3),
                           ((unsigned long long)tag << 32) | __builtin_bit_cast(unsigned, cs[0]), RLX_AGENT);
    };
    row_load(rs_, rsX, lane);
    constexpr int CW = CWIN;
    u32x4 c[CW];
    rsrc_t rs0, rs1; unsigned cv0, cv1, st0, st1;
    cs_src(gw, rs0, cv0, st0); cs_src(gw + 126, rs1, cv1, st1);
    auto cld = [&](int j) { return j < 27 ? __builtin_amdgcn_raw_buffer_load_b128(rs0, cv0 + j * st0, 0, 0) : __builtin_amdgcn_raw_buffer_load_b128(rs1, cv1 + (j - 27) * st1, 0, 0); };
#pragma unroll
    for (int j = 0; j < CW; ++j) c[j] = cld(j);
    SB;
    asm volatile("s_waitcnt vmcnt(%2) lgkmcnt(0)\n\tv_readfirstlane_b32 %0, %1\n\ts_nop 3" : "=s"(tag) : "v"(epoch), "i"(CW) : "memory");
    tag = rfl(tag) + 1;
    static_for<0, 14>([&](auto K) { row_step<K.v>(rs_, lane); });
    lbar();
    row_publish(rs_, p.ws, tag, tid);
    float cs[8] = {};
    static_for<0, 54>([&](auto J) {
      constexpr int j = J.v;
#pragma unroll
      for (int k = 0; k < 4; ++k) { cs[2 * k] = dot2(c[j % CW][k], k10, cs[2 * k]); cs[2 * k + 1] = dot2(c[j % CW][k], k01, cs[2 * k + 1]); }
      if constexpr (j + CW < 54) c[j % CW] = cld(j + CW);
      SB;
      if constexpr (j == 26) {
        cs_publish(gw, cs);
        STAMP(11);
#pragma unroll
        for (int k = 0; k < 8; ++k) cs[k] = 0.f;
      }
    });
    cs_publish(gw + 126, cs);
    STAMP(12);
    bar();
#pragma unroll 1
    for (unsigned i = w; i < 144; i += 8) dma16(i * 1024, v2, rsWd, urow(i >> 2, rot) + (i & 3) * 10240u + cb * 128u);
    drain_vm();
    bar();
  }
  if (!has2) { STAMP_END; return; }
  relane();

  const unsigned rbA = (G * 6) >> 4, rbB = (G * 6 + 5 > 127 ? 127 : G * 6 + 5) >> 4;
  if (w >= 4) {
    __builtin_amdgcn_s_setprio(HPRIO);
    const unsigned lw = w - 4, ld0 = lw * 1024;
    gu32* fo = (gu32*)(p.ws + WS_FLAGO + ((lane < 32 ? rbA : rbB) * 32 + (lane & 3)) * FS);
    gu32* fh = (gu32*)(p.ws + WS_FLAGH + ((lane < 32 ? rbA : rbB) * 32 + ((lane & 31) < 18 ? lane & 31 : 17)) * FS);
    __builtin_amdgcn_s_sleep(HSLEEP);
    if (jtype == J_KV) {
#pragma unroll
      for (int u = 31; u < 36; ++u) dma16(u * 4096 + ld0, v2, rsWd, w2_off(W, u, lw));
    }
    if (jtype != J_MLP) {
#pragma unroll
      for (int u = 0; u < NPRE; ++u) { pre2[u] = w2_load(W, 36 + u, lw, v2); SB; }
    }
#pragma unroll
    for (int u = 36 + NPRE; u < PSPLIT; ++u) { pre2[u - 36] = w2_load(W, u, lw, v2); SB; }
    if (w == 7) {
      __builtin_amdgcn_s_setprio(3);
      const unsigned v = poll_flags(fh, (lane & 31) < 18 && (lane & 31) >= 10, tag);
      lds_set(L_GO);
      if (!all64((lane & 31) >= 18 || v == tag)) poll_flags(fh, (lane & 31) < 18, tag);
      lds_set(L_GO + 4);
      __builtin_amdgcn_s_setprio(HPRIO);
    }
#pragma unroll
    for (int u = PSPLIT; u < HSPLIT; ++u) { pre2[u - 36] = w2_load(W, u, lw, v2); SB; }
    STAMP(8);
    unsigned la[3], wb[3], xo[3], st[3];
    u32x4 xr[3];
    const unsigned ch = lane & 7;
#pragma unroll
    for (int i = 0; i < 3; ++i) {
      const unsigned row = lw * 24 + i * 8 + (lane >> 3), rl = row >> 4, rr = row & 15;
      la[i] = (rl >= 3 ? 24576u + (rl - 3) * 4096u : rl * 4096u) + (ch >> 1) * 1024 + (ch & 1) * 512 + ((rr + 2 * ch) & 15) * 16;
      xo[i] = (G * 96 + row) * 1280 + cb * 128 + ch * 16;
      wb[i] = ld0 + lane * 16 + i * 49152;
      asm volatile("" : "+v"(la[i]), "+v"(wb[i]), "+v"(xo[i]));
    }
    float ri[3], rl[3];
    unsigned of = 0;
    f32x4 xf[3][2], dd[3][2], d2[3][2];
#pragma unroll
    for (int k = 2; k <= 6; ++k) {
      if (k == O_BAR && w == 7 && !all64((lane & 31) >= 4 || of == tag)) poll_flags(fo, (lane & 31) < 4, tag);
      if (k == 6) {
        wait_vm<0>();
#pragma unroll
        for (int h = 0; h < 3; ++h) {
          ri[h] = 2.0f * bfhi(st[h]) * bfhi(st[h]);
          rl[h] = -1.4426950408889634f * __builtin_amdgcn_rcpf(ri[h]);
          xf[h][0] = f32x4{bflo(xr[h][0]), bfhi(xr[h][0]), bflo(xr[h][1]), bfhi(xr[h][1])};
          xf[h][1] = f32x4{bflo(xr[h][2]), bfhi(xr[h][2]), bflo(xr[h][3]), bfhi(xr[h][3])};
          asm volatile("" : "+v"(ri[h]), "+v"(rl[h]), "+v"(xf[h][0]), "+v"(xf[h][1]));
        }
      }
      lbar();
      if (k == O_BAR - 1) { of = __hip_atomic_load(fo, RLX_AGENT); SB; }
      if (k < 6) {
        const int kk = k - 1;
#pragma unroll
        for (int u = 9 * (kk - 1) + ((kk - 1) & 1); u < 9 * kk + (kk & 1); ++u) {
          lds_st<u32x4>(wb[u / 12] + (u % 12) * 4096, pre2[u]);
          if (WSLEEP == 1) __builtin_amdgcn_s_sleep(1);
          if (WSLEEP == 2) asm volatile("s_nop 7");
          SB;
        }
      }
      if (k == 2 || k == 3) {
        constexpr int mid = (HSPLIT + 74) / 2 < 72 ? (HSPLIT + 74) / 2 : 72;
#pragma unroll
        for (int u = (k == 2 ? HSPLIT : mid); u < (k == 2 ? mid : 72); ++u) { pre2[u - 36] = w2_load(W, u, lw, v2); SB; }
        if (k == 3) { dma16(36 * 4096 + ld0, v2, W.wo, w2_off(W, 72, lw)); dma16(37 * 4096 + ld0, v2, W.wo, w2_off(W, 73, lw)); }
      }
      if (k == XK) {
#pragma unroll
        for (int h = 0; h < 3; ++h) {
          const unsigned row = G * 96 + lw * 24 + h * 8 + (lane >> 3);
          st[h] = __builtin_amdgcn_raw_buffer_load_b32(rsWS, WS_STAT + (row < S ? row : 0) * 8, 0, 16);
          xr[h] = __builtin_amdgcn_raw_buffer_load_b128(rsX, xo[h], 0, 0);
        }
      }
    }
    bar();
#pragma unroll
    for (int i = 0; i < 3; ++i) {
      dd[i][0] = lds_ld<f32x4>(la[i]); d2[i][0] = lds_ld<f32x4>(la[i] + 12288);
      dd[i][1] = lds_ld<f32x4>(la[i] + 256); d2[i][1] = lds_ld<f32x4>(la[i] + 12288 + 256);
      la[i] += 49152;
      asm volatile("" : "+v"(la[i]));
    }
    __builtin_amdgcn_s_setprio(GPRIO);
#pragma unroll
    for (int i = 0; i < 3; ++i) {
      float v[8], e[8];
#pragma unroll
      for (int j = 0; j < 8; ++j) v[j] = sadd(dd[i][j >> 2][j & 3], d2[i][j >> 2][j & 3]);
      SB;
#pragma unroll
      for (int j = 0; j < 8; ++j) e[j] = smul(v[j], rl[i]);
      SB;
#pragma unroll
      for (int j = 0; j < 8; ++j) e[j] = __builtin_amdgcn_exp2f(e[j]);
      SB;
#pragma unroll
      for (int j = 0; j < 8; ++j) e[j] = sfma(e[j], ri[i], ri[i]);
      SB;
#pragma unroll
      for (int j = 0; j < 8; ++j) e[j] = __builtin_amdgcn_rcpf(e[j]);
      SB;
#pragma unroll
      for (int j = 0; j < 8; ++j) dd[i][j >> 2][j & 3] = smul(v[j], e[j]);
      asm volatile("" : "+v"(dd[i][0]), "+v"(dd[i][1]));
      SB;
    }
    const rsrc_t rsO = mk_rsrc(p.out, S * D * 2);
    STAMP(9);
    bar();
    f32x4 ss[3][2];
#pragma unroll
    for (int i = 0; i < 3; ++i) {
      ss[i][0] = lds_ld<f32x4>(la[i]) + lds_ld<f32x4>(la[i] + 12288);
      ss[i][1] = lds_ld<f32x4>(la[i] + 256) + lds_ld<f32x4>(la[i] + 12288 + 256);
    }
#pragma unroll
    for (int i = 0; i < 3; ++i)
      __builtin_amdgcn_raw_buffer_store_b128(cat2(pk4(xf[i][0] + ss[i][0] * dd[i][0]), pk4(xf[i][1] + ss[i][1] * dd[i][1])), rsO, xo[i], 0, 0);
    STAMP(5);
    STAMP_END;
    return;
  }
  {
    const unsigned kh = w & 1, rt = G * 6 + 3 * (w >> 1);
    const rsrc_t rsH = mk_rsrc(p.ws + WS_HA + (rt < 128 ? rt : 0) * (NSTEP * 1024), rt < 128 ? (rt > 125 ? 128 - rt : 3) * (NSTEP * 1024) : 0);
    unsigned tb[3][4];
#pragma unroll
    for (int t = 0; t < 4; ++t) {
      tb[0][t] = (4 * g + q4) * 128 + ((t ^ ((2 * g + (q4 >> 1)) & 3)) << 5) + 8 * p4 + kh * 4096;
      tb[1][t] = tb[0][t] + 49152; tb[2][t] = tb[0][t] + 98304;
      asm volatile("" : "+v"(tb[0][t]), "+v"(tb[1][t]), "+v"(tb[2][t]));
    }
    auto rd1 = [&](int i, int t, int hf) {
      const int sl = i == 36 ? 36 : (2 * i) % 36, bi = sl / 12 > 2 ? 2 : sl / 12;
      return __builtin_bit_cast(u32x2, lds_tr(tb[bi][t] + (sl - 12 * bi) * 4096 + hf * 2048));
    };
    constexpr int NW = P2NW, W0 = P2W0;
    constexpr int OI = O_BAR == 4 ? 17 : (O_BAR == 5 ? 22 : 26);
    constexpr int OB0 = OI == 26 ? NW - 1 : 0;
    f32x4 acc[2][3][4];
    u32x4 a[NW][3];
    u32x2 bw[2][4][2];
    unsigned va[3];
#pragma unroll
    for (int h = 0; h < 3; ++h) va[h] = lane * 16 + h * NSTEP * 1024;
    unsigned xa[4];
#pragma unroll
    for (int t = 0; t < 4; ++t) {
      xa[t] = w * 12288u + t * 1024 + g * 256 + ((r + 4 * t + (g & 2)) & 15) * 16;
      asm volatile("" : "+v"(xa[t]));
    }
    auto put = [&](auto Z, auto E) {
      constexpr int z = Z.v, t = E.v / 3, h = 2 - E.v % 3;
      asm volatile("" : "+v"(acc[z][h][t]));
      lds_st<f32x4>(xa[t] + z * 49152 + h * 4096, acc[z][h][t]);
    };
    unsigned so = (rot + kh) * 1024;
    auto lda = [&](auto I, auto H) {
      constexpr int i = I.v, h = H.v;
      if constexpr (i < 27) {
        if (h == 0 && so >= 54 * 1024) so -= 54 * 1024;
        a[i % NW][h] = __builtin_amdgcn_raw_buffer_load_b128(rsH, va[h], so, 16);
        if (h == 2) so += 2048;
      } else {
        a[i % NW][h] = __builtin_amdgcn_raw_buffer_load_b128(rsH, va[h], (2 * i + kh) * 1024, 16);
      }
      SB;
    };
    lds_wait(L_GO);
    unsigned go2 = 0;
    STAMP(8);
    static_for<0, W0>([&](auto I) { lda(I, IC<0>{}); lda(I, IC<1>{}); lda(I, IC<2>{}); });
#pragma unroll
    for (int t = 0; t < 4; ++t) { bw[0][t][0] = rd1(0, t, 0); bw[0][t][1] = rd1(0, t, 1); }
    static_for<0, 37>([&](auto I) {
      constexpr int i = I.v;
      constexpr int lim = i + NW - 1;
      constexpr int nxt = i <= OI ? (W0 + 2 * i < lim ? W0 + 2 * i : lim) : (27 + OB0 + 2 * (i - OI - 1) < lim ? 27 + OB0 + 2 * (i - OI - 1) : lim);
      constexpr int end = i <= OI ? 27 : 37;
      constexpr int l0 = nxt < end ? nxt : -1, l1 = (nxt + 1 <= lim && nxt + 1 < end) ? nxt + 1 : -1;
      SB;
      static_for<0, 12>([&](auto E) {
        constexpr int e = E.v, t = e / 3, h = e % 3;
        const u32x4 bf = cat2(bw[i & 1][t][0], bw[i & 1][t][1]);
        if constexpr (i == 0 || i == 27) mfma_z(acc[i >= 27][2 - h][t], bf, a[i % NW][2 - h]); else mfma_ip(acc[i >= 27][2 - h][t], bf, a[i % NW][2 - h]);
        SB;
        if constexpr (i == 4 && e == 2) go2 = lds_get(L_GO + 4);
        if constexpr ((i == 27 || i == 28) && (e & 1)) put(IC<0>{}, IC<(i - 27) * 6 + e / 2>{});
        if constexpr (i == 36 && e >= 8) put(IC<1>{}, IC<e - 8>{});
        if constexpr (i < 36 && h < 2) bw[(i + 1) & 1][t][h] = rd1(i + 1, t, h);
        if constexpr (l1 < 0) { if constexpr (h == 2 && t < 3 && l0 >= 0) lda(IC<l0>{}, IC<t>{}); }
        else if constexpr (e & 1) lda(IC<(e < 6 ? l0 : l1)>{}, IC<(e >> 1) % 3>{});
        SB;
      });
      if constexpr (i == 29) { asm volatile("s_waitcnt lgkmcnt(8)" ::: "memory"); bar(); }
      if constexpr (i == 4) { if (__builtin_expect(!rfl(go2), 0)) lds_wait(L_GO + 4); asm volatile("" ::: "memory"); STAMP(17); }
      if constexpr (i > 4 && i < 27 && (i % 9 == 4 || i % 9 == 8)) {
        bar();
        if constexpr (i == 26 && OI == 26) static_for<27, 27 + OB0>([&](auto J) { lda(J, IC<0>{}); lda(J, IC<1>{}); lda(J, IC<2>{}); });
        STAMP(17 + (2 * i) / 9);
      }
    });
    STAMP(24);
    static_for<4, 12>([&](auto E) { if constexpr (E.v == 8) asm volatile("s_nop 3" ::: "memory"); put(IC<1>{}, E); SB; });
    lbar();
    STAMP(5);
    STAMP_END;
  }
}

extern "C" void kernel_launch(void* const* d_in, const int* in_sizes, int n_in, void* d_out, int out_size, void* d_ws,
                              size_t ws_size, hipStream_t stream) {
  Params p;
  p.x = d_in[0]; p.wqkv = d_in[1]; p.wo = d_in[2]; p.wg = d_in[4]; p.wu = d_in[5]; p.wd = d_in[6];
  p.out = d_out; p.ws = (char*)d_ws;
  fused_block<<<256, 512, 0, stream>>>(p);
}
```

```cpp
#include <hip/hip_runtime.h>
#include <stdint.h>

typedef __attribute__((ext_vector_type(8))) __bf16 bf16x8;
typedef __attribute__((ext_vector_type(4))) __bf16 bf16x4;
typedef __attribute__((ext_vector_type(2))) __bf16 bf16x2;
typedef __attribute__((ext_vector_type(4))) float f32x4;
typedef __attribute__((ext_vector_type(2))) float f32x2;
typedef __attribute__((ext_vector_type(4))) unsigned u32x4;
typedef __attribute__((ext_vector_type(2))) unsigned u32x2;
typedef __amdgpu_buffer_rsrc_t rsrc_t;
#define LAS __attribute__((address_space(3)))
#define GAS __attribute__((address_space(1)))
#define DI __device__ __forceinline__
#define RLX_AGENT __ATOMIC_RELAXED, __HIP_MEMORY_SCOPE_AGENT
typedef GAS unsigned gu32;
typedef GAS unsigned long long gu64;

constexpr int S = 2048, D = 640, F = 1728;
constexpr int NCOL = 5376;
constexpr int NSTEP = 74;
constexpr unsigned WS_EPOCH = 0;
#define FS 128
constexpr unsigned WS_FLAGH = 768u << 10;
constexpr unsigned WS_FLAGO = WS_FLAGH + 8 * 32 * FS;
constexpr unsigned WS_FLAGKV = WS_FLAGO + 8 * 32 * FS;
constexpr unsigned WS_STAT = 4096;
constexpr unsigned WS_CSUM = WS_STAT + S * 8;
constexpr unsigned WS_KVP = WS_CSUM + 3 * NCOL * 8;
constexpr unsigned WS_HA = 1u << 20;
constexpr unsigned WS_DBG = 12u << 20;
constexpr unsigned WS_END = 13u << 20;
constexpr unsigned L_B = 65536;
constexpr unsigned L_SCR = 147456;
constexpr unsigned L_MISC = 159744;
constexpr unsigned L_GO = L_MISC + 3072;
constexpr unsigned SPIN_CAP = 1u << 18;
#define HSLEEP 4
#define HSPLIT 64
#define PPIPE 0
#define STA 0
#define STB 2
#define SNAKE 0
#define EORD 0
#define EARLY1 1
#define ROTW 1
#define NPRE 12
#define WSCH 0
#define PSPLIT 48
#define XK 2
#define GRP 1
#define GPRIO 0
#define HPRIO 0
#define PSLEEP 2
#define P2NW 7
#define P2W0 6
#define WSLEEP 0
#define ROTM 18
#define NA1 2
#define O_BAR 5
#define PRIO_MODE 0
#define DMA_SPACE_A T
#define DMA_SPACE_B (4 * T / 3)

struct Params {
  const void* x; const void* wqkv; const void* wo; const void* wg; const void* wu; const void* wd;
  void* out; char* ws;
};

__shared__ __attribute__((aligned(16))) char smem[163840];

DI float sadd(float a, float b) { float o; asm("v_add_f32 %0, %1, %2" : "=v"(o) : "v"(a), "v"(b)); return o; }
DI float smul(float a, float b) { float o; asm("v_mul_f32 %0, %1, %2" : "=v"(o) : "v"(a), "v"(b)); return o; }
DI float sfma(float a, float b, float c) { float o; asm("v_fma_f32 %0, %1, %2, %3" : "=v"(o) : "v"(a), "v"(b), "v"(c)); return o; }
DI unsigned bsel(unsigned m, unsigned a, unsigned b) { unsigned o; asm("v_bfi_b32 %0, %1, %2, %3" : "=v"(o) : "v"(m), "v"(a), "v"(b)); return o; }
DI float bself(unsigned m, float a, float b) { return __builtin_bit_cast(float, bsel(m, __builtin_bit_cast(unsigned, a), __builtin_bit_cast(unsigned, b))); }
DI bool all64(bool c) { return __builtin_amdgcn_ballot_w64(!c) == 0; }
DI float shfl_x(float v, unsigned lane, unsigned m) {
  return __builtin_bit_cast(float, __builtin_amdgcn_ds_bpermute((int)((lane ^ m) << 2), __builtin_bit_cast(int, v)));
}
DI unsigned umin(unsigned a, unsigned b) { return a < b ? a : b; }
DI unsigned rfl(unsigned v) { return __builtin_amdgcn_readfirstlane(v); }
DI rsrc_t mk_rsrc(const void* p, unsigned bytes) { return __builtin_amdgcn_make_buffer_rsrc((void*)p, (short)0, (int)bytes, 0x00020000); }
DI void dma16(unsigned ldsdst, unsigned voff, rsrc_t rs, unsigned soff) {
  asm volatile("s_mov_b32 m0, %0\n\ts_nop 0\n\tbuffer_load_dwordx4 %1, %2, %3 offen lds" ::"s"(ldsdst), "v"(voff), "s"(rs), "s"(soff) : "memory");
}
template <int N> DI void wait_vm() { asm volatile("s_waitcnt vmcnt(%0)" ::"i"(N) : "memory"); }
DI void drain_vm() { wait_vm<0>(); __builtin_amdgcn_s_waitcnt(0x0F70); }
DI void wait_lgkm0() { asm volatile("s_waitcnt lgkmcnt(0)" ::: "memory"); }
DI void bar() { asm volatile("" ::: "memory"); __builtin_amdgcn_s_barrier(); asm volatile("" ::: "memory"); }
DI void lbar() { wait_lgkm0(); bar(); }
template <class V> DI V lds_ld(unsigned a) { return *(LAS V*)((LAS char*)smem + a); }
template <class V> DI void lds_st(unsigned a, V v) { *(LAS V*)((LAS char*)smem + a) = v; }
DI bf16x4 lds_tr(unsigned a) { return __builtin_amdgcn_ds_read_tr16_b64_v4bf16((LAS bf16x4*)((LAS char*)smem + a)); }
DI bf16x8 cat(bf16x4 a, bf16x4 b) { return __builtin_shufflevector(a, b, 0, 1, 2, 3, 4, 5, 6, 7); }
DI f32x4 mfma(bf16x8 a, bf16x8 b, f32x4 c) { return __builtin_amdgcn_mfma_f32_16x16x32_bf16(a, b, c, 0, 0, 0); }
DI unsigned pk2(float a, float b) { f32x2 v = {a, b}; return __builtin_bit_cast(unsigned, __builtin_convertvector(v, bf16x2)); }
DI u32x2 pk4(f32x4 v) { u32x2 r = {pk2(v[0], v[1]), pk2(v[2], v[3])}; return r; }
DI bf16x8 frag(u32x2 lo, u32x2 hi) { u32x4 v = {lo[0], lo[1], hi[0], hi[1]}; return __builtin_bit_cast(bf16x8, v); }
DI float bflo(unsigned v) { return __builtin_bit_cast(float, v << 16); }
DI float bfhi(unsigned v) { return __builtin_bit_cast(float, v & 0xffff0000u); }
DI float rbf(float v) { return bflo(pk2(v, 0.f)); }
DI float relu2(float v) { float o; asm("v_add_f32 %0, %1, |%1|" : "=v"(o) : "v"(v)); return o; }
DI f32x4 phi4(f32x4 u, f32x4 rl, f32x4 rh) {
  const f32x4 l = u * rl;
  f32x4 e, a;
#pragma unroll
  for (int j = 0; j < 4; ++j) {
    e[j] = __builtin_amdgcn_fmed3f(__builtin_amdgcn_exp2f(l[j]), 0.f, 1.f);
    a[j] = relu2(u[j]);
  }
  return a * rh + e + 1e-6f;
}
DI f32x4 splat(float v) { return f32x4{v, v, v, v}; }
template <int CTRL> DI float dpp_add(float v) {
  return v + __builtin_bit_cast(float, __builtin_amdgcn_update_dpp(0, __builtin_bit_cast(int, v), CTRL, 0xf, 0xf, true));
}
DI float dot2(unsigned a, unsigned b, float c) {
  return __builtin_amdgcn_fdot2_f32_bf16(__builtin_bit_cast(bf16x2, a), __builtin_bit_cast(bf16x2, b), c, false);
}
DI u32x4 ldtr2(unsigned a, unsigned d) { return __builtin_bit_cast(u32x4, cat(lds_tr(a), lds_tr(a + d))); }
DI void mfma_ip(f32x4& c, const u32x4& a, const u32x4& b) { asm volatile("v_mfma_f32_16x16x32_bf16 %0, %1, %2, %0" : "+v"(c) : "v"(a), "v"(b)); }
DI void mfma_c(f32x4& d, const u32x4& a, const u32x4& b, const f32x4& c) { asm volatile("v_mfma_f32_16x16x32_bf16 %0, %1, %2, %3" : "=&v"(d) : "v"(a), "v"(b), "v"(c)); }
DI void mfma_z(f32x4& c, const u32x4& a, const u32x4& b) { asm volatile("v_mfma_f32_16x16x32_bf16 %0, %1, %2, 0" : "=&v"(c) : "v"(a), "v"(b)); }
template <int N> DI void acc_fence(f32x4* c) {
  asm volatile("s_nop 7" ::: "memory");
#pragma unroll
  for (int i = 0; i < N; ++i) asm volatile("" : "+v"(c[i]));
}
DI u32x4 cat2(u32x2 a, u32x2 b) { return u32x4{a[0], a[1], b[0], b[1]}; }
DI u32x2 ldtr1(unsigned a) { return __builtin_bit_cast(u32x2, lds_tr(a)); }
template <int IMM> DI void set_m0i(unsigned v) { asm volatile("s_add_i32 m0, %0, %1" ::"s"(v), "i"(IMM) : "memory", "scc"); }
template <int OFF> DI void dma_goi(unsigned voff, rsrc_t rs, unsigned soff) {
  asm volatile("buffer_load_dwordx4 %0, %1, %2 offen offset:%3 lds" ::"v"(voff), "s"(rs), "s"(soff), "i"(OFF) : "memory");
}
DI void set_m0(unsigned v) { asm volatile("s_mov_b32 m0, %0" ::"s"(v) : "memory"); }
DI void dma_go(unsigned voff, rsrc_t rs, unsigned soff) {
  asm volatile("buffer_load_dwordx4 %0, %1, %2 offen lds" ::"v"(voff), "s"(rs), "s"(soff) : "memory");
}
DI f32x4 mfma_u(u32x4 a, u32x4 b, f32x4 c) { return mfma(__builtin_bit_cast(bf16x8, a), __builtin_bit_cast(bf16x8, b), c); }
struct Stamps { unsigned long long t; };
#define STAMP_DECL Stamps stv_ = {0ull}
#define STAMP_ARG , Stamps& stv_
#define STAMP_PASS , stv_
#define STAMPX(c, i) do { if ((i) == 14) { asm volatile("s_memrealtime %0\n\ts_waitcnt lgkmcnt(0)" : "=s"(stv_.t)); } } while (0)
#define STAMP_END do { if (blockIdx.x == 8 && threadIdx.x == 0) __hip_atomic_store((gu64*)(p.ws + WS_DBG), stv_.t, RLX_AGENT); } while (0)
#define STAMP(i) STAMPX(__builtin_amdgcn_workitem_id_x() == 0, i)

DI unsigned poll_flags(gu32* word, bool active, unsigned tag) {
  unsigned v = 0;
#pragma unroll 1
  for (unsigned spins = 0; spins < SPIN_CAP; ++spins) {
    v = __hip_atomic_load(word, RLX_AGENT);
    if (all64(!active || v == tag)) break;
    if (PSLEEP) __builtin_amdgcn_s_sleep(PSLEEP);
  }
  asm volatile("" ::: "memory");
  return v;
}

DI void lds_set(unsigned a) { __hip_atomic_store((LAS unsigned*)smem + a / 4, 1u, __ATOMIC_RELAXED, __HIP_MEMORY_SCOPE_WORKGROUP); }
DI unsigned lds_get(unsigned a) { return __hip_atomic_load((LAS unsigned*)smem + a / 4, __ATOMIC_RELAXED, __HIP_MEMORY_SCOPE_WORKGROUP); }
DI void lds_wait(unsigned a) {
#pragma unroll 1
  for (unsigned spins = 0; spins < (SPIN_CAP << 4); ++spins) {
    if (rfl(lds_get(a))) break;
    __builtin_amdgcn_s_sleep(1);
  }
  asm volatile("" ::: "memory");
}

template <int I> struct IC { static constexpr int v = I; };
template <int B, int E, class Fn> DI void static_for(Fn&& f) {
  if constexpr (B < E) { f(IC<B>{}); static_for<B + 1, E>(f); }
}

enum { J_MLP = 0, J_KV = 1, J_Q = 2, J_SPARE = 3 };

struct Dma {
  rsrc_t rsA, rsB;
  unsigned vA0, vA1, vB0, vB1, vB2;
  unsigned sA[4], sB, advB;
  unsigned wa, wq;
  unsigned mA, mB, mW;
};

constexpr unsigned a_slot(unsigned s3) { return s3 == 0 ? 126976u : (s3 - 1) * 32768u; }
#define SB __builtin_amdgcn_sched_barrier(0)

template <int T> DI unsigned bofs(const unsigned (&boff)[T], int t) { return T == 6 && t >= 3 ? boff[t - 3] + 6144 : boff[t]; }
template <int T, bool TR, int KS, class Extra>
DI void kstep(f32x4 (&acc)[4][T], const u32x4 (&ac)[4], const u32x2 (&bc)[T][2], u32x4 (&an)[4], u32x2 (&bn)[T][2],
              const unsigned (&aoff)[4], const unsigned (&boff)[T], const Dma& d, Extra&& extra) {
  constexpr int s = KS >> 1, KIND = KS & 1, NP = KIND == 0 ? 4 : 3, ST = KIND == 0 ? (STA ? STA : (4 * T - 1) / NP) : (STB ? STB : (4 * T - 1) / NP);
  constexpr int sn = (KS + 1) >> 1;
  constexpr unsigned asl = a_slot(sn % 3), bsl = ((KS + 1) % 5) * 12288u;
  static_for<0, 4 * T>([&](auto E) {
    constexpr int e = E.v, t = e >> 2, i = (SNAKE && (t & 1)) ? 3 - (e & 3) : (e & 3);
    const u32x4 bf = cat2(bc[t][0], bc[t][1]);
    if constexpr (KS == 0) { if (TR) mfma_z(acc[i][t], bf, ac[i]); else mfma_z(acc[i][t], ac[i], bf); }
    else { if (TR) mfma_ip(acc[i][t], bf, ac[i]); else mfma_ip(acc[i][t], ac[i], bf); }
    SB;
    if constexpr (KS < 19) {
      if constexpr (e < 4) an[e] = lds_ld<u32x4>(aoff[((KS + 1) & 1) + (sn % 3 == 0 ? 2 : 0)] + (sn % 3 == 0 ? 0u : asl) + e * 2048);
      else if constexpr (e < 4 + 2 * T) bn[(e - 4) >> 1][e & 1] = ldtr1(bofs<T>(boff, (e - 4) >> 1) + bsl + (e & 1) * 768);
    }
    constexpr int pc = e / ST;
    if constexpr (KIND == 0) {
      constexpr int adv = s < 8 ? (s + 2) * 128 : 0, dst = (int)a_slot((s + 2) % 3) + pc * 1024, off = dst >= adv ? adv : 0;
      if constexpr (pc < NP && e % ST == ST - 2) set_m0i<dst - off>(d.mA);
      if constexpr (pc < NP && e % ST == ST - 1) dma_goi<off>((pc & 1) ? d.vA1 : d.vA0, d.rsA, d.sA[pc] + (adv - off));
    } else {
      constexpr int h0 = (2 * s) % 5;
      if constexpr (pc < NP && e % ST == ST - 2) set_m0i<(h0 == 4 ? 0 : h0 * 12288) + pc * 1024>(h0 == 4 ? d.mW : d.mB);
      if constexpr (pc < NP && e % ST == ST - 1) dma_goi<0>(pc == 0 ? d.vB0 : (pc == 1 ? d.vB1 : d.vB2), d.rsB, d.sB);
    }
    extra(E);
    SB;
  });
}

struct RowStat { u32x4 xa; unsigned xb, row; float s, q; };
DI void row_load(RowStat& R, rsrc_t rsX, unsigned lane) {
  asm volatile("buffer_load_dwordx4 %0, %2, %4, %5 offen\n\tbuffer_load_dword %1, %3, %4, %5 offen offset:1024"
               : "=&v"(R.xa), "=&v"(R.xb) : "v"(lane * 16), "v"(lane * 4), "s"(rsX), "s"(R.row * 1280) : "memory");
}
template <int K> DI void row_step(RowStat& R, unsigned lane) {
  if constexpr (K < 5) {
    if constexpr (K == 0) { R.s = 0.f; R.q = 0.f; }
    const unsigned v = K < 4 ? R.xa[K & 3] : R.xb;
    asm volatile("v_dot2c_f32_bf16 %0, %2, %3\n\tv_dot2c_f32_bf16 %1, %2, %2" : "+v"(R.s), "+v"(R.q) : "v"(v), "v"(0x3f803f80u));
  } else if constexpr (K < 11) {
    constexpr int C = K == 5 ? 0xB1 : (K == 6 ? 0x4E : (K == 7 ? 0x141 : (K == 8 ? 0x140 : (K == 9 ? 0x142 : 0x143))));
    if constexpr (K == 5) asm volatile("s_nop 2" : "+v"(R.s), "+v"(R.q));
    R.s = dpp_add<C>(R.s); R.q = dpp_add<C>(R.q);
  } else if constexpr (K == 11) {
    R.s *= 1.0f / D;
    R.q = __builtin_fmaxf(R.q * (1.0f / D) - R.s * R.s, 0.f);
  } else if constexpr (K == 12) {
    R.q = __builtin_amdgcn_sqrtf(rbf(rbf(R.q) + 1e-5f));
  } else {
    lds_st<unsigned>(lane == 63 ? L_GO + 32 + (R.row & 7) * 4 : L_GO + 64, pk2(R.s, R.q));
  }
  if constexpr (K >= 5 && K < 13) asm volatile("" : "+v"(R.s), "+v"(R.q));
}
DI void row_publish(const RowStat& R, char* ws, unsigned tag, unsigned tid) {
  if (tid < 8) __hip_atomic_store((gu64*)(ws + WS_STAT) + R.row + tid, ((unsigned long long)tag << 32) | lds_ld<unsigned>(L_GO + 32 + tid * 4), RLX_AGENT);
}

struct Gran { u32x2 g0, g1, g2; unsigned off, step; };
DI void gran_issue(Gran& G, rsrc_t rs) {
  asm volatile("buffer_load_dwordx2 %0, %3, %4, 0 offen sc1\n\tbuffer_load_dwordx2 %1, %3, %4, %5 offen sc1\n\t"
               "buffer_load_dwordx2 %2, %3, %4, %6 offen sc1"
               : "=&v"(G.g0), "=&v"(G.g1), "=&v"(G.g2) : "v"(G.off), "s"(rs), "s"(G.step), "s"(2 * G.step) : "memory");
}
DI void gran_load(Gran& G, rsrc_t rs) {
  asm volatile("buffer_load_dwordx2 %0, %3, %4, 0 offen sc1\n\tbuffer_load_dwordx2 %1, %3, %4, %5 offen sc1\n\t"
               "buffer_load_dwordx2 %2, %3, %4, %6 offen sc1\n\ts_waitcnt vmcnt(0)"
               : "=&v"(G.g0), "=&v"(G.g1), "=&v"(G.g2) : "v"(G.off), "s"(rs), "s"(G.step), "s"(2 * G.step) : "memory");
}

DI unsigned urow(unsigned pos, unsigned rot) { const unsigned u = pos + rot; return (u >= 54 ? u - 54 : u) * 40960u; }

constexpr unsigned wo_tile(unsigned v) { return (v / 5) * 10 + (v % 5 == 4 ? 4 : (v % 5 < 2 ? 2 * (v % 5) : 2 * (v % 5) + 1)); }
struct W2 { rsrc_t wd, wo; unsigned rot, col; };
DI unsigned w2_off(const W2& W, int u, unsigned lw) {
  if (u < 54) return urow(u, W.rot) + lw * 10240u + W.col;
  return wo_tile(u - 54) * 20480u + (lw >> 1) * ((u - 54) % 5 == 4 ? 102400u : 20480u) + (lw & 1) * 10240u + W.col;
}
DI u32x4 w2_load(const W2& W, int u, unsigned lw, unsigned voff) {
  return __builtin_amdgcn_raw_buffer_load_b128(u < 54 ? W.wd : W.wo, voff, w2_off(W, u, lw), 0);
}

template <int T, bool TR, bool PF, bool LASTA, class Pre>
DI void main_loop(Pre&& pre, RowStat& R, char* ws, f32x4 (&acc)[4][T], unsigned (&boff)[T], Dma& d, unsigned w, unsigned lane, unsigned tid,
                  unsigned epoch, unsigned& tag, unsigned rb, unsigned ccol, rsrc_t rsB2, rsrc_t rsWS, unsigned cb, unsigned rot, u32x4 (&pre2)[36] STAMP_ARG) {
  const unsigned g = lane >> 4, r = lane & 15, m = w & 3;
  unsigned aoff[4];
  aoff[0] = (64 * m + r) * 128 + ((g ^ (r >> 1)) << 4);
  aoff[1] = aoff[0] ^ 64;
  aoff[2] = aoff[0] + a_slot(0); aoff[3] = aoff[1] + a_slot(0);
  const unsigned wq = d.wq, wa = d.wa;
  asm volatile("" : "+v"(aoff[0]), "+v"(aoff[1]), "+v"(aoff[2]), "+v"(aoff[3]), "+v"(boff[0]), "+v"(boff[1]), "+v"(boff[2]));
  if (T == 5) asm volatile("" : "+v"(boff[3]), "+v"(boff[4]));
  Gran G;
  unsigned v2, vb2, vpf, tA[3], tB[3];
  row_load(R, d.rsA, lane);
  {
    asm volatile("s_waitcnt vmcnt(%2) lgkmcnt(0)\n\tv_readfirstlane_b32 %0, %1\n\ts_nop 3" : "=s"(tag) : "v"(epoch), "i"(5 + NA1) : "memory");
    tag = rfl(tag) + 1;
    bar();
  }
  STAMP(1);
  u32x4 a0[4], a1[4];
  u32x2 b0[T][2], b1[T][2];
#pragma unroll
  for (int i = 0; i < 4; ++i) a0[i] = lds_ld<u32x4>(aoff[2] + i * 2048);
#pragma unroll
  for (int t = 0; t < T; ++t) { b0[t][0] = ldtr1(bofs<T>(boff, t)); b0[t][1] = ldtr1(bofs<T>(boff, t) + 768); }
  SB;
#pragma unroll
  for (int i = NA1; i < 4; ++i) dma16(a_slot(1) + d.mA + i * 1024, (i & 1) ? d.vA1 : d.vA0, d.rsA, d.sA[i] + 128);
  const bool y = w >= 4;
  bool ok = true;
  auto ln_store = [&] {
    const float v0 = y ? -bflo(G.g0[0]) : __builtin_bit_cast(float, G.g0[0]) + __builtin_bit_cast(float, G.g1[0]) + __builtin_bit_cast(float, G.g2[0]);
    const unsigned la = L_MISC + (y ? tid * 4 - 1024 : 2048 + tid * 4);
    lds_st<float>(la, v0);
    lds_st<float>(y ? la + 1024 : L_GO + 64 + (lane & 7) * 4, __builtin_amdgcn_rcpf(bfhi(G.g0[0])));
  };
  static_for<0, 10>([&](auto S_) {
    constexpr int s = S_.v, L = 4 * T;
    constexpr int NX = (PF && s >= 1 && 2 * s <= NPRE) ? 2 : 0;
    kstep<T, TR, 2 * s>(acc, a0, b0, a1, b1, aoff, boff, d, [&](auto E) {
      constexpr int e = E.v;
      if constexpr (s == 2) {
        if constexpr (e == 2) { vb2 = v2 + (3 * wq) * 10240u; asm volatile("" : "+v"(vb2)); }
        if constexpr (e == 5) { G.off = w < 4 ? WS_CSUM + ccol * 8 : WS_STAT + (rb * 256 + tid - 256) * 8; asm volatile("" : "+v"(G.off)); }
        if constexpr (e == 8) { G.step = w < 4 ? NCOL * 8 : 0; tA[0] = urow(wa, rot) + cb * 128u; asm volatile("" : "+s"(G.step), "+s"(tA[0])); }
        if constexpr (e == 10) { tA[1] = urow(8 + wa, rot) + cb * 128u; asm volatile("" : "+s"(tA[1])); }
        if constexpr (e == 12) { tA[2] = urow(31 + wa, rot) + cb * 128u; asm volatile("" : "+s"(tA[2])); }
        if constexpr (e == 14) {
          tB[0] = urow(16, rot) + cb * 128u; tB[1] = urow(w < 4 ? 19u : 22u, rot) + cb * 128u; tB[2] = urow(w < 4 ? 25u : 28u, rot) + cb * 128u;
          asm volatile("" : "+s"(tB[0]), "+s"(tB[1]), "+s"(tB[2]));
        }
      }
      if constexpr (s == 3 && e == 2) pre();
      if constexpr (NX && e >= L - 2) {
        constexpr int u = 2 * (s - 1) + e - (L - 2);
        pre2[u] = __builtin_amdgcn_raw_buffer_load_b128(rsB2, vpf, urow(36 + u, rot) + (w & 3) * 10240u + cb * 128u, 0);
      }
      if constexpr (s < 7) { if constexpr (e == L - 3) d.sB += s == 0 ? d.advB * (1 + (w >> 2)) : d.advB; }
      else if constexpr (s == 7) {
        if constexpr (e == L - 6) d.vB0 = y ? vb2 : d.vB0;
        if constexpr (e == L - 4) d.vB1 = y ? vb2 + 10240u : d.vB1;
        if constexpr (e == L - 3) d.vB2 = y ? vb2 + 20480u : d.vB2;
        if constexpr (e == L - 2) d.sB = y ? tB[0] : d.sB + d.advB;
        if constexpr (e == L - 1) d.rsB = y ? rsB2 : d.rsB;
      } else {
        if constexpr (e == L - 3) { d.sB = tB[s - 7]; d.rsB = rsB2; d.vB0 = vb2; d.vB1 = vb2 + 10240u; d.vB2 = vb2 + 20480u; }
      }
      if constexpr (s == 9 && e == L - 4) {
        asm volatile("s_waitcnt vmcnt(7)" : "+v"(G.g0), "+v"(G.g1), "+v"(G.g2)::"memory");
        ok = all64(G.g0[1] == tag && G.g1[1] == tag && G.g2[1] == tag);
      }
      if constexpr (s == 9 && e == L - 3) ln_store();
    });
    if constexpr (s == 0) {
      wait_vm<EARLY1 ? 8 - NA1 : 11>();
      static_for<0, 14>([&](auto K) { row_step<K.v>(R, lane); });
    }
    if constexpr (s == 9) {
      if (__builtin_expect(!ok, 0)) {
#pragma unroll 1
        for (unsigned spins = 0; spins < SPIN_CAP; ++spins) {
          __builtin_amdgcn_s_sleep(2);
          gran_load(G, rsWS);
          if (all64(G.g0[1] == tag && G.g1[1] == tag && G.g2[1] == tag)) break;
        }
        ln_store();
      }
    }
    wait_lgkm0();
    if constexpr (s < 9) wait_vm<4 + NX>();
    bar();
    if constexpr (s == 0) { STAMP(12); }
    SB;
    kstep<T, TR, 2 * s + 1>(acc, a1, b1, a0, b0, aoff, boff, d, [&](auto E) {
      constexpr int e = E.v;
      if constexpr (s == 0 && e == 6) {
        if (y) {
          const unsigned so = d.sB - d.advB;
          dma16(d.mW + 49152, d.vB0, d.rsB, so); dma16(d.mW + 49152 + 1024, d.vB1, d.rsB, so); dma16(d.mW + 49152 + 2048, d.vB2, d.rsB, so);
        }
      }
      if constexpr (s == 0 && e == L - 4) {
        v2 = (lane >> 3) * 1280 + (((lane & 7) ^ (((lane >> 4) & 3) << 1)) << 4);
        vpf = y ? v2 : 0x7ffffff0u;
        asm volatile("" : "+v"(v2), "+v"(vpf));
      }
      if constexpr (s == 0 && e == L - 6) row_publish(R, ws, tag, tid);
      if constexpr (s == 8 && e == 0) gran_issue(G, rsWS);
      if constexpr (s >= 7 && s < 9 && e >= L - 4) {
        d.rsA = rsB2; d.vA0 = v2; d.vA1 = v2;
        d.sA[e - (L - 4)] = tA[s - 7] + (e - (L - 4)) * 10240;
      }
      if constexpr (s == 9 && LASTA && e == 7) {
        if (wa < 5) {
#pragma unroll
          for (int i = 0; i < 4; ++i) dma16(a_slot(0) + d.mA + i * 1024, v2, rsB2, tA[2] + i * 10240);
        }
      }
    });
    if constexpr (s == 8) { STAMP(13); }
    if constexpr (s == 2) { STAMP(26); }
    if constexpr (s == 6) { STAMP(27); }
  });
  acc_fence<4 * T>(&acc[0][0]);
  STAMP(14);
}

DI void st16(u32x4 v, rsrc_t rs, unsigned voff, unsigned soff) {
  __builtin_amdgcn_raw_buffer_store_b128(v, rs, voff, soff, 16);
  asm volatile("s_nop 1" ::"v"(v) : "memory");
}
DI void publish_flag(const Params& p, unsigned flag_off, unsigned tag, unsigned tid) {
  drain_vm();
  bar();
  if (tid == 0) __hip_atomic_store((gu32*)(p.ws + flag_off), tag, RLX_AGENT);
}
DI void st_frag(rsrc_t rs, unsigned lane, unsigned rt, unsigned step, u32x2 lo, u32x2 hi) {
  u32x4 v = {lo[0], lo[1], hi[0], hi[1]};
  st16(v, rs, lane * 16, WS_HA + (rt * NSTEP + step) * 1024);
}
DI void shared_send(unsigned w, unsigned lane, const u32x2 (&sh)[4]) {
  if (w >= 4) lds_st<u32x4>(L_SCR + w * 1024 + lane * 16, cat2(sh[0], sh[1]));
  else lds_st<u32x4>(L_SCR + w * 1024 + lane * 16, cat2(sh[2], sh[3]));
  asm volatile("" ::: "memory");
}
DI void shared_recv(rsrc_t rs, unsigned w, unsigned lane, unsigned rt0, unsigned step, const u32x2 (&sh)[4]) {
  lbar();
  u32x4 rcv = lds_ld<u32x4>(L_SCR + (w ^ 4) * 1024 + lane * 16);
  u32x2 r0 = {rcv[0], rcv[1]}, r1 = {rcv[2], rcv[3]};
  if (w >= 4) { st_frag(rs, lane, rt0 + 2, step, r0, sh[2]); st_frag(rs, lane, rt0 + 3, step, r1, sh[3]); }
  else        { st_frag(rs, lane, rt0 + 0, step, sh[0], r0); st_frag(rs, lane, rt0 + 1, step, sh[1], r1); }
}

__global__ __launch_bounds__(512, 2) void fused_block(Params p) {
  unsigned tid = __builtin_amdgcn_workitem_id_x(), lane = tid & 63;
  const unsigned w = rfl(tid >> 6);
  const unsigned b = __builtin_amdgcn_workgroup_id_x(), xcd = b & 7, slot = b >> 3;
  const unsigned rb = 4 * (xcd & 1) + (slot & 3), jq = xcd >> 1, ji = slot >> 2;
  const unsigned spi = (xcd - 4) * 4 + (slot & 3);
  const unsigned long long jl0 = (jq & 1) ? 0x4123220908070605ull : 0x4021200403020100ull, jl1 = (jq & 1) ? 0x4327266011100f0eull : 0x422524600d0c0b0aull;
  const unsigned jb = (unsigned)(((jq & 2) ? jl1 : jl0) >> (8 * ji)) & 0xff, jtype = jb >> 5, jidx = jb & 31;
  STAMP_DECL;
  STAMP(0);

  Dma d;
  RowStat rs_;
  const bool mlp = jtype == J_MLP;
  const unsigned pitch = mlp ? 3456u : 3840u, wq = (w + (ROTW ? slot : 0)) & 3, wa = (w + (ROTW ? ji : 0)) & 7;
  d.wa = wa; d.wq = wq;
  const rsrc_t rsX = mk_rsrc(p.x, S * D * 2);
  d.vA0 = (lane >> 3) * 1280 + (((lane & 7) ^ (lane >> 4)) << 4); d.vA1 = d.vA0 ^ 64;
  d.rsA = rsX; d.mA = wa * 4096u;
#pragma unroll
  for (int i = 0; i < 4; ++i) d.sA[i] = (rb * 256 + 32 * wa + 8 * i) * 1280;
  unsigned epoch, tag;
  asm volatile("s_nop 4" :: "s"(rsX) : "memory");
  STAMP(6);
#pragma unroll
  for (int i = 0; i < 4; ++i) dma16(a_slot(0) + d.mA + i * 1024, (i & 1) ? d.vA1 : d.vA0, d.rsA, d.sA[i]);
  asm volatile("global_load_dword %0, %1, %2 sc1" : "=&v"(epoch) : "v"(0u), "s"(p.ws) : "memory");
  SB;
  {
    unsigned vb[3];
    const unsigned lim = mlp ? 176u : 144u;
    {
      const unsigned r0 = (lane * 43) >> 9, p0 = lane - 12 * r0;
      const bool w1 = p0 >= 8, w2 = p0 >= 4;
      const unsigned r1 = r0 + (w1 ? 6 : 5), p1 = p0 + (w1 ? -8 : 4), r2 = r0 + (w2 ? 11 : 10), p2 = p0 + (w2 ? -4 : 8);
      vb[0] = r0 * pitch + umin(p0 * 16, lim);
      vb[1] = r1 * pitch + umin((p1 * 16) ^ ((r1 & 8) << 2), lim);
      vb[2] = r2 * pitch + umin((p2 * 16) ^ 32, lim);
    }
    d.vB0 = vb[0]; d.vB1 = vb[1]; d.vB2 = vb[2];
    const unsigned mh = wq >> 1;
    const void* bw = mlp ? (mh ? p.wu : p.wg) : p.wqkv;
    const unsigned cj = mlp ? 192u : 160u * jtype, cm = mlp ? 0u : (jtype == J_KV ? 1280u : 160u), c0 = jtype == J_KV ? 1280u : 0u;
    d.rsB = mk_rsrc(bw, D * pitch);
    d.sB = (16 * (wq & 1) + (w < 4 ? 32 : 0)) * pitch + c0 + cj * jidx + cm * mh; d.advB = 64 * pitch;
    d.mB = L_B + wq * 3072u + (w < 4 ? 0u : 12288u); d.mW = L_B + wq * 3072u + (w < 4 ? 49152u : 0u);
    const unsigned bdst = L_B + (w < 4 ? 12288u : 0u) + wq * 3072u;
    dma16(bdst, d.vB0, d.rsB, d.sB); dma16(bdst + 1024, d.vB1, d.rsB, d.sB); dma16(bdst + 2048, d.vB2, d.rsB, d.sB);
    rs_.row = rfl(b * 8 + w);
    d.sB += d.advB;
    dma16(bdst + 24576, d.vB0, d.rsB, d.sB); dma16(bdst + 24576 + 1024, d.vB1, d.rsB, d.sB); dma16(bdst + 24576 + 2048, d.vB2, d.rsB, d.sB);
#pragma unroll
    for (int i = 0; i < NA1; ++i) dma16(a_slot(1) + d.mA + i * 1024, (i & 1) ? d.vA1 : d.vA0, d.rsA, d.sA[i] + 128);
  }
  STAMP(10);
  lds_st<unsigned>(L_GO + (tid & 1) * 4, 0u);

  const unsigned t2 = 28 * xcd + slot;
  const bool has2 = slot < 28 && t2 < 220;
  const unsigned G = t2 / 10, cb = t2 % 10;
  constexpr unsigned rot = 32;
  unsigned g = lane >> 4, r = lane & 15, q4 = r >> 2, p4 = lane & 3;
  const rsrc_t rsWd = mk_rsrc(p.wd, has2 ? F * D * 2 : 0);
  const rsrc_t rsWS = mk_rsrc(p.ws, WS_END);
  unsigned v2 = (lane >> 3) * 1280 + (((lane & 7) ^ (((lane >> 4) & 3) << 1)) << 4);
  auto relane = [&] {
    asm volatile("" : "+v"(tid));
    lane = tid & 63; g = lane >> 4; r = lane & 15; q4 = r >> 2; p4 = lane & 3;
    v2 = (lane >> 3) * 1280 + (((lane & 7) ^ (((lane >> 4) & 3) << 1)) << 4);
  };
  const unsigned m = w & 3, n = w >> 2;
  const unsigned rt0 = rb * 16 + m * 4;
  const unsigned brow = L_B + (8 * g + q4) * 192;
  const unsigned bx = (g & 1) << 5;

  u32x4 pre2[36];
  W2 W;
  W.wd = rsWd; W.wo = mk_rsrc(p.wo, has2 ? D * D * 2 : 0); W.rot = rot; W.col = cb * 128u;
  if (jtype == J_MLP) {
    f32x4 acc[4][6];
    unsigned boff[6];
#pragma unroll
    for (int t = 0; t < 3; ++t) {
      const unsigned gt = n ? (t == 2 ? 3 : 4 + t) : t;
      boff[t] = brow + ((32 * gt + 8 * p4) ^ bx); boff[t + 3] = boff[t] + 6144;
    }
    const unsigned ccol = tid < 96 ? 1920 + 96 * jidx + tid : 3648 + 96 * jidx + (tid < 192 ? tid - 96 : 0);
    main_loop<6, true, true, true>([] {}, rs_, p.ws, acc, boff, d, w, lane, tid, epoch, tag, rb, ccol, rsWd, rsWS, cb, rot, pre2 STAMP_PASS);
    relane();
    float mm[4];
    f32x4 cg[3], cu[3];
#pragma unroll
    for (int i = 0; i < 4; ++i) mm[i] = lds_ld<float>(L_MISC + (64 * m + 16 * i + r) * 4);
#pragma unroll
    for (int t = 0; t < 3; ++t) {
      const unsigned gt = n ? (t == 2 ? 3 : 4 + t) : t;
      cg[t] = lds_ld<f32x4>(L_MISC + 2048 + (16 * gt + 4 * g) * 4); cu[t] = lds_ld<f32x4>(L_MISC + 2048 + (96 + 16 * gt + 4 * g) * 4);
    }
    auto hval = [&](int i, int t) {
      f32x4 gg = acc[i][t] + mm[i] * cg[t];
      const f32x4 uu = acc[i][t + 3] + mm[i] * cu[t];
#pragma unroll
      for (int j = 0; j < 4; ++j) gg[j] = relu2(gg[j]);
      return pk4(gg * uu);
    };
#pragma unroll
    for (int i = 0; i < 4; ++i) { st_frag(rsWS, lane, rt0 + i, 3 * jidx + 2 * n, hval(i, 0), hval(i, 1)); SB; }
    const u32x2 sh[4] = {hval(0, 2), hval(1, 2), hval(2, 2), hval(3, 2)};
    shared_send(w, lane, sh);
    shared_recv(rsWS, w, lane, rt0, 3 * jidx + 1, sh);
    STAMP(15);
    publish_flag(p, WS_FLAGH + (rb * 32 + jidx) * FS, tag, tid);
    STAMP(3);
  } else if (jtype == J_KV) {
    f32x4 acc[4][5];
    unsigned boff[5];
    boff[0] = brow + ((80 * n + 8 * p4) ^ bx); boff[1] = boff[0] + 6144;
    boff[2] = brow + ((80 * n + 40 + 8 * p4) ^ bx); boff[3] = boff[2] + 6144;
    boff[4] = brow + ((80 * n + 32 + 40 * (p4 >> 1)) ^ bx) + (p4 & 1) * 6144;
    const unsigned kb = 126976u + m * 6720 + n * 3360, dump = L_MISC - 2048 + lane * 16;
    const unsigned ea = (q4 == 1 || r == 0) ? kb + (r == 0 ? 20 : 12 + r) * 80 : 0u, eb = (q4 == 3 || r == 0) ? kb + 1680 + (r == 0 ? 20 : 4 + r) * 80 : 0u;
    unsigned ka[6] = {kb + r * 80 + g * 16, ea ? ea + g * 16 : dump, eb ? eb + g * 16 : dump, g == 0 ? kb + r * 80 + 64 : dump,
                      g == 2 ? kb + 1680 + r * 80 + 64 : dump, (g == 0 && ea) ? ea + 64 : ((g == 2 && eb) ? eb + 64 : dump)};
    auto pin = [&] { asm volatile("" : "+v"(ka[0]), "+v"(ka[1]), "+v"(ka[2]), "+v"(ka[3]), "+v"(ka[4]), "+v"(ka[5])); };
    const unsigned ccol = tid < 80 ? 640 + 80 * jidx + tid : 1280 + 80 * jidx + (tid < 160 ? tid - 80 : 0);
    main_loop<5, false, false, false>(pin, rs_, p.ws, acc, boff, d, w, lane, tid, epoch, tag, rb, ccol, rsWd, rsWS, cb, rot, pre2 STAMP_PASS);
    relane();
    float cs[5];
    cs[0] = lds_ld<float>(L_MISC + 2048 + (40 * n + r) * 4);
    cs[1] = lds_ld<float>(L_MISC + 2048 + (80 + 40 * n + r) * 4);
    cs[2] = lds_ld<float>(L_MISC + 2048 + (40 * n + 20 + r) * 4);
    cs[3] = lds_ld<float>(L_MISC + 2048 + (80 + 40 * n + 20 + r) * 4);
    cs[4] = lds_ld<float>(L_MISC + 2048 + ((q4 & 1) * 80 + 40 * n + 16 + 20 * (q4 >> 1) + (r & 3)) * 4);
    u32x2 pk[4][3], vv[4][3];
    const unsigned vmask = 0u - (q4 & 1);
    f32x4 mm4[4], rr4[4];
#pragma unroll
    for (int i = 0; i < 4; ++i) {
      mm4[i] = lds_ld<f32x4>(L_MISC + (64 * m + 16 * i + 4 * g) * 4); rr4[i] = lds_ld<f32x4>(L_MISC + 1024 + (64 * m + 16 * i + 4 * g) * 4);
    }
    SB;
#pragma unroll
    for (int i = 0; i < 4; ++i) {
      const f32x4 mm = mm4[i], rr = rr4[i];
      const f32x4 rl = rr * 1.4426950408889634f, rh = rr * 0.5f;
      f32x4 val[5];
#pragma unroll
      for (int t = 0; t < 5; ++t) val[t] = acc[i][t] + mm * cs[t];
#pragma unroll
      for (int t = 0; t < 3; ++t) pk[i][t] = pk4(phi4(val[2 * t], rl, rh));
      vv[i][0] = pk4(val[1] * rr); vv[i][1] = pk4(val[3] * rr);
      const u32x2 v4 = pk4(val[4] * rr);
      vv[i][2] = u32x2{bsel(vmask, v4[0], 0x3f803f80u), bsel(vmask, v4[1], 0x3f803f80u)};
    }
    STAMP(25);
    f32x4 P[7] = {};
#pragma unroll
    for (int i = 0; i < 4; i += 2) {
      const bf16x8 k0 = frag(pk[i][0], pk[i + 1][0]), k2 = frag(pk[i][1], pk[i + 1][1]), k4 = frag(pk[i][2], pk[i + 1][2]);
      const bf16x8 v1 = frag(vv[i][0], vv[i + 1][0]), v3 = frag(vv[i][1], vv[i + 1][1]), v4 = frag(vv[i][2], vv[i + 1][2]);
      P[0] = mfma(k0, v1, P[0]); P[1] = mfma(k0, v4, P[1]); P[2] = mfma(k4, v1, P[2]);
      P[3] = mfma(k2, v3, P[3]); P[4] = mfma(k2, v4, P[4]); P[5] = mfma(k4, v3, P[5]); P[6] = mfma(k4, v4, P[6]);
    }
    lds_st<f32x4>(ka[0], P[0]); lds_st<f32x4>(ka[0] + 1680, P[3]); lds_st<f32x4>(ka[1], P[1]); lds_st<f32x4>(ka[2], P[4]);
    lds_st<f32x4>(ka[3], P[2]); lds_st<f32x4>(ka[4], P[5]); lds_st<f32x4>(ka[5], P[6]);
    lbar();
    if (tid < 210) {
      const unsigned a = 126976u + tid * 32;
      const f32x4 t0 = (lds_ld<f32x4>(a) + lds_ld<f32x4>(a + 6720)) + (lds_ld<f32x4>(a + 13440) + lds_ld<f32x4>(a + 20160));
      const f32x4 t1 = (lds_ld<f32x4>(a + 16) + lds_ld<f32x4>(a + 6736)) + (lds_ld<f32x4>(a + 13456) + lds_ld<f32x4>(a + 20176));
      st16(cat2(pk4(t0), pk4(t1)), rsWS, tid * 16, WS_KVP + (rb * 32 + 4 * jidx) * 840);
    }
    wait_lgkm0();
    STAMP(15);
    publish_flag(p, WS_FLAGKV + ((jidx >> 1) * 32 + (jidx & 1) * 8 + rb) * FS, tag, tid);
    STAMP(3);
  } else if (jtype == J_Q) {
    f32x4 acc[4][5];
    unsigned boff[5];
#pragma unroll
    for (int t = 0; t < 5; ++t) boff[t] = brow + n * 6144 + ((32 * t + 8 * p4) ^ bx);
    const unsigned ccol = 160 * jidx + (tid < 160 ? tid : 0);
    main_loop<5, true, false, false>([] {}, rs_, p.ws, acc, boff, d, w, lane, tid, epoch, tag, rb, ccol, rsWd, rsWS, cb, rot, pre2 STAMP_PASS);
    relane();
    u32x2 pq[4][5];
    {
      float mm[4], rr[4];
      f32x4 c[5];
#pragma unroll
      for (int i = 0; i < 4; ++i) { mm[i] = lds_ld<float>(L_MISC + (64 * m + 16 * i + r) * 4); rr[i] = lds_ld<float>(L_MISC + 1024 + (64 * m + 16 * i + r) * 4); }
#pragma unroll
      for (int t = 0; t < 5; ++t) c[t] = lds_ld<f32x4>(L_MISC + 2048 + (80 * n + 16 * t + 4 * g) * 4);
      SB;
#pragma unroll
      for (int i = 0; i < 4; ++i)
#pragma unroll
        for (int t = 0; t < 5; ++t) pq[i][t] = pk4(phi4(acc[i][t] + mm[i] * c[t], splat(rr[i] * 1.4426950408889634f), splat(rr[i] * 0.5f)));
#pragma unroll
      for (int i = 0; i < 4; ++i) asm volatile("" : "+v"(pq[i][0]), "+v"(pq[i][1]), "+v"(pq[i][2]), "+v"(pq[i][3]), "+v"(pq[i][4]));
    }
    constexpr unsigned KP = 208, KQ = 84 * KP;
    unsigned wa[2];
#pragma unroll
    for (int k = 0; k < 2; ++k) {
      const unsigned qd = 2 * (tid < 420 ? tid : 0) + k, hd = qd / 105, e = (qd % 105) / 5, dq = qd % 5, hl = hd & 3;
      wa[k] = (hd >> 2) * KQ + (e < 20 ? hl * 20 + e : 80 + hl) * KP + hl * 40 + dq * 8;
    }
    unsigned fk = n * KQ + r * KP + g * 8, fz = n * KQ + (80 + (r & 3)) * KP + g * 8;
    unsigned gm[3] = {g < 1 ? ~0u : 0u, g < 2 ? ~0u : 0u, g < 3 ? ~0u : 0u};
    u32x4 B[4][3];
    f32x4 eps4 = splat(1e-6f);
#pragma unroll
    for (int i = 0; i < 4; ++i) {
      B[i][0] = cat2(pq[i][0], pq[i][1]); B[i][1] = cat2(pq[i][2], pq[i][3]); B[i][2] = cat2(pq[i][4], u32x2{0u, 0u});
      asm volatile("" : "+v"(B[i][0]), "+v"(B[i][1]), "+v"(B[i][2]));
    }
    asm volatile("" : "+v"(wa[0]), "+v"(wa[1]), "+v"(fk), "+v"(fz), "+v"(gm[0]), "+v"(gm[1]), "+v"(gm[2]), "+v"(eps4));
    STAMP(25);
    wait_vm<0>();
    bar();
#pragma unroll
    for (int k = 0; k < 5; ++k) lds_st<u32x4>(k * 8192 + tid * 16, u32x4{0u, 0u, 0u, 0u});
    if (w == 0) poll_flags((gu32*)(p.ws + WS_FLAGKV + (jidx * 32 + (lane & 15)) * FS), true, tag);
    lbar();
    STAMP(4);
    if (w < 7) {
      u32x4 v[8];
#pragma unroll
      for (int j = 0; j < 8; ++j)
        v[j] = __builtin_amdgcn_raw_buffer_load_b128(rsWS, (tid < 420 ? tid : 0) * 16, WS_KVP + (j * 32 + 8 * jidx) * 840, 16);
      SB;
      f32x4 t0 = {0.f, 0.f, 0.f, 0.f}, t1 = t0;
#pragma unroll
      for (int j = 0; j < 8; ++j) {
        t0 += f32x4{bflo(v[j][0]), bfhi(v[j][0]), bflo(v[j][1]), bfhi(v[j][1])};
        t1 += f32x4{bflo(v[j][2]), bfhi(v[j][2]), bflo(v[j][3]), bfhi(v[j][3])};
      }
      lds_st<u32x2>(wa[0], pk4(t0)); lds_st<u32x2>(wa[1], pk4(t1));
    }
    lbar();
    STAMP(16);
    u32x4 A[12];
#pragma unroll
    for (int P = 0; P < 3; ++P) A[P] = cat2(lds_ld<u32x2>(fz + P * 64), lds_ld<u32x2>(fz + P * 64 + 32));
#pragma unroll
    for (int u = 0; u < 5; ++u) {
      const unsigned a0 = fk + u * 16 * KP + (u < 3 ? 0 : 64);
      A[3 + u] = cat2(lds_ld<u32x2>(a0), lds_ld<u32x2>(a0 + 32));
    }
#pragma unroll
    for (int u = 1; u < 5; ++u) {
      const unsigned a0 = fk + u * 16 * KP + (u < 3 ? 64 : 128);
      A[7 + u] = cat2(lds_ld<u32x2>(a0), lds_ld<u32x2>(a0 + 32));
    }
    SB;
    u32x2 sh[4];
    f32x4 den[4], num[2][5];
    static_for<0, 12>([&](auto E) {
      constexpr int P = E.v >> 2, i = E.v & 3;
      if constexpr (P == 0) mfma_c(den[i], A[0], B[i][0], eps4); else mfma_ip(den[i], A[P], B[i][P]);
    });
    u32x2 o[5];
    auto fin = [&](auto I, auto K) {
      constexpr int i = I.v, k = K.v, u = k < 8 ? k >> 1 : 4;
      if constexpr (k == 8 || !(k & 1)) {
        asm volatile("" : "+v"(num[i & 1][u]));
        const float rr = u == 0 ? den[i][0] : (u == 4 ? den[i][3] : bself(gm[(u + 2) % 3], den[i][(u + 3) & 3], den[i][u & 3]));
#pragma unroll
        for (int j = 0; j < 4; ++j) num[i & 1][u][j] = smul(num[i & 1][u][j], rr);
      }
      if constexpr (k == 8 || (k & 1)) o[u] = pk4(num[i & 1][u]);
      if constexpr (k == 3) st_frag(rsWS, lane, rt0 + i, 54 + 5 * jidx + 2 * n, o[0], o[1]);
      if constexpr (k == 7) st_frag(rsWS, lane, rt0 + i, 54 + 5 * jidx + 2 * n + 1, o[2], o[3]);
      if constexpr (k == 8) { sh[i] = o[4]; asm volatile("" : "+v"(sh[i])); }
    };
    static_for<0, 4>([&](auto I) {
      constexpr int i = I.v;
      static_for<0, 9>([&](auto K) {
        constexpr int k = K.v, u = k < 5 ? k : k - 4;
        if constexpr (k < 5) mfma_z(num[i & 1][u], A[3 + u], B[i][u < 3 ? 0 : 1]); else mfma_ip(num[i & 1][u], A[7 + u], B[i][u < 3 ? 1 : 2]);
        SB;
        if constexpr (i == 0) {
          if constexpr (k >= 5) {
            asm volatile("" : "+v"(den[k - 5]));
#pragma unroll
            for (int j = 0; j < 4; ++j) den[k - 5][j] = __builtin_amdgcn_rcpf(den[k - 5][j]);
            asm volatile("" : "+v"(den[k - 5]));
          }
        } else {
          fin(IC<i - 1>{}, K);
        }
        SB;
      });
    });
    static_for<0, 9>([&](auto K) { fin(IC<3>{}, K); SB; });
    shared_send(w, lane, sh);
    shared_recv(rsWS, w, lane, rt0, 54 + 5 * jidx + 4, sh);
    STAMP(15);
    publish_flag(p, WS_FLAGO + (rb * 32 + jidx) * FS, tag, tid);
    STAMP(3);
    if (b == 224) {
      poll_flags((gu32*)(p.ws + WS_FLAGH + tid * FS), (tid & 31) < (tid < 256 ? 18u : 4u), tag);
      bar();
      if (tid == 0) __hip_atomic_store((gu32*)(p.ws + WS_EPOCH), tag, RLX_AGENT);
    }
  } else {
    const unsigned gw = spi * 8 + w;
    unsigned k10 = 0x00003f80u, k01 = 0x3f800000u;
    asm volatile("" : "+v"(k10), "+v"(k01));
    auto cs_src = [&](unsigned u, rsrc_t& rs, unsigned& cv, unsigned& st) {
      const unsigned cline = u / 3, third = u % 3;
      const void* cw = cline < 30 ? p.wqkv : (cline < 57 ? p.wg : p.wu);
      const unsigned cpitch = cline < 30 ? 3840u : 3456u;
      const unsigned clocal = cline < 30 ? cline : (cline < 57 ? cline - 30 : cline - 57);
      rs = mk_rsrc(cw, gw < 126 ? D * cpitch : 0);
      cv = (third * 216 + (lane >> 3)) * cpitch + clocal * 128 + (lane & 7) * 16;
      st = 8 * cpitch;
    };
    auto cs_publish = [&](unsigned u, float (&cs)[8]) {
#pragma unroll
      for (int k = 0; k < 4; ++k) { const bool hi = lane & 32; const float o = shfl_x(hi ? cs[k] : cs[k + 4], lane, 32); cs[k] = (hi ? cs[k + 4] : cs[k]) + o; }
#pragma unroll
      for (int k = 0; k < 2; ++k) { const bool hi = lane & 16; const float o = shfl_x(hi ? cs[k] : cs[k + 2], lane, 16); cs[k] = (hi ? cs[k + 2] : cs[k]) + o; }
      { const bool hi = lane & 8; const float o = shfl_x(hi ? cs[0] : cs[1], lane, 8); cs[0] = (hi ? cs[1] : cs[0]) + o; }
      if (gw < 126)
        __hip_atomic_store((gu64*)(p.ws + WS_CSUM) + (u % 3) * NCOL + (u / 3) * 64 + (lane & 7) * 8 + (lane >> 3),
                           ((unsigned long long)tag << 32) | __builtin_bit_cast(unsigned, cs[0]), RLX_AGENT);
    };
    row_load(rs_, rsX, lane);
    asm volatile("s_waitcnt vmcnt(0) lgkmcnt(0)\n\tv_readfirstlane_b32 %0, %1\n\ts_nop 3" : "=s"(tag) : "v"(epoch) : "memory");
    tag = rfl(tag) + 1;
    static_for<0, 14>([&](auto K) { row_step<K.v>(rs_, lane); });
    u32x4 c[27];
    rsrc_t rs0, rs1; unsigned cv0, cv1, st0, st1;
    cs_src(gw, rs0, cv0, st0); cs_src(gw + 126, rs1, cv1, st1);
#pragma unroll
    for (int j = 0; j < 27; ++j) c[j] = __builtin_amdgcn_raw_buffer_load_b128(rs0, cv0 + j * st0, 0, 0);
    lbar();
    row_publish(rs_, p.ws, tag, tid);
    float cs[8] = {};
#pragma unroll
    for (int j = 0; j < 27; ++j) {
#pragma unroll
      for (int k = 0; k < 4; ++k) { cs[2 * k] = dot2(c[j][k], k10, cs[2 * k]); cs[2 * k + 1] = dot2(c[j][k], k01, cs[2 * k + 1]); }
      if (j < 8) c[j] = __builtin_amdgcn_raw_buffer_load_b128(rs1, cv1 + j * st1, 0, 0);
      SB;
    }
    cs_publish(gw, cs);
    SB;
#pragma unroll
    for (int j = 8; j < 27; ++j) c[j] = __builtin_amdgcn_raw_buffer_load_b128(rs1, cv1 + j * st1, 0, 0);
    SB;
    STAMP(11);
#pragma unroll
    for (int k = 0; k < 8; ++k) cs[k] = 0.f;
#pragma unroll
    for (int j = 0; j < 27; ++j)
#pragma unroll
      for (int k = 0; k < 4; ++k) { cs[2 * k] = dot2(c[j][k], k10, cs[2 * k]); cs[2 * k + 1] = dot2(c[j][k], k01, cs[2 * k + 1]); }
    cs_publish(gw + 126, cs);
    STAMP(12);
#pragma unroll 1
    for (unsigned i = w; i < 144; i += 8) dma16(i * 1024, v2, rsWd, urow(i >> 2, rot) + (i & 3) * 10240u + cb * 128u);
    drain_vm();
    bar();
  }
  if (!has2) { STAMP_END; return; }
  relane();

  const unsigned rbA = (G * 6) >> 4, rbB = (G * 6 + 5 > 127 ? 127 : G * 6 + 5) >> 4;
  if (w >= 4) {
    __builtin_amdgcn_s_setprio(HPRIO);
    const unsigned lw = w - 4, ld0 = lw * 1024;
    gu32* fo = (gu32*)(p.ws + WS_FLAGO + ((lane < 32 ? rbA : rbB) * 32 + (lane & 3)) * FS);
    gu32* fh = (gu32*)(p.ws + WS_FLAGH + ((lane < 32 ? rbA : rbB) * 32 + ((lane & 31) < 18 ? lane & 31 : 17)) * FS);
    __builtin_amdgcn_s_sleep(HSLEEP);
    if (jtype == J_KV) {
#pragma unroll
      for (int u = 31; u < 36; ++u) dma16(u * 4096 + ld0, v2, rsWd, w2_off(W, u, lw));
    }
    if (jtype != J_MLP) {
#pragma unroll
      for (int u = 0; u < NPRE; ++u) { pre2[u] = w2_load(W, 36 + u, lw, v2); SB; }
    }
#pragma unroll
    for (int u = 36 + NPRE; u < PSPLIT; ++u) { pre2[u - 36] = w2_load(W, u, lw, v2); SB; }
    if (w == 7) {
      __builtin_amdgcn_s_setprio(3);
      const unsigned v = poll_flags(fh, (lane & 31) < 18 && (lane & 31) >= 10, tag);
      lds_set(L_GO);
      if (!all64((lane & 31) >= 18 || v == tag)) poll_flags(fh, (lane & 31) < 18, tag);
      lds_set(L_GO + 4);
      __builtin_amdgcn_s_setprio(HPRIO);
    }
#pragma unroll
    for (int u = PSPLIT; u < HSPLIT; ++u) { pre2[u - 36] = w2_load(W, u, lw, v2); SB; }
    STAMP(8);
    unsigned la[3], wb[3], xo[3], st[3];
    u32x4 xr[3];
    const unsigned ch = lane & 7;
#pragma unroll
    for (int i = 0; i < 3; ++i) {
      const unsigned row = lw * 24 + i * 8 + (lane >> 3), rl = row >> 4, rr = row & 15;
      la[i] = (rl >= 3 ? 24576u + (rl - 3) * 4096u : rl * 4096u) + (ch >> 1) * 1024 + (ch & 1) * 512 + ((rr + 2 * ch) & 15) * 16;
      xo[i] = (G * 96 + row) * 1280 + cb * 128 + ch * 16;
      wb[i] = ld0 + lane * 16 + i * 49152;
      asm volatile("" : "+v"(la[i]), "+v"(wb[i]), "+v"(xo[i]));
    }
    float ri[3], rl[3];
    unsigned of = 0;
    f32x4 xf[3][2], dd[3][2], d2[3][2];
#pragma unroll
    for (int k = 2; k <= 6; ++k) {
      if (k == O_BAR && w == 7 && !all64((lane & 31) >= 4 || of == tag)) poll_flags(fo, (lane & 31) < 4, tag);
      if (k == 6) {
        wait_vm<0>();
#pragma unroll
        for (int h = 0; h < 3; ++h) {
          ri[h] = 2.0f * bfhi(st[h]) * bfhi(st[h]);
          rl[h] = -1.4426950408889634f * __builtin_amdgcn_rcpf(ri[h]);
          xf[h][0] = f32x4{bflo(xr[h][0]), bfhi(xr[h][0]), bflo(xr[h][1]), bfhi(xr[h][1])};
          xf[h][1] = f32x4{bflo(xr[h][2]), bfhi(xr[h][2]), bflo(xr[h][3]), bfhi(xr[h][3])};
          asm volatile("" : "+v"(ri[h]), "+v"(rl[h]), "+v"(xf[h][0]), "+v"(xf[h][1]));
        }
      }
      lbar();
      if (k == O_BAR - 1) { of = __hip_atomic_load(fo, RLX_AGENT); SB; }
      if (k < 6) {
        const int kk = k - 1;
#pragma unroll
        for (int u = 9 * (kk - 1) + ((kk - 1) & 1); u < 9 * kk + (kk & 1); ++u) {
          lds_st<u32x4>(wb[u / 12] + (u % 12) * 4096, pre2[u]);
          if (WSLEEP == 1) __builtin_amdgcn_s_sleep(1);
          if (WSLEEP == 2) asm volatile("s_nop 7");
          SB;
        }
      }
      if (k == 2 || k == 3) {
        constexpr int mid = (HSPLIT + 74) / 2 < 72 ? (HSPLIT + 74) / 2 : 72;
#pragma unroll
        for (int u = (k == 2 ? HSPLIT : mid); u < (k == 2 ? mid : 72); ++u) { pre2[u - 36] = w2_load(W, u, lw, v2); SB; }
        if (k == 3) { dma16(36 * 4096 + ld0, v2, W.wo, w2_off(W, 72, lw)); dma16(37 * 4096 + ld0, v2, W.wo, w2_off(W, 73, lw)); }
      }
      if (k == XK) {
#pragma unroll
        for (int h = 0; h < 3; ++h) {
          const unsigned row = G * 96 + lw * 24 + h * 8 + (lane >> 3);
          st[h] = __builtin_amdgcn_raw_buffer_load_b32(rsWS, WS_STAT + (row < S ? row : 0) * 8, 0, 16);
          xr[h] = __builtin_amdgcn_raw_buffer_load_b128(rsX, xo[h], 0, 0);
        }
      }
    }
    bar();
#pragma unroll
    for (int i = 0; i < 3; ++i) {
      dd[i][0] = lds_ld<f32x4>(la[i]); d2[i][0] = lds_ld<f32x4>(la[i] + 12288);
      dd[i][1] = lds_ld<f32x4>(la[i] + 256); d2[i][1] = lds_ld<f32x4>(la[i] + 12288 + 256);
      la[i] += 49152;
      asm volatile("" : "+v"(la[i]));
    }
    __builtin_amdgcn_s_setprio(GPRIO);
#pragma unroll
    for (int i = 0; i < 3; ++i) {
      float v[8], e[8];
#pragma unroll
      for (int j = 0; j < 8; ++j) v[j] = sadd(dd[i][j >> 2][j & 3], d2[i][j >> 2][j & 3]);
      SB;
#pragma unroll
      for (int j = 0; j < 8; ++j) e[j] = smul(v[j], rl[i]);
      SB;
#pragma unroll
      for (int j = 0; j < 8; ++j) e[j] = __builtin_amdgcn_exp2f(e[j]);
      SB;
#pragma unroll
      for (int j = 0; j < 8; ++j) e[j] = sfma(e[j], ri[i], ri[i]);
      SB;
#pragma unroll
      for (int j = 0; j < 8; ++j) e[j] = __builtin_amdgcn_rcpf(e[j]);
      SB;
#pragma unroll
      for (int j = 0; j < 8; ++j) dd[i][j >> 2][j & 3] = smul(v[j], e[j]);
      asm volatile("" : "+v"(dd[i][0]), "+v"(dd[i][1]));
      SB;
    }
    const rsrc_t rsO = mk_rsrc(p.out, S * D * 2);
    STAMP(9);
    bar();
    f32x4 ss[3][2];
#pragma unroll
    for (int i = 0; i < 3; ++i) {
      ss[i][0] = lds_ld<f32x4>(la[i]) + lds_ld<f32x4>(la[i] + 12288);
      ss[i][1] = lds_ld<f32x4>(la[i] + 256) + lds_ld<f32x4>(la[i] + 12288 + 256);
    }
#pragma unroll
    for (int i = 0; i < 3; ++i)
      __builtin_amdgcn_raw_buffer_store_b128(cat2(pk4(xf[i][0] + ss[i][0] * dd[i][0]), pk4(xf[i][1] + ss[i][1] * dd[i][1])), rsO, xo[i], 0, 0);
    STAMP(5);
    STAMP_END;
    return;
  }
  {
    const unsigned kh = w & 1, rt = G * 6 + 3 * (w >> 1);
    const rsrc_t rsH = mk_rsrc(p.ws + WS_HA + (rt < 128 ? rt : 0) * (NSTEP * 1024), rt < 128 ? (rt > 125 ? 128 - rt : 3) * (NSTEP * 1024) : 0);
    unsigned tb[3][4];
#pragma unroll
    for (int t = 0; t < 4; ++t) {
      tb[0][t] = (4 * g + q4) * 128 + ((t ^ ((2 * g + (q4 >> 1)) & 3)) << 5) + 8 * p4 + kh * 4096;
      tb[1][t] = tb[0][t] + 49152; tb[2][t] = tb[0][t] + 98304;
      asm volatile("" : "+v"(tb[0][t]), "+v"(tb[1][t]), "+v"(tb[2][t]));
    }
    auto rd1 = [&](int i, int t, int hf) {
      const int sl = i == 36 ? 36 : (2 * i) % 36, bi = sl / 12 > 2 ? 2 : sl / 12;
      return __builtin_bit_cast(u32x2, lds_tr(tb[bi][t] + (sl - 12 * bi) * 4096 + hf * 2048));
    };
    constexpr int NW = P2NW, W0 = P2W0;
    constexpr int OI = O_BAR == 4 ? 17 : (O_BAR == 5 ? 22 : 26);
    constexpr int OB0 = OI == 26 ? NW - 1 : 0;
    f32x4 acc[2][3][4];
    u32x4 a[NW][3];
    u32x2 bw[2][4][2];
    unsigned va[3];
#pragma unroll
    for (int h = 0; h < 3; ++h) va[h] = lane * 16 + h * NSTEP * 1024;
    unsigned xa[4];
#pragma unroll
    for (int t = 0; t < 4; ++t) {
      xa[t] = w * 12288u + t * 1024 + g * 256 + ((r + 4 * t + (g & 2)) & 15) * 16;
      asm volatile("" : "+v"(xa[t]));
    }
    auto put = [&](auto Z, auto E) {
      constexpr int z = Z.v, t = E.v / 3, h = 2 - E.v % 3;
      asm volatile("" : "+v"(acc[z][h][t]));
      lds_st<f32x4>(xa[t] + z * 49152 + h * 4096, acc[z][h][t]);
    };
    unsigned so = (rot + kh) * 1024;
    auto lda = [&](auto I, auto H) {
      constexpr int i = I.v, h = H.v;
      if constexpr (i < 27) {
        if (h == 0 && so >= 54 * 1024) so -= 54 * 1024;
        a[i % NW][h] = __builtin_amdgcn_raw_buffer_load_b128(rsH, va[h], so, 16);
        if (h == 2) so += 2048;
      } else {
        a[i % NW][h] = __builtin_amdgcn_raw_buffer_load_b128(rsH, va[h], (2 * i + kh) * 1024, 16);
      }
      SB;
    };
    lds_wait(L_GO);
    unsigned go2 = 0;
    STAMP(8);
    static_for<0, W0>([&](auto I) { lda(I, IC<0>{}); lda(I, IC<1>{}); lda(I, IC<2>{}); });
#pragma unroll
    for (int t = 0; t < 4; ++t) { bw[0][t][0] = rd1(0, t, 0); bw[0][t][1] = rd1(0, t, 1); }
    static_for<0, 37>([&](auto I) {
      constexpr int i = I.v;
      constexpr int lim = i + NW - 1;
      constexpr int nxt = i <= OI ? (W0 + 2 * i < lim ? W0 + 2 * i : lim) : (27 + OB0 + 2 * (i - OI - 1) < lim ? 27 + OB0 + 2 * (i - OI - 1) : lim);
      constexpr int end = i <= OI ? 27 : 37;
      constexpr int l0 = nxt < end ? nxt : -1, l1 = (nxt + 1 <= lim && nxt + 1 < end) ? nxt + 1 : -1;
      SB;
      static_for<0, 12>([&](auto E) {
        constexpr int e = E.v, t = e / 3, h = e % 3;
        const u32x4 bf = cat2(bw[i & 1][t][0], bw[i & 1][t][1]);
        if constexpr (i == 0 || i == 27) mfma_z(acc[i >= 27][2 - h][t], bf, a[i % NW][2 - h]); else mfma_ip(acc[i >= 27][2 - h][t], bf, a[i % NW][2 - h]);
        SB;
        if constexpr (i == 4 && e == 2) go2 = lds_get(L_GO + 4);
        if constexpr ((i == 27 || i == 28) && (e & 1)) put(IC<0>{}, IC<(i - 27) * 6 + e / 2>{});
        if constexpr (i == 36 && e >= 8) put(IC<1>{}, IC<e - 8>{});
        if constexpr (i < 36 && h < 2) bw[(i + 1) & 1][t][h] = rd1(i + 1, t, h);
        if constexpr (l1 < 0) { if constexpr (h == 2 && t < 3 && l0 >= 0) lda(IC<l0>{}, IC<t>{}); }
        else if constexpr (e & 1) lda(IC<(e < 6 ? l0 : l1)>{}, IC<(e >> 1) % 3>{});
        SB;
      });
      if constexpr (i == 29) { asm volatile("s_waitcnt lgkmcnt(8)" ::: "memory"); bar(); }
      if constexpr (i == 4) { if (__builtin_expect(!rfl(go2), 0)) lds_wait(L_GO + 4); asm volatile("" ::: "memory"); STAMP(17); }
      if constexpr (i > 4 && i < 27 && (i % 9 == 4 || i % 9 == 8)) {
        bar();
        if constexpr (i == 26 && OI == 26) static_for<27, 27 + OB0>([&](auto J) { lda(J, IC<0>{}); lda(J, IC<1>{}); lda(J, IC<2>{}); });
        STAMP(17 + (2 * i) / 9);
      }
    });
    STAMP(24);
    static_for<4, 12>([&](auto E) { if constexpr (E.v == 8) asm volatile("s_nop 3" ::: "memory"); put(IC<1>{}, E); SB; });
    lbar();
    STAMP(5);
    STAMP_END;
  }
}

__global__ __launch_bounds__(64) void tailk(Params p) {
  const unsigned long long t0 = __hip_atomic_load((gu64*)(p.ws + WS_DBG), RLX_AGENT);
  for (unsigned i = 0; i < (1u << 22) && __builtin_amdgcn_s_memrealtime() - t0 < 6000; ++i) __builtin_amdgcn_s_sleep(1);
}

extern "C" void kernel_launch(void* const* d_in, const int* in_sizes, int n_in, void* d_out, int out_size, void* d_ws,
                              size_t ws_size, hipStream_t stream) {
  Params p;
  p.x = d_in[0]; p.wqkv = d_in[1]; p.wo = d_in[2]; p.wg = d_in[4]; p.wu = d_in[5]; p.wd = d_in[6];
  p.out = d_out; p.ws = (char*)d_ws;
  fused_block<<<256, 512, 0, stream>>>(p);
  tailk<<<1, 64, 0, stream>>>(p);
}
```

```cpp
#include <hip/hip_runtime.h>
#include <stdint.h>

typedef __attribute__((ext_vector_type(8))) __bf16 bf16x8;
typedef __attribute__((ext_vector_type(4))) __bf16 bf16x4;
typedef __attribute__((ext_vector_type(2))) __bf16 bf16x2;
typedef __attribute__((ext_vector_type(4))) float f32x4;
typedef __attribute__((ext_vector_type(2))) float f32x2;
typedef __attribute__((ext_vector_type(4))) unsigned u32x4;
typedef __attribute__((ext_vector_type(2))) unsigned u32x2;
typedef __amdgpu_buffer_rsrc_t rsrc_t;
#define LAS __attribute__((address_space(3)))
#define GAS __attribute__((address_space(1)))
#define DI __device__ __forceinline__
#define RLX_AGENT __ATOMIC_RELAXED, __HIP_MEMORY_SCOPE_AGENT
typedef GAS unsigned gu32;
typedef GAS unsigned long long gu64;

constexpr int S = 2048, D = 640, F = 1728;
constexpr int NCOL = 5376;
constexpr int NSTEP = 74;
constexpr unsigned WS_EPOCH = 0;
#define FS 128
constexpr unsigned WS_FLAGH = 768u << 10;
constexpr unsigned WS_FLAGO = WS_FLAGH + 8 * 32 * FS;
constexpr unsigned WS_FLAGKV = WS_FLAGO + 8 * 32 * FS;
constexpr unsigned WS_STAT = 4096;
constexpr unsigned WS_CSUM = WS_STAT + S * 8;
constexpr unsigned WS_KVP = WS_CSUM + 3 * NCOL * 8;
constexpr unsigned WS_HA = 1u << 20;
constexpr unsigned WS_DBG = 12u << 20;
constexpr unsigned WS_END = 13u << 20;
constexpr unsigned L_B = 65536;
constexpr unsigned L_SCR = 147456;
constexpr unsigned L_MISC = 159744;
constexpr unsigned L_GO = L_MISC + 3072;
constexpr unsigned SPIN_CAP = 1u << 18;
#define HSLEEP 4
#define HSPLIT 64
#define PPIPE 0
#define STA 0
#define STB 2
#define SNAKE 0
#define EORD 0
#define EARLY1 1
#define ROTW 1
#define NPRE 12
#define WSCH 0
#define PSPLIT 48
#define WARM 1
#define XK 2
#define GRP 1
#define GPRIO 0
#define HPRIO 0
#define PSLEEP 2
#define P2NW 7
#define P2W0 6
#define WSLEEP 0
#define ROTM 18
#define NA1 2
#define O_BAR 5
#define PRIO_MODE 0
#define DMA_SPACE_A T
#define DMA_SPACE_B (4 * T / 3)

struct Params {
  const void* x; const void* wqkv; const void* wo; const void* wg; const void* wu; const void* wd;
  void* out; char* ws;
};

__shared__ __attribute__((aligned(16))) char smem[163840];

DI float sadd(float a, float b) { float o; asm("v_add_f32 %0, %1, %2" : "=v"(o) : "v"(a), "v"(b)); return o; }
DI float smul(float a, float b) { float o; asm("v_mul_f32 %0, %1, %2" : "=v"(o) : "v"(a), "v"(b)); return o; }
DI float sfma(float a, float b, float c) { float o; asm("v_fma_f32 %0, %1, %2, %3" : "=v"(o) : "v"(a), "v"(b), "v"(c)); return o; }
DI unsigned bsel(unsigned m, unsigned a, unsigned b) { unsigned o; asm("v_bfi_b32 %0, %1, %2, %3" : "=v"(o) : "v"(m), "v"(a), "v"(b)); return o; }
DI float bself(unsigned m, float a, float b) { return __builtin_bit_cast(float, bsel(m, __builtin_bit_cast(unsigned, a), __builtin_bit_cast(unsigned, b))); }
DI bool all64(bool c) { return __builtin_amdgcn_ballot_w64(!c) == 0; }
DI float shfl_x(float v, unsigned lane, unsigned m) {
  return __builtin_bit_cast(float, __builtin_amdgcn_ds_bpermute((int)((lane ^ m) << 2), __builtin_bit_cast(int, v)));
}
DI unsigned umin(unsigned a, unsigned b) { return a < b ? a : b; }
DI unsigned rfl(unsigned v) { return __builtin_amdgcn_readfirstlane(v); }
DI rsrc_t mk_rsrc(const void* p, unsigned bytes) { return __builtin_amdgcn_make_buffer_rsrc((void*)p, (short)0, (int)bytes, 0x00020000); }
DI void dma16(unsigned ldsdst, unsigned voff, rsrc_t rs, unsigned soff) {
  asm volatile("s_mov_b32 m0, %0\n\ts_nop 0\n\tbuffer_load_dwordx4 %1, %2, %3 offen lds" ::"s"(ldsdst), "v"(voff), "s"(rs), "s"(soff) : "memory");
}
template <int N> DI void wait_vm() { asm volatile("s_waitcnt vmcnt(%0)" ::"i"(N) : "memory"); }
DI void drain_vm() { wait_vm<0>(); __builtin_amdgcn_s_waitcnt(0x0F70); }
DI void wait_lgkm0() { asm volatile("s_waitcnt lgkmcnt(0)" ::: "memory"); }
DI void bar() { asm volatile("" ::: "memory"); __builtin_amdgcn_s_barrier(); asm volatile("" ::: "memory"); }
DI void lbar() { wait_lgkm0(); bar(); }
template <class V> DI V lds_ld(unsigned a) { return *(LAS V*)((LAS char*)smem + a); }
template <class V> DI void lds_st(unsigned a, V v) { *(LAS V*)((LAS char*)smem + a) = v; }
DI bf16x4 lds_tr(unsigned a) { return __builtin_amdgcn_ds_read_tr16_b64_v4bf16((LAS bf16x4*)((LAS char*)smem + a)); }
DI bf16x8 cat(bf16x4 a, bf16x4 b) { return __builtin_shufflevector(a, b, 0, 1, 2, 3, 4, 5, 6, 7); }
DI f32x4 mfma(bf16x8 a, bf16x8 b, f32x4 c) { return __builtin_amdgcn_mfma_f32_16x16x32_bf16(a, b, c, 0, 0, 0); }
DI unsigned pk2(float a, float b) { f32x2 v = {a, b}; return __builtin_bit_cast(unsigned, __builtin_convertvector(v, bf16x2)); }
DI u32x2 pk4(f32x4 v) { u32x2 r = {pk2(v[0], v[1]), pk2(v[2], v[3])}; return r; }
DI bf16x8 frag(u32x2 lo, u32x2 hi) { u32x4 v = {lo[0], lo[1], hi[0], hi[1]}; return __builtin_bit_cast(bf16x8, v); }
DI float bflo(unsigned v) { return __builtin_bit_cast(float, v << 16); }
DI float bfhi(unsigned v) { return __builtin_bit_cast(float, v & 0xffff0000u); }
DI float rbf(float v) { return bflo(pk2(v, 0.f)); }
DI float relu2(float v) { float o; asm("v_add_f32 %0, %1, |%1|" : "=v"(o) : "v"(v)); return o; }
DI f32x4 phi4(f32x4 u, f32x4 rl, f32x4 rh) {
  const f32x4 l = u * rl;
  f32x4 e, a;
#pragma unroll
  for (int j = 0; j < 4; ++j) {
    e[j] = __builtin_amdgcn_fmed3f(__builtin_amdgcn_exp2f(l[j]), 0.f, 1.f);
    a[j] = relu2(u[j]);
  }
  return a * rh + e + 1e-6f;
}
DI f32x4 splat(float v) { return f32x4{v, v, v, v}; }
template <int CTRL> DI float dpp_add(float v) {
  return v + __builtin_bit_cast(float, __builtin_amdgcn_update_dpp(0, __builtin_bit_cast(int, v), CTRL, 0xf, 0xf, true));
}
DI float dot2(unsigned a, unsigned b, float c) {
  return __builtin_amdgcn_fdot2_f32_bf16(__builtin_bit_cast(bf16x2, a), __builtin_bit_cast(bf16x2, b), c, false);
}
DI u32x4 ldtr2(unsigned a, unsigned d) { return __builtin_bit_cast(u32x4, cat(lds_tr(a), lds_tr(a + d))); }
DI void mfma_ip(f32x4& c, const u32x4& a, const u32x4& b) { asm volatile("v_mfma_f32_16x16x32_bf16 %0, %1, %2, %0" : "+v"(c) : "v"(a), "v"(b)); }
DI void mfma_c(f32x4& d, const u32x4& a, const u32x4& b, const f32x4& c) { asm volatile("v_mfma_f32_16x16x32_bf16 %0, %1, %2, %3" : "=&v"(d) : "v"(a), "v"(b), "v"(c)); }
DI void mfma_z(f32x4& c, const u32x4& a, const u32x4& b) { asm volatile("v_mfma_f32_16x16x32_bf16 %0, %1, %2, 0" : "=&v"(c) : "v"(a), "v"(b)); }
template <int N> DI void acc_fence(f32x4* c) {
  asm volatile("s_nop 7" ::: "memory");
#pragma unroll
  for (int i = 0; i < N; ++i) asm volatile("" : "+v"(c[i]));
}
DI u32x4 cat2(u32x2 a, u32x2 b) { return u32x4{a[0], a[1], b[0], b[1]}; }
DI u32x2 ldtr1(unsigned a) { return __builtin_bit_cast(u32x2, lds_tr(a)); }
template <int IMM> DI void set_m0i(unsigned v) { asm volatile("s_add_i32 m0, %0, %1" ::"s"(v), "i"(IMM) : "memory", "scc"); }
template <int OFF> DI void dma_goi(unsigned voff, rsrc_t rs, unsigned soff) {
  asm volatile("buffer_load_dwordx4 %0, %1, %2 offen offset:%3 lds" ::"v"(voff), "s"(rs), "s"(soff), "i"(OFF) : "memory");
}
DI void set_m0(unsigned v) { asm volatile("s_mov_b32 m0, %0" ::"s"(v) : "memory"); }
DI void dma_go(unsigned voff, rsrc_t rs, unsigned soff) {
  asm volatile("buffer_load_dwordx4 %0, %1, %2 offen lds" ::"v"(voff), "s"(rs), "s"(soff) : "memory");
}
DI f32x4 mfma_u(u32x4 a, u32x4 b, f32x4 c) { return mfma(__builtin_bit_cast(bf16x8, a), __builtin_bit_cast(bf16x8, b), c); }
#define STAMP_DECL
#define STAMP_ARG
#define STAMP_PASS
#define STAMPX(c, i)
#define STAMP_END
#define STAMP(i) STAMPX(__builtin_amdgcn_workitem_id_x() == 0, i)

DI unsigned poll_flags(gu32* word, bool active, unsigned tag) {
  unsigned v = 0;
#pragma unroll 1
  for (unsigned spins = 0; spins < SPIN_CAP; ++spins) {
    v = __hip_atomic_load(word, RLX_AGENT);
    if (all64(!active || v == tag)) break;
    if (PSLEEP) __builtin_amdgcn_s_sleep(PSLEEP);
  }
  asm volatile("" ::: "memory");
  return v;
}

DI void lds_set(unsigned a) { __hip_atomic_store((LAS unsigned*)smem + a / 4, 1u, __ATOMIC_RELAXED, __HIP_MEMORY_SCOPE_WORKGROUP); }
DI unsigned lds_get(unsigned a) { return __hip_atomic_load((LAS unsigned*)smem + a / 4, __ATOMIC_RELAXED, __HIP_MEMORY_SCOPE_WORKGROUP); }
DI void lds_wait(unsigned a) {
#pragma unroll 1
  for (unsigned spins = 0; spins < (SPIN_CAP << 4); ++spins) {
    if (rfl(lds_get(a))) break;
    __builtin_amdgcn_s_sleep(1);
  }
  asm volatile("" ::: "memory");
}

template <int I> struct IC { static constexpr int v = I; };
template <int B, int E, class Fn> DI void static_for(Fn&& f) {
  if constexpr (B < E) { f(IC<B>{}); static_for<B + 1, E>(f); }
}

enum { J_MLP = 0, J_KV = 1, J_Q = 2, J_SPARE = 3 };

struct Dma {
  rsrc_t rsA, rsB;
  unsigned vA0, vA1, vB0, vB1, vB2;
  unsigned sA[4], sB, advB;
  unsigned wa, wq;
  unsigned mA, mB, mW;
};

constexpr unsigned a_slot(unsigned s3) { return s3 == 0 ? 126976u : (s3 - 1) * 32768u; }
#define SB __builtin_amdgcn_sched_barrier(0)

template <int T> DI unsigned bofs(const unsigned (&boff)[T], int t) { return T == 6 && t >= 3 ? boff[t - 3] + 6144 : boff[t]; }
template <int T, bool TR, int KS, class Extra>
DI void kstep(f32x4 (&acc)[4][T], const u32x4 (&ac)[4], const u32x2 (&bc)[T][2], u32x4 (&an)[4], u32x2 (&bn)[T][2],
              const unsigned (&aoff)[4], const unsigned (&boff)[T], const Dma& d, Extra&& extra) {
  constexpr int s = KS >> 1, KIND = KS & 1, NP = KIND == 0 ? 4 : 3, ST = KIND == 0 ? (STA ? STA : (4 * T - 1) / NP) : (STB ? STB : (4 * T - 1) / NP);
  constexpr int sn = (KS + 1) >> 1;
  constexpr unsigned asl = a_slot(sn % 3), bsl = ((KS + 1) % 5) * 12288u;
  static_for<0, 4 * T>([&](auto E) {
    constexpr int e = E.v, t = e >> 2, i = (SNAKE && (t & 1)) ? 3 - (e & 3) : (e & 3);
    const u32x4 bf = cat2(bc[t][0], bc[t][1]);
    if constexpr (KS == 0) { if (TR) mfma_z(acc[i][t], bf, ac[i]); else mfma_z(acc[i][t], ac[i], bf); }
    else { if (TR) mfma_ip(acc[i][t], bf, ac[i]); else mfma_ip(acc[i][t], ac[i], bf); }
    SB;
    if constexpr (KS < 19) {
      if constexpr (e < 4) an[e] = lds_ld<u32x4>(aoff[((KS + 1) & 1) + (sn % 3 == 0 ? 2 : 0)] + (sn % 3 == 0 ? 0u : asl) + e * 2048);
      else if constexpr (e < 4 + 2 * T) bn[(e - 4) >> 1][e & 1] = ldtr1(bofs<T>(boff, (e - 4) >> 1) + bsl + (e & 1) * 768);
    }
    constexpr int pc = e / ST;
    if constexpr (KIND == 0) {
      constexpr int adv = s < 8 ? (s + 2) * 128 : 0, dst = (int)a_slot((s + 2) % 3) + pc * 1024, off = dst >= adv ? adv : 0;
      if constexpr (pc < NP && e % ST == ST - 2) set_m0i<dst - off>(d.mA);
      if constexpr (pc < NP && e % ST == ST - 1) dma_goi<off>((pc & 1) ? d.vA1 : d.vA0, d.rsA, d.sA[pc] + (adv - off));
    } else {
      constexpr int h0 = (2 * s) % 5;
      if constexpr (pc < NP && e % ST == ST - 2) set_m0i<(h0 == 4 ? 0 : h0 * 12288) + pc * 1024>(h0 == 4 ? d.mW : d.mB);
      if constexpr (pc < NP && e % ST == ST - 1) dma_goi<0>(pc == 0 ? d.vB0 : (pc == 1 ? d.vB1 : d.vB2), d.rsB, d.sB);
    }
    extra(E);
    SB;
  });
}

struct RowStat { u32x4 xa; unsigned xb, row; float s, q; };
DI void row_load(RowStat& R, rsrc_t rsX, unsigned lane) {
  asm volatile("buffer_load_dwordx4 %0, %2, %4, %5 offen\n\tbuffer_load_dword %1, %3, %4, %5 offen offset:1024"
               : "=&v"(R.xa), "=&v"(R.xb) : "v"(lane * 16), "v"(lane * 4), "s"(rsX), "s"(R.row * 1280) : "memory");
}
template <int K> DI void row_step(RowStat& R, unsigned lane) {
  if constexpr (K < 5) {
    if constexpr (K == 0) { R.s = 0.f; R.q = 0.f; }
    const unsigned v = K < 4 ? R.xa[K & 3] : R.xb;
    asm volatile("v_dot2c_f32_bf16 %0, %2, %3\n\tv_dot2c_f32_bf16 %1, %2, %2" : "+v"(R.s), "+v"(R.q) : "v"(v), "v"(0x3f803f80u));
  } else if constexpr (K < 11) {
    constexpr int C = K == 5 ? 0xB1 : (K == 6 ? 0x4E : (K == 7 ? 0x141 : (K == 8 ? 0x140 : (K == 9 ? 0x142 : 0x143))));
    if constexpr (K == 5) asm volatile("s_nop 2" : "+v"(R.s), "+v"(R.q));
    R.s = dpp_add<C>(R.s); R.q = dpp_add<C>(R.q);
  } else if constexpr (K == 11) {
    R.s *= 1.0f / D;
    R.q = __builtin_fmaxf(R.q * (1.0f / D) - R.s * R.s, 0.f);
  } else if constexpr (K == 12) {
    R.q = __builtin_amdgcn_sqrtf(rbf(rbf(R.q) + 1e-5f));
  } else {
    lds_st<unsigned>(lane == 63 ? L_GO + 32 + (R.row & 7) * 4 : L_GO + 64, pk2(R.s, R.q));
  }
  if constexpr (K >= 5 && K < 13) asm volatile("" : "+v"(R.s), "+v"(R.q));
}
DI void row_publish(const RowStat& R, char* ws, unsigned tag, unsigned tid) {
  if (tid < 8) __hip_atomic_store((gu64*)(ws + WS_STAT) + R.row + tid, ((unsigned long long)tag << 32) | lds_ld<unsigned>(L_GO + 32 + tid * 4), RLX_AGENT);
}

struct Gran { u32x2 g0, g1, g2; unsigned off, step; };
DI void gran_issue(Gran& G, rsrc_t rs) {
  asm volatile("buffer_load_dwordx2 %0, %3, %4, 0 offen sc1\n\tbuffer_load_dwordx2 %1, %3, %4, %5 offen sc1\n\t"
               "buffer_load_dwordx2 %2, %3, %4, %6 offen sc1"
               : "=&v"(G.g0), "=&v"(G.g1), "=&v"(G.g2) : "v"(G.off), "s"(rs), "s"(G.step), "s"(2 * G.step) : "memory");
}
DI void gran_load(Gran& G, rsrc_t rs) {
  asm volatile("buffer_load_dwordx2 %0, %3, %4, 0 offen sc1\n\tbuffer_load_dwordx2 %1, %3, %4, %5 offen sc1\n\t"
               "buffer_load_dwordx2 %2, %3, %4, %6 offen sc1\n\ts_waitcnt vmcnt(0)"
               : "=&v"(G.g0), "=&v"(G.g1), "=&v"(G.g2) : "v"(G.off), "s"(rs), "s"(G.step), "s"(2 * G.step) : "memory");
}

DI unsigned urow(unsigned pos, unsigned rot) { const unsigned u = pos + rot; return (u >= 54 ? u - 54 : u) * 40960u; }

constexpr unsigned wo_tile(unsigned v) { return (v / 5) * 10 + (v % 5 == 4 ? 4 : (v % 5 < 2 ? 2 * (v % 5) : 2 * (v % 5) + 1)); }
struct W2 { rsrc_t wd, wo; unsigned rot, col; };
DI unsigned w2_off(const W2& W, int u, unsigned lw) {
  if (u < 54) return urow(u, W.rot) + lw * 10240u + W.col;
  return wo_tile(u - 54) * 20480u + (lw >> 1) * ((u - 54) % 5 == 4 ? 102400u : 20480u) + (lw & 1) * 10240u + W.col;
}
DI u32x4 w2_load(const W2& W, int u, unsigned lw, unsigned voff) {
  return __builtin_amdgcn_raw_buffer_load_b128(u < 54 ? W.wd : W.wo, voff, w2_off(W, u, lw), 0);
}

template <int T, bool TR, bool PF, bool LASTA, class Pre>
DI void main_loop(Pre&& pre, RowStat& R, char* ws, f32x4 (&acc)[4][T], unsigned (&boff)[T], Dma& d, unsigned w, unsigned lane, unsigned tid,
                  unsigned epoch, unsigned& tag, unsigned rb, unsigned ccol, rsrc_t rsB2, rsrc_t rsWS, unsigned cb, unsigned rot, u32x4 (&pre2)[36] STAMP_ARG) {
  const unsigned g = lane >> 4, r = lane & 15, m = w & 3;
  unsigned aoff[4];
  aoff[0] = (64 * m + r) * 128 + ((g ^ (r >> 1)) << 4);
  aoff[1] = aoff[0] ^ 64;
  aoff[2] = aoff[0] + a_slot(0); aoff[3] = aoff[1] + a_slot(0);
  const unsigned wq = d.wq, wa = d.wa;
  asm volatile("" : "+v"(aoff[0]), "+v"(aoff[1]), "+v"(aoff[2]), "+v"(aoff[3]), "+v"(boff[0]), "+v"(boff[1]), "+v"(boff[2]));
  if (T == 5) asm volatile("" : "+v"(boff[3]), "+v"(boff[4]));
  Gran G;
  unsigned v2, vb2, vpf, tA[3], tB[3];
  row_load(R, d.rsA, lane);
  {
    asm volatile("s_waitcnt vmcnt(%2) lgkmcnt(0)\n\tv_readfirstlane_b32 %0, %1\n\ts_nop 3" : "=s"(tag) : "v"(epoch), "i"(5 + NA1) : "memory");
    tag = rfl(tag) + 1;
    bar();
  }
  STAMP(1);
  u32x4 a0[4], a1[4];
  u32x2 b0[T][2], b1[T][2];
#pragma unroll
  for (int i = 0; i < 4; ++i) a0[i] = lds_ld<u32x4>(aoff[2] + i * 2048);
#pragma unroll
  for (int t = 0; t < T; ++t) { b0[t][0] = ldtr1(bofs<T>(boff, t)); b0[t][1] = ldtr1(bofs<T>(boff, t) + 768); }
  SB;
#pragma unroll
  for (int i = NA1; i < 4; ++i) dma16(a_slot(1) + d.mA + i * 1024, (i & 1) ? d.vA1 : d.vA0, d.rsA, d.sA[i] + 128);
  const bool y = w >= 4;
  bool ok = true;
  auto ln_store = [&] {
    const float v0 = y ? -bflo(G.g0[0]) : __builtin_bit_cast(float, G.g0[0]) + __builtin_bit_cast(float, G.g1[0]) + __builtin_bit_cast(float, G.g2[0]);
    const unsigned la = L_MISC + (y ? tid * 4 - 1024 : 2048 + tid * 4);
    lds_st<float>(la, v0);
    lds_st<float>(y ? la + 1024 : L_GO + 64 + (lane & 7) * 4, __builtin_amdgcn_rcpf(bfhi(G.g0[0])));
  };
  static_for<0, 10>([&](auto S_) {
    constexpr int s = S_.v, L = 4 * T;
    constexpr int NX = (PF && s >= 1 && 2 * s <= NPRE) ? 2 : 0;
    kstep<T, TR, 2 * s>(acc, a0, b0, a1, b1, aoff, boff, d, [&](auto E) {
      constexpr int e = E.v;
      if constexpr (s == 2) {
        if constexpr (e == 2) { vb2 = v2 + (3 * wq) * 10240u; asm volatile("" : "+v"(vb2)); }
        if constexpr (e == 5) { G.off = w < 4 ? WS_CSUM + ccol * 8 : WS_STAT + (rb * 256 + tid - 256) * 8; asm volatile("" : "+v"(G.off)); }
        if constexpr (e == 8) { G.step = w < 4 ? NCOL * 8 : 0; tA[0] = urow(wa, rot) + cb * 128u; asm volatile("" : "+s"(G.step), "+s"(tA[0])); }
        if constexpr (e == 10) { tA[1] = urow(8 + wa, rot) + cb * 128u; asm volatile("" : "+s"(tA[1])); }
        if constexpr (e == 12) { tA[2] = urow(31 + wa, rot) + cb * 128u; asm volatile("" : "+s"(tA[2])); }
        if constexpr (e == 14) {
          tB[0] = urow(16, rot) + cb * 128u; tB[1] = urow(w < 4 ? 19u : 22u, rot) + cb * 128u; tB[2] = urow(w < 4 ? 25u : 28u, rot) + cb * 128u;
          asm volatile("" : "+s"(tB[0]), "+s"(tB[1]), "+s"(tB[2]));
        }
      }
      if constexpr (s == (TR ? 1 : 3) && e == 2) pre();
      if constexpr (NX && e >= L - 2) {
        constexpr int u = 2 * (s - 1) + e - (L - 2);
        pre2[u] = __builtin_amdgcn_raw_buffer_load_b128(rsB2, vpf, urow(36 + u, rot) + (w & 3) * 10240u + cb * 128u, 0);
      }
      if constexpr (s < 7) { if constexpr (e == L - 3) d.sB += s == 0 ? d.advB * (1 + (w >> 2)) : d.advB; }
      else if constexpr (s == 7) {
        if constexpr (e == L - 6) d.vB0 = y ? vb2 : d.vB0;
        if constexpr (e == L - 4) d.vB1 = y ? vb2 + 10240u : d.vB1;
        if constexpr (e == L - 3) d.vB2 = y ? vb2 + 20480u : d.vB2;
        if constexpr (e == L - 2) d.sB = y ? tB[0] : d.sB + d.advB;
        if constexpr (e == L - 1) d.rsB = y ? rsB2 : d.rsB;
      } else {
        if constexpr (e == L - 3) { d.sB = tB[s - 7]; d.rsB = rsB2; d.vB0 = vb2; d.vB1 = vb2 + 10240u; d.vB2 = vb2 + 20480u; }
      }
      if constexpr (s == 9 && e == L - 4) {
        asm volatile("s_waitcnt vmcnt(7)" : "+v"(G.g0), "+v"(G.g1), "+v"(G.g2)::"memory");
        ok = all64(G.g0[1] == tag && G.g1[1] == tag && G.g2[1] == tag);
      }
      if constexpr (s == 9 && e == L - 3) ln_store();
    });
    if constexpr (s == 0) {
      wait_vm<EARLY1 ? 8 - NA1 : 11>();
      static_for<0, 14>([&](auto K) { row_step<K.v>(R, lane); });
    }
    if constexpr (s == 9) {
      if (__builtin_expect(!ok, 0)) {
#pragma unroll 1
        for (unsigned spins = 0; spins < SPIN_CAP; ++spins) {
          __builtin_amdgcn_s_sleep(2);
          gran_load(G, rsWS);
          if (all64(G.g0[1] == tag && G.g1[1] == tag && G.g2[1] == tag)) break;
        }
        ln_store();
      }
    }
    wait_lgkm0();
    if constexpr (s < 9) wait_vm<4 + NX>();
    bar();
    if constexpr (s == 0) { STAMP(12); }
    SB;
    kstep<T, TR, 2 * s + 1>(acc, a1, b1, a0, b0, aoff, boff, d, [&](auto E) {
      constexpr int e = E.v;
      if constexpr (s == 0 && e == 6) {
        if (y) {
          const unsigned so = d.sB - d.advB;
          dma16(d.mW + 49152, d.vB0, d.rsB, so); dma16(d.mW + 49152 + 1024, d.vB1, d.rsB, so); dma16(d.mW + 49152 + 2048, d.vB2, d.rsB, so);
        }
      }
      if constexpr (s == 0 && e == L - 4) {
        v2 = (lane >> 3) * 1280 + (((lane & 7) ^ (((lane >> 4) & 3) << 1)) << 4);
        vpf = y ? v2 : 0x7ffffff0u;
        asm volatile("" : "+v"(v2), "+v"(vpf));
      }
      if constexpr (s == 0 && e == L - 6) row_publish(R, ws, tag, tid);
      if constexpr (s == 8 && e == 0) gran_issue(G, rsWS);
      if constexpr (s >= 7 && s < 9 && e >= L - 4) {
        d.rsA = rsB2; d.vA0 = v2; d.vA1 = v2;
        d.sA[e - (L - 4)] = tA[s - 7] + (e - (L - 4)) * 10240;
      }
      if constexpr (s == 9 && LASTA && e == 7) {
        if (wa < 5) {
#pragma unroll
          for (int i = 0; i < 4; ++i) dma16(a_slot(0) + d.mA + i * 1024, v2, rsB2, tA[2] + i * 10240);
        }
      }
    });
    if constexpr (s == 8) { STAMP(13); }
    if constexpr (s == 2) { STAMP(26); }
    if constexpr (s == 6) { STAMP(27); }
  });
  acc_fence<4 * T>(&acc[0][0]);
  STAMP(14);
}

DI void st16(u32x4 v, rsrc_t rs, unsigned voff, unsigned soff) {
  __builtin_amdgcn_raw_buffer_store_b128(v, rs, voff, soff, 16);
  asm volatile("s_nop 1" ::"v"(v) : "memory");
}
DI void publish_flag(const Params& p, unsigned flag_off, unsigned tag, unsigned tid) {
  drain_vm();
  bar();
  if (tid == 0) __hip_atomic_store((gu32*)(p.ws + flag_off), tag, RLX_AGENT);
}
DI void st_frag(rsrc_t rs, unsigned lane, unsigned rt, unsigned step, u32x2 lo, u32x2 hi) {
  u32x4 v = {lo[0], lo[1], hi[0], hi[1]};
  st16(v, rs, lane * 16, WS_HA + (rt * NSTEP + step) * 1024);
}
DI void shared_send(unsigned w, unsigned lane, const u32x2 (&sh)[4]) {
  if (w >= 4) lds_st<u32x4>(L_SCR + w * 1024 + lane * 16, cat2(sh[0], sh[1]));
  else lds_st<u32x4>(L_SCR + w * 1024 + lane * 16, cat2(sh[2], sh[3]));
  asm volatile("" ::: "memory");
}
DI void shared_recv(rsrc_t rs, unsigned w, unsigned lane, unsigned rt0, unsigned step, const u32x2 (&sh)[4]) {
  lbar();
  u32x4 rcv = lds_ld<u32x4>(L_SCR + (w ^ 4) * 1024 + lane * 16);
  u32x2 r0 = {rcv[0], rcv[1]}, r1 = {rcv[2], rcv[3]};
  if (w >= 4) { st_frag(rs, lane, rt0 + 2, step, r0, sh[2]); st_frag(rs, lane, rt0 + 3, step, r1, sh[3]); }
  else        { st_frag(rs, lane, rt0 + 0, step, sh[0], r0); st_frag(rs, lane, rt0 + 1, step, sh[1], r1); }
}

__global__ __launch_bounds__(512, 2) void fused_block(Params p) {
  unsigned tid = __builtin_amdgcn_workitem_id_x(), lane = tid & 63;
  const unsigned w = rfl(tid >> 6);
  const unsigned b = __builtin_amdgcn_workgroup_id_x(), xcd = b & 7, slot = b >> 3;
  const unsigned rb = 4 * (xcd & 1) + (slot & 3), jq = xcd >> 1, ji = slot >> 2;
  const unsigned spi = (xcd - 4) * 4 + (slot & 3);
  const unsigned long long jl0 = (jq & 1) ? 0x4123220908070605ull : 0x4021200403020100ull, jl1 = (jq & 1) ? 0x4327266011100f0eull : 0x422524600d0c0b0aull;
  const unsigned jb = (unsigned)(((jq & 2) ? jl1 : jl0) >> (8 * ji)) & 0xff, jtype = jb >> 5, jidx = jb & 31;
  STAMP_DECL;
  STAMP(0);

  Dma d;
  RowStat rs_;
  const bool mlp = jtype == J_MLP;
  const unsigned pitch = mlp ? 3456u : 3840u, wq = (w + (ROTW ? slot : 0)) & 3, wa = (w + (ROTW ? ji : 0)) & 7;
  d.wa = wa; d.wq = wq;
  const rsrc_t rsX = mk_rsrc(p.x, S * D * 2);
  d.vA0 = (lane >> 3) * 1280 + (((lane & 7) ^ (lane >> 4)) << 4); d.vA1 = d.vA0 ^ 64;
  d.rsA = rsX; d.mA = wa * 4096u;
#pragma unroll
  for (int i = 0; i < 4; ++i) d.sA[i] = (rb * 256 + 32 * wa + 8 * i) * 1280;
  unsigned epoch, tag;
  asm volatile("s_nop 4" :: "s"(rsX) : "memory");
  STAMP(6);
#pragma unroll
  for (int i = 0; i < 4; ++i) dma16(a_slot(0) + d.mA + i * 1024, (i & 1) ? d.vA1 : d.vA0, d.rsA, d.sA[i]);
  asm volatile("global_load_dword %0, %1, %2 sc1" : "=&v"(epoch) : "v"(0u), "s"(p.ws) : "memory");
  SB;
  {
    unsigned vb[3];
    const unsigned lim = mlp ? 176u : 144u;
    {
      const unsigned r0 = (lane * 43) >> 9, p0 = lane - 12 * r0;
      const bool w1 = p0 >= 8, w2 = p0 >= 4;
      const unsigned r1 = r0 + (w1 ? 6 : 5), p1 = p0 + (w1 ? -8 : 4), r2 = r0 + (w2 ? 11 : 10), p2 = p0 + (w2 ? -4 : 8);
      vb[0] = r0 * pitch + umin(p0 * 16, lim);
      vb[1] = r1 * pitch + umin((p1 * 16) ^ ((r1 & 8) << 2), lim);
      vb[2] = r2 * pitch + umin((p2 * 16) ^ 32, lim);
    }
    d.vB0 = vb[0]; d.vB1 = vb[1]; d.vB2 = vb[2];
    const unsigned mh = wq >> 1;
    const void* bw = mlp ? (mh ? p.wu : p.wg) : p.wqkv;
    const unsigned cj = mlp ? 192u : 160u * jtype, cm = mlp ? 0u : (jtype == J_KV ? 1280u : 160u), c0 = jtype == J_KV ? 1280u : 0u;
    d.rsB = mk_rsrc(bw, D * pitch);
    d.sB = (16 * (wq & 1) + (w < 4 ? 32 : 0)) * pitch + c0 + cj * jidx + cm * mh; d.advB = 64 * pitch;
    d.mB = L_B + wq * 3072u + (w < 4 ? 0u : 12288u); d.mW = L_B + wq * 3072u + (w < 4 ? 49152u : 0u);
    const unsigned bdst = L_B + (w < 4 ? 12288u : 0u) + wq * 3072u;
    dma16(bdst, d.vB0, d.rsB, d.sB); dma16(bdst + 1024, d.vB1, d.rsB, d.sB); dma16(bdst + 2048, d.vB2, d.rsB, d.sB);
    rs_.row = rfl(b * 8 + w);
    d.sB += d.advB;
    dma16(bdst + 24576, d.vB0, d.rsB, d.sB); dma16(bdst + 24576 + 1024, d.vB1, d.rsB, d.sB); dma16(bdst + 24576 + 2048, d.vB2, d.rsB, d.sB);
#pragma unroll
    for (int i = 0; i < NA1; ++i) dma16(a_slot(1) + d.mA + i * 1024, (i & 1) ? d.vA1 : d.vA0, d.rsA, d.sA[i] + 128);
  }
  STAMP(10);
  lds_st<unsigned>(L_GO + (tid & 1) * 4, 0u);

  const unsigned t2 = 28 * xcd + slot;
  const bool has2 = slot < 28 && t2 < 220;
  const unsigned G = t2 / 10, cb = t2 % 10;
  constexpr unsigned rot = 32;
  unsigned g = lane >> 4, r = lane & 15, q4 = r >> 2, p4 = lane & 3;
  const rsrc_t rsWd = mk_rsrc(p.wd, has2 ? F * D * 2 : 0);
  const rsrc_t rsWS = mk_rsrc(p.ws, WS_END);
  unsigned v2 = (lane >> 3) * 1280 + (((lane & 7) ^ (((lane >> 4) & 3) << 1)) << 4);
  auto relane = [&] {
    asm volatile("" : "+v"(tid));
    lane = tid & 63; g = lane >> 4; r = lane & 15; q4 = r >> 2; p4 = lane & 3;
    v2 = (lane >> 3) * 1280 + (((lane & 7) ^ (((lane >> 4) & 3) << 1)) << 4);
  };
  const unsigned m = w & 3, n = w >> 2;
  const unsigned rt0 = rb * 16 + m * 4;
  const unsigned brow = L_B + (8 * g + q4) * 192;
  const unsigned bx = (g & 1) << 5;

  u32x4 pre2[36];
  W2 W;
  W.wd = rsWd; W.wo = mk_rsrc(p.wo, has2 ? D * D * 2 : 0); W.rot = rot; W.col = cb * 128u;
  if (jtype == J_MLP) {
    f32x4 acc[4][6];
    unsigned boff[6];
#pragma unroll
    for (int t = 0; t < 3; ++t) {
      const unsigned gt = n ? (t == 2 ? 3 : 4 + t) : t;
      boff[t] = brow + ((32 * gt + 8 * p4) ^ bx); boff[t + 3] = boff[t] + 6144;
    }
    const unsigned ccol = tid < 96 ? 1920 + 96 * jidx + tid : 3648 + 96 * jidx + (tid < 192 ? tid - 96 : 0);
    main_loop<6, true, true, true>([] {}, rs_, p.ws, acc, boff, d, w, lane, tid, epoch, tag, rb, ccol, rsWd, rsWS, cb, rot, pre2 STAMP_PASS);
    relane();
    float mm[4];
    f32x4 cg[3], cu[3];
#pragma unroll
    for (int i = 0; i < 4; ++i) mm[i] = lds_ld<float>(L_MISC + (64 * m + 16 * i + r) * 4);
#pragma unroll
    for (int t = 0; t < 3; ++t) {
      const unsigned gt = n ? (t == 2 ? 3 : 4 + t) : t;
      cg[t] = lds_ld<f32x4>(L_MISC + 2048 + (16 * gt + 4 * g) * 4); cu[t] = lds_ld<f32x4>(L_MISC + 2048 + (96 + 16 * gt + 4 * g) * 4);
    }
    auto hval = [&](int i, int t) {
      f32x4 gg = acc[i][t] + mm[i] * cg[t];
      const f32x4 uu = acc[i][t + 3] + mm[i] * cu[t];
#pragma unroll
      for (int j = 0; j < 4; ++j) gg[j] = relu2(gg[j]);
      return pk4(gg * uu);
    };
#pragma unroll
    for (int i = 0; i < 4; ++i) { st_frag(rsWS, lane, rt0 + i, 3 * jidx + 2 * n, hval(i, 0), hval(i, 1)); SB; }
    const u32x2 sh[4] = {hval(0, 2), hval(1, 2), hval(2, 2), hval(3, 2)};
    shared_send(w, lane, sh);
    shared_recv(rsWS, w, lane, rt0, 3 * jidx + 1, sh);
    STAMP(15);
    publish_flag(p, WS_FLAGH + (rb * 32 + jidx) * FS, tag, tid);
    STAMP(3);
  } else if (jtype == J_KV) {
    f32x4 acc[4][5];
    unsigned boff[5];
    boff[0] = brow + ((80 * n + 8 * p4) ^ bx); boff[1] = boff[0] + 6144;
    boff[2] = brow + ((80 * n + 40 + 8 * p4) ^ bx); boff[3] = boff[2] + 6144;
    boff[4] = brow + ((80 * n + 32 + 40 * (p4 >> 1)) ^ bx) + (p4 & 1) * 6144;
    const unsigned kb = 126976u + m * 6720 + n * 3360, dump = L_MISC - 2048 + lane * 16;
    const unsigned ea = (q4 == 1 || r == 0) ? kb + (r == 0 ? 20 : 12 + r) * 80 : 0u, eb = (q4 == 3 || r == 0) ? kb + 1680 + (r == 0 ? 20 : 4 + r) * 80 : 0u;
    unsigned ka[6] = {kb + r * 80 + g * 16, ea ? ea + g * 16 : dump, eb ? eb + g * 16 : dump, g == 0 ? kb + r * 80 + 64 : dump,
                      g == 2 ? kb + 1680 + r * 80 + 64 : dump, (g == 0 && ea) ? ea + 64 : ((g == 2 && eb) ? eb + 64 : dump)};
    auto pin = [&] { asm volatile("" : "+v"(ka[0]), "+v"(ka[1]), "+v"(ka[2]), "+v"(ka[3]), "+v"(ka[4]), "+v"(ka[5])); };
    const unsigned ccol = tid < 80 ? 640 + 80 * jidx + tid : 1280 + 80 * jidx + (tid < 160 ? tid - 80 : 0);
    main_loop<5, false, false, false>(pin, rs_, p.ws, acc, boff, d, w, lane, tid, epoch, tag, rb, ccol, rsWd, rsWS, cb, rot, pre2 STAMP_PASS);
    relane();
    float cs[5];
    cs[0] = lds_ld<float>(L_MISC + 2048 + (40 * n + r) * 4);
    cs[1] = lds_ld<float>(L_MISC + 2048 + (80 + 40 * n + r) * 4);
    cs[2] = lds_ld<float>(L_MISC + 2048 + (40 * n + 20 + r) * 4);
    cs[3] = lds_ld<float>(L_MISC + 2048 + (80 + 40 * n + 20 + r) * 4);
    cs[4] = lds_ld<float>(L_MISC + 2048 + ((q4 & 1) * 80 + 40 * n + 16 + 20 * (q4 >> 1) + (r & 3)) * 4);
    u32x2 pk[4][3], vv[4][3];
    const unsigned vmask = 0u - (q4 & 1);
    f32x4 mm4[4], rr4[4];
#pragma unroll
    for (int i = 0; i < 4; ++i) {
      mm4[i] = lds_ld<f32x4>(L_MISC + (64 * m + 16 * i + 4 * g) * 4); rr4[i] = lds_ld<f32x4>(L_MISC + 1024 + (64 * m + 16 * i + 4 * g) * 4);
    }
    SB;
#pragma unroll
    for (int i = 0; i < 4; ++i) {
      const f32x4 mm = mm4[i], rr = rr4[i];
      const f32x4 rl = rr * 1.4426950408889634f, rh = rr * 0.5f;
      f32x4 val[5];
#pragma unroll
      for (int t = 0; t < 5; ++t) val[t] = acc[i][t] + mm * cs[t];
#pragma unroll
      for (int t = 0; t < 3; ++t) pk[i][t] = pk4(phi4(val[2 * t], rl, rh));
      vv[i][0] = pk4(val[1] * rr); vv[i][1] = pk4(val[3] * rr);
      const u32x2 v4 = pk4(val[4] * rr);
      vv[i][2] = u32x2{bsel(vmask, v4[0], 0x3f803f80u), bsel(vmask, v4[1], 0x3f803f80u)};
    }
    STAMP(25);
    f32x4 P[7] = {};
#pragma unroll
    for (int i = 0; i < 4; i += 2) {
      const bf16x8 k0 = frag(pk[i][0], pk[i + 1][0]), k2 = frag(pk[i][1], pk[i + 1][1]), k4 = frag(pk[i][2], pk[i + 1][2]);
      const bf16x8 v1 = frag(vv[i][0], vv[i + 1][0]), v3 = frag(vv[i][1], vv[i + 1][1]), v4 = frag(vv[i][2], vv[i + 1][2]);
      P[0] = mfma(k0, v1, P[0]); P[1] = mfma(k0, v4, P[1]); P[2] = mfma(k4, v1, P[2]);
      P[3] = mfma(k2, v3, P[3]); P[4] = mfma(k2, v4, P[4]); P[5] = mfma(k4, v3, P[5]); P[6] = mfma(k4, v4, P[6]);
    }
    lds_st<f32x4>(ka[0], P[0]); lds_st<f32x4>(ka[0] + 1680, P[3]); lds_st<f32x4>(ka[1], P[1]); lds_st<f32x4>(ka[2], P[4]);
    lds_st<f32x4>(ka[3], P[2]); lds_st<f32x4>(ka[4], P[5]); lds_st<f32x4>(ka[5], P[6]);
    lbar();
    if (tid < 210) {
      const unsigned a = 126976u + tid * 32;
      const f32x4 t0 = (lds_ld<f32x4>(a) + lds_ld<f32x4>(a + 6720)) + (lds_ld<f32x4>(a + 13440) + lds_ld<f32x4>(a + 20160));
      const f32x4 t1 = (lds_ld<f32x4>(a + 16) + lds_ld<f32x4>(a + 6736)) + (lds_ld<f32x4>(a + 13456) + lds_ld<f32x4>(a + 20176));
      st16(cat2(pk4(t0), pk4(t1)), rsWS, tid * 16, WS_KVP + (rb * 32 + 4 * jidx) * 840);
    }
    wait_lgkm0();
    STAMP(15);
    publish_flag(p, WS_FLAGKV + ((jidx >> 1) * 32 + (jidx & 1) * 8 + rb) * FS, tag, tid);
    STAMP(3);
  } else if (jtype == J_Q) {
    f32x4 acc[4][5];
    unsigned boff[5];
#pragma unroll
    for (int t = 0; t < 5; ++t) boff[t] = brow + n * 6144 + ((32 * t + 8 * p4) ^ bx);
    const unsigned ccol = 160 * jidx + (tid < 160 ? tid : 0);
    unsigned tch[3];
    auto warm = [&] {
      const unsigned gq = (rb * 4 + jidx) * 8 + w;
      asm volatile("buffer_load_dword %0, %3, %5, 0 offen\n\tbuffer_load_dword %1, %3, %5, %7 offen\n\tbuffer_load_dword %2, %4, %6, 0 offen"
                   : "=&v"(tch[0]), "=&v"(tch[1]), "=&v"(tch[2])
                   : "v"((gq * 34 + (lane < 34 ? lane : 0)) * 128), "v"((gq * 25 + (lane < 25 ? lane : 0)) * 128), "s"(mk_rsrc(p.wd, F * D * 2)),
                     "s"(mk_rsrc(p.wo, D * D * 2)), "s"(8576u * 128u) : "memory");
    };
    main_loop<5, true, false, false>(warm, rs_, p.ws, acc, boff, d, w, lane, tid, epoch, tag, rb, ccol, rsWd, rsWS, cb, rot, pre2 STAMP_PASS);
    relane();
    asm volatile("" :: "v"(tch[0]), "v"(tch[1]), "v"(tch[2]));
    u32x2 pq[4][5];
    {
      float mm[4], rr[4];
      f32x4 c[5];
#pragma unroll
      for (int i = 0; i < 4; ++i) { mm[i] = lds_ld<float>(L_MISC + (64 * m + 16 * i + r) * 4); rr[i] = lds_ld<float>(L_MISC + 1024 + (64 * m + 16 * i + r) * 4); }
#pragma unroll
      for (int t = 0; t < 5; ++t) c[t] = lds_ld<f32x4>(L_MISC + 2048 + (80 * n + 16 * t + 4 * g) * 4);
      SB;
#pragma unroll
      for (int i = 0; i < 4; ++i)
#pragma unroll
        for (int t = 0; t < 5; ++t) pq[i][t] = pk4(phi4(acc[i][t] + mm[i] * c[t], splat(rr[i] * 1.4426950408889634f), splat(rr[i] * 0.5f)));
#pragma unroll
      for (int i = 0; i < 4; ++i) asm volatile("" : "+v"(pq[i][0]), "+v"(pq[i][1]), "+v"(pq[i][2]), "+v"(pq[i][3]), "+v"(pq[i][4]));
    }
    constexpr unsigned KP = 208, KQ = 84 * KP;
    unsigned wa[2];
#pragma unroll
    for (int k = 0; k < 2; ++k) {
      const unsigned qd = 2 * (tid < 420 ? tid : 0) + k, hd = qd / 105, e = (qd % 105) / 5, dq = qd % 5, hl = hd & 3;
      wa[k] = (hd >> 2) * KQ + (e < 20 ? hl * 20 + e : 80 + hl) * KP + hl * 40 + dq * 8;
    }
    unsigned fk = n * KQ + r * KP + g * 8, fz = n * KQ + (80 + (r & 3)) * KP + g * 8;
    unsigned gm[3] = {g < 1 ? ~0u : 0u, g < 2 ? ~0u : 0u, g < 3 ? ~0u : 0u};
    u32x4 B[4][3];
    f32x4 eps4 = splat(1e-6f);
#pragma unroll
    for (int i = 0; i < 4; ++i) {
      B[i][0] = cat2(pq[i][0], pq[i][1]); B[i][1] = cat2(pq[i][2], pq[i][3]); B[i][2] = cat2(pq[i][4], u32x2{0u, 0u});
      asm volatile("" : "+v"(B[i][0]), "+v"(B[i][1]), "+v"(B[i][2]));
    }
    asm volatile("" : "+v"(wa[0]), "+v"(wa[1]), "+v"(fk), "+v"(fz), "+v"(gm[0]), "+v"(gm[1]), "+v"(gm[2]), "+v"(eps4));
    STAMP(25);
    wait_vm<0>();
    bar();
#pragma unroll
    for (int k = 0; k < 5; ++k) lds_st<u32x4>(k * 8192 + tid * 16, u32x4{0u, 0u, 0u, 0u});
    if (w == 0) poll_flags((gu32*)(p.ws + WS_FLAGKV + (jidx * 32 + (lane & 15)) * FS), true, tag);
    lbar();
    STAMP(4);
    if (w < 7) {
      u32x4 v[8];
#pragma unroll
      for (int j = 0; j < 8; ++j)
        v[j] = __builtin_amdgcn_raw_buffer_load_b128(rsWS, (tid < 420 ? tid : 0) * 16, WS_KVP + (j * 32 + 8 * jidx) * 840, 16);
      SB;
      f32x4 t0 = {0.f, 0.f, 0.f, 0.f}, t1 = t0;
#pragma unroll
      for (int j = 0; j < 8; ++j) {
        t0 += f32x4{bflo(v[j][0]), bfhi(v[j][0]), bflo(v[j][1]), bfhi(v[j][1])};
        t1 += f32x4{bflo(v[j][2]), bfhi(v[j][2]), bflo(v[j][3]), bfhi(v[j][3])};
      }
      lds_st<u32x2>(wa[0], pk4(t0)); lds_st<u32x2>(wa[1], pk4(t1));
    }
    lbar();
    STAMP(16);
    u32x4 A[12];
#pragma unroll
    for (int P = 0; P < 3; ++P) A[P] = cat2(lds_ld<u32x2>(fz + P * 64), lds_ld<u32x2>(fz + P * 64 + 32));
#pragma unroll
    for (int u = 0; u < 5; ++u) {
      const unsigned a0 = fk + u * 16 * KP + (u < 3 ? 0 : 64);
      A[3 + u] = cat2(lds_ld<u32x2>(a0), lds_ld<u32x2>(a0 + 32));
    }
#pragma unroll
    for (int u = 1; u < 5; ++u) {
      const unsigned a0 = fk + u * 16 * KP + (u < 3 ? 64 : 128);
      A[7 + u] = cat2(lds_ld<u32x2>(a0), lds_ld<u32x2>(a0 + 32));
    }
    SB;
    u32x2 sh[4];
    f32x4 den[4], num[2][5];
    static_for<0, 12>([&](auto E) {
      constexpr int P = E.v >> 2, i = E.v & 3;
      if constexpr (P == 0) mfma_c(den[i], A[0], B[i][0], eps4); else mfma_ip(den[i], A[P], B[i][P]);
    });
    u32x2 o[5];
    auto fin = [&](auto I, auto K) {
      constexpr int i = I.v, k = K.v, u = k < 8 ? k >> 1 : 4;
      if constexpr (k == 8 || !(k & 1)) {
        asm volatile("" : "+v"(num[i & 1][u]));
        const float rr = u == 0 ? den[i][0] : (u == 4 ? den[i][3] : bself(gm[(u + 2) % 3], den[i][(u + 3) & 3], den[i][u & 3]));
#pragma unroll
        for (int j = 0; j < 4; ++j) num[i & 1][u][j] = smul(num[i & 1][u][j], rr);
      }
      if constexpr (k == 8 || (k & 1)) o[u] = pk4(num[i & 1][u]);
      if constexpr (k == 3) st_frag(rsWS, lane, rt0 + i, 54 + 5 * jidx + 2 * n, o[0], o[1]);
      if constexpr (k == 7) st_frag(rsWS, lane, rt0 + i, 54 + 5 * jidx + 2 * n + 1, o[2], o[3]);
      if constexpr (k == 8) { sh[i] = o[4]; asm volatile("" : "+v"(sh[i])); }
    };
    static_for<0, 4>([&](auto I) {
      constexpr int i = I.v;
      static_for<0, 9>([&](auto K) {
        constexpr int k = K.v, u = k < 5 ? k : k - 4;
        if constexpr (k < 5) mfma_z(num[i & 1][u], A[3 + u], B[i][u < 3 ? 0 : 1]); else mfma_ip(num[i & 1][u], A[7 + u], B[i][u < 3 ? 1 : 2]);
        SB;
        if constexpr (i == 0) {
          if constexpr (k >= 5) {
            asm volatile("" : "+v"(den[k - 5]));
#pragma unroll
            for (int j = 0; j < 4; ++j) den[k - 5][j] = __builtin_amdgcn_rcpf(den[k - 5][j]);
            asm volatile("" : "+v"(den[k - 5]));
          }
        } else {
          fin(IC<i - 1>{}, K);
        }
        SB;
      });
    });
    static_for<0, 9>([&](auto K) { fin(IC<3>{}, K); SB; });
    shared_send(w, lane, sh);
    shared_recv(rsWS, w, lane, rt0, 54 + 5 * jidx + 4, sh);
    STAMP(15);
    publish_flag(p, WS_FLAGO + (rb * 32 + jidx) * FS, tag, tid);
    STAMP(3);
    if (b == 224) {
      poll_flags((gu32*)(p.ws + WS_FLAGH + tid * FS), (tid & 31) < (tid < 256 ? 18u : 4u), tag);
      bar();
      if (tid == 0) __hip_atomic_store((gu32*)(p.ws + WS_EPOCH), tag, RLX_AGENT);
    }
  } else {
    const unsigned gw = spi * 8 + w;
    unsigned k10 = 0x00003f80u, k01 = 0x3f800000u;
    asm volatile("" : "+v"(k10), "+v"(k01));
    auto cs_src = [&](unsigned u, rsrc_t& rs, unsigned& cv, unsigned& st) {
      const unsigned cline = u / 3, third = u % 3;
      const void* cw = cline < 30 ? p.wqkv : (cline < 57 ? p.wg : p.wu);
      const unsigned cpitch = cline < 30 ? 3840u : 3456u;
      const unsigned clocal = cline < 30 ? cline : (cline < 57 ? cline - 30 : cline - 57);
      rs = mk_rsrc(cw, gw < 126 ? D * cpitch : 0);
      cv = (third * 216 + (lane >> 3)) * cpitch + clocal * 128 + (lane & 7) * 16;
      st = 8 * cpitch;
    };
    auto cs_publish = [&](unsigned u, float (&cs)[8]) {
#pragma unroll
      for (int k = 0; k < 4; ++k) { const bool hi = lane & 32; const float o = shfl_x(hi ? cs[k] : cs[k + 4], lane, 32); cs[k] = (hi ? cs[k + 4] : cs[k]) + o; }
#pragma unroll
      for (int k = 0; k < 2; ++k) { const bool hi = lane & 16; const float o = shfl_x(hi ? cs[k] : cs[k + 2], lane, 16); cs[k] = (hi ? cs[k + 2] : cs[k]) + o; }
      { const bool hi = lane & 8; const float o = shfl_x(hi ? cs[0] : cs[1], lane, 8); cs[0] = (hi ? cs[1] : cs[0]) + o; }
      if (gw < 126)
        __hip_atomic_store((gu64*)(p.ws + WS_CSUM) + (u % 3) * NCOL + (u / 3) * 64 + (lane & 7) * 8 + (lane >> 3),
                           ((unsigned long long)tag << 32) | __builtin_bit_cast(unsigned, cs[0]), RLX_AGENT);
    };
    row_load(rs_, rsX, lane);
    asm volatile("s_waitcnt vmcnt(0) lgkmcnt(0)\n\tv_readfirstlane_b32 %0, %1\n\ts_nop 3" : "=s"(tag) : "v"(epoch) : "memory");
    tag = rfl(tag) + 1;
    static_for<0, 14>([&](auto K) { row_step<K.v>(rs_, lane); });
    u32x4 c[27];
    rsrc_t rs0, rs1; unsigned cv0, cv1, st0, st1;
    cs_src(gw, rs0, cv0, st0); cs_src(gw + 126, rs1, cv1, st1);
#pragma unroll
    for (int j = 0; j < 27; ++j) c[j] = __builtin_amdgcn_raw_buffer_load_b128(rs0, cv0 + j * st0, 0, 0);
    lbar();
    row_publish(rs_, p.ws, tag, tid);
    float cs[8] = {};
#pragma unroll
    for (int j = 0; j < 27; ++j) {
#pragma unroll
      for (int k = 0; k < 4; ++k) { cs[2 * k] = dot2(c[j][k], k10, cs[2 * k]); cs[2 * k + 1] = dot2(c[j][k], k01, cs[2 * k + 1]); }
      if (j < 8) c[j] = __builtin_amdgcn_raw_buffer_load_b128(rs1, cv1 + j * st1, 0, 0);
      SB;
    }
    cs_publish(gw, cs);
    SB;
#pragma unroll
    for (int j = 8; j < 27; ++j) c[j] = __builtin_amdgcn_raw_buffer_load_b128(rs1, cv1 + j * st1, 0, 0);
    SB;
    STAMP(11);
#pragma unroll
    for (int k = 0; k < 8; ++k) cs[k] = 0.f;
#pragma unroll
    for (int j = 0; j < 27; ++j)
#pragma unroll
      for (int k = 0; k < 4; ++k) { cs[2 * k] = dot2(c[j][k], k10, cs[2 * k]); cs[2 * k + 1] = dot2(c[j][k], k01, cs[2 * k + 1]); }
    cs_publish(gw + 126, cs);
    STAMP(12);
#pragma unroll 1
    for (unsigned i = w; i < 144; i += 8) dma16(i * 1024, v2, rsWd, urow(i >> 2, rot) + (i & 3) * 10240u + cb * 128u);
    drain_vm();
    bar();
  }
  if (!has2) { STAMP_END; return; }
  relane();

  const unsigned rbA = (G * 6) >> 4, rbB = (G * 6 + 5 > 127 ? 127 : G * 6 + 5) >> 4;
  if (w >= 4) {
    __builtin_amdgcn_s_setprio(HPRIO);
    const unsigned lw = w - 4, ld0 = lw * 1024;
    gu32* fo = (gu32*)(p.ws + WS_FLAGO + ((lane < 32 ? rbA : rbB) * 32 + (lane & 3)) * FS);
    gu32* fh = (gu32*)(p.ws + WS_FLAGH + ((lane < 32 ? rbA : rbB) * 32 + ((lane & 31) < 18 ? lane & 31 : 17)) * FS);
    __builtin_amdgcn_s_sleep(HSLEEP);
    if (jtype == J_KV) {
#pragma unroll
      for (int u = 31; u < 36; ++u) dma16(u * 4096 + ld0, v2, rsWd, w2_off(W, u, lw));
    }
    if (jtype != J_MLP) {
#pragma unroll
      for (int u = 0; u < NPRE; ++u) { pre2[u] = w2_load(W, 36 + u, lw, v2); SB; }
    }
#pragma unroll
    for (int u = 36 + NPRE; u < PSPLIT; ++u) { pre2[u - 36] = w2_load(W, u, lw, v2); SB; }
    if (w == 7) {
      __builtin_amdgcn_s_setprio(3);
      const unsigned v = poll_flags(fh, (lane & 31) < 18 && (lane & 31) >= 10, tag);
      lds_set(L_GO);
      if (!all64((lane & 31) >= 18 || v == tag)) poll_flags(fh, (lane & 31) < 18, tag);
      lds_set(L_GO + 4);
      __builtin_amdgcn_s_setprio(HPRIO);
    }
#pragma unroll
    for (int u = PSPLIT; u < HSPLIT; ++u) { pre2[u - 36] = w2_load(W, u, lw, v2); SB; }
    STAMP(8);
    unsigned la[3], wb[3], xo[3], st[3];
    u32x4 xr[3];
    const unsigned ch = lane & 7;
#pragma unroll
    for (int i = 0; i < 3; ++i) {
      const unsigned row = lw * 24 + i * 8 + (lane >> 3), rl = row >> 4, rr = row & 15;
      la[i] = (rl >= 3 ? 24576u + (rl - 3) * 4096u : rl * 4096u) + (ch >> 1) * 1024 + (ch & 1) * 512 + ((rr + 2 * ch) & 15) * 16;
      xo[i] = (G * 96 + row) * 1280 + cb * 128 + ch * 16;
      wb[i] = ld0 + lane * 16 + i * 49152;
      asm volatile("" : "+v"(la[i]), "+v"(wb[i]), "+v"(xo[i]));
    }
    float ri[3], rl[3];
    unsigned of = 0;
    f32x4 xf[3][2], dd[3][2], d2[3][2];
#pragma unroll
    for (int k = 2; k <= 6; ++k) {
      if (k == O_BAR && w == 7 && !all64((lane & 31) >= 4 || of == tag)) poll_flags(fo, (lane & 31) < 4, tag);
      if (k == 6) {
        wait_vm<0>();
#pragma unroll
        for (int h = 0; h < 3; ++h) {
          ri[h] = 2.0f * bfhi(st[h]) * bfhi(st[h]);
          rl[h] = -1.4426950408889634f * __builtin_amdgcn_rcpf(ri[h]);
          xf[h][0] = f32x4{bflo(xr[h][0]), bfhi(xr[h][0]), bflo(xr[h][1]), bfhi(xr[h][1])};
          xf[h][1] = f32x4{bflo(xr[h][2]), bfhi(xr[h][2]), bflo(xr[h][3]), bfhi(xr[h][3])};
          asm volatile("" : "+v"(ri[h]), "+v"(rl[h]), "+v"(xf[h][0]), "+v"(xf[h][1]));
        }
      }
      lbar();
      if (k == O_BAR - 1) { of = __hip_atomic_load(fo, RLX_AGENT); SB; }
      if (k < 6) {
        const int kk = k - 1;
#pragma unroll
        for (int u = 9 * (kk - 1) + ((kk - 1) & 1); u < 9 * kk + (kk & 1); ++u) {
          lds_st<u32x4>(wb[u / 12] + (u % 12) * 4096, pre2[u]);
          if (WSLEEP == 1) __builtin_amdgcn_s_sleep(1);
          if (WSLEEP == 2) asm volatile("s_nop 7");
          SB;
        }
      }
      if (k == 2 || k == 3) {
        constexpr int mid = (HSPLIT + 74) / 2 < 72 ? (HSPLIT + 74) / 2 : 72;
#pragma unroll
        for (int u = (k == 2 ? HSPLIT : mid); u < (k == 2 ? mid : 72); ++u) { pre2[u - 36] = w2_load(W, u, lw, v2); SB; }
        if (k == 3) { dma16(36 * 4096 + ld0, v2, W.wo, w2_off(W, 72, lw)); dma16(37 * 4096 + ld0, v2, W.wo, w2_off(W, 73, lw)); }
      }
      if (k == XK) {
#pragma unroll
        for (int h = 0; h < 3; ++h) {
          const unsigned row = G * 96 + lw * 24 + h * 8 + (lane >> 3);
          st[h] = __builtin_amdgcn_raw_buffer_load_b32(rsWS, WS_STAT + (row < S ? row : 0) * 8, 0, 16);
          xr[h] = __builtin_amdgcn_raw_buffer_load_b128(rsX, xo[h], 0, 0);
        }
      }
    }
    bar();
#pragma unroll
    for (int i = 0; i < 3; ++i) {
      dd[i][0] = lds_ld<f32x4>(la[i]); d2[i][0] = lds_ld<f32x4>(la[i] + 12288);
      dd[i][1] = lds_ld<f32x4>(la[i] + 256); d2[i][1] = lds_ld<f32x4>(la[i] + 12288 + 256);
      la[i] += 49152;
      asm volatile("" : "+v"(la[i]));
    }
    __builtin_amdgcn_s_setprio(GPRIO);
#pragma unroll
    for (int i = 0; i < 3; ++i) {
      float v[8], e[8];
#pragma unroll
      for (int j = 0; j < 8; ++j) v[j] = sadd(dd[i][j >> 2][j & 3], d2[i][j >> 2][j & 3]);
      SB;
#pragma unroll
      for (int j = 0; j < 8; ++j) e[j] = smul(v[j], rl[i]);
      SB;
#pragma unroll
      for (int j = 0; j < 8; ++j) e[j] = __builtin_amdgcn_exp2f(e[j]);
      SB;
#pragma unroll
      for (int j = 0; j < 8; ++j) e[j] = sfma(e[j], ri[i], ri[i]);
      SB;
#pragma unroll
      for (int j = 0; j < 8; ++j) e[j] = __builtin_amdgcn_rcpf(e[j]);
      SB;
#pragma unroll
      for (int j = 0; j < 8; ++j) dd[i][j >> 2][j & 3] = smul(v[j], e[j]);
      asm volatile("" : "+v"(dd[i][0]), "+v"(dd[i][1]));
      SB;
    }
    const rsrc_t rsO = mk_rsrc(p.out, S * D * 2);
    STAMP(9);
    bar();
    f32x4 ss[3][2];
#pragma unroll
    for (int i = 0; i < 3; ++i) {
      ss[i][0] = lds_ld<f32x4>(la[i]) + lds_ld<f32x4>(la[i] + 12288);
      ss[i][1] = lds_ld<f32x4>(la[i] + 256) + lds_ld<f32x4>(la[i] + 12288 + 256);
    }
#pragma unroll
    for (int i = 0; i < 3; ++i)
      __builtin_amdgcn_raw_buffer_store_b128(cat2(pk4(xf[i][0] + ss[i][0] * dd[i][0]), pk4(xf[i][1] + ss[i][1] * dd[i][1])), rsO, xo[i], 0, 0);
    STAMP(5);
    STAMP_END;
    return;
  }
  {
    const unsigned kh = w & 1, rt = G * 6 + 3 * (w >> 1);
    const rsrc_t rsH = mk_rsrc(p.ws + WS_HA + (rt < 128 ? rt : 0) * (NSTEP * 1024), rt < 128 ? (rt > 125 ? 128 - rt : 3) * (NSTEP * 1024) : 0);
    unsigned tb[3][4];
#pragma unroll
    for (int t = 0; t < 4; ++t) {
      tb[0][t] = (4 * g + q4) * 128 + ((t ^ ((2 * g + (q4 >> 1)) & 3)) << 5) + 8 * p4 + kh * 4096;
      tb[1][t] = tb[0][t] + 49152; tb[2][t] = tb[0][t] + 98304;
      asm volatile("" : "+v"(tb[0][t]), "+v"(tb[1][t]), "+v"(tb[2][t]));
    }
    auto rd1 = [&](int i, int t, int hf) {
      const int sl = i == 36 ? 36 : (2 * i) % 36, bi = sl / 12 > 2 ? 2 : sl / 12;
      return __builtin_bit_cast(u32x2, lds_tr(tb[bi][t] + (sl - 12 * bi) * 4096 + hf * 2048));
    };
    constexpr int NW = P2NW, W0 = P2W0;
    constexpr int OI = O_BAR == 4 ? 17 : (O_BAR == 5 ? 22 : 26);
    constexpr int OB0 = OI == 26 ? NW - 1 : 0;
    f32x4 acc[2][3][4];
    u32x4 a[NW][3];
    u32x2 bw[2][4][2];
    unsigned va[3];
#pragma unroll
    for (int h = 0; h < 3; ++h) va[h] = lane * 16 + h * NSTEP * 1024;
    unsigned xa[4];
#pragma unroll
    for (int t = 0; t < 4; ++t) {
      xa[t] = w * 12288u + t * 1024 + g * 256 + ((r + 4 * t + (g & 2)) & 15) * 16;
      asm volatile("" : "+v"(xa[t]));
    }
    auto put = [&](auto Z, auto E) {
      constexpr int z = Z.v, t = E.v / 3, h = 2 - E.v % 3;
      asm volatile("" : "+v"(acc[z][h][t]));
      lds_st<f32x4>(xa[t] + z * 49152 + h * 4096, acc[z][h][t]);
    };
    unsigned so = (rot + kh) * 1024;
    auto lda = [&](auto I, auto H) {
      constexpr int i = I.v, h = H.v;
      if constexpr (i < 27) {
        if (h == 0 && so >= 54 * 1024) so -= 54 * 1024;
        a[i % NW][h] = __builtin_amdgcn_raw_buffer_load_b128(rsH, va[h], so, 16);
        if (h == 2) so += 2048;
      } else {
        a[i % NW][h] = __builtin_amdgcn_raw_buffer_load_b128(rsH, va[h], (2 * i + kh) * 1024, 16);
      }
      SB;
    };
    lds_wait(L_GO);
    unsigned go2 = 0;
    STAMP(8);
    static_for<0, W0>([&](auto I) { lda(I, IC<0>{}); lda(I, IC<1>{}); lda(I, IC<2>{}); });
#pragma unroll
    for (int t = 0; t < 4; ++t) { bw[0][t][0] = rd1(0, t, 0); bw[0][t][1] = rd1(0, t, 1); }
    static_for<0, 37>([&](auto I) {
      constexpr int i = I.v;
      constexpr int lim = i + NW - 1;
      constexpr int nxt = i <= OI ? (W0 + 2 * i < lim ? W0 + 2 * i : lim) : (27 + OB0 + 2 * (i - OI - 1) < lim ? 27 + OB0 + 2 * (i - OI - 1) : lim);
      constexpr int end = i <= OI ? 27 : 37;
      constexpr int l0 = nxt < end ? nxt : -1, l1 = (nxt + 1 <= lim && nxt + 1 < end) ? nxt + 1 : -1;
      SB;
      static_for<0, 12>([&](auto E) {
        constexpr int e = E.v, t = e / 3, h = e % 3;
        const u32x4 bf = cat2(bw[i & 1][t][0], bw[i & 1][t][1]);
        if constexpr (i == 0 || i == 27) mfma_z(acc[i >= 27][2 - h][t], bf, a[i % NW][2 - h]); else mfma_ip(acc[i >= 27][2 - h][t], bf, a[i % NW][2 - h]);
        SB;
        if constexpr (i == 4 && e == 2) go2 = lds_get(L_GO + 4);
        if constexpr ((i == 27 || i == 28) && (e & 1)) put(IC<0>{}, IC<(i - 27) * 6 + e / 2>{});
        if constexpr (i == 36 && e >= 8) put(IC<1>{}, IC<e - 8>{});
        if constexpr (i < 36 && h < 2) bw[(i + 1) & 1][t][h] = rd1(i + 1, t, h);
        if constexpr (l1 < 0) { if constexpr (h == 2 && t < 3 && l0 >= 0) lda(IC<l0>{}, IC<t>{}); }
        else if constexpr (e & 1) lda(IC<(e < 6 ? l0 : l1)>{}, IC<(e >> 1) % 3>{});
        SB;
      });
      if constexpr (i == 29) { asm volatile("s_waitcnt lgkmcnt(8)" ::: "memory"); bar(); }
      if constexpr (i == 4) { if (__builtin_expect(!rfl(go2), 0)) lds_wait(L_GO + 4); asm volatile("" ::: "memory"); STAMP(17); }
      if constexpr (i > 4 && i < 27 && (i % 9 == 4 || i % 9 == 8)) {
        bar();
        if constexpr (i == 26 && OI == 26) static_for<27, 27 + OB0>([&](auto J) { lda(J, IC<0>{}); lda(J, IC<1>{}); lda(J, IC<2>{}); });
        STAMP(17 + (2 * i) / 9);
      }
    });
    STAMP(24);
    static_for<4, 12>([&](auto E) { if constexpr (E.v == 8) asm volatile("s_nop 3" ::: "memory"); put(IC<1>{}, E); SB; });
    lbar();
    STAMP(5);
    STAMP_END;
  }
}

extern "C" void kernel_launch(void* const* d_in, const int* in_sizes, int n_in, void* d_out, int out_size, void* d_ws,
                              size_t ws_size, hipStream_t stream) {
  Params p;
  p.x = d_in[0]; p.wqkv = d_in[1]; p.wo = d_in[2]; p.wg = d_in[4]; p.wu = d_in[5]; p.wd = d_in[6];
  p.out = d_out; p.ws = (char*)d_ws;
  fused_block<<<256, 512, 0, stream>>>(p);
}
```

```cpp
#include <hip/hip_runtime.h>
#include <stdint.h>

typedef __attribute__((ext_vector_type(8))) __bf16 bf16x8;
typedef __attribute__((ext_vector_type(4))) __bf16 bf16x4;
typedef __attribute__((ext_vector_type(2))) __bf16 bf16x2;
typedef __attribute__((ext_vector_type(4))) float f32x4;
typedef __attribute__((ext_vector_type(2))) float f32x2;
typedef __attribute__((ext_vector_type(4))) unsigned u32x4;
typedef __attribute__((ext_vector_type(2))) unsigned u32x2;
typedef __amdgpu_buffer_rsrc_t rsrc_t;
#define LAS __attribute__((address_space(3)))
#define GAS __attribute__((address_space(1)))
#define DI __device__ __forceinline__
#define RLX_AGENT __ATOMIC_RELAXED, __HIP_MEMORY_SCOPE_AGENT
typedef GAS unsigned gu32;
typedef GAS unsigned long long gu64;

constexpr int S = 2048, D = 640, F = 1728;
constexpr int NCOL = 5376;
constexpr int NSTEP = 74;
constexpr unsigned WS_EPOCH = 0;
#define FS 128
constexpr unsigned WS_FLAGH = 768u << 10;
constexpr unsigned WS_FLAGO = WS_FLAGH + 8 * 32 * FS;
constexpr unsigned WS_FLAGKV = WS_FLAGO + 8 * 32 * FS;
constexpr unsigned WS_STAT = 4096;
constexpr unsigned WS_CSUM = WS_STAT + S * 8;
constexpr unsigned WS_KVP = WS_CSUM + 3 * NCOL * 8;
constexpr unsigned WS_HA = 1u << 20;
constexpr unsigned RBSZ = 16 * NSTEP * 1024;
constexpr unsigned step_off(unsigned st) { return st < 54 ? (st / 3) * 49152 + (st % 3) * 1024 : 18 * 49152 + ((st - 54) / 5) * 81920 + ((st - 54) % 5) * 1024; }
constexpr unsigned WS_DBG = 12u << 20;
constexpr unsigned WS_END = 13u << 20;
constexpr unsigned L_B = 65536;
constexpr unsigned L_SCR = 147456;
constexpr unsigned L_MISC = 159744;
constexpr unsigned L_GO = L_MISC + 3072;
constexpr unsigned SPIN_CAP = 1u << 18;
#define HSLEEP 4
#define HSPLIT 64
#define PPIPE 0
#define STA 0
#define STB 2
#define SNAKE 0
#define EARLY1 1
#define ROTW 1
#define NPRE 12
#define WSCH 0
#define PSPLIT 48
#define WARM 1
#define XK 2
#define GRP 1
#define GPRIO 0
#define HPRIO 0
#define PSLEEP 2
#define P2NW 7
#define P2W0 6
#define WSLEEP 0
#define ROTM 18
#define NA1 2
#define O_BAR 5
#define PRIO_MODE 0
#define DMA_SPACE_A T
#define DMA_SPACE_B (4 * T / 3)

struct Params {
  const void* x; const void* wqkv; const void* wo; const void* wg; const void* wu; const void* wd;
  void* out; char* ws;
};

__shared__ __attribute__((aligned(16))) char smem[163840];

DI float sadd(float a, float b) { float o; asm("v_add_f32 %0, %1, %2" : "=v"(o) : "v"(a), "v"(b)); return o; }
DI float smul(float a, float b) { float o; asm("v_mul_f32 %0, %1, %2" : "=v"(o) : "v"(a), "v"(b)); return o; }
DI float sfma(float a, float b, float c) { float o; asm("v_fma_f32 %0, %1, %2, %3" : "=v"(o) : "v"(a), "v"(b), "v"(c)); return o; }
DI unsigned bsel(unsigned m, unsigned a, unsigned b) { unsigned o; asm("v_bfi_b32 %0, %1, %2, %3" : "=v"(o) : "v"(m), "v"(a), "v"(b)); return o; }
DI float bself(unsigned m, float a, float b) { return __builtin_bit_cast(float, bsel(m, __builtin_bit_cast(unsigned, a), __builtin_bit_cast(unsigned, b))); }
DI bool all64(bool c) { return __builtin_amdgcn_ballot_w64(!c) == 0; }
DI float shfl_x(float v, unsigned lane, unsigned m) {
  return __builtin_bit_cast(float, __builtin_amdgcn_ds_bpermute((int)((lane ^ m) << 2), __builtin_bit_cast(int, v)));
}
DI unsigned umin(unsigned a, unsigned b) { return a < b ? a : b; }
DI unsigned rfl(unsigned v) { return __builtin_amdgcn_readfirstlane(v); }
DI rsrc_t mk_rsrc(const void* p, unsigned bytes) { return __builtin_amdgcn_make_buffer_rsrc((void*)p, (short)0, (int)bytes, 0x00020000); }
DI void dma16(unsigned ldsdst, unsigned voff, rsrc_t rs, unsigned soff) {
  asm volatile("s_mov_b32 m0, %0\n\ts_nop 0\n\tbuffer_load_dwordx4 %1, %2, %3 offen lds" ::"s"(ldsdst), "v"(voff), "s"(rs), "s"(soff) : "memory");
}
template <int N> DI void wait_vm() { asm volatile("s_waitcnt vmcnt(%0)" ::"i"(N) : "memory"); }
DI void drain_vm() { wait_vm<0>(); __builtin_amdgcn_s_waitcnt(0x0F70); }
DI void wait_lgkm0() { asm volatile("s_waitcnt lgkmcnt(0)" ::: "memory"); }
DI void bar() { asm volatile("" ::: "memory"); __builtin_amdgcn_s_barrier(); asm volatile("" ::: "memory"); }
DI void lbar() { wait_lgkm0(); bar(); }
template <class V> DI V lds_ld(unsigned a) { return *(LAS V*)((LAS char*)smem + a); }
template <class V> DI void lds_st(unsigned a, V v) { *(LAS V*)((LAS char*)smem + a) = v; }
DI bf16x4 lds_tr(unsigned a) { return __builtin_amdgcn_ds_read_tr16_b64_v4bf16((LAS bf16x4*)((LAS char*)smem + a)); }
DI bf16x8 cat(bf16x4 a, bf16x4 b) { return __builtin_shufflevector(a, b, 0, 1, 2, 3, 4, 5, 6, 7); }
DI f32x4 mfma(bf16x8 a, bf16x8 b, f32x4 c) { return __builtin_amdgcn_mfma_f32_16x16x32_bf16(a, b, c, 0, 0, 0); }
DI unsigned pk2(float a, float b) { f32x2 v = {a, b}; return __builtin_bit_cast(unsigned, __builtin_convertvector(v, bf16x2)); }
DI u32x2 pk4(f32x4 v) { u32x2 r = {pk2(v[0], v[1]), pk2(v[2], v[3])}; return r; }
DI bf16x8 frag(u32x2 lo, u32x2 hi) { u32x4 v = {lo[0], lo[1], hi[0], hi[1]}; return __builtin_bit_cast(bf16x8, v); }
DI float bflo(unsigned v) { return __builtin_bit_cast(float, v << 16); }
DI float bfhi(unsigned v) { return __builtin_bit_cast(float, v & 0xffff0000u); }
DI float rbf(float v) { return bflo(pk2(v, 0.f)); }
DI float relu2(float v) { float o; asm("v_add_f32 %0, %1, |%1|" : "=v"(o) : "v"(v)); return o; }
DI f32x4 phi4(f32x4 u, f32x4 rl, f32x4 rh) {
  const f32x4 l = u * rl;
  f32x4 e, a;
#pragma unroll
  for (int j = 0; j < 4; ++j) {
    e[j] = __builtin_amdgcn_fmed3f(__builtin_amdgcn_exp2f(l[j]), 0.f, 1.f);
    a[j] = relu2(u[j]);
  }
  return a * rh + e + 1e-6f;
}
DI f32x4 splat(float v) { return f32x4{v, v, v, v}; }
template <int CTRL> DI float dpp_add(float v) {
  return v + __builtin_bit_cast(float, __builtin_amdgcn_update_dpp(0, __builtin_bit_cast(int, v), CTRL, 0xf, 0xf, true));
}
DI float dot2(unsigned a, unsigned b, float c) {
  return __builtin_amdgcn_fdot2_f32_bf16(__builtin_bit_cast(bf16x2, a), __builtin_bit_cast(bf16x2, b), c, false);
}
DI u32x4 ldtr2(unsigned a, unsigned d) { return __builtin_bit_cast(u32x4, cat(lds_tr(a), lds_tr(a + d))); }
DI void mfma_ip(f32x4& c, const u32x4& a, const u32x4& b) { asm volatile("v_mfma_f32_16x16x32_bf16 %0, %1, %2, %0" : "+v"(c) : "v"(a), "v"(b)); }
DI void mfma_c(f32x4& d, const u32x4& a, const u32x4& b, const f32x4& c) { asm volatile("v_mfma_f32_16x16x32_bf16 %0, %1, %2, %3" : "=&v"(d) : "v"(a), "v"(b), "v"(c)); }
DI void mfma_z(f32x4& c, const u32x4& a, const u32x4& b) { asm volatile("v_mfma_f32_16x16x32_bf16 %0, %1, %2, 0" : "=&v"(c) : "v"(a), "v"(b)); }
template <int N> DI void acc_fence(f32x4* c) {
  asm volatile("s_nop 7" ::: "memory");
#pragma unroll
  for (int i = 0; i < N; ++i) asm volatile("" : "+v"(c[i]));
}
DI u32x4 cat2(u32x2 a, u32x2 b) { return u32x4{a[0], a[1], b[0], b[1]}; }
DI u32x2 ldtr1(unsigned a) { return __builtin_bit_cast(u32x2, lds_tr(a)); }
template <int IMM> DI void set_m0i(unsigned v) { asm volatile("s_add_i32 m0, %0, %1" ::"s"(v), "i"(IMM) : "memory", "scc"); }
template <int OFF> DI void dma_goi(unsigned voff, rsrc_t rs, unsigned soff) {
  asm volatile("buffer_load_dwordx4 %0, %1, %2 offen offset:%3 lds" ::"v"(voff), "s"(rs), "s"(soff), "i"(OFF) : "memory");
}
DI void set_m0(unsigned v) { asm volatile("s_mov_b32 m0, %0" ::"s"(v) : "memory"); }
DI void dma_go(unsigned voff, rsrc_t rs, unsigned soff) {
  asm volatile("buffer_load_dwordx4 %0, %1, %2 offen lds" ::"v"(voff), "s"(rs), "s"(soff) : "memory");
}
DI f32x4 mfma_u(u32x4 a, u32x4 b, f32x4 c) { return mfma(__builtin_bit_cast(bf16x8, a), __builtin_bit_cast(bf16x8, b), c); }
#define STAMP_DECL
#define STAMP_ARG
#define STAMP_PASS
#define STAMPX(c, i)
#define STAMP_END
#define STAMP(i) STAMPX(__builtin_amdgcn_workitem_id_x() == 0, i)

DI unsigned poll_flags(gu32* word, bool active, unsigned tag) {
  unsigned v = 0;
#pragma unroll 1
  for (unsigned spins = 0; spins < SPIN_CAP; ++spins) {
    v = __hip_atomic_load(word, RLX_AGENT);
    if (all64(!active || v == tag)) break;
    if (PSLEEP) __builtin_amdgcn_s_sleep(PSLEEP);
  }
  asm volatile("" ::: "memory");
  return v;
}

DI void lds_set(unsigned a) { __hip_atomic_store((LAS unsigned*)smem + a / 4, 1u, __ATOMIC_RELAXED, __HIP_MEMORY_SCOPE_WORKGROUP); }
DI unsigned lds_get(unsigned a) { return __hip_atomic_load((LAS unsigned*)smem + a / 4, __ATOMIC_RELAXED, __HIP_MEMORY_SCOPE_WORKGROUP); }
DI void lds_wait(unsigned a) {
#pragma unroll 1
  for (unsigned spins = 0; spins < (SPIN_CAP << 4); ++spins) {
    if (rfl(lds_get(a))) break;
    __builtin_amdgcn_s_sleep(1);
  }
  asm volatile("" ::: "memory");
}

template <int I> struct IC { static constexpr int v = I; };
template <int B, int E, class Fn> DI void static_for(Fn&& f) {
  if constexpr (B < E) { f(IC<B>{}); static_for<B + 1, E>(f); }
}

enum { J_MLP = 0, J_KV = 1, J_Q = 2, J_SPARE = 3 };

struct Dma {
  rsrc_t rsA, rsB;
  unsigned vA0, vA1, vB0, vB1, vB2;
  unsigned sA[4], sB, advB;
  unsigned wa, wq;
  unsigned mA, mB, mW;
};

constexpr unsigned a_slot(unsigned s3) { return s3 == 0 ? 126976u : (s3 - 1) * 32768u; }
#define SB __builtin_amdgcn_sched_barrier(0)

template <int T> DI unsigned bofs(const unsigned (&boff)[T], int t) { return T == 6 && t >= 3 ? boff[t - 3] + 6144 : boff[t]; }
template <int T, bool TR, int KS, class Extra>
DI void kstep(f32x4 (&acc)[4][T], const u32x4 (&ac)[4], const u32x2 (&bc)[T][2], u32x4 (&an)[4], u32x2 (&bn)[T][2],
              const unsigned (&aoff)[4], const unsigned (&boff)[T], const Dma& d, Extra&& extra) {
  constexpr int s = KS >> 1, KIND = KS & 1, NP = KIND == 0 ? 4 : 3, ST = KIND == 0 ? (STA ? STA : (4 * T - 1) / NP) : (STB ? STB : (4 * T - 1) / NP);
  constexpr int sn = (KS + 1) >> 1;
  constexpr unsigned asl = a_slot(sn % 3), bsl = ((KS + 1) % 5) * 12288u;
  static_for<0, 4 * T>([&](auto E) {
    constexpr int e = E.v, t = e >> 2, i = (SNAKE && (t & 1)) ? 3 - (e & 3) : (e & 3);
    const u32x4 bf = cat2(bc[t][0], bc[t][1]);
    if constexpr (KS == 0) { if (TR) mfma_z(acc[i][t], bf, ac[i]); else mfma_z(acc[i][t], ac[i], bf); }
    else { if (TR) mfma_ip(acc[i][t], bf, ac[i]); else mfma_ip(acc[i][t], ac[i], bf); }
    SB;
    if constexpr (KS < 19) {
      if constexpr (e < 4) an[e] = lds_ld<u32x4>(aoff[((KS + 1) & 1) + (sn % 3 == 0 ? 2 : 0)] + (sn % 3 == 0 ? 0u : asl) + e * 2048);
      else if constexpr (e < 4 + 2 * T) bn[(e - 4) >> 1][e & 1] = ldtr1(bofs<T>(boff, (e - 4) >> 1) + bsl + (e & 1) * 768);
    }
    constexpr int pc = e / ST;
    if constexpr (KIND == 0) {
      constexpr int adv = s < 8 ? (s + 2) * 128 : 0, dst = (int)a_slot((s + 2) % 3) + pc * 1024, off = dst >= adv ? adv : 0;
      if constexpr (pc < NP && e % ST == ST - 2) set_m0i<dst - off>(d.mA);
      if constexpr (pc < NP && e % ST == ST - 1) dma_goi<off>((pc & 1) ? d.vA1 : d.vA0, d.rsA, d.sA[pc] + (adv - off));
    } else {
      constexpr int h0 = (2 * s) % 5;
      if constexpr (pc < NP && e % ST == ST - 2) set_m0i<(h0 == 4 ? 0 : h0 * 12288) + pc * 1024>(h0 == 4 ? d.mW : d.mB);
      if constexpr (pc < NP && e % ST == ST - 1) dma_goi<0>(pc == 0 ? d.vB0 : (pc == 1 ? d.vB1 : d.vB2), d.rsB, d.sB);
    }
    extra(E);
    SB;
  });
}

struct RowStat { u32x4 xa; unsigned xb, row; float s, q; };
DI void row_load(RowStat& R, rsrc_t rsX, unsigned lane) {
  asm volatile("buffer_load_dwordx4 %0, %2, %4, %5 offen\n\tbuffer_load_dword %1, %3, %4, %5 offen offset:1024"
               : "=&v"(R.xa), "=&v"(R.xb) : "v"(lane * 16), "v"(lane * 4), "s"(rsX), "s"(R.row * 1280) : "memory");
}
template <int K> DI void row_step(RowStat& R, unsigned lane) {
  if constexpr (K < 5) {
    if constexpr (K == 0) { R.s = 0.f; R.q = 0.f; }
    const unsigned v = K < 4 ? R.xa[K & 3] : R.xb;
    asm volatile("v_dot2c_f32_bf16 %0, %2, %3\n\tv_dot2c_f32_bf16 %1, %2, %2" : "+v"(R.s), "+v"(R.q) : "v"(v), "v"(0x3f803f80u));
  } else if constexpr (K < 11) {
    constexpr int C = K == 5 ? 0xB1 : (K == 6 ? 0x4E : (K == 7 ? 0x141 : (K == 8 ? 0x140 : (K == 9 ? 0x142 : 0x143))));
    if constexpr (K == 5) asm volatile("s_nop 2" : "+v"(R.s), "+v"(R.q));
    R.s = dpp_add<C>(R.s); R.q = dpp_add<C>(R.q);
  } else if constexpr (K == 11) {
    R.s *= 1.0f / D;
    R.q = __builtin_fmaxf(R.q * (1.0f / D) - R.s * R.s, 0.f);
  } else if constexpr (K == 12) {
    R.q = __builtin_amdgcn_sqrtf(rbf(rbf(R.q) + 1e-5f));
  } else {
    lds_st<unsigned>(lane == 63 ? L_GO + 32 + (R.row & 7) * 4 : L_GO + 64, pk2(R.s, R.q));
  }
  if constexpr (K >= 5 && K < 13) asm volatile("" : "+v"(R.s), "+v"(R.q));
}
DI void row_publish(const RowStat& R, char* ws, unsigned tag, unsigned tid) {
  if (tid < 8) __hip_atomic_store((gu64*)(ws + WS_STAT) + R.row + tid, ((unsigned long long)tag << 32) | lds_ld<unsigned>(L_GO + 32 + tid * 4), RLX_AGENT);
}

struct Gran { u32x2 g0, g1, g2; unsigned off, step; };
DI void gran_issue(Gran& G, rsrc_t rs) {
  asm volatile("buffer_load_dwordx2 %0, %3, %4, 0 offen sc1\n\tbuffer_load_dwordx2 %1, %3, %4, %5 offen sc1\n\t"
               "buffer_load_dwordx2 %2, %3, %4, %6 offen sc1"
               : "=&v"(G.g0), "=&v"(G.g1), "=&v"(G.g2) : "v"(G.off), "s"(rs), "s"(G.step), "s"(2 * G.step) : "memory");
}
DI void gran_load(Gran& G, rsrc_t rs) {
  asm volatile("buffer_load_dwordx2 %0, %3, %4, 0 offen sc1\n\tbuffer_load_dwordx2 %1, %3, %4, %5 offen sc1\n\t"
               "buffer_load_dwordx2 %2, %3, %4, %6 offen sc1\n\ts_waitcnt vmcnt(0)"
               : "=&v"(G.g0), "=&v"(G.g1), "=&v"(G.g2) : "v"(G.off), "s"(rs), "s"(G.step), "s"(2 * G.step) : "memory");
}

DI unsigned urow(unsigned pos, unsigned rot) { const unsigned u = pos + rot; return (u >= 54 ? u - 54 : u) * 40960u; }

constexpr unsigned wo_tile(unsigned v) { return (v / 5) * 10 + (v % 5 == 4 ? 4 : (v % 5 < 2 ? 2 * (v % 5) : 2 * (v % 5) + 1)); }
struct W2 { rsrc_t wd, wo; unsigned rot, col; };
DI unsigned w2_off(const W2& W, int u, unsigned lw) {
  if (u < 54) return urow(u, W.rot) + lw * 10240u + W.col;
  return wo_tile(u - 54) * 20480u + (lw >> 1) * ((u - 54) % 5 == 4 ? 102400u : 20480u) + (lw & 1) * 10240u + W.col;
}
DI u32x4 w2_load(const W2& W, int u, unsigned lw, unsigned voff) {
  return __builtin_amdgcn_raw_buffer_load_b128(u < 54 ? W.wd : W.wo, voff, w2_off(W, u, lw), 0);
}

template <int T, bool TR, bool PF, bool LASTA, class Pre>
DI void main_loop(Pre&& pre, RowStat& R, char* ws, f32x4 (&acc)[4][T], unsigned (&boff)[T], Dma& d, unsigned w, unsigned lane, unsigned tid,
                  unsigned epoch, unsigned& tag, unsigned rb, unsigned ccol, rsrc_t rsB2, rsrc_t rsWS, unsigned cb, unsigned rot, u32x4 (&pre2)[36] STAMP_ARG) {
  const unsigned g = lane >> 4, r = lane & 15, m = w & 3;
  unsigned aoff[4];
  aoff[0] = (64 * m + r) * 128 + ((g ^ (r >> 1)) << 4);
  aoff[1] = aoff[0] ^ 64;
  aoff[2] = aoff[0] + a_slot(0); aoff[3] = aoff[1] + a_slot(0);
  const unsigned wq = d.wq, wa = d.wa;
  asm volatile("" : "+v"(aoff[0]), "+v"(aoff[1]), "+v"(aoff[2]), "+v"(aoff[3]), "+v"(boff[0]), "+v"(boff[1]), "+v"(boff[2]));
  if (T == 5) asm volatile("" : "+v"(boff[3]), "+v"(boff[4]));
  Gran G;
  unsigned v2, vb2, vpf, tA[3], tB[3];
  row_load(R, d.rsA, lane);
  {
    asm volatile("s_waitcnt vmcnt(%2) lgkmcnt(0)\n\tv_readfirstlane_b32 %0, %1\n\ts_nop 3" : "=s"(tag) : "v"(epoch), "i"(5 + NA1) : "memory");
    tag = rfl(tag) + 1;
    bar();
  }
  STAMP(1);
  u32x4 a0[4], a1[4];
  u32x2 b0[T][2], b1[T][2];
#pragma unroll
  for (int i = 0; i < 4; ++i) a0[i] = lds_ld<u32x4>(aoff[2] + i * 2048);
#pragma unroll
  for (int t = 0; t < T; ++t) { b0[t][0] = ldtr1(bofs<T>(boff, t)); b0[t][1] = ldtr1(bofs<T>(boff, t) + 768); }
  SB;
#pragma unroll
  for (int i = NA1; i < 4; ++i) dma16(a_slot(1) + d.mA + i * 1024, (i & 1) ? d.vA1 : d.vA0, d.rsA, d.sA[i] + 128);
  const bool y = w >= 4;
  bool ok = true;
  auto ln_store = [&] {
    const float v0 = y ? -bflo(G.g0[0]) : __builtin_bit_cast(float, G.g0[0]) + __builtin_bit_cast(float, G.g1[0]) + __builtin_bit_cast(float, G.g2[0]);
    const unsigned la = L_MISC + (y ? tid * 4 - 1024 : 2048 + tid * 4);
    lds_st<float>(la, v0);
    lds_st<float>(y ? la + 1024 : L_GO + 64 + (lane & 7) * 4, __builtin_amdgcn_rcpf(bfhi(G.g0[0])));
  };
  static_for<0, 10>([&](auto S_) {
    constexpr int s = S_.v, L = 4 * T;
    constexpr int NX = (PF && s >= 1 && 2 * s <= NPRE) ? 2 : 0;
    kstep<T, TR, 2 * s>(acc, a0, b0, a1, b1, aoff, boff, d, [&](auto E) {
      constexpr int e = E.v;
      if constexpr (s == 2) {
        if constexpr (e == 2) { vb2 = v2 + (3 * wq) * 10240u; asm volatile("" : "+v"(vb2)); }
        if constexpr (e == 5) { G.off = w < 4 ? WS_CSUM + ccol * 8 : WS_STAT + (rb * 256 + tid - 256) * 8; asm volatile("" : "+v"(G.off)); }
        if constexpr (e == 8) { G.step = w < 4 ? NCOL * 8 : 0; tA[0] = urow(wa, rot) + cb * 128u; asm volatile("" : "+s"(G.step), "+s"(tA[0])); }
        if constexpr (e == 10) { tA[1] = urow(8 + wa, rot) + cb * 128u; asm volatile("" : "+s"(tA[1])); }
        if constexpr (e == 12) { tA[2] = urow(31 + wa, rot) + cb * 128u; asm volatile("" : "+s"(tA[2])); }
        if constexpr (e == 14) {
          tB[0] = urow(16, rot) + cb * 128u; tB[1] = urow(w < 4 ? 19u : 22u, rot) + cb * 128u; tB[2] = urow(w < 4 ? 25u : 28u, rot) + cb * 128u;
          asm volatile("" : "+s"(tB[0]), "+s"(tB[1]), "+s"(tB[2]));
        }
      }
      if constexpr (s == (TR ? 1 : 3) && e == 2) pre();
      if constexpr (NX && e >= L - 2) {
        constexpr int u = 2 * (s - 1) + e - (L - 2);
        pre2[u] = __builtin_amdgcn_raw_buffer_load_b128(rsB2, vpf, urow(36 + u, rot) + (w & 3) * 10240u + cb * 128u, 0);
      }
      if constexpr (s < 7) { if constexpr (e == L - 3) d.sB += s == 0 ? d.advB * (1 + (w >> 2)) : d.advB; }
      else if constexpr (s == 7) {
        if constexpr (e == L - 6) d.vB0 = y ? vb2 : d.vB0;
        if constexpr (e == L - 4) d.vB1 = y ? vb2 + 10240u : d.vB1;
        if constexpr (e == L - 3) d.vB2 = y ? vb2 + 20480u : d.vB2;
        if constexpr (e == L - 2) d.sB = y ? tB[0] : d.sB + d.advB;
        if constexpr (e == L - 1) d.rsB = y ? rsB2 : d.rsB;
      } else {
        if constexpr (e == L - 3) { d.sB = tB[s - 7]; d.rsB = rsB2; d.vB0 = vb2; d.vB1 = vb2 + 10240u; d.vB2 = vb2 + 20480u; }
      }
      if constexpr (s == 9 && e == L - 4) {
        asm volatile("s_waitcnt vmcnt(7)" : "+v"(G.g0), "+v"(G.g1), "+v"(G.g2)::"memory");
        ok = all64(G.g0[1] == tag && G.g1[1] == tag && G.g2[1] == tag);
      }
      if constexpr (s == 9 && e == L - 3) ln_store();
    });
    if constexpr (s == 0) {
      wait_vm<EARLY1 ? 8 - NA1 : 11>();
      static_for<0, 14>([&](auto K) { row_step<K.v>(R, lane); });
    }
    if constexpr (s == 9) {
      if (__builtin_expect(!ok, 0)) {
#pragma unroll 1
        for (unsigned spins = 0; spins < SPIN_CAP; ++spins) {
          __builtin_amdgcn_s_sleep(2);
          gran_load(G, rsWS);
          if (all64(G.g0[1] == tag && G.g1[1] == tag && G.g2[1] == tag)) break;
        }
        ln_store();
      }
    }
    wait_lgkm0();
    if constexpr (s < 9) wait_vm<4 + NX>();
    bar();
    if constexpr (s == 0) { STAMP(12); }
    SB;
    kstep<T, TR, 2 * s + 1>(acc, a1, b1, a0, b0, aoff, boff, d, [&](auto E) {
      constexpr int e = E.v;
      if constexpr (s == 0 && e == 6) {
        if (y) {
          const unsigned so = d.sB - d.advB;
          dma16(d.mW + 49152, d.vB0, d.rsB, so); dma16(d.mW + 49152 + 1024, d.vB1, d.rsB, so); dma16(d.mW + 49152 + 2048, d.vB2, d.rsB, so);
        }
      }
      if constexpr (s == 0 && e == L - 4) {
        v2 = (lane >> 3) * 1280 + (((lane & 7) ^ (((lane >> 4) & 3) << 1)) << 4);
        vpf = y ? v2 : 0x7ffffff0u;
        asm volatile("" : "+v"(v2), "+v"(vpf));
      }
      if constexpr (s == 0 && e == L - 6) row_publish(R, ws, tag, tid);
      if constexpr (s == 8 && e == 0) gran_issue(G, rsWS);
      if constexpr (s >= 7 && s < 9 && e >= L - 4) {
        d.rsA = rsB2; d.vA0 = v2; d.vA1 = v2;
        d.sA[e - (L - 4)] = tA[s - 7] + (e - (L - 4)) * 10240;
      }
      if constexpr (s == 9 && LASTA && e == 7) {
        if (wa < 5) {
#pragma unroll
          for (int i = 0; i < 4; ++i) dma16(a_slot(0) + d.mA + i * 1024, v2, rsB2, tA[2] + i * 10240);
        }
      }
    });
    if constexpr (s == 8) { STAMP(13); }
    if constexpr (s == 2) { STAMP(26); }
    if constexpr (s == 6) { STAMP(27); }
  });
  acc_fence<4 * T>(&acc[0][0]);
  STAMP(14);
}

DI void st16(u32x4 v, rsrc_t rs, unsigned voff, unsigned soff) {
  __builtin_amdgcn_raw_buffer_store_b128(v, rs, voff, soff, 16);
  asm volatile("s_nop 1" ::"v"(v) : "memory");
}
DI void publish_flag(const Params& p, unsigned flag_off, unsigned tag, unsigned tid) {
  drain_vm();
  bar();
  if (tid == 0) __hip_atomic_store((gu32*)(p.ws + flag_off), tag, RLX_AGENT);
}
DI void st_frag(rsrc_t rs, unsigned lane, unsigned off, u32x2 lo, u32x2 hi) { st16(cat2(lo, hi), rs, lane * 16, off); }
DI void shared_send(unsigned w, unsigned lane, const u32x2 (&sh)[4]) {
  if (w >= 4) lds_st<u32x4>(L_SCR + w * 1024 + lane * 16, cat2(sh[0], sh[1]));
  else lds_st<u32x4>(L_SCR + w * 1024 + lane * 16, cat2(sh[2], sh[3]));
  asm volatile("" ::: "memory");
}
DI void shared_recv(rsrc_t rs, unsigned w, unsigned lane, unsigned off, unsigned str, const u32x2 (&sh)[4]) {
  lbar();
  u32x4 rcv = lds_ld<u32x4>(L_SCR + (w ^ 4) * 1024 + lane * 16);
  u32x2 r0 = {rcv[0], rcv[1]}, r1 = {rcv[2], rcv[3]};
  if (w >= 4) { st_frag(rs, lane, off + 2 * str, r0, sh[2]); st_frag(rs, lane, off + 3 * str, r1, sh[3]); }
  else        { st_frag(rs, lane, off, sh[0], r0); st_frag(rs, lane, off + str, sh[1], r1); }
}

__global__ __launch_bounds__(512, 2) void fused_block(Params p) {
  unsigned tid = __builtin_amdgcn_workitem_id_x(), lane = tid & 63;
  const unsigned w = rfl(tid >> 6);
  const unsigned b = __builtin_amdgcn_workgroup_id_x(), xcd = b & 7, slot = b >> 3;
  const unsigned rb = 4 * (xcd & 1) + (slot & 3), jq = xcd >> 1, ji = slot >> 2;
  const unsigned spi = (xcd - 4) * 4 + (slot & 3);
  const unsigned long long jl0 = (jq & 1) ? 0x4123220908070605ull : 0x4021200403020100ull, jl1 = (jq & 1) ? 0x4327266011100f0eull : 0x422524600d0c0b0aull;
  const unsigned jb = (unsigned)(((jq & 2) ? jl1 : jl0) >> (8 * ji)) & 0xff, jtype = jb >> 5, jidx = jb & 31;
  STAMP_DECL;
  STAMP(0);

  Dma d;
  RowStat rs_;
  const bool mlp = jtype == J_MLP;
  const unsigned pitch = mlp ? 3456u : 3840u, wq = (w + (ROTW ? slot : 0)) & 3, wa = (w + (ROTW ? ji : 0)) & 7;
  d.wa = wa; d.wq = wq;
  const rsrc_t rsX = mk_rsrc(p.x, S * D * 2);
  d.vA0 = (lane >> 3) * 1280 + (((lane & 7) ^ (lane >> 4)) << 4); d.vA1 = d.vA0 ^ 64;
  d.rsA = rsX; d.mA = wa * 4096u;
#pragma unroll
  for (int i = 0; i < 4; ++i) d.sA[i] = (rb * 256 + 32 * wa + 8 * i) * 1280;
  unsigned epoch, tag;
  asm volatile("s_nop 4" :: "s"(rsX) : "memory");
  STAMP(6);
#pragma unroll
  for (int i = 0; i < 4; ++i) dma16(a_slot(0) + d.mA + i * 1024, (i & 1) ? d.vA1 : d.vA0, d.rsA, d.sA[i]);
  asm volatile("global_load_dword %0, %1, %2 sc1" : "=&v"(epoch) : "v"(0u), "s"(p.ws) : "memory");
  SB;
  {
    unsigned vb[3];
    const unsigned lim = mlp ? 176u : 144u;
    {
      const unsigned r0 = (lane * 43) >> 9, p0 = lane - 12 * r0;
      const bool w1 = p0 >= 8, w2 = p0 >= 4;
      const unsigned r1 = r0 + (w1 ? 6 : 5), p1 = p0 + (w1 ? -8 : 4), r2 = r0 + (w2 ? 11 : 10), p2 = p0 + (w2 ? -4 : 8);
      vb[0] = r0 * pitch + umin(p0 * 16, lim);
      vb[1] = r1 * pitch + umin((p1 * 16) ^ ((r1 & 8) << 2), lim);
      vb[2] = r2 * pitch + umin((p2 * 16) ^ 32, lim);
    }
    d.vB0 = vb[0]; d.vB1 = vb[1]; d.vB2 = vb[2];
    const unsigned mh = wq >> 1;
    const void* bw = mlp ? (mh ? p.wu : p.wg) : p.wqkv;
    const unsigned cj = mlp ? 192u : 160u * jtype, cm = mlp ? 0u : (jtype == J_KV ? 1280u : 160u), c0 = jtype == J_KV ? 1280u : 0u;
    d.rsB = mk_rsrc(bw, D * pitch);
    d.sB = (16 * (wq & 1) + (w < 4 ? 32 : 0)) * pitch + c0 + cj * jidx + cm * mh; d.advB = 64 * pitch;
    d.mB = L_B + wq * 3072u + (w < 4 ? 0u : 12288u); d.mW = L_B + wq * 3072u + (w < 4 ? 49152u : 0u);
    const unsigned bdst = L_B + (w < 4 ? 12288u : 0u) + wq * 3072u;
    dma16(bdst, d.vB0, d.rsB, d.sB); dma16(bdst + 1024, d.vB1, d.rsB, d.sB); dma16(bdst + 2048, d.vB2, d.rsB, d.sB);
    rs_.row = rfl(b * 8 + w);
    d.sB += d.advB;
    dma16(bdst + 24576, d.vB0, d.rsB, d.sB); dma16(bdst + 24576 + 1024, d.vB1, d.rsB, d.sB); dma16(bdst + 24576 + 2048, d.vB2, d.rsB, d.sB);
#pragma unroll
    for (int i = 0; i < NA1; ++i) dma16(a_slot(1) + d.mA + i * 1024, (i & 1) ? d.vA1 : d.vA0, d.rsA, d.sA[i] + 128);
  }
  STAMP(10);
  lds_st<unsigned>(L_GO + (tid & 1) * 4, 0u);

  const unsigned t2 = 28 * xcd + slot;
  const bool has2 = slot < 28 && t2 < 220;
  const unsigned G = t2 / 10, cb = t2 % 10;
  constexpr unsigned rot = 32;
  unsigned g = lane >> 4, r = lane & 15, q4 = r >> 2, p4 = lane & 3;
  const rsrc_t rsWd = mk_rsrc(p.wd, has2 ? F * D * 2 : 0);
  const rsrc_t rsWS = mk_rsrc(p.ws, WS_END);
  unsigned v2 = (lane >> 3) * 1280 + (((lane & 7) ^ (((lane >> 4) & 3) << 1)) << 4);
  auto relane = [&] {
    asm volatile("" : "+v"(tid));
    lane = tid & 63; g = lane >> 4; r = lane & 15; q4 = r >> 2; p4 = lane & 3;
    v2 = (lane >> 3) * 1280 + (((lane & 7) ^ (((lane >> 4) & 3) << 1)) << 4);
  };
  const unsigned m = w & 3, n = w >> 2;
  const unsigned brow = L_B + (8 * g + q4) * 192;
  const unsigned bx = (g & 1) << 5;

  u32x4 pre2[36];
  W2 W;
  W.wd = rsWd; W.wo = mk_rsrc(p.wo, has2 ? D * D * 2 : 0); W.rot = rot; W.col = cb * 128u;
  if (jtype == J_MLP) {
    f32x4 acc[4][6];
    unsigned boff[6];
#pragma unroll
    for (int t = 0; t < 3; ++t) {
      const unsigned gt = n ? (t == 2 ? 3 : 4 + t) : t;
      boff[t] = brow + ((32 * gt + 8 * p4) ^ bx); boff[t + 3] = boff[t] + 6144;
    }
    const unsigned ccol = tid < 96 ? 1920 + 96 * jidx + tid : 3648 + 96 * jidx + (tid < 192 ? tid - 96 : 0);
    main_loop<6, true, true, true>([] {}, rs_, p.ws, acc, boff, d, w, lane, tid, epoch, tag, rb, ccol, rsWd, rsWS, cb, rot, pre2 STAMP_PASS);
    relane();
    float mm[4];
    f32x4 cg[3], cu[3];
#pragma unroll
    for (int i = 0; i < 4; ++i) mm[i] = lds_ld<float>(L_MISC + (64 * m + 16 * i + r) * 4);
#pragma unroll
    for (int t = 0; t < 3; ++t) {
      const unsigned gt = n ? (t == 2 ? 3 : 4 + t) : t;
      cg[t] = lds_ld<f32x4>(L_MISC + 2048 + (16 * gt + 4 * g) * 4); cu[t] = lds_ld<f32x4>(L_MISC + 2048 + (96 + 16 * gt + 4 * g) * 4);
    }
    auto hval = [&](int i, int t) {
      f32x4 gg = acc[i][t] + mm[i] * cg[t];
      const f32x4 uu = acc[i][t + 3] + mm[i] * cu[t];
#pragma unroll
      for (int j = 0; j < 4; ++j) gg[j] = relu2(gg[j]);
      return pk4(gg * uu);
    };
    const unsigned ho = WS_HA + rb * RBSZ + jidx * 49152 + m * 4 * 3072;
#pragma unroll
    for (int i = 0; i < 4; ++i) { st_frag(rsWS, lane, ho + i * 3072 + n * 2048, hval(i, 0), hval(i, 1)); SB; }
    const u32x2 sh[4] = {hval(0, 2), hval(1, 2), hval(2, 2), hval(3, 2)};
    shared_send(w, lane, sh);
    shared_recv(rsWS, w, lane, ho + 1024, 3072, sh);
    STAMP(15);
    publish_flag(p, WS_FLAGH + (rb * 32 + jidx) * FS, tag, tid);
    STAMP(3);
  } else if (jtype == J_KV) {
    f32x4 acc[4][5];
    unsigned boff[5];
    boff[0] = brow + ((80 * n + 8 * p4) ^ bx); boff[1] = boff[0] + 6144;
    boff[2] = brow + ((80 * n + 40 + 8 * p4) ^ bx); boff[3] = boff[2] + 6144;
    boff[4] = brow + ((80 * n + 32 + 40 * (p4 >> 1)) ^ bx) + (p4 & 1) * 6144;
    const unsigned kb = 126976u + m * 6720 + n * 3360, dump = L_MISC - 2048 + lane * 16;
    const unsigned ea = (q4 == 1 || r == 0) ? kb + (r == 0 ? 20 : 12 + r) * 80 : 0u, eb = (q4 == 3 || r == 0) ? kb + 1680 + (r == 0 ? 20 : 4 + r) * 80 : 0u;
    unsigned ka[6] = {kb + r * 80 + g * 16, ea ? ea + g * 16 : dump, eb ? eb + g * 16 : dump, g == 0 ? kb + r * 80 + 64 : dump,
                      g == 2 ? kb + 1680 + r * 80 + 64 : dump, (g == 0 && ea) ? ea + 64 : ((g == 2 && eb) ? eb + 64 : dump)};
    auto pin = [&] { asm volatile("" : "+v"(ka[0]), "+v"(ka[1]), "+v"(ka[2]), "+v"(ka[3]), "+v"(ka[4]), "+v"(ka[5])); };
    const unsigned ccol = tid < 80 ? 640 + 80 * jidx + tid : 1280 + 80 * jidx + (tid < 160 ? tid - 80 : 0);
    main_loop<5, false, false, false>(pin, rs_, p.ws, acc, boff, d, w, lane, tid, epoch, tag, rb, ccol, rsWd, rsWS, cb, rot, pre2 STAMP_PASS);
    relane();
    float cs[5];
    cs[0] = lds_ld<float>(L_MISC + 2048 + (40 * n + r) * 4);
    cs[1] = lds_ld<float>(L_MISC + 2048 + (80 + 40 * n + r) * 4);
    cs[2] = lds_ld<float>(L_MISC + 2048 + (40 * n + 20 + r) * 4);
    cs[3] = lds_ld<float>(L_MISC + 2048 + (80 + 40 * n + 20 + r) * 4);
    cs[4] = lds_ld<float>(L_MISC + 2048 + ((q4 & 1) * 80 + 40 * n + 16 + 20 * (q4 >> 1) + (r & 3)) * 4);
    u32x2 pk[4][3], vv[4][3];
    const unsigned vmask = 0u - (q4 & 1);
    f32x4 mm4[4], rr4[4];
#pragma unroll
    for (int i = 0; i < 4; ++i) {
      mm4[i] = lds_ld<f32x4>(L_MISC + (64 * m + 16 * i + 4 * g) * 4); rr4[i] = lds_ld<f32x4>(L_MISC + 1024 + (64 * m + 16 * i + 4 * g) * 4);
    }
    SB;
#pragma unroll
    for (int i = 0; i < 4; ++i) {
      const f32x4 mm = mm4[i], rr = rr4[i];
      const f32x4 rl = rr * 1.4426950408889634f, rh = rr * 0.5f;
      f32x4 val[5];
#pragma unroll
      for (int t = 0; t < 5; ++t) val[t] = acc[i][t] + mm * cs[t];
#pragma unroll
      for (int t = 0; t < 3; ++t) pk[i][t] = pk4(phi4(val[2 * t], rl, rh));
      vv[i][0] = pk4(val[1] * rr); vv[i][1] = pk4(val[3] * rr);
      const u32x2 v4 = pk4(val[4] * rr);
      vv[i][2] = u32x2{bsel(vmask, v4[0], 0x3f803f80u), bsel(vmask, v4[1], 0x3f803f80u)};
    }
    STAMP(25);
    f32x4 P[7] = {};
#pragma unroll
    for (int i = 0; i < 4; i += 2) {
      const bf16x8 k0 = frag(pk[i][0], pk[i + 1][0]), k2 = frag(pk[i][1], pk[i + 1][1]), k4 = frag(pk[i][2], pk[i + 1][2]);
      const bf16x8 v1 = frag(vv[i][0], vv[i + 1][0]), v3 = frag(vv[i][1], vv[i + 1][1]), v4 = frag(vv[i][2], vv[i + 1][2]);
      P[0] = mfma(k0, v1, P[0]); P[1] = mfma(k0, v4, P[1]); P[2] = mfma(k4, v1, P[2]);
      P[3] = mfma(k2, v3, P[3]); P[4] = mfma(k2, v4, P[4]); P[5] = mfma(k4, v3, P[5]); P[6] = mfma(k4, v4, P[6]);
    }
    lds_st<f32x4>(ka[0], P[0]); lds_st<f32x4>(ka[0] + 1680, P[3]); lds_st<f32x4>(ka[1], P[1]); lds_st<f32x4>(ka[2], P[4]);
    lds_st<f32x4>(ka[3], P[2]); lds_st<f32x4>(ka[4], P[5]); lds_st<f32x4>(ka[5], P[6]);
    lbar();
    if (tid < 210) {
      const unsigned a = 126976u + tid * 32;
      const f32x4 t0 = (lds_ld<f32x4>(a) + lds_ld<f32x4>(a + 6720)) + (lds_ld<f32x4>(a + 13440) + lds_ld<f32x4>(a + 20160));
      const f32x4 t1 = (lds_ld<f32x4>(a + 16) + lds_ld<f32x4>(a + 6736)) + (lds_ld<f32x4>(a + 13456) + lds_ld<f32x4>(a + 20176));
      st16(cat2(pk4(t0), pk4(t1)), rsWS, tid * 16, WS_KVP + (rb * 32 + 4 * jidx) * 840);
    }
    wait_lgkm0();
    STAMP(15);
    publish_flag(p, WS_FLAGKV + ((jidx >> 1) * 32 + (jidx & 1) * 8 + rb) * FS, tag, tid);
    STAMP(3);
  } else if (jtype == J_Q) {
    f32x4 acc[4][5];
    unsigned boff[5];
#pragma unroll
    for (int t = 0; t < 5; ++t) boff[t] = brow + n * 6144 + ((32 * t + 8 * p4) ^ bx);
    const unsigned ccol = 160 * jidx + (tid < 160 ? tid : 0);
    unsigned tch[3];
    auto warm = [&] {
      const unsigned gq = (rb * 4 + jidx) * 8 + w;
      asm volatile("buffer_load_dword %0, %3, %5, 0 offen\n\tbuffer_load_dword %1, %3, %5, %7 offen\n\tbuffer_load_dword %2, %4, %6, 0 offen"
                   : "=&v"(tch[0]), "=&v"(tch[1]), "=&v"(tch[2])
                   : "v"((gq * 34 + (lane < 34 ? lane : 0)) * 128), "v"((gq * 25 + (lane < 25 ? lane : 0)) * 128), "s"(mk_rsrc(p.wd, F * D * 2)),
                     "s"(mk_rsrc(p.wo, D * D * 2)), "s"(8576u * 128u) : "memory");
    };
    main_loop<5, true, false, false>(warm, rs_, p.ws, acc, boff, d, w, lane, tid, epoch, tag, rb, ccol, rsWd, rsWS, cb, rot, pre2 STAMP_PASS);
    relane();
    asm volatile("" :: "v"(tch[0]), "v"(tch[1]), "v"(tch[2]));
    u32x2 pq[4][5];
    {
      float mm[4], rr[4];
      f32x4 c[5];
#pragma unroll
      for (int i = 0; i < 4; ++i) { mm[i] = lds_ld<float>(L_MISC + (64 * m + 16 * i + r) * 4); rr[i] = lds_ld<float>(L_MISC + 1024 + (64 * m + 16 * i + r) * 4); }
#pragma unroll
      for (int t = 0; t < 5; ++t) c[t] = lds_ld<f32x4>(L_MISC + 2048 + (80 * n + 16 * t + 4 * g) * 4);
      SB;
#pragma unroll
      for (int i = 0; i < 4; ++i)
#pragma unroll
        for (int t = 0; t < 5; ++t) pq[i][t] = pk4(phi4(acc[i][t] + mm[i] * c[t], splat(rr[i] * 1.4426950408889634f), splat(rr[i] * 0.5f)));
#pragma unroll
      for (int i = 0; i < 4; ++i) asm volatile("" : "+v"(pq[i][0]), "+v"(pq[i][1]), "+v"(pq[i][2]), "+v"(pq[i][3]), "+v"(pq[i][4]));
    }
    constexpr unsigned KP = 208, KQ = 84 * KP;
    unsigned wa[2];
#pragma unroll
    for (int k = 0; k < 2; ++k) {
      const unsigned qd = 2 * (tid < 420 ? tid : 0) + k, hd = qd / 105, e = (qd % 105) / 5, dq = qd % 5, hl = hd & 3;
      wa[k] = (hd >> 2) * KQ + (e < 20 ? hl * 20 + e : 80 + hl) * KP + hl * 40 + dq * 8;
    }
    unsigned fk = n * KQ + r * KP + g * 8, fz = n * KQ + (80 + (r & 3)) * KP + g * 8;
    unsigned gm[3] = {g < 1 ? ~0u : 0u, g < 2 ? ~0u : 0u, g < 3 ? ~0u : 0u};
    u32x4 B[4][3];
    f32x4 eps4 = splat(1e-6f);
#pragma unroll
    for (int i = 0; i < 4; ++i) {
      B[i][0] = cat2(pq[i][0], pq[i][1]); B[i][1] = cat2(pq[i][2], pq[i][3]); B[i][2] = cat2(pq[i][4], u32x2{0u, 0u});
      asm volatile("" : "+v"(B[i][0]), "+v"(B[i][1]), "+v"(B[i][2]));
    }
    asm volatile("" : "+v"(wa[0]), "+v"(wa[1]), "+v"(fk), "+v"(fz), "+v"(gm[0]), "+v"(gm[1]), "+v"(gm[2]), "+v"(eps4));
    STAMP(25);
    wait_vm<0>();
    bar();
#pragma unroll
    for (int k = 0; k < 5; ++k) lds_st<u32x4>(k * 8192 + tid * 16, u32x4{0u, 0u, 0u, 0u});
    if (w == 0) poll_flags((gu32*)(p.ws + WS_FLAGKV + (jidx * 32 + (lane & 15)) * FS), true, tag);
    lbar();
    STAMP(4);
    if (w < 7) {
      u32x4 v[8];
#pragma unroll
      for (int j = 0; j < 8; ++j)
        v[j] = __builtin_amdgcn_raw_buffer_load_b128(rsWS, (tid < 420 ? tid : 0) * 16, WS_KVP + (j * 32 + 8 * jidx) * 840, 16);
      SB;
      f32x4 t0 = {0.f, 0.f, 0.f, 0.f}, t1 = t0;
#pragma unroll
      for (int j = 0; j < 8; ++j) {
        t0 += f32x4{bflo(v[j][0]), bfhi(v[j][0]), bflo(v[j][1]), bfhi(v[j][1])};
        t1 += f32x4{bflo(v[j][2]), bfhi(v[j][2]), bflo(v[j][3]), bfhi(v[j][3])};
      }
      lds_st<u32x2>(wa[0], pk4(t0)); lds_st<u32x2>(wa[1], pk4(t1));
    }
    lbar();
    STAMP(16);
    u32x4 A[12];
#pragma unroll
    for (int P = 0; P < 3; ++P) A[P] = cat2(lds_ld<u32x2>(fz + P * 64), lds_ld<u32x2>(fz + P * 64 + 32));
#pragma unroll
    for (int u = 0; u < 5; ++u) {
      const unsigned a0 = fk + u * 16 * KP + (u < 3 ? 0 : 64);
      A[3 + u] = cat2(lds_ld<u32x2>(a0), lds_ld<u32x2>(a0 + 32));
    }
#pragma unroll
    for (int u = 1; u < 5; ++u) {
      const unsigned a0 = fk + u * 16 * KP + (u < 3 ? 64 : 128);
      A[7 + u] = cat2(lds_ld<u32x2>(a0), lds_ld<u32x2>(a0 + 32));
    }
    SB;
    u32x2 sh[4];
    f32x4 den[4], num[2][5];
    static_for<0, 12>([&](auto E) {
      constexpr int P = E.v >> 2, i = E.v & 3;
      if constexpr (P == 0) mfma_c(den[i], A[0], B[i][0], eps4); else mfma_ip(den[i], A[P], B[i][P]);
    });
    u32x2 o[5];
    const unsigned oo = WS_HA + rb * RBSZ + 18 * 49152 + jidx * 81920 + m * 4 * 5120;
    auto fin = [&](auto I, auto K) {
      constexpr int i = I.v, k = K.v, u = k < 8 ? k >> 1 : 4;
      if constexpr (k == 8 || !(k & 1)) {
        asm volatile("" : "+v"(num[i & 1][u]));
        const float rr = u == 0 ? den[i][0] : (u == 4 ? den[i][3] : bself(gm[(u + 2) % 3], den[i][(u + 3) & 3], den[i][u & 3]));
#pragma unroll
        for (int j = 0; j < 4; ++j) num[i & 1][u][j] = smul(num[i & 1][u][j], rr);
      }
      if constexpr (k == 8 || (k & 1)) o[u] = pk4(num[i & 1][u]);
      if constexpr (k == 3) st_frag(rsWS, lane, oo + i * 5120 + n * 2048, o[0], o[1]);
      if constexpr (k == 7) st_frag(rsWS, lane, oo + i * 5120 + n * 2048 + 1024, o[2], o[3]);
      if constexpr (k == 8) { sh[i] = o[4]; asm volatile("" : "+v"(sh[i])); }
    };
    static_for<0, 4>([&](auto I) {
      constexpr int i = I.v;
      static_for<0, 9>([&](auto K) {
        constexpr int k = K.v, u = k < 5 ? k : k - 4;
        if constexpr (k < 5) mfma_z(num[i & 1][u], A[3 + u], B[i][u < 3 ? 0 : 1]); else mfma_ip(num[i & 1][u], A[7 + u], B[i][u < 3 ? 1 : 2]);
        SB;
        if constexpr (i == 0) {
          if constexpr (k >= 5) {
            asm volatile("" : "+v"(den[k - 5]));
#pragma unroll
            for (int j = 0; j < 4; ++j) den[k - 5][j] = __builtin_amdgcn_rcpf(den[k - 5][j]);
            asm volatile("" : "+v"(den[k - 5]));
          }
        } else {
          fin(IC<i - 1>{}, K);
        }
        SB;
      });
    });
    static_for<0, 9>([&](auto K) { fin(IC<3>{}, K); SB; });
    shared_send(w, lane, sh);
    shared_recv(rsWS, w, lane, oo + 4096, 5120, sh);
    STAMP(15);
    publish_flag(p, WS_FLAGO + (rb * 32 + jidx) * FS, tag, tid);
    STAMP(3);
    if (b == 224) {
      poll_flags((gu32*)(p.ws + WS_FLAGH + tid * FS), (tid & 31) < (tid < 256 ? 18u : 4u), tag);
      bar();
      if (tid == 0) __hip_atomic_store((gu32*)(p.ws + WS_EPOCH), tag, RLX_AGENT);
    }
  } else {
    const unsigned gw = spi * 8 + w;
    unsigned k10 = 0x00003f80u, k01 = 0x3f800000u;
    asm volatile("" : "+v"(k10), "+v"(k01));
    auto cs_src = [&](unsigned u, rsrc_t& rs, unsigned& cv, unsigned& st) {
      const unsigned cline = u / 3, third = u % 3;
      const void* cw = cline < 30 ? p.wqkv : (cline < 57 ? p.wg : p.wu);
      const unsigned cpitch = cline < 30 ? 3840u : 3456u;
      const unsigned clocal = cline < 30 ? cline : (cline < 57 ? cline - 30 : cline - 57);
      rs = mk_rsrc(cw, gw < 126 ? D * cpitch : 0);
      cv = (third * 216 + (lane >> 3)) * cpitch + clocal * 128 + (lane & 7) * 16;
      st = 8 * cpitch;
    };
    auto cs_publish = [&](unsigned u, float (&cs)[8]) {
#pragma unroll
      for (int k = 0; k < 4; ++k) { const bool hi = lane & 32; const float o = shfl_x(hi ? cs[k] : cs[k + 4], lane, 32); cs[k] = (hi ? cs[k + 4] : cs[k]) + o; }
#pragma unroll
      for (int k = 0; k < 2; ++k) { const bool hi = lane & 16; const float o = shfl_x(hi ? cs[k] : cs[k + 2], lane, 16); cs[k] = (hi ? cs[k + 2] : cs[k]) + o; }
      { const bool hi = lane & 8; const float o = shfl_x(hi ? cs[0] : cs[1], lane, 8); cs[0] = (hi ? cs[1] : cs[0]) + o; }
      if (gw < 126)
        __hip_atomic_store((gu64*)(p.ws + WS_CSUM) + (u % 3) * NCOL + (u / 3) * 64 + (lane & 7) * 8 + (lane >> 3),
                           ((unsigned long long)tag << 32) | __builtin_bit_cast(unsigned, cs[0]), RLX_AGENT);
    };
    row_load(rs_, rsX, lane);
    asm volatile("s_waitcnt vmcnt(0) lgkmcnt(0)\n\tv_readfirstlane_b32 %0, %1\n\ts_nop 3" : "=s"(tag) : "v"(epoch) : "memory");
    tag = rfl(tag) + 1;
    static_for<0, 14>([&](auto K) { row_step<K.v>(rs_, lane); });
    u32x4 c[27];
    rsrc_t rs0, rs1; unsigned cv0, cv1, st0, st1;
    cs_src(gw, rs0, cv0, st0); cs_src(gw + 126, rs1, cv1, st1);
#pragma unroll
    for (int j = 0; j < 27; ++j) c[j] = __builtin_amdgcn_raw_buffer_load_b128(rs0, cv0 + j * st0, 0, 0);
    lbar();
    row_publish(rs_, p.ws, tag, tid);
    float cs[8] = {};
#pragma unroll
    for (int j = 0; j < 27; ++j) {
#pragma unroll
      for (int k = 0; k < 4; ++k) { cs[2 * k] = dot2(c[j][k], k10, cs[2 * k]); cs[2 * k + 1] = dot2(c[j][k], k01, cs[2 * k + 1]); }
      if (j < 8) c[j] = __builtin_amdgcn_raw_buffer_load_b128(rs1, cv1 + j * st1, 0, 0);
      SB;
    }
    cs_publish(gw, cs);
    SB;
#pragma unroll
    for (int j = 8; j < 27; ++j) c[j] = __builtin_amdgcn_raw_buffer_load_b128(rs1, cv1 + j * st1, 0, 0);
    SB;
    STAMP(11);
#pragma unroll
    for (int k = 0; k < 8; ++k) cs[k] = 0.f;
#pragma unroll
    for (int j = 0; j < 27; ++j)
#pragma unroll
      for (int k = 0; k < 4; ++k) { cs[2 * k] = dot2(c[j][k], k10, cs[2 * k]); cs[2 * k + 1] = dot2(c[j][k], k01, cs[2 * k + 1]); }
    cs_publish(gw + 126, cs);
    STAMP(12);
#pragma unroll 1
    for (unsigned i = w; i < 144; i += 8) dma16(i * 1024, v2, rsWd, urow(i >> 2, rot) + (i & 3) * 10240u + cb * 128u);
    drain_vm();
    bar();
  }
  if (!has2) { STAMP_END; return; }
  relane();

  const unsigned rbA = (G * 6) >> 4, rbB = (G * 6 + 5 > 127 ? 127 : G * 6 + 5) >> 4;
  if (w >= 4) {
    __builtin_amdgcn_s_setprio(HPRIO);
    const unsigned lw = w - 4, ld0 = lw * 1024;
    gu32* fo = (gu32*)(p.ws + WS_FLAGO + ((lane < 32 ? rbA : rbB) * 32 + (lane & 3)) * FS);
    gu32* fh = (gu32*)(p.ws + WS_FLAGH + ((lane < 32 ? rbA : rbB) * 32 + ((lane & 31) < 18 ? lane & 31 : 17)) * FS);
    __builtin_amdgcn_s_sleep(HSLEEP);
    if (jtype == J_KV) {
#pragma unroll
      for (int u = 31; u < 36; ++u) dma16(u * 4096 + ld0, v2, rsWd, w2_off(W, u, lw));
    }
    if (jtype != J_MLP) {
#pragma unroll
      for (int u = 0; u < NPRE; ++u) { pre2[u] = w2_load(W, 36 + u, lw, v2); SB; }
    }
#pragma unroll
    for (int u = 36 + NPRE; u < PSPLIT; ++u) { pre2[u - 36] = w2_load(W, u, lw, v2); SB; }
    if (w == 7) {
      __builtin_amdgcn_s_setprio(3);
      const unsigned v = poll_flags(fh, (lane & 31) < 18 && (lane & 31) >= 10, tag);
      lds_set(L_GO);
      if (!all64((lane & 31) >= 18 || v == tag)) poll_flags(fh, (lane & 31) < 18, tag);
      lds_set(L_GO + 4);
      __builtin_amdgcn_s_setprio(HPRIO);
    }
#pragma unroll
    for (int u = PSPLIT; u < HSPLIT; ++u) { pre2[u - 36] = w2_load(W, u, lw, v2); SB; }
    STAMP(8);
    unsigned la[3], wb[3], xo[3], st[3];
    u32x4 xr[3];
    const unsigned ch = lane & 7;
#pragma unroll
    for (int i = 0; i < 3; ++i) {
      const unsigned row = lw * 24 + i * 8 + (lane >> 3), rl = row >> 4, rr = row & 15;
      la[i] = (rl >= 3 ? 24576u + (rl - 3) * 4096u : rl * 4096u) + (ch >> 1) * 1024 + (ch & 1) * 512 + ((rr + 2 * ch) & 15) * 16;
      xo[i] = (G * 96 + row) * 1280 + cb * 128 + ch * 16;
      wb[i] = ld0 + lane * 16 + i * 49152;
      asm volatile("" : "+v"(la[i]), "+v"(wb[i]), "+v"(xo[i]));
    }
    float ri[3], rl[3];
    unsigned of = 0;
    f32x4 xf[3][2], dd[3][2], d2[3][2];
#pragma unroll
    for (int k = 2; k <= 6; ++k) {
      if (k == O_BAR && w == 7 && !all64((lane & 31) >= 4 || of == tag)) poll_flags(fo, (lane & 31) < 4, tag);
      if (k == 6) {
        wait_vm<0>();
#pragma unroll
        for (int h = 0; h < 3; ++h) {
          ri[h] = 2.0f * bfhi(st[h]) * bfhi(st[h]);
          rl[h] = -1.4426950408889634f * __builtin_amdgcn_rcpf(ri[h]);
          xf[h][0] = f32x4{bflo(xr[h][0]), bfhi(xr[h][0]), bflo(xr[h][1]), bfhi(xr[h][1])};
          xf[h][1] = f32x4{bflo(xr[h][2]), bfhi(xr[h][2]), bflo(xr[h][3]), bfhi(xr[h][3])};
          asm volatile("" : "+v"(ri[h]), "+v"(rl[h]), "+v"(xf[h][0]), "+v"(xf[h][1]));
        }
      }
      lbar();
      if (k == O_BAR - 1) { of = __hip_atomic_load(fo, RLX_AGENT); SB; }
      if (k < 6) {
        const int kk = k - 1;
#pragma unroll
        for (int u = 9 * (kk - 1) + ((kk - 1) & 1); u < 9 * kk + (kk & 1); ++u) {
          lds_st<u32x4>(wb[u / 12] + (u % 12) * 4096, pre2[u]);
          if (WSLEEP == 1) __builtin_amdgcn_s_sleep(1);
          if (WSLEEP == 2) asm volatile("s_nop 7");
          SB;
        }
      }
      if (k == 2 || k == 3) {
        constexpr int mid = (HSPLIT + 74) / 2 < 72 ? (HSPLIT + 74) / 2 : 72;
#pragma unroll
        for (int u = (k == 2 ? HSPLIT : mid); u < (k == 2 ? mid : 72); ++u) { pre2[u - 36] = w2_load(W, u, lw, v2); SB; }
        if (k == 3) { dma16(36 * 4096 + ld0, v2, W.wo, w2_off(W, 72, lw)); dma16(37 * 4096 + ld0, v2, W.wo, w2_off(W, 73, lw)); }
      }
      if (k == XK) {
#pragma unroll
        for (int h = 0; h < 3; ++h) {
          const unsigned row = G * 96 + lw * 24 + h * 8 + (lane >> 3);
          st[h] = __builtin_amdgcn_raw_buffer_load_b32(rsWS, WS_STAT + (row < S ? row : 0) * 8, 0, 16);
          xr[h] = __builtin_amdgcn_raw_buffer_load_b128(rsX, xo[h], 0, 0);
        }
      }
    }
    bar();
#pragma unroll
    for (int i = 0; i < 3; ++i) {
      dd[i][0] = lds_ld<f32x4>(la[i]); d2[i][0] = lds_ld<f32x4>(la[i] + 12288);
      dd[i][1] = lds_ld<f32x4>(la[i] + 256); d2[i][1] = lds_ld<f32x4>(la[i] + 12288 + 256);
      la[i] += 49152;
      asm volatile("" : "+v"(la[i]));
    }
    __builtin_amdgcn_s_setprio(GPRIO);
#pragma unroll
    for (int i = 0; i < 3; ++i) {
      float v[8], e[8];
#pragma unroll
      for (int j = 0; j < 8; ++j) v[j] = sadd(dd[i][j >> 2][j & 3], d2[i][j >> 2][j & 3]);
      SB;
#pragma unroll
      for (int j = 0; j < 8; ++j) e[j] = smul(v[j], rl[i]);
      SB;
#pragma unroll
      for (int j = 0; j < 8; ++j) e[j] = __builtin_amdgcn_exp2f(e[j]);
      SB;
#pragma unroll
      for (int j = 0; j < 8; ++j) e[j] = sfma(e[j], ri[i], ri[i]);
      SB;
#pragma unroll
      for (int j = 0; j < 8; ++j) e[j] = __builtin_amdgcn_rcpf(e[j]);
      SB;
#pragma unroll
      for (int j = 0; j < 8; ++j) dd[i][j >> 2][j & 3] = smul(v[j], e[j]);
      asm volatile("" : "+v"(dd[i][0]), "+v"(dd[i][1]));
      SB;
    }
    const rsrc_t rsO = mk_rsrc(p.out, S * D * 2);
    STAMP(9);
    bar();
    f32x4 ss[3][2];
#pragma unroll
    for (int i = 0; i < 3; ++i) {
      ss[i][0] = lds_ld<f32x4>(la[i]) + lds_ld<f32x4>(la[i] + 12288);
      ss[i][1] = lds_ld<f32x4>(la[i] + 256) + lds_ld<f32x4>(la[i] + 12288 + 256);
    }
#pragma unroll
    for (int i = 0; i < 3; ++i)
      __builtin_amdgcn_raw_buffer_store_b128(cat2(pk4(xf[i][0] + ss[i][0] * dd[i][0]), pk4(xf[i][1] + ss[i][1] * dd[i][1])), rsO, xo[i], 0, 0);
    STAMP(5);
    STAMP_END;
    return;
  }
  {
    const unsigned kh = w & 1, rt = G * 6 + 3 * (w >> 1);
    const rsrc_t rsH = mk_rsrc(p.ws + WS_HA, 8 * RBSZ);
    unsigned tb[3][4];
#pragma unroll
    for (int t = 0; t < 4; ++t) {
      tb[0][t] = (4 * g + q4) * 128 + ((t ^ ((2 * g + (q4 >> 1)) & 3)) << 5) + 8 * p4 + kh * 4096;
      tb[1][t] = tb[0][t] + 49152; tb[2][t] = tb[0][t] + 98304;
      asm volatile("" : "+v"(tb[0][t]), "+v"(tb[1][t]), "+v"(tb[2][t]));
    }
    auto rd1 = [&](int i, int t, int hf) {
      const int sl = i == 36 ? 36 : (2 * i) % 36, bi = sl / 12 > 2 ? 2 : sl / 12;
      return __builtin_bit_cast(u32x2, lds_tr(tb[bi][t] + (sl - 12 * bi) * 4096 + hf * 2048));
    };
    constexpr int NW = P2NW, W0 = P2W0;
    constexpr int OI = O_BAR == 4 ? 17 : (O_BAR == 5 ? 22 : 26);
    constexpr int OB0 = OI == 26 ? NW - 1 : 0;
    f32x4 acc[2][3][4];
    u32x4 a[NW][3];
    u32x2 bw[2][4][2];
    unsigned va[2][3];
#pragma unroll
    for (int h = 0; h < 3; ++h) {
      va[0][h] = lane * 16 + ((rt + h) >> 4) * RBSZ + ((rt + h) & 15) * 3072;
      va[1][h] = va[0][h] + ((rt + h) & 15) * 2048;
      asm volatile("" : "+v"(va[0][h]), "+v"(va[1][h]));
    }
    unsigned xa[4];
#pragma unroll
    for (int t = 0; t < 4; ++t) {
      xa[t] = w * 12288u + t * 1024 + g * 256 + ((r + 4 * t + (g & 2)) & 15) * 16;
      asm volatile("" : "+v"(xa[t]));
    }
    auto put = [&](auto Z, auto E) {
      constexpr int z = Z.v, t = E.v / 3, h = 2 - E.v % 3;
      asm volatile("" : "+v"(acc[z][h][t]));
      lds_st<f32x4>(xa[t] + z * 49152 + h * 4096, acc[z][h][t]);
    };
    auto lda = [&](auto I, auto H) {
      constexpr int i = I.v, h = H.v;
      constexpr unsigned s0 = i < 27 ? (2 * i + rot) % 54 : 2 * i, s1 = i < 27 ? (2 * i + 1 + rot) % 54 : 2 * i + 1;
      a[i % NW][h] = __builtin_amdgcn_raw_buffer_load_b128(rsH, va[i >= 27][h], step_off(s0) + kh * (step_off(s1) - step_off(s0)), 16);
      SB;
    };
    lds_wait(L_GO);
    unsigned go2 = 0;
    STAMP(8);
    static_for<0, W0>([&](auto I) { lda(I, IC<0>{}); lda(I, IC<1>{}); lda(I, IC<2>{}); });
#pragma unroll
    for (int t = 0; t < 4; ++t) { bw[0][t][0] = rd1(0, t, 0); bw[0][t][1] = rd1(0, t, 1); }
    static_for<0, 37>([&](auto I) {
      constexpr int i = I.v;
      constexpr int lim = i + NW - 1;
      constexpr int nxt = i <= OI ? (W0 + 2 * i < lim ? W0 + 2 * i : lim) : (27 + OB0 + 2 * (i - OI - 1) < lim ? 27 + OB0 + 2 * (i - OI - 1) : lim);
      constexpr int end = i <= OI ? 27 : 37;
      constexpr int l0 = nxt < end ? nxt : -1, l1 = (nxt + 1 <= lim && nxt + 1 < end) ? nxt + 1 : -1;
      SB;
      static_for<0, 12>([&](auto E) {
        constexpr int e = E.v, t = e / 3, h = e % 3;
        const u32x4 bf = cat2(bw[i & 1][t][0], bw[i & 1][t][1]);
        if constexpr (i == 0 || i == 27) mfma_z(acc[i >= 27][2 - h][t], bf, a[i % NW][2 - h]); else mfma_ip(acc[i >= 27][2 - h][t], bf, a[i % NW][2 - h]);
        SB;
        if constexpr (i == 4 && e == 2) go2 = lds_get(L_GO + 4);
        if constexpr ((i == 27 || i == 28) && (e & 1)) put(IC<0>{}, IC<(i - 27) * 6 + e / 2>{});
        if constexpr (i == 36 && e >= 8) put(IC<1>{}, IC<e - 8>{});
        if constexpr (i < 36 && h < 2) bw[(i + 1) & 1][t][h] = rd1(i + 1, t, h);
        if constexpr (l1 < 0) { if constexpr (h == 2 && t < 3 && l0 >= 0) lda(IC<l0>{}, IC<t>{}); }
        else if constexpr (e & 1) lda(IC<(e < 6 ? l0 : l1)>{}, IC<(e >> 1) % 3>{});
        SB;
      });
      if constexpr (i == 29) { asm volatile("s_waitcnt lgkmcnt(8)" ::: "memory"); bar(); }
      if constexpr (i == 4) { if (__builtin_expect(!rfl(go2), 0)) lds_wait(L_GO + 4); asm volatile("" ::: "memory"); STAMP(17); }
      if constexpr (i > 4 && i < 27 && (i % 9 == 4 || i % 9 == 8)) {
        bar();
        if constexpr (i == 26 && OI == 26) static_for<27, 27 + OB0>([&](auto J) { lda(J, IC<0>{}); lda(J, IC<1>{}); lda(J, IC<2>{}); });
        STAMP(17 + (2 * i) / 9);
      }
    });
    STAMP(24);
    static_for<4, 12>([&](auto E) { if constexpr (E.v == 8) asm volatile("s_nop 3" ::: "memory"); put(IC<1>{}, E); SB; });
    lbar();
    STAMP(5);
    STAMP_END;
  }
}

extern "C" void kernel_launch(void* const* d_in, const int* in_sizes, int n_in, void* d_out, int out_size, void* d_ws,
                              size_t ws_size, hipStream_t stream) {
  Params p;
  p.x = d_in[0]; p.wqkv = d_in[1]; p.wo = d_in[2]; p.wg = d_in[4]; p.wu = d_in[5]; p.wd = d_in[6];
  p.out = d_out; p.ws = (char*)d_ws + ((ws_size / 2) & ~(size_t)0x1fffff);
  fused_block<<<256, 512, 0, stream>>>(p);
}
```

```cpp
#include <hip/hip_runtime.h>
#include <stdint.h>

typedef __attribute__((ext_vector_type(8))) __bf16 bf16x8;
typedef __attribute__((ext_vector_type(4))) __bf16 bf16x4;
typedef __attribute__((ext_vector_type(2))) __bf16 bf16x2;
typedef __attribute__((ext_vector_type(4))) float f32x4;
typedef __attribute__((ext_vector_type(2))) float f32x2;
typedef __attribute__((ext_vector_type(4))) unsigned u32x4;
typedef __attribute__((ext_vector_type(2))) unsigned u32x2;
typedef __amdgpu_buffer_rsrc_t rsrc_t;
#define LAS __attribute__((address_space(3)))
#define GAS __attribute__((address_space(1)))
#define DI __device__ __forceinline__
#define RLX_AGENT __ATOMIC_RELAXED, __HIP_MEMORY_SCOPE_AGENT
typedef GAS unsigned gu32;
typedef GAS unsigned long long gu64;

constexpr int S = 2048, D = 640, F = 1728;
constexpr int NCOL = 5376;
constexpr int NSTEP = 74;
constexpr unsigned WS_EPOCH = 0;
#define FS 128
constexpr unsigned WS_FLAGH = 768u << 10;
constexpr unsigned WS_FLAGO = WS_FLAGH + 8 * 32 * FS;
constexpr unsigned WS_FLAGKV = WS_FLAGO + 8 * 32 * FS;
constexpr unsigned WS_STAT = 4096;
constexpr unsigned WS_CSUM = WS_STAT + S * 8;
constexpr unsigned WS_KVP = WS_CSUM + 3 * NCOL * 8;
constexpr unsigned WS_HA = 1u << 20;
constexpr unsigned RBSZ = 16 * NSTEP * 1024;
constexpr unsigned step_off(unsigned st) { return st < 54 ? (st / 3) * 49152 + (st % 3) * 1024 : 18 * 49152 + ((st - 54) / 5) * 81920 + ((st - 54) % 5) * 1024; }
constexpr unsigned WS_DBG = 12u << 20;
constexpr unsigned WS_END = 13u << 20;
constexpr unsigned L_B = 65536;
constexpr unsigned L_SCR = 147456;
constexpr unsigned L_MISC = 159744;
constexpr unsigned L_GO = L_MISC + 3072;
constexpr unsigned SPIN_CAP = 1u << 18;
#define HSLEEP 4
#define HSPLIT 64
#define PPIPE 0
#define STA 0
#define STB 2
#define SNAKE 0
#define EARLY1 1
#define ROTW 1
#define NPRE 12
#define WSCH 0
#define PSPLIT 48
#define WARM 1
#define XK 2
#define GRP 1
#define GPRIO 0
#define HPRIO 0
#define PSLEEP 2
#define P2NW 7
#define P2W0 6
#define WSLEEP 0
#define ROTM 18
#define NA1 2
#define O_BAR 5
#define PRIO_MODE 0
#define DMA_SPACE_A T
#define DMA_SPACE_B (4 * T / 3)

struct Params {
  const void* x; const void* wqkv; const void* wo; const void* wg; const void* wu; const void* wd;
  void* out; char* ws;
  unsigned long long wsal;
};

__shared__ __attribute__((aligned(16))) char smem[163840];

DI float sadd(float a, float b) { float o; asm("v_add_f32 %0, %1, %2" : "=v"(o) : "v"(a), "v"(b)); return o; }
DI float smul(float a, float b) { float o; asm("v_mul_f32 %0, %1, %2" : "=v"(o) : "v"(a), "v"(b)); return o; }
DI float sfma(float a, float b, float c) { float o; asm("v_fma_f32 %0, %1, %2, %3" : "=v"(o) : "v"(a), "v"(b), "v"(c)); return o; }
DI unsigned bsel(unsigned m, unsigned a, unsigned b) { unsigned o; asm("v_bfi_b32 %0, %1, %2, %3" : "=v"(o) : "v"(m), "v"(a), "v"(b)); return o; }
DI float bself(unsigned m, float a, float b) { return __builtin_bit_cast(float, bsel(m, __builtin_bit_cast(unsigned, a), __builtin_bit_cast(unsigned, b))); }
DI bool all64(bool c) { return __builtin_amdgcn_ballot_w64(!c) == 0; }
DI float shfl_x(float v, unsigned lane, unsigned m) {
  return __builtin_bit_cast(float, __builtin_amdgcn_ds_bpermute((int)((lane ^ m) << 2), __builtin_bit_cast(int, v)));
}
DI unsigned umin(unsigned a, unsigned b) { return a < b ? a : b; }
DI unsigned rfl(unsigned v) { return __builtin_amdgcn_readfirstlane(v); }
DI rsrc_t mk_rsrc(const void* p, unsigned bytes) { return __builtin_amdgcn_make_buffer_rsrc((void*)p, (short)0, (int)bytes, 0x00020000); }
DI void dma16(unsigned ldsdst, unsigned voff, rsrc_t rs, unsigned soff) {
  asm volatile("s_mov_b32 m0, %0\n\ts_nop 0\n\tbuffer_load_dwordx4 %1, %2, %3 offen lds" ::"s"(ldsdst), "v"(voff), "s"(rs), "s"(soff) : "memory");
}
template <int N> DI void wait_vm() { asm volatile("s_waitcnt vmcnt(%0)" ::"i"(N) : "memory"); }
DI void drain_vm() { wait_vm<0>(); __builtin_amdgcn_s_waitcnt(0x0F70); }
DI void wait_lgkm0() { asm volatile("s_waitcnt lgkmcnt(0)" ::: "memory"); }
DI void bar() { asm volatile("" ::: "memory"); __builtin_amdgcn_s_barrier(); asm volatile("" ::: "memory"); }
DI void lbar() { wait_lgkm0(); bar(); }
template <class V> DI V lds_ld(unsigned a) { return *(LAS V*)((LAS char*)smem + a); }
template <class V> DI void lds_st(unsigned a, V v) { *(LAS V*)((LAS char*)smem + a) = v; }
DI bf16x4 lds_tr(unsigned a) { return __builtin_amdgcn_ds_read_tr16_b64_v4bf16((LAS bf16x4*)((LAS char*)smem + a)); }
DI bf16x8 cat(bf16x4 a, bf16x4 b) { return __builtin_shufflevector(a, b, 0, 1, 2, 3, 4, 5, 6, 7); }
DI f32x4 mfma(bf16x8 a, bf16x8 b, f32x4 c) { return __builtin_amdgcn_mfma_f32_16x16x32_bf16(a, b, c, 0, 0, 0); }
DI unsigned pk2(float a, float b) { f32x2 v = {a, b}; return __builtin_bit_cast(unsigned, __builtin_convertvector(v, bf16x2)); }
DI u32x2 pk4(f32x4 v) { u32x2 r = {pk2(v[0], v[1]), pk2(v[2], v[3])}; return r; }
DI bf16x8 frag(u32x2 lo, u32x2 hi) { u32x4 v = {lo[0], lo[1], hi[0], hi[1]}; return __builtin_bit_cast(bf16x8, v); }
DI float bflo(unsigned v) { return __builtin_bit_cast(float, v << 16); }
DI float bfhi(unsigned v) { return __builtin_bit_cast(float, v & 0xffff0000u); }
DI float rbf(float v) { return bflo(pk2(v, 0.f)); }
DI float relu2(float v) { float o; asm("v_add_f32 %0, %1, |%1|" : "=v"(o) : "v"(v)); return o; }
DI f32x4 phi4(f32x4 u, f32x4 rl, f32x4 rh) {
  const f32x4 l = u * rl;
  f32x4 e, a;
#pragma unroll
  for (int j = 0; j < 4; ++j) {
    e[j] = __builtin_amdgcn_fmed3f(__builtin_amdgcn_exp2f(l[j]), 0.f, 1.f);
    a[j] = relu2(u[j]);
  }
  return a * rh + e + 1e-6f;
}
DI f32x4 splat(float v) { return f32x4{v, v, v, v}; }
template <int CTRL> DI float dpp_add(float v) {
  return v + __builtin_bit_cast(float, __builtin_amdgcn_update_dpp(0, __builtin_bit_cast(int, v), CTRL, 0xf, 0xf, true));
}
DI float dot2(unsigned a, unsigned b, float c) {
  return __builtin_amdgcn_fdot2_f32_bf16(__builtin_bit_cast(bf16x2, a), __builtin_bit_cast(bf16x2, b), c, false);
}
DI u32x4 ldtr2(unsigned a, unsigned d) { return __builtin_bit_cast(u32x4, cat(lds_tr(a), lds_tr(a + d))); }
DI void mfma_ip(f32x4& c, const u32x4& a, const u32x4& b) { asm volatile("v_mfma_f32_16x16x32_bf16 %0, %1, %2, %0" : "+v"(c) : "v"(a), "v"(b)); }
DI void mfma_c(f32x4& d, const u32x4& a, const u32x4& b, const f32x4& c) { asm volatile("v_mfma_f32_16x16x32_bf16 %0, %1, %2, %3" : "=&v"(d) : "v"(a), "v"(b), "v"(c)); }
DI void mfma_z(f32x4& c, const u32x4& a, const u32x4& b) { asm volatile("v_mfma_f32_16x16x32_bf16 %0, %1, %2, 0" : "=&v"(c) : "v"(a), "v"(b)); }
template <int N> DI void acc_fence(f32x4* c) {
  asm volatile("s_nop 7" ::: "memory");
#pragma unroll
  for (int i = 0; i < N; ++i) asm volatile("" : "+v"(c[i]));
}
DI u32x4 cat2(u32x2 a, u32x2 b) { return u32x4{a[0], a[1], b[0], b[1]}; }
DI u32x2 ldtr1(unsigned a) { return __builtin_bit_cast(u32x2, lds_tr(a)); }
template <int IMM> DI void set_m0i(unsigned v) { asm volatile("s_add_i32 m0, %0, %1" ::"s"(v), "i"(IMM) : "memory", "scc"); }
template <int OFF> DI void dma_goi(unsigned voff, rsrc_t rs, unsigned soff) {
  asm volatile("buffer_load_dwordx4 %0, %1, %2 offen offset:%3 lds" ::"v"(voff), "s"(rs), "s"(soff), "i"(OFF) : "memory");
}
DI void set_m0(unsigned v) { asm volatile("s_mov_b32 m0, %0" ::"s"(v) : "memory"); }
DI void dma_go(unsigned voff, rsrc_t rs, unsigned soff) {
  asm volatile("buffer_load_dwordx4 %0, %1, %2 offen lds" ::"v"(voff), "s"(rs), "s"(soff) : "memory");
}
DI f32x4 mfma_u(u32x4 a, u32x4 b, f32x4 c) { return mfma(__builtin_bit_cast(bf16x8, a), __builtin_bit_cast(bf16x8, b), c); }
#define STAMP_DECL
#define STAMP_ARG
#define STAMP_PASS
#define STAMPX(c, i)
#define STAMP_END
#define STAMP(i) STAMPX(__builtin_amdgcn_workitem_id_x() == 0, i)

DI unsigned poll_flags(gu32* word, bool active, unsigned tag) {
  unsigned v = 0;
#pragma unroll 1
  for (unsigned spins = 0; spins < SPIN_CAP; ++spins) {
    v = __hip_atomic_load(word, RLX_AGENT);
    if (all64(!active || v == tag)) break;
    if (PSLEEP) __builtin_amdgcn_s_sleep(PSLEEP);
  }
  asm volatile("" ::: "memory");
  return v;
}

DI void lds_set(unsigned a) { __hip_atomic_store((LAS unsigned*)smem + a / 4, 1u, __ATOMIC_RELAXED, __HIP_MEMORY_SCOPE_WORKGROUP); }
DI unsigned lds_get(unsigned a) { return __hip_atomic_load((LAS unsigned*)smem + a / 4, __ATOMIC_RELAXED, __HIP_MEMORY_SCOPE_WORKGROUP); }
DI void lds_wait(unsigned a) {
#pragma unroll 1
  for (unsigned spins = 0; spins < (SPIN_CAP << 4); ++spins) {
    if (rfl(lds_get(a))) break;
    __builtin_amdgcn_s_sleep(1);
  }
  asm volatile("" ::: "memory");
}

template <int I> struct IC { static constexpr int v = I; };
template <int B, int E, class Fn> DI void static_for(Fn&& f) {
  if constexpr (B < E) { f(IC<B>{}); static_for<B + 1, E>(f); }
}

enum { J_MLP = 0, J_KV = 1, J_Q = 2, J_SPARE = 3 };

struct Dma {
  rsrc_t rsA, rsB;
  unsigned vA0, vA1, vB0, vB1, vB2;
  unsigned sA[4], sB, advB;
  unsigned wa, wq;
  unsigned mA, mB, mW;
};

constexpr unsigned a_slot(unsigned s3) { return s3 == 0 ? 126976u : (s3 - 1) * 32768u; }
#define SB __builtin_amdgcn_sched_barrier(0)

template <int T> DI unsigned bofs(const unsigned (&boff)[T], int t) { return T == 6 && t >= 3 ? boff[t - 3] + 6144 : boff[t]; }
template <int T, bool TR, int KS, class Extra>
DI void kstep(f32x4 (&acc)[4][T], const u32x4 (&ac)[4], const u32x2 (&bc)[T][2], u32x4 (&an)[4], u32x2 (&bn)[T][2],
              const unsigned (&aoff)[4], const unsigned (&boff)[T], const Dma& d, Extra&& extra) {
  constexpr int s = KS >> 1, KIND = KS & 1, NP = KIND == 0 ? 4 : 3, ST = KIND == 0 ? (STA ? STA : (4 * T - 1) / NP) : (STB ? STB : (4 * T - 1) / NP);
  constexpr int sn = (KS + 1) >> 1;
  constexpr unsigned asl = a_slot(sn % 3), bsl = ((KS + 1) % 5) * 12288u;
  static_for<0, 4 * T>([&](auto E) {
    constexpr int e = E.v, t = e >> 2, i = (SNAKE && (t & 1)) ? 3 - (e & 3) : (e & 3);
    const u32x4 bf = cat2(bc[t][0], bc[t][1]);
    if constexpr (KS == 0) { if (TR) mfma_z(acc[i][t], bf, ac[i]); else mfma_z(acc[i][t], ac[i], bf); }
    else { if (TR) mfma_ip(acc[i][t], bf, ac[i]); else mfma_ip(acc[i][t], ac[i], bf); }
    SB;
    if constexpr (KS < 19) {
      if constexpr (e < 4) an[e] = lds_ld<u32x4>(aoff[((KS + 1) & 1) + (sn % 3 == 0 ? 2 : 0)] + (sn % 3 == 0 ? 0u : asl) + e * 2048);
      else if constexpr (e < 4 + 2 * T) bn[(e - 4) >> 1][e & 1] = ldtr1(bofs<T>(boff, (e - 4) >> 1) + bsl + (e & 1) * 768);
    }
    constexpr int pc = e / ST;
    if constexpr (KIND == 0) {
      constexpr int adv = s < 8 ? (s + 2) * 128 : 0, dst = (int)a_slot((s + 2) % 3) + pc * 1024, off = dst >= adv ? adv : 0;
      if constexpr (pc < NP && e % ST == ST - 2) set_m0i<dst - off>(d.mA);
      if constexpr (pc < NP && e % ST == ST - 1) dma_goi<off>((pc & 1) ? d.vA1 : d.vA0, d.rsA, d.sA[pc] + (adv - off));
    } else {
      constexpr int h0 = (2 * s) % 5;
      if constexpr (pc < NP && e % ST == ST - 2) set_m0i<(h0 == 4 ? 0 : h0 * 12288) + pc * 1024>(h0 == 4 ? d.mW : d.mB);
      if constexpr (pc < NP && e % ST == ST - 1) dma_goi<0>(pc == 0 ? d.vB0 : (pc == 1 ? d.vB1 : d.vB2), d.rsB, d.sB);
    }
    extra(E);
    SB;
  });
}

struct RowStat { u32x4 xa; unsigned xb, row; float s, q; };
DI void row_load(RowStat& R, rsrc_t rsX, unsigned lane) {
  asm volatile("buffer_load_dwordx4 %0, %2, %4, %5 offen\n\tbuffer_load_dword %1, %3, %4, %5 offen offset:1024"
               : "=&v"(R.xa), "=&v"(R.xb) : "v"(lane * 16), "v"(lane * 4), "s"(rsX), "s"(R.row * 1280) : "memory");
}
template <int K> DI void row_step(RowStat& R, unsigned lane) {
  if constexpr (K < 5) {
    if constexpr (K == 0) { R.s = 0.f; R.q = 0.f; }
    const unsigned v = K < 4 ? R.xa[K & 3] : R.xb;
    asm volatile("v_dot2c_f32_bf16 %0, %2, %3\n\tv_dot2c_f32_bf16 %1, %2, %2" : "+v"(R.s), "+v"(R.q) : "v"(v), "v"(0x3f803f80u));
  } else if constexpr (K < 11) {
    constexpr int C = K == 5 ? 0xB1 : (K == 6 ? 0x4E : (K == 7 ? 0x141 : (K == 8 ? 0x140 : (K == 9 ? 0x142 : 0x143))));
    if constexpr (K == 5) asm volatile("s_nop 2" : "+v"(R.s), "+v"(R.q));
    R.s = dpp_add<C>(R.s); R.q = dpp_add<C>(R.q);
  } else if constexpr (K == 11) {
    R.s *= 1.0f / D;
    R.q = __builtin_fmaxf(R.q * (1.0f / D) - R.s * R.s, 0.f);
  } else if constexpr (K == 12) {
    R.q = __builtin_amdgcn_sqrtf(rbf(rbf(R.q) + 1e-5f));
  } else {
    lds_st<unsigned>(lane == 63 ? L_GO + 32 + (R.row & 7) * 4 : L_GO + 64, pk2(R.s, R.q));
  }
  if constexpr (K >= 5 && K < 13) asm volatile("" : "+v"(R.s), "+v"(R.q));
}
DI void row_publish(const RowStat& R, char* ws, unsigned tag, unsigned tid) {
  if (tid < 8) __hip_atomic_store((gu64*)(ws + WS_STAT) + R.row + tid, ((unsigned long long)tag << 32) | lds_ld<unsigned>(L_GO + 32 + tid * 4), RLX_AGENT);
}

struct Gran { u32x2 g0, g1, g2; unsigned off, step; };
DI void gran_issue(Gran& G, rsrc_t rs) {
  asm volatile("buffer_load_dwordx2 %0, %3, %4, 0 offen sc1\n\tbuffer_load_dwordx2 %1, %3, %4, %5 offen sc1\n\t"
               "buffer_load_dwordx2 %2, %3, %4, %6 offen sc1"
               : "=&v"(G.g0), "=&v"(G.g1), "=&v"(G.g2) : "v"(G.off), "s"(rs), "s"(G.step), "s"(2 * G.step) : "memory");
}
DI void gran_load(Gran& G, rsrc_t rs) {
  asm volatile("buffer_load_dwordx2 %0, %3, %4, 0 offen sc1\n\tbuffer_load_dwordx2 %1, %3, %4, %5 offen sc1\n\t"
               "buffer_load_dwordx2 %2, %3, %4, %6 offen sc1\n\ts_waitcnt vmcnt(0)"
               : "=&v"(G.g0), "=&v"(G.g1), "=&v"(G.g2) : "v"(G.off), "s"(rs), "s"(G.step), "s"(2 * G.step) : "memory");
}

DI unsigned urow(unsigned pos, unsigned rot) { const unsigned u = pos + rot; return (u >= 54 ? u - 54 : u) * 40960u; }

constexpr unsigned wo_tile(unsigned v) { return (v / 5) * 10 + (v % 5 == 4 ? 4 : (v % 5 < 2 ? 2 * (v % 5) : 2 * (v % 5) + 1)); }
struct W2 { rsrc_t wd, wo; unsigned rot, col; };
DI unsigned w2_off(const W2& W, int u, unsigned lw) {
  if (u < 54) return urow(u, W.rot) + lw * 10240u + W.col;
  return wo_tile(u - 54) * 20480u + (lw >> 1) * ((u - 54) % 5 == 4 ? 102400u : 20480u) + (lw & 1) * 10240u + W.col;
}
DI u32x4 w2_load(const W2& W, int u, unsigned lw, unsigned voff) {
  return __builtin_amdgcn_raw_buffer_load_b128(u < 54 ? W.wd : W.wo, voff, w2_off(W, u, lw), 0);
}

template <int T, bool TR, bool PF, bool LASTA, class Pre>
DI void main_loop(Pre&& pre, RowStat& R, char* ws, f32x4 (&acc)[4][T], unsigned (&boff)[T], Dma& d, unsigned w, unsigned lane, unsigned tid,
                  unsigned epoch, unsigned& tag, unsigned rb, unsigned ccol, rsrc_t rsB2, rsrc_t rsWS, unsigned cb, unsigned rot, u32x4 (&pre2)[36] STAMP_ARG) {
  const unsigned g = lane >> 4, r = lane & 15, m = w & 3;
  unsigned aoff[4];
  aoff[0] = (64 * m + r) * 128 + ((g ^ (r >> 1)) << 4);
  aoff[1] = aoff[0] ^ 64;
  aoff[2] = aoff[0] + a_slot(0); aoff[3] = aoff[1] + a_slot(0);
  const unsigned wq = d.wq, wa = d.wa;
  asm volatile("" : "+v"(aoff[0]), "+v"(aoff[1]), "+v"(aoff[2]), "+v"(aoff[3]), "+v"(boff[0]), "+v"(boff[1]), "+v"(boff[2]));
  if (T == 5) asm volatile("" : "+v"(boff[3]), "+v"(boff[4]));
  Gran G;
  unsigned v2, vb2, vpf, tA[3], tB[3];
  row_load(R, d.rsA, lane);
  {
    asm volatile("s_waitcnt vmcnt(%2) lgkmcnt(0)\n\tv_readfirstlane_b32 %0, %1\n\ts_nop 3" : "=s"(tag) : "v"(epoch), "i"(5 + NA1) : "memory");
    tag = rfl(tag) + 1;
    bar();
  }
  STAMP(1);
  u32x4 a0[4], a1[4];
  u32x2 b0[T][2], b1[T][2];
#pragma unroll
  for (int i = 0; i < 4; ++i) a0[i] = lds_ld<u32x4>(aoff[2] + i * 2048);
#pragma unroll
  for (int t = 0; t < T; ++t) { b0[t][0] = ldtr1(bofs<T>(boff, t)); b0[t][1] = ldtr1(bofs<T>(boff, t) + 768); }
  SB;
#pragma unroll
  for (int i = NA1; i < 4; ++i) dma16(a_slot(1) + d.mA + i * 1024, (i & 1) ? d.vA1 : d.vA0, d.rsA, d.sA[i] + 128);
  const bool y = w >= 4;
  bool ok = true;
  auto ln_store = [&] {
    const float v0 = y ? -bflo(G.g0[0]) : __builtin_bit_cast(float, G.g0[0]) + __builtin_bit_cast(float, G.g1[0]) + __builtin_bit_cast(float, G.g2[0]);
    const unsigned la = L_MISC + (y ? tid * 4 - 1024 : 2048 + tid * 4);
    lds_st<float>(la, v0);
    lds_st<float>(y ? la + 1024 : L_GO + 64 + (lane & 7) * 4, __builtin_amdgcn_rcpf(bfhi(G.g0[0])));
  };
  static_for<0, 10>([&](auto S_) {
    constexpr int s = S_.v, L = 4 * T;
    constexpr int NX = (PF && s >= 1 && 2 * s <= NPRE) ? 2 : 0;
    kstep<T, TR, 2 * s>(acc, a0, b0, a1, b1, aoff, boff, d, [&](auto E) {
      constexpr int e = E.v;
      if constexpr (s == 2) {
        if constexpr (e == 2) { vb2 = v2 + (3 * wq) * 10240u; asm volatile("" : "+v"(vb2)); }
        if constexpr (e == 5) { G.off = w < 4 ? WS_CSUM + ccol * 8 : WS_STAT + (rb * 256 + tid - 256) * 8; asm volatile("" : "+v"(G.off)); }
        if constexpr (e == 8) { G.step = w < 4 ? NCOL * 8 : 0; tA[0] = urow(wa, rot) + cb * 128u; asm volatile("" : "+s"(G.step), "+s"(tA[0])); }
        if constexpr (e == 10) { tA[1] = urow(8 + wa, rot) + cb * 128u; asm volatile("" : "+s"(tA[1])); }
        if constexpr (e == 12) { tA[2] = urow(31 + wa, rot) + cb * 128u; asm volatile("" : "+s"(tA[2])); }
        if constexpr (e == 14) {
          tB[0] = urow(16, rot) + cb * 128u; tB[1] = urow(w < 4 ? 19u : 22u, rot) + cb * 128u; tB[2] = urow(w < 4 ? 25u : 28u, rot) + cb * 128u;
          asm volatile("" : "+s"(tB[0]), "+s"(tB[1]), "+s"(tB[2]));
        }
      }
      if constexpr (s == (TR ? 1 : 3) && e == 2) pre();
      if constexpr (NX && e >= L - 2) {
        constexpr int u = 2 * (s - 1) + e - (L - 2);
        pre2[u] = __builtin_amdgcn_raw_buffer_load_b128(rsB2, vpf, urow(36 + u, rot) + (w & 3) * 10240u + cb * 128u, 0);
      }
      if constexpr (s < 7) { if constexpr (e == L - 3) d.sB += s == 0 ? d.advB * (1 + (w >> 2)) : d.advB; }
      else if constexpr (s == 7) {
        if constexpr (e == L - 6) d.vB0 = y ? vb2 : d.vB0;
        if constexpr (e == L - 4) d.vB1 = y ? vb2 + 10240u : d.vB1;
        if constexpr (e == L - 3) d.vB2 = y ? vb2 + 20480u : d.vB2;
        if constexpr (e == L - 2) d.sB = y ? tB[0] : d.sB + d.advB;
        if constexpr (e == L - 1) d.rsB = y ? rsB2 : d.rsB;
      } else {
        if constexpr (e == L - 3) { d.sB = tB[s - 7]; d.rsB = rsB2; d.vB0 = vb2; d.vB1 = vb2 + 10240u; d.vB2 = vb2 + 20480u; }
      }
      if constexpr (s == 9 && e == L - 4) {
        asm volatile("s_waitcnt vmcnt(7)" : "+v"(G.g0), "+v"(G.g1), "+v"(G.g2)::"memory");
        ok = all64(G.g0[1] == tag && G.g1[1] == tag && G.g2[1] == tag);
      }
      if constexpr (s == 9 && e == L - 3) ln_store();
    });
    if constexpr (s == 0) {
      wait_vm<EARLY1 ? 8 - NA1 : 11>();
      static_for<0, 14>([&](auto K) { row_step<K.v>(R, lane); });
    }
    if constexpr (s == 9) {
      if (__builtin_expect(!ok, 0)) {
#pragma unroll 1
        for (unsigned spins = 0; spins < SPIN_CAP; ++spins) {
          __builtin_amdgcn_s_sleep(2);
          gran_load(G, rsWS);
          if (all64(G.g0[1] == tag && G.g1[1] == tag && G.g2[1] == tag)) break;
        }
        ln_store();
      }
    }
    wait_lgkm0();
    if constexpr (s < 9) wait_vm<4 + NX>();
    bar();
    if constexpr (s == 0) { STAMP(12); }
    SB;
    kstep<T, TR, 2 * s + 1>(acc, a1, b1, a0, b0, aoff, boff, d, [&](auto E) {
      constexpr int e = E.v;
      if constexpr (s == 0 && e == 6) {
        if (y) {
          const unsigned so = d.sB - d.advB;
          dma16(d.mW + 49152, d.vB0, d.rsB, so); dma16(d.mW + 49152 + 1024, d.vB1, d.rsB, so); dma16(d.mW + 49152 + 2048, d.vB2, d.rsB, so);
        }
      }
      if constexpr (s == 0 && e == L - 4) {
        v2 = (lane >> 3) * 1280 + (((lane & 7) ^ (((lane >> 4) & 3) << 1)) << 4);
        vpf = y ? v2 : 0x7ffffff0u;
        asm volatile("" : "+v"(v2), "+v"(vpf));
      }
      if constexpr (s == 0 && e == L - 6) row_publish(R, ws, tag, tid);
      if constexpr (s == 8 && e == 0) gran_issue(G, rsWS);
      if constexpr (s >= 7 && s < 9 && e >= L - 4) {
        d.rsA = rsB2; d.vA0 = v2; d.vA1 = v2;
        d.sA[e - (L - 4)] = tA[s - 7] + (e - (L - 4)) * 10240;
      }
      if constexpr (s == 9 && LASTA && e == 7) {
        if (wa < 5) {
#pragma unroll
          for (int i = 0; i < 4; ++i) dma16(a_slot(0) + d.mA + i * 1024, v2, rsB2, tA[2] + i * 10240);
        }
      }
    });
    if constexpr (s == 8) { STAMP(13); }
    if constexpr (s == 2) { STAMP(26); }
    if constexpr (s == 6) { STAMP(27); }
  });
  acc_fence<4 * T>(&acc[0][0]);
  STAMP(14);
}

DI void st16(u32x4 v, rsrc_t rs, unsigned voff, unsigned soff) {
  __builtin_amdgcn_raw_buffer_store_b128(v, rs, voff, soff, 16);
  asm volatile("s_nop 1" ::"v"(v) : "memory");
}
DI void publish_flag(const Params& p, unsigned flag_off, unsigned tag, unsigned tid) {
  drain_vm();
  bar();
  if (tid == 0) __hip_atomic_store((gu32*)(p.ws + flag_off), tag, RLX_AGENT);
}
DI void st_frag(rsrc_t rs, unsigned lane, unsigned off, u32x2 lo, u32x2 hi) { st16(cat2(lo, hi), rs, lane * 16, off); }
DI void shared_send(unsigned w, unsigned lane, const u32x2 (&sh)[4]) {
  if (w >= 4) lds_st<u32x4>(L_SCR + w * 1024 + lane * 16, cat2(sh[0], sh[1]));
  else lds_st<u32x4>(L_SCR + w * 1024 + lane * 16, cat2(sh[2], sh[3]));
  asm volatile("" ::: "memory");
}
DI void shared_recv(rsrc_t rs, unsigned w, unsigned lane, unsigned off, unsigned str, const u32x2 (&sh)[4]) {
  lbar();
  u32x4 rcv = lds_ld<u32x4>(L_SCR + (w ^ 4) * 1024 + lane * 16);
  u32x2 r0 = {rcv[0], rcv[1]}, r1 = {rcv[2], rcv[3]};
  if (w >= 4) { st_frag(rs, lane, off + 2 * str, r0, sh[2]); st_frag(rs, lane, off + 3 * str, r1, sh[3]); }
  else        { st_frag(rs, lane, off, sh[0], r0); st_frag(rs, lane, off + str, sh[1], r1); }
}

__global__ __launch_bounds__(512, 2) void fused_block(Params p) {
  p.ws = (char*)(((unsigned long long)p.ws + p.wsal) & ~p.wsal);
  unsigned tid = __builtin_amdgcn_workitem_id_x(), lane = tid & 63;
  const unsigned w = rfl(tid >> 6);
  const unsigned b = __builtin_amdgcn_workgroup_id_x(), xcd = b & 7, slot = b >> 3;
  const unsigned rb = 4 * (xcd & 1) + (slot & 3), jq = xcd >> 1, ji = slot >> 2;
  const unsigned spi = (xcd - 4) * 4 + (slot & 3);
  const unsigned long long jl0 = (jq & 1) ? 0x4123220908070605ull : 0x4021200403020100ull, jl1 = (jq & 1) ? 0x4327266011100f0eull : 0x422524600d0c0b0aull;
  const unsigned jb = (unsigned)(((jq & 2) ? jl1 : jl0) >> (8 * ji)) & 0xff, jtype = jb >> 5, jidx = jb & 31;
  STAMP_DECL;
  STAMP(0);

  Dma d;
  RowStat rs_;
  const bool mlp = jtype == J_MLP;
  const unsigned pitch = mlp ? 3456u : 3840u, wq = (w + (ROTW ? slot : 0)) & 3, wa = (w + (ROTW ? ji : 0)) & 7;
  d.wa = wa; d.wq = wq;
  const rsrc_t rsX = mk_rsrc(p.x, S * D * 2);
  d.vA0 = (lane >> 3) * 1280 + (((lane & 7) ^ (lane >> 4)) << 4); d.vA1 = d.vA0 ^ 64;
  d.rsA = rsX; d.mA = wa * 4096u;
#pragma unroll
  for (int i = 0; i < 4; ++i) d.sA[i] = (rb * 256 + 32 * wa + 8 * i) * 1280;
  unsigned epoch, tag;
  asm volatile("s_nop 4" :: "s"(rsX) : "memory");
  STAMP(6);
#pragma unroll
  for (int i = 0; i < 4; ++i) dma16(a_slot(0) + d.mA + i * 1024, (i & 1) ? d.vA1 : d.vA0, d.rsA, d.sA[i]);
  asm volatile("global_load_dword %0, %1, %2 sc1" : "=&v"(epoch) : "v"(0u), "s"(p.ws) : "memory");
  SB;
  {
    unsigned vb[3];
    const unsigned lim = mlp ? 176u : 144u;
    {
      const unsigned r0 = (lane * 43) >> 9, p0 = lane - 12 * r0;
      const bool w1 = p0 >= 8, w2 = p0 >= 4;
      const unsigned r1 = r0 + (w1 ? 6 : 5), p1 = p0 + (w1 ? -8 : 4), r2 = r0 + (w2 ? 11 : 10), p2 = p0 + (w2 ? -4 : 8);
      vb[0] = r0 * pitch + umin(p0 * 16, lim);
      vb[1] = r1 * pitch + umin((p1 * 16) ^ ((r1 & 8) << 2), lim);
      vb[2] = r2 * pitch + umin((p2 * 16) ^ 32, lim);
    }
    d.vB0 = vb[0]; d.vB1 = vb[1]; d.vB2 = vb[2];
    const unsigned mh = wq >> 1;
    const void* bw = mlp ? (mh ? p.wu : p.wg) : p.wqkv;
    const unsigned cj = mlp ? 192u : 160u * jtype, cm = mlp ? 0u : (jtype == J_KV ? 1280u : 160u), c0 = jtype == J_KV ? 1280u : 0u;
    d.rsB = mk_rsrc(bw, D * pitch);
    d.sB = (16 * (wq & 1) + (w < 4 ? 32 : 0)) * pitch + c0 + cj * jidx + cm * mh; d.advB = 64 * pitch;
    d.mB = L_B + wq * 3072u + (w < 4 ? 0u : 12288u); d.mW = L_B + wq * 3072u + (w < 4 ? 49152u : 0u);
    const unsigned bdst = L_B + (w < 4 ? 12288u : 0u) + wq * 3072u;
    dma16(bdst, d.vB0, d.rsB, d.sB); dma16(bdst + 1024, d.vB1, d.rsB, d.sB); dma16(bdst + 2048, d.vB2, d.rsB, d.sB);
    rs_.row = rfl(b * 8 + w);
    d.sB += d.advB;
    dma16(bdst + 24576, d.vB0, d.rsB, d.sB); dma16(bdst + 24576 + 1024, d.vB1, d.rsB, d.sB); dma16(bdst + 24576 + 2048, d.vB2, d.rsB, d.sB);
#pragma unroll
    for (int i = 0; i < NA1; ++i) dma16(a_slot(1) + d.mA + i * 1024, (i & 1) ? d.vA1 : d.vA0, d.rsA, d.sA[i] + 128);
  }
  STAMP(10);
  lds_st<unsigned>(L_GO + (tid & 1) * 4, 0u);

  const unsigned t2 = 28 * xcd + slot;
  const bool has2 = slot < 28 && t2 < 220;
  const unsigned G = t2 / 10, cb = t2 % 10;
  constexpr unsigned rot = 32;
  unsigned g = lane >> 4, r = lane & 15, q4 = r >> 2, p4 = lane & 3;
  const rsrc_t rsWd = mk_rsrc(p.wd, has2 ? F * D * 2 : 0);
  const rsrc_t rsWS = mk_rsrc(p.ws, WS_END);
  unsigned v2 = (lane >> 3) * 1280 + (((lane & 7) ^ (((lane >> 4) & 3) << 1)) << 4);
  auto relane = [&] {
    asm volatile("" : "+v"(tid));
    lane = tid & 63; g = lane >> 4; r = lane & 15; q4 = r >> 2; p4 = lane & 3;
    v2 = (lane >> 3) * 1280 + (((lane & 7) ^ (((lane >> 4) & 3) << 1)) << 4);
  };
  const unsigned m = w & 3, n = w >> 2;
  const unsigned brow = L_B + (8 * g + q4) * 192;
  const unsigned bx = (g & 1) << 5;

  u32x4 pre2[36];
  W2 W;
  W.wd = rsWd; W.wo = mk_rsrc(p.wo, has2 ? D * D * 2 : 0); W.rot = rot; W.col = cb * 128u;
  if (jtype == J_MLP) {
    f32x4 acc[4][6];
    unsigned boff[6];
#pragma unroll
    for (int t = 0; t < 3; ++t) {
      const unsigned gt = n ? (t == 2 ? 3 : 4 + t) : t;
      boff[t] = brow + ((32 * gt + 8 * p4) ^ bx); boff[t + 3] = boff[t] + 6144;
    }
    const unsigned ccol = tid < 96 ? 1920 + 96 * jidx + tid : 3648 + 96 * jidx + (tid < 192 ? tid - 96 : 0);
    main_loop<6, true, true, true>([] {}, rs_, p.ws, acc, boff, d, w, lane, tid, epoch, tag, rb, ccol, rsWd, rsWS, cb, rot, pre2 STAMP_PASS);
    relane();
    float mm[4];
    f32x4 cg[3], cu[3];
#pragma unroll
    for (int i = 0; i < 4; ++i) mm[i] = lds_ld<float>(L_MISC + (64 * m + 16 * i + r) * 4);
#pragma unroll
    for (int t = 0; t < 3; ++t) {
      const unsigned gt = n ? (t == 2 ? 3 : 4 + t) : t;
      cg[t] = lds_ld<f32x4>(L_MISC + 2048 + (16 * gt + 4 * g) * 4); cu[t] = lds_ld<f32x4>(L_MISC + 2048 + (96 + 16 * gt + 4 * g) * 4);
    }
    auto hval = [&](int i, int t) {
      f32x4 gg = acc[i][t] + mm[i] * cg[t];
      const f32x4 uu = acc[i][t + 3] + mm[i] * cu[t];
#pragma unroll
      for (int j = 0; j < 4; ++j) gg[j] = relu2(gg[j]);
      return pk4(gg * uu);
    };
    const unsigned ho = WS_HA + rb * RBSZ + jidx * 49152 + m * 4 * 3072;
#pragma unroll
    for (int i = 0; i < 4; ++i) { st_frag(rsWS, lane, ho + i * 3072 + n * 2048, hval(i, 0), hval(i, 1)); SB; }
    const u32x2 sh[4] = {hval(0, 2), hval(1, 2), hval(2, 2), hval(3, 2)};
    shared_send(w, lane, sh);
    shared_recv(rsWS, w, lane, ho + 1024, 3072, sh);
    STAMP(15);
    publish_flag(p, WS_FLAGH + (rb * 32 + jidx) * FS, tag, tid);
    STAMP(3);
  } else if (jtype == J_KV) {
    f32x4 acc[4][5];
    unsigned boff[5];
    boff[0] = brow + ((80 * n + 8 * p4) ^ bx); boff[1] = boff[0] + 6144;
    boff[2] = brow + ((80 * n + 40 + 8 * p4) ^ bx); boff[3] = boff[2] + 6144;
    boff[4] = brow + ((80 * n + 32 + 40 * (p4 >> 1)) ^ bx) + (p4 & 1) * 6144;
    const unsigned kb = 126976u + m * 6720 + n * 3360, dump = L_MISC - 2048 + lane * 16;
    const unsigned ea = (q4 == 1 || r == 0) ? kb + (r == 0 ? 20 : 12 + r) * 80 : 0u, eb = (q4 == 3 || r == 0) ? kb + 1680 + (r == 0 ? 20 : 4 + r) * 80 : 0u;
    unsigned ka[6] = {kb + r * 80 + g * 16, ea ? ea + g * 16 : dump, eb ? eb + g * 16 : dump, g == 0 ? kb + r * 80 + 64 : dump,
                      g == 2 ? kb + 1680 + r * 80 + 64 : dump, (g == 0 && ea) ? ea + 64 : ((g == 2 && eb) ? eb + 64 : dump)};
    auto pin = [&] { asm volatile("" : "+v"(ka[0]), "+v"(ka[1]), "+v"(ka[2]), "+v"(ka[3]), "+v"(ka[4]), "+v"(ka[5])); };
    const unsigned ccol = tid < 80 ? 640 + 80 * jidx + tid : 1280 + 80 * jidx + (tid < 160 ? tid - 80 : 0);
    main_loop<5, false, false, false>(pin, rs_, p.ws, acc, boff, d, w, lane, tid, epoch, tag, rb, ccol, rsWd, rsWS, cb, rot, pre2 STAMP_PASS);
    relane();
    float cs[5];
    cs[0] = lds_ld<float>(L_MISC + 2048 + (40 * n + r) * 4);
    cs[1] = lds_ld<float>(L_MISC + 2048 + (80 + 40 * n + r) * 4);
    cs[2] = lds_ld<float>(L_MISC + 2048 + (40 * n + 20 + r) * 4);
    cs[3] = lds_ld<float>(L_MISC + 2048 + (80 + 40 * n + 20 + r) * 4);
    cs[4] = lds_ld<float>(L_MISC + 2048 + ((q4 & 1) * 80 + 40 * n + 16 + 20 * (q4 >> 1) + (r & 3)) * 4);
    u32x2 pk[4][3], vv[4][3];
    const unsigned vmask = 0u - (q4 & 1);
    f32x4 mm4[4], rr4[4];
#pragma unroll
    for (int i = 0; i < 4; ++i) {
      mm4[i] = lds_ld<f32x4>(L_MISC + (64 * m + 16 * i + 4 * g) * 4); rr4[i] = lds_ld<f32x4>(L_MISC + 1024 + (64 * m + 16 * i + 4 * g) * 4);
    }
    SB;
#pragma unroll
    for (int i = 0; i < 4; ++i) {
      const f32x4 mm = mm4[i], rr = rr4[i];
      const f32x4 rl = rr * 1.4426950408889634f, rh = rr * 0.5f;
      f32x4 val[5];
#pragma unroll
      for (int t = 0; t < 5; ++t) val[t] = acc[i][t] + mm * cs[t];
#pragma unroll
      for (int t = 0; t < 3; ++t) pk[i][t] = pk4(phi4(val[2 * t], rl, rh));
      vv[i][0] = pk4(val[1] * rr); vv[i][1] = pk4(val[3] * rr);
      const u32x2 v4 = pk4(val[4] * rr);
      vv[i][2] = u32x2{bsel(vmask, v4[0], 0x3f803f80u), bsel(vmask, v4[1], 0x3f803f80u)};
    }
    STAMP(25);
    f32x4 P[7] = {};
#pragma unroll
    for (int i = 0; i < 4; i += 2) {
      const bf16x8 k0 = frag(pk[i][0], pk[i + 1][0]), k2 = frag(pk[i][1], pk[i + 1][1]), k4 = frag(pk[i][2], pk[i + 1][2]);
      const bf16x8 v1 = frag(vv[i][0], vv[i + 1][0]), v3 = frag(vv[i][1], vv[i + 1][1]), v4 = frag(vv[i][2], vv[i + 1][2]);
      P[0] = mfma(k0, v1, P[0]); P[1] = mfma(k0, v4, P[1]); P[2] = mfma(k4, v1, P[2]);
      P[3] = mfma(k2, v3, P[3]); P[4] = mfma(k2, v4, P[4]); P[5] = mfma(k4, v3, P[5]); P[6] = mfma(k4, v4, P[6]);
    }
    lds_st<f32x4>(ka[0], P[0]); lds_st<f32x4>(ka[0] + 1680, P[3]); lds_st<f32x4>(ka[1], P[1]); lds_st<f32x4>(ka[2], P[4]);
    lds_st<f32x4>(ka[3], P[2]); lds_st<f32x4>(ka[4], P[5]); lds_st<f32x4>(ka[5], P[6]);
    lbar();
    if (tid < 210) {
      const unsigned a = 126976u + tid * 32;
      const f32x4 t0 = (lds_ld<f32x4>(a) + lds_ld<f32x4>(a + 6720)) + (lds_ld<f32x4>(a + 13440) + lds_ld<f32x4>(a + 20160));
      const f32x4 t1 = (lds_ld<f32x4>(a + 16) + lds_ld<f32x4>(a + 6736)) + (lds_ld<f32x4>(a + 13456) + lds_ld<f32x4>(a + 20176));
      st16(cat2(pk4(t0), pk4(t1)), rsWS, tid * 16, WS_KVP + (rb * 32 + 4 * jidx) * 840);
    }
    wait_lgkm0();
    STAMP(15);
    publish_flag(p, WS_FLAGKV + ((jidx >> 1) * 32 + (jidx & 1) * 8 + rb) * FS, tag, tid);
    STAMP(3);
  } else if (jtype == J_Q) {
    f32x4 acc[4][5];
    unsigned boff[5];
#pragma unroll
    for (int t = 0; t < 5; ++t) boff[t] = brow + n * 6144 + ((32 * t + 8 * p4) ^ bx);
    const unsigned ccol = 160 * jidx + (tid < 160 ? tid : 0);
    unsigned tch[3];
    auto warm = [&] {
      const unsigned gq = (rb * 4 + jidx) * 8 + w;
      asm volatile("buffer_load_dword %0, %3, %5, 0 offen\n\tbuffer_load_dword %1, %3, %5, %7 offen\n\tbuffer_load_dword %2, %4, %6, 0 offen"
                   : "=&v"(tch[0]), "=&v"(tch[1]), "=&v"(tch[2])
                   : "v"((gq * 34 + (lane < 34 ? lane : 0)) * 128), "v"((gq * 25 + (lane < 25 ? lane : 0)) * 128), "s"(mk_rsrc(p.wd, F * D * 2)),
                     "s"(mk_rsrc(p.wo, D * D * 2)), "s"(8576u * 128u) : "memory");
    };
    main_loop<5, true, false, false>(warm, rs_, p.ws, acc, boff, d, w, lane, tid, epoch, tag, rb, ccol, rsWd, rsWS, cb, rot, pre2 STAMP_PASS);
    relane();
    asm volatile("" :: "v"(tch[0]), "v"(tch[1]), "v"(tch[2]));
    u32x2 pq[4][5];
    {
      float mm[4], rr[4];
      f32x4 c[5];
#pragma unroll
      for (int i = 0; i < 4; ++i) { mm[i] = lds_ld<float>(L_MISC + (64 * m + 16 * i + r) * 4); rr[i] = lds_ld<float>(L_MISC + 1024 + (64 * m + 16 * i + r) * 4); }
#pragma unroll
      for (int t = 0; t < 5; ++t) c[t] = lds_ld<f32x4>(L_MISC + 2048 + (80 * n + 16 * t + 4 * g) * 4);
      SB;
#pragma unroll
      for (int i = 0; i < 4; ++i)
#pragma unroll
        for (int t = 0; t < 5; ++t) pq[i][t] = pk4(phi4(acc[i][t] + mm[i] * c[t], splat(rr[i] * 1.4426950408889634f), splat(rr[i] * 0.5f)));
#pragma unroll
      for (int i = 0; i < 4; ++i) asm volatile("" : "+v"(pq[i][0]), "+v"(pq[i][1]), "+v"(pq[i][2]), "+v"(pq[i][3]), "+v"(pq[i][4]));
    }
    constexpr unsigned KP = 208, KQ = 84 * KP;
    unsigned wa[2];
#pragma unroll
    for (int k = 0; k < 2; ++k) {
      const unsigned qd = 2 * (tid < 420 ? tid : 0) + k, hd = qd / 105, e = (qd % 105) / 5, dq = qd % 5, hl = hd & 3;
      wa[k] = (hd >> 2) * KQ + (e < 20 ? hl * 20 + e : 80 + hl) * KP + hl * 40 + dq * 8;
    }
    unsigned fk = n * KQ + r * KP + g * 8, fz = n * KQ + (80 + (r & 3)) * KP + g * 8;
    unsigned gm[3] = {g < 1 ? ~0u : 0u, g < 2 ? ~0u : 0u, g < 3 ? ~0u : 0u};
    u32x4 B[4][3];
    f32x4 eps4 = splat(1e-6f);
#pragma unroll
    for (int i = 0; i < 4; ++i) {
      B[i][0] = cat2(pq[i][0], pq[i][1]); B[i][1] = cat2(pq[i][2], pq[i][3]); B[i][2] = cat2(pq[i][4], u32x2{0u, 0u});
      asm volatile("" : "+v"(B[i][0]), "+v"(B[i][1]), "+v"(B[i][2]));
    }
    asm volatile("" : "+v"(wa[0]), "+v"(wa[1]), "+v"(fk), "+v"(fz), "+v"(gm[0]), "+v"(gm[1]), "+v"(gm[2]), "+v"(eps4));
    STAMP(25);
    wait_vm<0>();
    bar();
#pragma unroll
    for (int k = 0; k < 5; ++k) lds_st<u32x4>(k * 8192 + tid * 16, u32x4{0u, 0u, 0u, 0u});
    if (w == 0) poll_flags((gu32*)(p.ws + WS_FLAGKV + (jidx * 32 + (lane & 15)) * FS), true, tag);
    lbar();
    STAMP(4);
    if (w < 7) {
      u32x4 v[8];
#pragma unroll
      for (int j = 0; j < 8; ++j)
        v[j] = __builtin_amdgcn_raw_buffer_load_b128(rsWS, (tid < 420 ? tid : 0) * 16, WS_KVP + (j * 32 + 8 * jidx) * 840, 16);
      SB;
      f32x4 t0 = {0.f, 0.f, 0.f, 0.f}, t1 = t0;
#pragma unroll
      for (int j = 0; j < 8; ++j) {
        t0 += f32x4{bflo(v[j][0]), bfhi(v[j][0]), bflo(v[j][1]), bfhi(v[j][1])};
        t1 += f32x4{bflo(v[j][2]), bfhi(v[j][2]), bflo(v[j][3]), bfhi(v[j][3])};
      }
      lds_st<u32x2>(wa[0], pk4(t0)); lds_st<u32x2>(wa[1], pk4(t1));
    }
    lbar();
    STAMP(16);
    u32x4 A[12];
#pragma unroll
    for (int P = 0; P < 3; ++P) A[P] = cat2(lds_ld<u32x2>(fz + P * 64), lds_ld<u32x2>(fz + P * 64 + 32));
#pragma unroll
    for (int u = 0; u < 5; ++u) {
      const unsigned a0 = fk + u * 16 * KP + (u < 3 ? 0 : 64);
      A[3 + u] = cat2(lds_ld<u32x2>(a0), lds_ld<u32x2>(a0 + 32));
    }
#pragma unroll
    for (int u = 1; u < 5; ++u) {
      const unsigned a0 = fk + u * 16 * KP + (u < 3 ? 64 : 128);
      A[7 + u] = cat2(lds_ld<u32x2>(a0), lds_ld<u32x2>(a0 + 32));
    }
    SB;
    u32x2 sh[4];
    f32x4 den[4], num[2][5];
    static_for<0, 12>([&](auto E) {
      constexpr int P = E.v >> 2, i = E.v & 3;
      if constexpr (P == 0) mfma_c(den[i], A[0], B[i][0], eps4); else mfma_ip(den[i], A[P], B[i][P]);
    });
    u32x2 o[5];
    const unsigned oo = WS_HA + rb * RBSZ + 18 * 49152 + jidx * 81920 + m * 4 * 5120;
    auto fin = [&](auto I, auto K) {
      constexpr int i = I.v, k = K.v, u = k < 8 ? k >> 1 : 4;
      if constexpr (k == 8 || !(k & 1)) {
        asm volatile("" : "+v"(num[i & 1][u]));
        const float rr = u == 0 ? den[i][0] : (u == 4 ? den[i][3] : bself(gm[(u + 2) % 3], den[i][(u + 3) & 3], den[i][u & 3]));
#pragma unroll
        for (int j = 0; j < 4; ++j) num[i & 1][u][j] = smul(num[i & 1][u][j], rr);
      }
      if constexpr (k == 8 || (k & 1)) o[u] = pk4(num[i & 1][u]);
      if constexpr (k == 3) st_frag(rsWS, lane, oo + i * 5120 + n * 2048, o[0], o[1]);
      if constexpr (k == 7) st_frag(rsWS, lane, oo + i * 5120 + n * 2048 + 1024, o[2], o[3]);
      if constexpr (k == 8) { sh[i] = o[4]; asm volatile("" : "+v"(sh[i])); }
    };
    static_for<0, 4>([&](auto I) {
      constexpr int i = I.v;
      static_for<0, 9>([&](auto K) {
        constexpr int k = K.v, u = k < 5 ? k : k - 4;
        if constexpr (k < 5) mfma_z(num[i & 1][u], A[3 + u], B[i][u < 3 ? 0 : 1]); else mfma_ip(num[i & 1][u], A[7 + u], B[i][u < 3 ? 1 : 2]);
        SB;
        if constexpr (i == 0) {
          if constexpr (k >= 5) {
            asm volatile("" : "+v"(den[k - 5]));
#pragma unroll
            for (int j = 0; j < 4; ++j) den[k - 5][j] = __builtin_amdgcn_rcpf(den[k - 5][j]);
            asm volatile("" : "+v"(den[k - 5]));
          }
        } else {
          fin(IC<i - 1>{}, K);
        }
        SB;
      });
    });
    static_for<0, 9>([&](auto K) { fin(IC<3>{}, K); SB; });
    shared_send(w, lane, sh);
    shared_recv(rsWS, w, lane, oo + 4096, 5120, sh);
    STAMP(15);
    publish_flag(p, WS_FLAGO + (rb * 32 + jidx) * FS, tag, tid);
    STAMP(3);
    if (b == 224) {
      poll_flags((gu32*)(p.ws + WS_FLAGH + tid * FS), (tid & 31) < (tid < 256 ? 18u : 4u), tag);
      bar();
      if (tid == 0) __hip_atomic_store((gu32*)(p.ws + WS_EPOCH), tag, RLX_AGENT);
    }
  } else {
    const unsigned gw = spi * 8 + w;
    unsigned k10 = 0x00003f80u, k01 = 0x3f800000u;
    asm volatile("" : "+v"(k10), "+v"(k01));
    auto cs_src = [&](unsigned u, rsrc_t& rs, unsigned& cv, unsigned& st) {
      const unsigned cline = u / 3, third = u % 3;
      const void* cw = cline < 30 ? p.wqkv : (cline < 57 ? p.wg : p.wu);
      const unsigned cpitch = cline < 30 ? 3840u : 3456u;
      const unsigned clocal = cline < 30 ? cline : (cline < 57 ? cline - 30 : cline - 57);
      rs = mk_rsrc(cw, gw < 126 ? D * cpitch : 0);
      cv = (third * 216 + (lane >> 3)) * cpitch + clocal * 128 + (lane & 7) * 16;
      st = 8 * cpitch;
    };
    auto cs_publish = [&](unsigned u, float (&cs)[8]) {
#pragma unroll
      for (int k = 0; k < 4; ++k) { const bool hi = lane & 32; const float o = shfl_x(hi ? cs[k] : cs[k + 4], lane, 32); cs[k] = (hi ? cs[k + 4] : cs[k]) + o; }
#pragma unroll
      for (int k = 0; k < 2; ++k) { const bool hi = lane & 16; const float o = shfl_x(hi ? cs[k] : cs[k + 2], lane, 16); cs[k] = (hi ? cs[k + 2] : cs[k]) + o; }
      { const bool hi = lane & 8; const float o = shfl_x(hi ? cs[0] : cs[1], lane, 8); cs[0] = (hi ? cs[1] : cs[0]) + o; }
      if (gw < 126)
        __hip_atomic_store((gu64*)(p.ws + WS_CSUM) + (u % 3) * NCOL + (u / 3) * 64 + (lane & 7) * 8 + (lane >> 3),
                           ((unsigned long long)tag << 32) | __builtin_bit_cast(unsigned, cs[0]), RLX_AGENT);
    };
    row_load(rs_, rsX, lane);
    asm volatile("s_waitcnt vmcnt(0) lgkmcnt(0)\n\tv_readfirstlane_b32 %0, %1\n\ts_nop 3" : "=s"(tag) : "v"(epoch) : "memory");
    tag = rfl(tag) + 1;
    static_for<0, 14>([&](auto K) { row_step<K.v>(rs_, lane); });
    u32x4 c[27];
    rsrc_t rs0, rs1; unsigned cv0, cv1, st0, st1;
    cs_src(gw, rs0, cv0, st0); cs_src(gw + 126, rs1, cv1, st1);
#pragma unroll
    for (int j = 0; j < 27; ++j) c[j] = __builtin_amdgcn_raw_buffer_load_b128(rs0, cv0 + j * st0, 0, 0);
    lbar();
    row_publish(rs_, p.ws, tag, tid);
    float cs[8] = {};
#pragma unroll
    for (int j = 0; j < 27; ++j) {
#pragma unroll
      for (int k = 0; k < 4; ++k) { cs[2 * k] = dot2(c[j][k], k10, cs[2 * k]); cs[2 * k + 1] = dot2(c[j][k], k01, cs[2 * k + 1]); }
      if (j < 8) c[j] = __builtin_amdgcn_raw_buffer_load_b128(rs1, cv1 + j * st1, 0, 0);
      SB;
    }
    cs_publish(gw, cs);
    SB;
#pragma unroll
    for (int j = 8; j < 27; ++j) c[j] = __builtin_amdgcn_raw_buffer_load_b128(rs1, cv1 + j * st1, 0, 0);
    SB;
    STAMP(11);
#pragma unroll
    for (int k = 0; k < 8; ++k) cs[k] = 0.f;
#pragma unroll
    for (int j = 0; j < 27; ++j)
#pragma unroll
      for (int k = 0; k < 4; ++k) { cs[2 * k] = dot2(c[j][k], k10, cs[2 * k]); cs[2 * k + 1] = dot2(c[j][k], k01, cs[2 * k + 1]); }
    cs_publish(gw + 126, cs);
    STAMP(12);
#pragma unroll 1
    for (unsigned i = w; i < 144; i += 8) dma16(i * 1024, v2, rsWd, urow(i >> 2, rot) + (i & 3) * 10240u + cb * 128u);
    drain_vm();
    bar();
  }
  if (!has2) { STAMP_END; return; }
  relane();

  const unsigned rbA = (G * 6) >> 4, rbB = (G * 6 + 5 > 127 ? 127 : G * 6 + 5) >> 4;
  if (w >= 4) {
    __builtin_amdgcn_s_setprio(HPRIO);
    const unsigned lw = w - 4, ld0 = lw * 1024;
    gu32* fo = (gu32*)(p.ws + WS_FLAGO + ((lane < 32 ? rbA : rbB) * 32 + (lane & 3)) * FS);
    gu32* fh = (gu32*)(p.ws + WS_FLAGH + ((lane < 32 ? rbA : rbB) * 32 + ((lane & 31) < 18 ? lane & 31 : 17)) * FS);
    __builtin_amdgcn_s_sleep(HSLEEP);
    if (jtype == J_KV) {
#pragma unroll
      for (int u = 31; u < 36; ++u) dma16(u * 4096 + ld0, v2, rsWd, w2_off(W, u, lw));
    }
    if (jtype != J_MLP) {
#pragma unroll
      for (int u = 0; u < NPRE; ++u) { pre2[u] = w2_load(W, 36 + u, lw, v2); SB; }
    }
#pragma unroll
    for (int u = 36 + NPRE; u < PSPLIT; ++u) { pre2[u - 36] = w2_load(W, u, lw, v2); SB; }
    if (w == 7) {
      __builtin_amdgcn_s_setprio(3);
      const unsigned v = poll_flags(fh, (lane & 31) < 18 && (lane & 31) >= 10, tag);
      lds_set(L_GO);
      if (!all64((lane & 31) >= 18 || v == tag)) poll_flags(fh, (lane & 31) < 18, tag);
      lds_set(L_GO + 4);
      __builtin_amdgcn_s_setprio(HPRIO);
    }
#pragma unroll
    for (int u = PSPLIT; u < HSPLIT; ++u) { pre2[u - 36] = w2_load(W, u, lw, v2); SB; }
    STAMP(8);
    unsigned la[3], wb[3], xo[3], st[3];
    u32x4 xr[3];
    const unsigned ch = lane & 7;
#pragma unroll
    for (int i = 0; i < 3; ++i) {
      const unsigned row = lw * 24 + i * 8 + (lane >> 3), rl = row >> 4, rr = row & 15;
      la[i] = (rl >= 3 ? 24576u + (rl - 3) * 4096u : rl * 4096u) + (ch >> 1) * 1024 + (ch & 1) * 512 + ((rr + 2 * ch) & 15) * 16;
      xo[i] = (G * 96 + row) * 1280 + cb * 128 + ch * 16;
      wb[i] = ld0 + lane * 16 + i * 49152;
      asm volatile("" : "+v"(la[i]), "+v"(wb[i]), "+v"(xo[i]));
    }
    float ri[3], rl[3];
    unsigned of = 0;
    f32x4 xf[3][2], dd[3][2], d2[3][2];
#pragma unroll
    for (int k = 2; k <= 6; ++k) {
      if (k == O_BAR && w == 7 && !all64((lane & 31) >= 4 || of == tag)) poll_flags(fo, (lane & 31) < 4, tag);
      if (k == 6) {
        wait_vm<0>();
#pragma unroll
        for (int h = 0; h < 3; ++h) {
          ri[h] = 2.0f * bfhi(st[h]) * bfhi(st[h]);
          rl[h] = -1.4426950408889634f * __builtin_amdgcn_rcpf(ri[h]);
          xf[h][0] = f32x4{bflo(xr[h][0]), bfhi(xr[h][0]), bflo(xr[h][1]), bfhi(xr[h][1])};
          xf[h][1] = f32x4{bflo(xr[h][2]), bfhi(xr[h][2]), bflo(xr[h][3]), bfhi(xr[h][3])};
          asm volatile("" : "+v"(ri[h]), "+v"(rl[h]), "+v"(xf[h][0]), "+v"(xf[h][1]));
        }
      }
      lbar();
      if (k == O_BAR - 1) { of = __hip_atomic_load(fo, RLX_AGENT); SB; }
      if (k < 6) {
        const int kk = k - 1;
#pragma unroll
        for (int u = 9 * (kk - 1) + ((kk - 1) & 1); u < 9 * kk + (kk & 1); ++u) {
          lds_st<u32x4>(wb[u / 12] + (u % 12) * 4096, pre2[u]);
          if (WSLEEP == 1) __builtin_amdgcn_s_sleep(1);
          if (WSLEEP == 2) asm volatile("s_nop 7");
          SB;
        }
      }
      if (!WSCH && (k == 2 || k == 3)) {
        constexpr int mid = (HSPLIT + 74) / 2 < 72 ? (HSPLIT + 74) / 2 : 72;
#pragma unroll
        for (int u = (k == 2 ? HSPLIT : mid); u < (k == 2 ? mid : 72); ++u) { pre2[u - 36] = w2_load(W, u, lw, v2); SB; }
        if (k == 3) { dma16(36 * 4096 + ld0, v2, W.wo, w2_off(W, 72, lw)); dma16(37 * 4096 + ld0, v2, W.wo, w2_off(W, 73, lw)); }
      }
      if (k == XK) {
#pragma unroll
        for (int h = 0; h < 3; ++h) {
          const unsigned row = G * 96 + lw * 24 + h * 8 + (lane >> 3);
          st[h] = __builtin_amdgcn_raw_buffer_load_b32(rsWS, WS_STAT + (row < S ? row : 0) * 8, 0, 16);
          xr[h] = __builtin_amdgcn_raw_buffer_load_b128(rsX, xo[h], 0, 0);
        }
      }
    }
    lbar();
#pragma unroll
    for (int i = 0; i < 3; ++i) {
      dd[i][0] = lds_ld<f32x4>(la[i]); d2[i][0] = lds_ld<f32x4>(la[i] + 12288);
      dd[i][1] = lds_ld<f32x4>(la[i] + 256); d2[i][1] = lds_ld<f32x4>(la[i] + 12288 + 256);
      la[i] += 49152;
      asm volatile("" : "+v"(la[i]));
    }
    __builtin_amdgcn_s_setprio(GPRIO);
#pragma unroll
    for (int i = 0; i < 3; ++i) {
      float v[8], e[8];
#pragma unroll
      for (int j = 0; j < 8; ++j) v[j] = sadd(dd[i][j >> 2][j & 3], d2[i][j >> 2][j & 3]);
      SB;
#pragma unroll
      for (int j = 0; j < 8; ++j) e[j] = smul(v[j], rl[i]);
      SB;
#pragma unroll
      for (int j = 0; j < 8; ++j) e[j] = __builtin_amdgcn_exp2f(e[j]);
      SB;
#pragma unroll
      for (int j = 0; j < 8; ++j) e[j] = sfma(e[j], ri[i], ri[i]);
      SB;
#pragma unroll
      for (int j = 0; j < 8; ++j) e[j] = __builtin_amdgcn_rcpf(e[j]);
      SB;
#pragma unroll
      for (int j = 0; j < 8; ++j) dd[i][j >> 2][j & 3] = smul(v[j], e[j]);
      asm volatile("" : "+v"(dd[i][0]), "+v"(dd[i][1]));
      SB;
    }
    const rsrc_t rsO = mk_rsrc(p.out, S * D * 2);
    STAMP(9);
    bar();
    f32x4 ss[3][2];
#pragma unroll
    for (int i = 0; i < 3; ++i) {
      ss[i][0] = lds_ld<f32x4>(la[i]) + lds_ld<f32x4>(la[i] + 12288);
      ss[i][1] = lds_ld<f32x4>(la[i] + 256) + lds_ld<f32x4>(la[i] + 12288 + 256);
    }
#pragma unroll
    for (int i = 0; i < 3; ++i)
      __builtin_amdgcn_raw_buffer_store_b128(cat2(pk4(xf[i][0] + ss[i][0] * dd[i][0]), pk4(xf[i][1] + ss[i][1] * dd[i][1])), rsO, xo[i], 0, 0);
    STAMP(5);
    STAMP_END;
    return;
  }
  {
    const unsigned kh = w & 1, rt = G * 6 + 3 * (w >> 1);
    const rsrc_t rsH = mk_rsrc(p.ws + WS_HA, 8 * RBSZ);
    unsigned tb[3][4];
#pragma unroll
    for (int t = 0; t < 4; ++t) {
      tb[0][t] = (4 * g + q4) * 128 + ((t ^ ((2 * g + (q4 >> 1)) & 3)) << 5) + 8 * p4 + kh * 4096;
      tb[1][t] = tb[0][t] + 49152; tb[2][t] = tb[0][t] + 98304;
      asm volatile("" : "+v"(tb[0][t]), "+v"(tb[1][t]), "+v"(tb[2][t]));
    }
    auto rd1 = [&](int i, int t, int hf) {
      const int sl = i == 36 ? 36 : (2 * i) % 36, bi = sl / 12 > 2 ? 2 : sl / 12;
      return __builtin_bit_cast(u32x2, lds_tr(tb[bi][t] + (sl - 12 * bi) * 4096 + hf * 2048));
    };
    constexpr int NW = P2NW, W0 = P2W0;
    constexpr int OI = O_BAR == 4 ? 17 : (O_BAR == 5 ? 22 : 26);
    constexpr int OB0 = OI == 26 ? NW - 1 : 0;
    f32x4 acc[2][3][4];
    u32x4 a[NW][3];
    u32x2 bw[2][4][2];
    unsigned va[2][3];
#pragma unroll
    for (int h = 0; h < 3; ++h) {
      va[0][h] = lane * 16 + ((rt + h) >> 4) * RBSZ + ((rt + h) & 15) * 3072;
      va[1][h] = va[0][h] + ((rt + h) & 15) * 2048;
      asm volatile("" : "+v"(va[0][h]), "+v"(va[1][h]));
    }
    unsigned xa[4];
#pragma unroll
    for (int t = 0; t < 4; ++t) {
      xa[t] = w * 12288u + t * 1024 + g * 256 + ((r + 4 * t + (g & 2)) & 15) * 16;
      asm volatile("" : "+v"(xa[t]));
    }
    auto put = [&](auto Z, auto E) {
      constexpr int z = Z.v, t = E.v / 3, h = 2 - E.v % 3;
      asm volatile("" : "+v"(acc[z][h][t]));
      lds_st<f32x4>(xa[t] + z * 49152 + h * 4096, acc[z][h][t]);
    };
    auto lda = [&](auto I, auto H) {
      constexpr int i = I.v, h = H.v;
      constexpr unsigned s0 = i < 27 ? (2 * i + rot) % 54 : 2 * i, s1 = i < 27 ? (2 * i + 1 + rot) % 54 : 2 * i + 1;
      a[i % NW][h] = __builtin_amdgcn_raw_buffer_load_b128(rsH, va[i >= 27][h], step_off(s0) + kh * (step_off(s1) - step_off(s0)), 16);
      SB;
    };
    lds_wait(L_GO);
    unsigned go2 = 0;
    STAMP(8);
    static_for<0, W0>([&](auto I) { lda(I, IC<0>{}); lda(I, IC<1>{}); lda(I, IC<2>{}); });
#pragma unroll
    for (int t = 0; t < 4; ++t) { bw[0][t][0] = rd1(0, t, 0); bw[0][t][1] = rd1(0, t, 1); }
    static_for<0, 37>([&](auto I) {
      constexpr int i = I.v;
      constexpr int lim = i + NW - 1;
      constexpr int nxt = i <= OI ? (W0 + 2 * i < lim ? W0 + 2 * i : lim) : (27 + OB0 + 2 * (i - OI - 1) < lim ? 27 + OB0 + 2 * (i - OI - 1) : lim);
      constexpr int end = i <= OI ? 27 : 37;
      constexpr int l0 = nxt < end ? nxt : -1, l1 = (nxt + 1 <= lim && nxt + 1 < end) ? nxt + 1 : -1;
      SB;
      static_for<0, 12>([&](auto E) {
        constexpr int e = E.v, t = e / 3, h = e % 3;
        const u32x4 bf = cat2(bw[i & 1][t][0], bw[i & 1][t][1]);
        if constexpr (i == 0 || i == 27) mfma_z(acc[i >= 27][2 - h][t], bf, a[i % NW][2 - h]); else mfma_ip(acc[i >= 27][2 - h][t], bf, a[i % NW][2 - h]);
        SB;
        if constexpr (i == 4 && e == 2) go2 = lds_get(L_GO + 4);
        if constexpr ((i == 27 || i == 28) && (e & 1)) put(IC<0>{}, IC<(i - 27) * 6 + e / 2>{});
        if constexpr (i == 36 && e >= 8) put(IC<1>{}, IC<e - 8>{});
        if constexpr (i < 36 && h < 2) bw[(i + 1) & 1][t][h] = rd1(i + 1, t, h);
        if constexpr (l1 < 0) { if constexpr (h == 2 && t < 3 && l0 >= 0) lda(IC<l0>{}, IC<t>{}); }
        else if constexpr (e & 1) lda(IC<(e < 6 ? l0 : l1)>{}, IC<(e >> 1) % 3>{});
        SB;
      });
      if constexpr (i == 29) { asm volatile("s_waitcnt lgkmcnt(8)" ::: "memory"); bar(); }
      if constexpr (i == 4) { if (__builtin_expect(!rfl(go2), 0)) lds_wait(L_GO + 4); asm volatile("" ::: "memory"); STAMP(17); }
      if constexpr (i > 4 && i < 27 && (i % 9 == 4 || i % 9 == 8)) {
        bar();
        if constexpr (i == 26 && OI == 26) static_for<27, 27 + OB0>([&](auto J) { lda(J, IC<0>{}); lda(J, IC<1>{}); lda(J, IC<2>{}); });
        STAMP(17 + (2 * i) / 9);
      }
    });
    STAMP(24);
    static_for<4, 12>([&](auto E) { if constexpr (E.v == 8) asm volatile("s_nop 3" ::: "memory"); put(IC<1>{}, E); SB; });
    lbar();
    STAMP(5);
    STAMP_END;
  }
}

extern "C" void kernel_launch(void* const* d_in, const int* in_sizes, int n_in, void* d_out, int out_size, void* d_ws,
                              size_t ws_size, hipStream_t stream) {
  Params p;
  p.x = d_in[0]; p.wqkv = d_in[1]; p.wo = d_in[2]; p.wg = d_in[4]; p.wu = d_in[5]; p.wd = d_in[6];
  p.out = d_out; p.ws = (char*)d_ws;
  p.wsal = ws_size >= (64ull << 20) + WS_END ? (64ull << 20) - 1 : 0;
  fused_block<<<256, 512, 0, stream>>>(p);
}
```

```cpp
#include <hip/hip_runtime.h>
#include <stdint.h>

typedef __attribute__((ext_vector_type(8))) __bf16 bf16x8;
typedef __attribute__((ext_vector_type(4))) __bf16 bf16x4;
typedef __attribute__((ext_vector_type(2))) __bf16 bf16x2;
typedef __attribute__((ext_vector_type(4))) float f32x4;
typedef __attribute__((ext_vector_type(2))) float f32x2;
typedef __attribute__((ext_vector_type(4))) unsigned u32x4;
typedef __attribute__((ext_vector_type(2))) unsigned u32x2;
typedef __amdgpu_buffer_rsrc_t rsrc_t;
#define LAS __attribute__((address_space(3)))
#define GAS __attribute__((address_space(1)))
#define DI __device__ __forceinline__
#define RLX_AGENT __ATOMIC_RELAXED, __HIP_MEMORY_SCOPE_AGENT
typedef GAS unsigned gu32;
typedef GAS unsigned long long gu64;

constexpr int S = 2048, D = 640, F = 1728;
constexpr int NCOL = 5376;
constexpr int NSTEP = 74;
constexpr unsigned WS_EPOCH = 0;
#define FS 128
constexpr unsigned WS_FLAGH = 768u << 10;
constexpr unsigned WS_FLAGO = WS_FLAGH + 8 * 32 * FS;
constexpr unsigned WS_FLAGKV = WS_FLAGO + 8 * 32 * FS;
constexpr unsigned WS_STAT = 4096;
constexpr unsigned WS_CSUM = WS_STAT + S * 8;
constexpr unsigned WS_KVP = WS_CSUM + 3 * NCOL * 8;
constexpr unsigned WS_HA = 1u << 20;
constexpr unsigned RBSZ = 16 * NSTEP * 1024;
constexpr unsigned step_off(unsigned st) { return st < 54 ? (st / 3) * 49152 + (st % 3) * 1024 : 18 * 49152 + ((st - 54) / 5) * 81920 + ((st - 54) % 5) * 1024; }
constexpr unsigned WS_DBG = 12u << 20;
constexpr unsigned WS_END = 13u << 20;
constexpr unsigned L_B = 65536;
constexpr unsigned L_SCR = 147456;
constexpr unsigned L_MISC = 159744;
constexpr unsigned L_GO = L_MISC + 3072;
constexpr unsigned SPIN_CAP = 1u << 18;
#define HSLEEP 4
#define HSPLIT 64
#define PPIPE 0
#define STA 0
#define STB 2
#define SNAKE 0
#define EARLY1 1
#define ROTW 1
#define NPRE 12
#define PUTB 0
#define OBN 6
#define PSPLIT 48
#define WARM 1
#define XK 2
#define GRP 1
#define GPRIO 0
#define HPRIO 0
#define PSLEEP 2
#define P2NW 7
#define P2W0 6
#define WSLEEP 0
#define ROTM 18
#define NA1 2
#define O_BAR 5
#define PRIO_MODE 0
#define DMA_SPACE_A T
#define DMA_SPACE_B (4 * T / 3)

struct Params {
  const void* x; const void* wqkv; const void* wo; const void* wg; const void* wu; const void* wd;
  void* out; char* ws;
  unsigned long long wsal;
};

__shared__ __attribute__((aligned(16))) char smem[163840];

DI float sadd(float a, float b) { float o; asm("v_add_f32 %0, %1, %2" : "=v"(o) : "v"(a), "v"(b)); return o; }
DI float smul(float a, float b) { float o; asm("v_mul_f32 %0, %1, %2" : "=v"(o) : "v"(a), "v"(b)); return o; }
DI float sfma(float a, float b, float c) { float o; asm("v_fma_f32 %0, %1, %2, %3" : "=v"(o) : "v"(a), "v"(b), "v"(c)); return o; }
DI unsigned bsel(unsigned m, unsigned a, unsigned b) { unsigned o; asm("v_bfi_b32 %0, %1, %2, %3" : "=v"(o) : "v"(m), "v"(a), "v"(b)); return o; }
DI float bself(unsigned m, float a, float b) { return __builtin_bit_cast(float, bsel(m, __builtin_bit_cast(unsigned, a), __builtin_bit_cast(unsigned, b))); }
DI bool all64(bool c) { return __builtin_amdgcn_ballot_w64(!c) == 0; }
DI float shfl_x(float v, unsigned lane, unsigned m) {
  return __builtin_bit_cast(float, __builtin_amdgcn_ds_bpermute((int)((lane ^ m) << 2), __builtin_bit_cast(int, v)));
}
DI unsigned umin(unsigned a, unsigned b) { return a < b ? a : b; }
DI unsigned rfl(unsigned v) { return __builtin_amdgcn_readfirstlane(v); }
DI rsrc_t mk_rsrc(const void* p, unsigned bytes) { return __builtin_amdgcn_make_buffer_rsrc((void*)p, (short)0, (int)bytes, 0x00020000); }
DI void dma16(unsigned ldsdst, unsigned voff, rsrc_t rs, unsigned soff) {
  asm volatile("s_mov_b32 m0, %0\n\ts_nop 0\n\tbuffer_load_dwordx4 %1, %2, %3 offen lds" ::"s"(ldsdst), "v"(voff), "s"(rs), "s"(soff) : "memory");
}
template <int N> DI void wait_vm() { asm volatile("s_waitcnt vmcnt(%0)" ::"i"(N) : "memory"); }
DI void drain_vm() { wait_vm<0>(); __builtin_amdgcn_s_waitcnt(0x0F70); }
DI void wait_lgkm0() { asm volatile("s_waitcnt lgkmcnt(0)" ::: "memory"); }
DI void bar() { asm volatile("" ::: "memory"); __builtin_amdgcn_s_barrier(); asm volatile("" ::: "memory"); }
DI void lbar() { wait_lgkm0(); bar(); }
template <class V> DI V lds_ld(unsigned a) { return *(LAS V*)((LAS char*)smem + a); }
template <class V> DI void lds_st(unsigned a, V v) { *(LAS V*)((LAS char*)smem + a) = v; }
DI bf16x4 lds_tr(unsigned a) { return __builtin_amdgcn_ds_read_tr16_b64_v4bf16((LAS bf16x4*)((LAS char*)smem + a)); }
DI bf16x8 cat(bf16x4 a, bf16x4 b) { return __builtin_shufflevector(a, b, 0, 1, 2, 3, 4, 5, 6, 7); }
DI f32x4 mfma(bf16x8 a, bf16x8 b, f32x4 c) { return __builtin_amdgcn_mfma_f32_16x16x32_bf16(a, b, c, 0, 0, 0); }
DI unsigned pk2(float a, float b) { f32x2 v = {a, b}; return __builtin_bit_cast(unsigned, __builtin_convertvector(v, bf16x2)); }
DI u32x2 pk4(f32x4 v) { u32x2 r = {pk2(v[0], v[1]), pk2(v[2], v[3])}; return r; }
DI bf16x8 frag(u32x2 lo, u32x2 hi) { u32x4 v = {lo[0], lo[1], hi[0], hi[1]}; return __builtin_bit_cast(bf16x8, v); }
DI float bflo(unsigned v) { return __builtin_bit_cast(float, v << 16); }
DI float bfhi(unsigned v) { return __builtin_bit_cast(float, v & 0xffff0000u); }
DI float rbf(float v) { return bflo(pk2(v, 0.f)); }
DI float relu2(float v) { float o; asm("v_add_f32 %0, %1, |%1|" : "=v"(o) : "v"(v)); return o; }
DI f32x4 phi4(f32x4 u, f32x4 rl, f32x4 rh) {
  const f32x4 l = u * rl;
  f32x4 e, a;
#pragma unroll
  for (int j = 0; j < 4; ++j) {
    e[j] = __builtin_amdgcn_fmed3f(__builtin_amdgcn_exp2f(l[j]), 0.f, 1.f);
    a[j] = relu2(u[j]);
  }
  return a * rh + e + 1e-6f;
}
DI f32x4 splat(float v) { return f32x4{v, v, v, v}; }
template <int CTRL> DI float dpp_add(float v) {
  return v + __builtin_bit_cast(float, __builtin_amdgcn_update_dpp(0, __builtin_bit_cast(int, v), CTRL, 0xf, 0xf, true));
}
DI float dot2(unsigned a, unsigned b, float c) {
  return __builtin_amdgcn_fdot2_f32_bf16(__builtin_bit_cast(bf16x2, a), __builtin_bit_cast(bf16x2, b), c, false);
}
DI u32x4 ldtr2(unsigned a, unsigned d) { return __builtin_bit_cast(u32x4, cat(lds_tr(a), lds_tr(a + d))); }
DI void mfma_ip(f32x4& c, const u32x4& a, const u32x4& b) { asm volatile("v_mfma_f32_16x16x32_bf16 %0, %1, %2, %0" : "+v"(c) : "v"(a), "v"(b)); }
DI void mfma_c(f32x4& d, const u32x4& a, const u32x4& b, const f32x4& c) { asm volatile("v_mfma_f32_16x16x32_bf16 %0, %1, %2, %3" : "=&v"(d) : "v"(a), "v"(b), "v"(c)); }
DI void mfma_z(f32x4& c, const u32x4& a, const u32x4& b) { asm volatile("v_mfma_f32_16x16x32_bf16 %0, %1, %2, 0" : "=&v"(c) : "v"(a), "v"(b)); }
template <int N> DI void acc_fence(f32x4* c) {
  asm volatile("s_nop 7" ::: "memory");
#pragma unroll
  for (int i = 0; i < N; ++i) asm volatile("" : "+v"(c[i]));
}
DI u32x4 cat2(u32x2 a, u32x2 b) { return u32x4{a[0], a[1], b[0], b[1]}; }
DI u32x2 ldtr1(unsigned a) { return __builtin_bit_cast(u32x2, lds_tr(a)); }
template <int IMM> DI void set_m0i(unsigned v) { asm volatile("s_add_i32 m0, %0, %1" ::"s"(v), "i"(IMM) : "memory", "scc"); }
template <int OFF> DI void dma_goi(unsigned voff, rsrc_t rs, unsigned soff) {
  asm volatile("buffer_load_dwordx4 %0, %1, %2 offen offset:%3 lds" ::"v"(voff), "s"(rs), "s"(soff), "i"(OFF) : "memory");
}
DI void set_m0(unsigned v) { asm volatile("s_mov_b32 m0, %0" ::"s"(v) : "memory"); }
DI void dma_go(unsigned voff, rsrc_t rs, unsigned soff) {
  asm volatile("buffer_load_dwordx4 %0, %1, %2 offen lds" ::"v"(voff), "s"(rs), "s"(soff) : "memory");
}
DI f32x4 mfma_u(u32x4 a, u32x4 b, f32x4 c) { return mfma(__builtin_bit_cast(bf16x8, a), __builtin_bit_cast(bf16x8, b), c); }
#define STAMP_DECL
#define STAMP_ARG
#define STAMP_PASS
#define STAMPX(c, i)
#define STAMP_END
#define STAMP(i) STAMPX(__builtin_amdgcn_workitem_id_x() == 0, i)

DI unsigned poll_flags(gu32* word, bool active, unsigned tag) {
  unsigned v = 0;
#pragma unroll 1
  for (unsigned spins = 0; spins < SPIN_CAP; ++spins) {
    v = __hip_atomic_load(word, RLX_AGENT);
    if (all64(!active || v == tag)) break;
    if (PSLEEP) __builtin_amdgcn_s_sleep(PSLEEP);
  }
  asm volatile("" ::: "memory");
  return v;
}

DI void lds_set(unsigned a) { __hip_atomic_store((LAS unsigned*)smem + a / 4, 1u, __ATOMIC_RELAXED, __HIP_MEMORY_SCOPE_WORKGROUP); }
DI unsigned lds_get(unsigned a) { return __hip_atomic_load((LAS unsigned*)smem + a / 4, __ATOMIC_RELAXED, __HIP_MEMORY_SCOPE_WORKGROUP); }
DI void lds_wait(unsigned a) {
#pragma unroll 1
  for (unsigned spins = 0; spins < (SPIN_CAP << 4); ++spins) {
    if (rfl(lds_get(a))) break;
    __builtin_amdgcn_s_sleep(1);
  }
  asm volatile("" ::: "memory");
}

template <int I> struct IC { static constexpr int v = I; };
template <int B, int E, class Fn> DI void static_for(Fn&& f) {
  if constexpr (B < E) { f(IC<B>{}); static_for<B + 1, E>(f); }
}

enum { J_MLP = 0, J_KV = 1, J_Q = 2, J_SPARE = 3 };

struct Dma {
  rsrc_t rsA, rsB;
  unsigned vA0, vA1, vB0, vB1, vB2;
  unsigned sA[4], sB, advB;
  unsigned wa, wq;
  unsigned mA, mB, mW;
};

constexpr unsigned a_slot(unsigned s3) { return s3 == 0 ? 126976u : (s3 - 1) * 32768u; }
#define SB __builtin_amdgcn_sched_barrier(0)

template <int T> DI unsigned bofs(const unsigned (&boff)[T], int t) { return T == 6 && t >= 3 ? boff[t - 3] + 6144 : boff[t]; }
template <int T, bool TR, int KS, class Extra>
DI void kstep(f32x4 (&acc)[4][T], const u32x4 (&ac)[4], const u32x2 (&bc)[T][2], u32x4 (&an)[4], u32x2 (&bn)[T][2],
              const unsigned (&aoff)[4], const unsigned (&boff)[T], const Dma& d, Extra&& extra) {
  constexpr int s = KS >> 1, KIND = KS & 1, NP = KIND == 0 ? 4 : 3, ST = KIND == 0 ? (STA ? STA : (4 * T - 1) / NP) : (STB ? STB : (4 * T - 1) / NP);
  constexpr int sn = (KS + 1) >> 1;
  constexpr unsigned asl = a_slot(sn % 3), bsl = ((KS + 1) % 5) * 12288u;
  static_for<0, 4 * T>([&](auto E) {
    constexpr int e = E.v, t = e >> 2, i = (SNAKE && (t & 1)) ? 3 - (e & 3) : (e & 3);
    const u32x4 bf = cat2(bc[t][0], bc[t][1]);
    if constexpr (KS == 0) { if (TR) mfma_z(acc[i][t], bf, ac[i]); else mfma_z(acc[i][t], ac[i], bf); }
    else { if (TR) mfma_ip(acc[i][t], bf, ac[i]); else mfma_ip(acc[i][t], ac[i], bf); }
    SB;
    if constexpr (KS < 19) {
      if constexpr (e < 4) an[e] = lds_ld<u32x4>(aoff[((KS + 1) & 1) + (sn % 3 == 0 ? 2 : 0)] + (sn % 3 == 0 ? 0u : asl) + e * 2048);
      else if constexpr (e < 4 + 2 * T) bn[(e - 4) >> 1][e & 1] = ldtr1(bofs<T>(boff, (e - 4) >> 1) + bsl + (e & 1) * 768);
    }
    constexpr int pc = e / ST;
    if constexpr (KIND == 0) {
      constexpr int adv = s < 8 ? (s + 2) * 128 : 0, dst = (int)a_slot((s + 2) % 3) + pc * 1024, off = dst >= adv ? adv : 0;
      if constexpr (pc < NP && e % ST == ST - 2) set_m0i<dst - off>(d.mA);
      if constexpr (pc < NP && e % ST == ST - 1) dma_goi<off>((pc & 1) ? d.vA1 : d.vA0, d.rsA, d.sA[pc] + (adv - off));
    } else {
      constexpr int h0 = (2 * s) % 5;
      if constexpr (pc < NP && e % ST == ST - 2) set_m0i<(h0 == 4 ? 0 : h0 * 12288) + pc * 1024>(h0 == 4 ? d.mW : d.mB);
      if constexpr (pc < NP && e % ST == ST - 1) dma_goi<0>(pc == 0 ? d.vB0 : (pc == 1 ? d.vB1 : d.vB2), d.rsB, d.sB);
    }
    extra(E);
    SB;
  });
}

struct RowStat { u32x4 xa; unsigned xb, row; float s, q; };
DI void row_load(RowStat& R, rsrc_t rsX, unsigned lane) {
  asm volatile("buffer_load_dwordx4 %0, %2, %4, %5 offen\n\tbuffer_load_dword %1, %3, %4, %5 offen offset:1024"
               : "=&v"(R.xa), "=&v"(R.xb) : "v"(lane * 16), "v"(lane * 4), "s"(rsX), "s"(R.row * 1280) : "memory");
}
template <int K> DI void row_step(RowStat& R, unsigned lane) {
  if constexpr (K < 5) {
    if constexpr (K == 0) { R.s = 0.f; R.q = 0.f; }
    const unsigned v = K < 4 ? R.xa[K & 3] : R.xb;
    asm volatile("v_dot2c_f32_bf16 %0, %2, %3\n\tv_dot2c_f32_bf16 %1, %2, %2" : "+v"(R.s), "+v"(R.q) : "v"(v), "v"(0x3f803f80u));
  } else if constexpr (K < 11) {
    constexpr int C = K == 5 ? 0xB1 : (K == 6 ? 0x4E : (K == 7 ? 0x141 : (K == 8 ? 0x140 : (K == 9 ? 0x142 : 0x143))));
    if constexpr (K == 5) asm volatile("s_nop 2" : "+v"(R.s), "+v"(R.q));
    R.s = dpp_add<C>(R.s); R.q = dpp_add<C>(R.q);
  } else if constexpr (K == 11) {
    R.s *= 1.0f / D;
    R.q = __builtin_fmaxf(R.q * (1.0f / D) - R.s * R.s, 0.f);
  } else if constexpr (K == 12) {
    R.q = __builtin_amdgcn_sqrtf(rbf(rbf(R.q) + 1e-5f));
  } else {
    lds_st<unsigned>(lane == 63 ? L_GO + 32 + (R.row & 7) * 4 : L_GO + 64, pk2(R.s, R.q));
  }
  if constexpr (K >= 5 && K < 13) asm volatile("" : "+v"(R.s), "+v"(R.q));
}
DI void row_publish(const RowStat& R, char* ws, unsigned tag, unsigned tid) {
  if (tid < 8) __hip_atomic_store((gu64*)(ws + WS_STAT) + R.row + tid, ((unsigned long long)tag << 32) | lds_ld<unsigned>(L_GO + 32 + tid * 4), RLX_AGENT);
}

struct Gran { u32x2 g0, g1, g2; unsigned off, step; };
DI void gran_issue(Gran& G, rsrc_t rs) {
  asm volatile("buffer_load_dwordx2 %0, %3, %4, 0 offen sc1\n\tbuffer_load_dwordx2 %1, %3, %4, %5 offen sc1\n\t"
               "buffer_load_dwordx2 %2, %3, %4, %6 offen sc1"
               : "=&v"(G.g0), "=&v"(G.g1), "=&v"(G.g2) : "v"(G.off), "s"(rs), "s"(G.step), "s"(2 * G.step) : "memory");
}
DI void gran_load(Gran& G, rsrc_t rs) {
  asm volatile("buffer_load_dwordx2 %0, %3, %4, 0 offen sc1\n\tbuffer_load_dwordx2 %1, %3, %4, %5 offen sc1\n\t"
               "buffer_load_dwordx2 %2, %3, %4, %6 offen sc1\n\ts_waitcnt vmcnt(0)"
               : "=&v"(G.g0), "=&v"(G.g1), "=&v"(G.g2) : "v"(G.off), "s"(rs), "s"(G.step), "s"(2 * G.step) : "memory");
}

DI unsigned urow(unsigned pos, unsigned rot) { const unsigned u = pos + rot; return (u >= 54 ? u - 54 : u) * 40960u; }

constexpr unsigned wo_tile(unsigned v) { return (v / 5) * 10 + (v % 5 == 4 ? 4 : (v % 5 < 2 ? 2 * (v % 5) : 2 * (v % 5) + 1)); }
struct W2 { rsrc_t wd, wo; unsigned rot, col; };
DI unsigned w2_off(const W2& W, int u, unsigned lw) {
  if (u < 54) return urow(u, W.rot) + lw * 10240u + W.col;
  return wo_tile(u - 54) * 20480u + (lw >> 1) * ((u - 54) % 5 == 4 ? 102400u : 20480u) + (lw & 1) * 10240u + W.col;
}
DI u32x4 w2_load(const W2& W, int u, unsigned lw, unsigned voff) {
  return __builtin_amdgcn_raw_buffer_load_b128(u < 54 ? W.wd : W.wo, voff, w2_off(W, u, lw), 0);
}

template <int T, bool TR, bool PF, bool LASTA, class Pre>
DI void main_loop(Pre&& pre, RowStat& R, char* ws, f32x4 (&acc)[4][T], unsigned (&boff)[T], Dma& d, unsigned w, unsigned lane, unsigned tid,
                  unsigned epoch, unsigned& tag, unsigned rb, unsigned ccol, rsrc_t rsB2, rsrc_t rsWS, unsigned cb, unsigned rot, u32x4 (&pre2)[36] STAMP_ARG) {
  const unsigned g = lane >> 4, r = lane & 15, m = w & 3;
  unsigned aoff[4];
  aoff[0] = (64 * m + r) * 128 + ((g ^ (r >> 1)) << 4);
  aoff[1] = aoff[0] ^ 64;
  aoff[2] = aoff[0] + a_slot(0); aoff[3] = aoff[1] + a_slot(0);
  const unsigned wq = d.wq, wa = d.wa;
  asm volatile("" : "+v"(aoff[0]), "+v"(aoff[1]), "+v"(aoff[2]), "+v"(aoff[3]), "+v"(boff[0]), "+v"(boff[1]), "+v"(boff[2]));
  if (T == 5) asm volatile("" : "+v"(boff[3]), "+v"(boff[4]));
  Gran G;
  unsigned v2, vb2, vpf, tA[3], tB[3];
  row_load(R, d.rsA, lane);
  {
    asm volatile("s_waitcnt vmcnt(%2) lgkmcnt(0)\n\tv_readfirstlane_b32 %0, %1\n\ts_nop 3" : "=s"(tag) : "v"(epoch), "i"(5 + NA1) : "memory");
    tag = rfl(tag) + 1;
    bar();
  }
  STAMP(1);
  u32x4 a0[4], a1[4];
  u32x2 b0[T][2], b1[T][2];
#pragma unroll
  for (int i = 0; i < 4; ++i) a0[i] = lds_ld<u32x4>(aoff[2] + i * 2048);
#pragma unroll
  for (int t = 0; t < T; ++t) { b0[t][0] = ldtr1(bofs<T>(boff, t)); b0[t][1] = ldtr1(bofs<T>(boff, t) + 768); }
  SB;
#pragma unroll
  for (int i = NA1; i < 4; ++i) dma16(a_slot(1) + d.mA + i * 1024, (i & 1) ? d.vA1 : d.vA0, d.rsA, d.sA[i] + 128);
  const bool y = w >= 4;
  bool ok = true;
  auto ln_store = [&] {
    const float v0 = y ? -bflo(G.g0[0]) : __builtin_bit_cast(float, G.g0[0]) + __builtin_bit_cast(float, G.g1[0]) + __builtin_bit_cast(float, G.g2[0]);
    const unsigned la = L_MISC + (y ? tid * 4 - 1024 : 2048 + tid * 4);
    lds_st<float>(la, v0);
    lds_st<float>(y ? la + 1024 : L_GO + 64 + (lane & 7) * 4, __builtin_amdgcn_rcpf(bfhi(G.g0[0])));
  };
  static_for<0, 10>([&](auto S_) {
    constexpr int s = S_.v, L = 4 * T;
    constexpr int NX = (PF && s >= 1 && 2 * s <= NPRE) ? 2 : 0;
    kstep<T, TR, 2 * s>(acc, a0, b0, a1, b1, aoff, boff, d, [&](auto E) {
      constexpr int e = E.v;
      if constexpr (s == 2) {
        if constexpr (e == 2) { vb2 = v2 + (3 * wq) * 10240u; asm volatile("" : "+v"(vb2)); }
        if constexpr (e == 5) { G.off = w < 4 ? WS_CSUM + ccol * 8 : WS_STAT + (rb * 256 + tid - 256) * 8; asm volatile("" : "+v"(G.off)); }
        if constexpr (e == 8) { G.step = w < 4 ? NCOL * 8 : 0; tA[0] = urow(wa, rot) + cb * 128u; asm volatile("" : "+s"(G.step), "+s"(tA[0])); }
        if constexpr (e == 10) { tA[1] = urow(8 + wa, rot) + cb * 128u; asm volatile("" : "+s"(tA[1])); }
        if constexpr (e == 12) { tA[2] = urow(31 + wa, rot) + cb * 128u; asm volatile("" : "+s"(tA[2])); }
        if constexpr (e == 14) {
          tB[0] = urow(16, rot) + cb * 128u; tB[1] = urow(w < 4 ? 19u : 22u, rot) + cb * 128u; tB[2] = urow(w < 4 ? 25u : 28u, rot) + cb * 128u;
          asm volatile("" : "+s"(tB[0]), "+s"(tB[1]), "+s"(tB[2]));
        }
      }
      if constexpr (s == (TR ? 1 : 3) && e == 2) pre();
      if constexpr (NX && e >= L - 2) {
        constexpr int u = 2 * (s - 1) + e - (L - 2);
        pre2[u] = __builtin_amdgcn_raw_buffer_load_b128(rsB2, vpf, urow(36 + u, rot) + (w & 3) * 10240u + cb * 128u, 0);
      }
      if constexpr (s < 7) { if constexpr (e == L - 3) d.sB += s == 0 ? d.advB * (1 + (w >> 2)) : d.advB; }
      else if constexpr (s == 7) {
        if constexpr (e == L - 6) d.vB0 = y ? vb2 : d.vB0;
        if constexpr (e == L - 4) d.vB1 = y ? vb2 + 10240u : d.vB1;
        if constexpr (e == L - 3) d.vB2 = y ? vb2 + 20480u : d.vB2;
        if constexpr (e == L - 2) d.sB = y ? tB[0] : d.sB + d.advB;
        if constexpr (e == L - 1) d.rsB = y ? rsB2 : d.rsB;
      } else {
        if constexpr (e == L - 3) { d.sB = tB[s - 7]; d.rsB = rsB2; d.vB0 = vb2; d.vB1 = vb2 + 10240u; d.vB2 = vb2 + 20480u; }
      }
      if constexpr (s == 9 && e == L - 4) {
        asm volatile("s_waitcnt vmcnt(7)" : "+v"(G.g0), "+v"(G.g1), "+v"(G.g2)::"memory");
        ok = all64(G.g0[1] == tag && G.g1[1] == tag && G.g2[1] == tag);
      }
      if constexpr (s == 9 && e == L - 3) ln_store();
    });
    if constexpr (s == 0) {
      wait_vm<EARLY1 ? 8 - NA1 : 11>();
      static_for<0, 14>([&](auto K) { row_step<K.v>(R, lane); });
    }
    if constexpr (s == 9) {
      if (__builtin_expect(!ok, 0)) {
#pragma unroll 1
        for (unsigned spins = 0; spins < SPIN_CAP; ++spins) {
          __builtin_amdgcn_s_sleep(2);
          gran_load(G, rsWS);
          if (all64(G.g0[1] == tag && G.g1[1] == tag && G.g2[1] == tag)) break;
        }
        ln_store();
      }
    }
    wait_lgkm0();
    if constexpr (s < 9) wait_vm<4 + NX>();
    bar();
    if constexpr (s == 0) { STAMP(12); }
    SB;
    kstep<T, TR, 2 * s + 1>(acc, a1, b1, a0, b0, aoff, boff, d, [&](auto E) {
      constexpr int e = E.v;
      if constexpr (s == 0 && e == 6) {
        if (y) {
          const unsigned so = d.sB - d.advB;
          dma16(d.mW + 49152, d.vB0, d.rsB, so); dma16(d.mW + 49152 + 1024, d.vB1, d.rsB, so); dma16(d.mW + 49152 + 2048, d.vB2, d.rsB, so);
        }
      }
      if constexpr (s == 0 && e == L - 4) {
        v2 = (lane >> 3) * 1280 + (((lane & 7) ^ (((lane >> 4) & 3) << 1)) << 4);
        vpf = y ? v2 : 0x7ffffff0u;
        asm volatile("" : "+v"(v2), "+v"(vpf));
      }
      if constexpr (s == 0 && e == L - 6) row_publish(R, ws, tag, tid);
      if constexpr (s == 8 && e == 0) gran_issue(G, rsWS);
      if constexpr (s >= 7 && s < 9 && e >= L - 4) {
        d.rsA = rsB2; d.vA0 = v2; d.vA1 = v2;
        d.sA[e - (L - 4)] = tA[s - 7] + (e - (L - 4)) * 10240;
      }
      if constexpr (s == 9 && LASTA && e == 7) {
        if (wa < 5) {
#pragma unroll
          for (int i = 0; i < 4; ++i) dma16(a_slot(0) + d.mA + i * 1024, v2, rsB2, tA[2] + i * 10240);
        }
      }
    });
    if constexpr (s == 8) { STAMP(13); }
    if constexpr (s == 2) { STAMP(26); }
    if constexpr (s == 6) { STAMP(27); }
  });
  acc_fence<4 * T>(&acc[0][0]);
  STAMP(14);
}

DI void st16(u32x4 v, rsrc_t rs, unsigned voff, unsigned soff) {
  __builtin_amdgcn_raw_buffer_store_b128(v, rs, voff, soff, 16);
  asm volatile("s_nop 1" ::"v"(v) : "memory");
}
DI void publish_flag(const Params& p, unsigned flag_off, unsigned tag, unsigned tid) {
  drain_vm();
  bar();
  if (tid == 0) __hip_atomic_store((gu32*)(p.ws + flag_off), tag, RLX_AGENT);
}
DI void st_frag(rsrc_t rs, unsigned lane, unsigned off, u32x2 lo, u32x2 hi) { st16(cat2(lo, hi), rs, lane * 16, off); }
DI void shared_send(unsigned w, unsigned lane, const u32x2 (&sh)[4]) {
  if (w >= 4) lds_st<u32x4>(L_SCR + w * 1024 + lane * 16, cat2(sh[0], sh[1]));
  else lds_st<u32x4>(L_SCR + w * 1024 + lane * 16, cat2(sh[2], sh[3]));
  asm volatile("" ::: "memory");
}
DI void shared_recv(rsrc_t rs, unsigned w, unsigned lane, unsigned off, unsigned str, const u32x2 (&sh)[4]) {
  lbar();
  u32x4 rcv = lds_ld<u32x4>(L_SCR + (w ^ 4) * 1024 + lane * 16);
  u32x2 r0 = {rcv[0], rcv[1]}, r1 = {rcv[2], rcv[3]};
  if (w >= 4) { st_frag(rs, lane, off + 2 * str, r0, sh[2]); st_frag(rs, lane, off + 3 * str, r1, sh[3]); }
  else        { st_frag(rs, lane, off, sh[0], r0); st_frag(rs, lane, off + str, sh[1], r1); }
}

__global__ __launch_bounds__(512, 2) void fused_block(Params p) {
  p.ws = (char*)(((unsigned long long)p.ws + p.wsal) & ~p.wsal);
  unsigned tid = __builtin_amdgcn_workitem_id_x(), lane = tid & 63;
  const unsigned w = rfl(tid >> 6);
  const unsigned b = __builtin_amdgcn_workgroup_id_x(), xcd = b & 7, slot = b >> 3;
  const unsigned rb = 4 * (xcd & 1) + (slot & 3), jq = xcd >> 1, ji = slot >> 2;
  const unsigned spi = (xcd - 4) * 4 + (slot & 3);
  const unsigned long long jl0 = (jq & 1) ? 0x4123220908070605ull : 0x4021200403020100ull, jl1 = (jq & 1) ? 0x4327266011100f0eull : 0x422524600d0c0b0aull;
  const unsigned jb = (unsigned)(((jq & 2) ? jl1 : jl0) >> (8 * ji)) & 0xff, jtype = jb >> 5, jidx = jb & 31;
  STAMP_DECL;
  STAMP(0);

  Dma d;
  RowStat rs_;
  const bool mlp = jtype == J_MLP;
  const unsigned pitch = mlp ? 3456u : 3840u, wq = (w + (ROTW ? slot : 0)) & 3, wa = (w + (ROTW ? ji : 0)) & 7;
  d.wa = wa; d.wq = wq;
  const rsrc_t rsX = mk_rsrc(p.x, S * D * 2);
  d.vA0 = (lane >> 3) * 1280 + (((lane & 7) ^ (lane >> 4)) << 4); d.vA1 = d.vA0 ^ 64;
  d.rsA = rsX; d.mA = wa * 4096u;
#pragma unroll
  for (int i = 0; i < 4; ++i) d.sA[i] = (rb * 256 + 32 * wa + 8 * i) * 1280;
  unsigned epoch, tag;
  asm volatile("s_nop 4" :: "s"(rsX) : "memory");
  STAMP(6);
#pragma unroll
  for (int i = 0; i < 4; ++i) dma16(a_slot(0) + d.mA + i * 1024, (i & 1) ? d.vA1 : d.vA0, d.rsA, d.sA[i]);
  asm volatile("global_load_dword %0, %1, %2 sc1" : "=&v"(epoch) : "v"(0u), "s"(p.ws) : "memory");
  SB;
  {
    unsigned vb[3];
    const unsigned lim = mlp ? 176u : 144u;
    {
      const unsigned r0 = (lane * 43) >> 9, p0 = lane - 12 * r0;
      const bool w1 = p0 >= 8, w2 = p0 >= 4;
      const unsigned r1 = r0 + (w1 ? 6 : 5), p1 = p0 + (w1 ? -8 : 4), r2 = r0 + (w2 ? 11 : 10), p2 = p0 + (w2 ? -4 : 8);
      vb[0] = r0 * pitch + umin(p0 * 16, lim);
      vb[1] = r1 * pitch + umin((p1 * 16) ^ ((r1 & 8) << 2), lim);
      vb[2] = r2 * pitch + umin((p2 * 16) ^ 32, lim);
    }
    d.vB0 = vb[0]; d.vB1 = vb[1]; d.vB2 = vb[2];
    const unsigned mh = wq >> 1;
    const void* bw = mlp ? (mh ? p.wu : p.wg) : p.wqkv;
    const unsigned cj = mlp ? 192u : 160u * jtype, cm = mlp ? 0u : (jtype == J_KV ? 1280u : 160u), c0 = jtype == J_KV ? 1280u : 0u;
    d.rsB = mk_rsrc(bw, D * pitch);
    d.sB = (16 * (wq & 1) + (w < 4 ? 32 : 0)) * pitch + c0 + cj * jidx + cm * mh; d.advB = 64 * pitch;
    d.mB = L_B + wq * 3072u + (w < 4 ? 0u : 12288u); d.mW = L_B + wq * 3072u + (w < 4 ? 49152u : 0u);
    const unsigned bdst = L_B + (w < 4 ? 12288u : 0u) + wq * 3072u;
    dma16(bdst, d.vB0, d.rsB, d.sB); dma16(bdst + 1024, d.vB1, d.rsB, d.sB); dma16(bdst + 2048, d.vB2, d.rsB, d.sB);
    rs_.row = rfl(b * 8 + w);
    d.sB += d.advB;
    dma16(bdst + 24576, d.vB0, d.rsB, d.sB); dma16(bdst + 24576 + 1024, d.vB1, d.rsB, d.sB); dma16(bdst + 24576 + 2048, d.vB2, d.rsB, d.sB);
#pragma unroll
    for (int i = 0; i < NA1; ++i) dma16(a_slot(1) + d.mA + i * 1024, (i & 1) ? d.vA1 : d.vA0, d.rsA, d.sA[i] + 128);
  }
  STAMP(10);
  lds_st<unsigned>(L_GO + (tid & 1) * 4, 0u);

  const unsigned t2 = 28 * xcd + slot;
  const bool has2 = slot < 28 && t2 < 220;
  const unsigned G = t2 / 10, cb = t2 % 10;
  constexpr unsigned rot = 32;
  unsigned g = lane >> 4, r = lane & 15, q4 = r >> 2, p4 = lane & 3;
  const rsrc_t rsWd = mk_rsrc(p.wd, has2 ? F * D * 2 : 0);
  const rsrc_t rsWS = mk_rsrc(p.ws, WS_END);
  unsigned v2 = (lane >> 3) * 1280 + (((lane & 7) ^ (((lane >> 4) & 3) << 1)) << 4);
  auto relane = [&] {
    asm volatile("" : "+v"(tid));
    lane = tid & 63; g = lane >> 4; r = lane & 15; q4 = r >> 2; p4 = lane & 3;
    v2 = (lane >> 3) * 1280 + (((lane & 7) ^ (((lane >> 4) & 3) << 1)) << 4);
  };
  const unsigned m = w & 3, n = w >> 2;
  const unsigned brow = L_B + (8 * g + q4) * 192;
  const unsigned bx = (g & 1) << 5;

  u32x4 pre2[36];
  W2 W;
  W.wd = rsWd; W.wo = mk_rsrc(p.wo, has2 ? D * D * 2 : 0); W.rot = rot; W.col = cb * 128u;
  if (jtype == J_MLP) {
    f32x4 acc[4][6];
    unsigned boff[6];
#pragma unroll
    for (int t = 0; t < 3; ++t) {
      const unsigned gt = n ? (t == 2 ? 3 : 4 + t) : t;
      boff[t] = brow + ((32 * gt + 8 * p4) ^ bx); boff[t + 3] = boff[t] + 6144;
    }
    const unsigned ccol = tid < 96 ? 1920 + 96 * jidx + tid : 3648 + 96 * jidx + (tid < 192 ? tid - 96 : 0);
    main_loop<6, true, true, true>([] {}, rs_, p.ws, acc, boff, d, w, lane, tid, epoch, tag, rb, ccol, rsWd, rsWS, cb, rot, pre2 STAMP_PASS);
    relane();
    float mm[4];
    f32x4 cg[3], cu[3];
#pragma unroll
    for (int i = 0; i < 4; ++i) mm[i] = lds_ld<float>(L_MISC + (64 * m + 16 * i + r) * 4);
#pragma unroll
    for (int t = 0; t < 3; ++t) {
      const unsigned gt = n ? (t == 2 ? 3 : 4 + t) : t;
      cg[t] = lds_ld<f32x4>(L_MISC + 2048 + (16 * gt + 4 * g) * 4); cu[t] = lds_ld<f32x4>(L_MISC + 2048 + (96 + 16 * gt + 4 * g) * 4);
    }
    auto hval = [&](int i, int t) {
      f32x4 gg = acc[i][t] + mm[i] * cg[t];
      const f32x4 uu = acc[i][t + 3] + mm[i] * cu[t];
#pragma unroll
      for (int j = 0; j < 4; ++j) gg[j] = relu2(gg[j]);
      return pk4(gg * uu);
    };
    const unsigned ho = WS_HA + rb * RBSZ + jidx * 49152 + m * 4 * 3072;
#pragma unroll
    for (int i = 0; i < 4; ++i) { st_frag(rsWS, lane, ho + i * 3072 + n * 2048, hval(i, 0), hval(i, 1)); SB; }
    const u32x2 sh[4] = {hval(0, 2), hval(1, 2), hval(2, 2), hval(3, 2)};
    shared_send(w, lane, sh);
    shared_recv(rsWS, w, lane, ho + 1024, 3072, sh);
    STAMP(15);
    publish_flag(p, WS_FLAGH + (rb * 32 + jidx) * FS, tag, tid);
    STAMP(3);
  } else if (jtype == J_KV) {
    f32x4 acc[4][5];
    unsigned boff[5];
    boff[0] = brow + ((80 * n + 8 * p4) ^ bx); boff[1] = boff[0] + 6144;
    boff[2] = brow + ((80 * n + 40 + 8 * p4) ^ bx); boff[3] = boff[2] + 6144;
    boff[4] = brow + ((80 * n + 32 + 40 * (p4 >> 1)) ^ bx) + (p4 & 1) * 6144;
    const unsigned kb = 126976u + m * 6720 + n * 3360, dump = L_MISC - 2048 + lane * 16;
    const unsigned ea = (q4 == 1 || r == 0) ? kb + (r == 0 ? 20 : 12 + r) * 80 : 0u, eb = (q4 == 3 || r == 0) ? kb + 1680 + (r == 0 ? 20 : 4 + r) * 80 : 0u;
    unsigned ka[6] = {kb + r * 80 + g * 16, ea ? ea + g * 16 : dump, eb ? eb + g * 16 : dump, g == 0 ? kb + r * 80 + 64 : dump,
                      g == 2 ? kb + 1680 + r * 80 + 64 : dump, (g == 0 && ea) ? ea + 64 : ((g == 2 && eb) ? eb + 64 : dump)};
    auto pin = [&] { asm volatile("" : "+v"(ka[0]), "+v"(ka[1]), "+v"(ka[2]), "+v"(ka[3]), "+v"(ka[4]), "+v"(ka[5])); };
    const unsigned ccol = tid < 80 ? 640 + 80 * jidx + tid : 1280 + 80 * jidx + (tid < 160 ? tid - 80 : 0);
    main_loop<5, false, false, false>(pin, rs_, p.ws, acc, boff, d, w, lane, tid, epoch, tag, rb, ccol, rsWd, rsWS, cb, rot, pre2 STAMP_PASS);
    relane();
    float cs[5];
    cs[0] = lds_ld<float>(L_MISC + 2048 + (40 * n + r) * 4);
    cs[1] = lds_ld<float>(L_MISC + 2048 + (80 + 40 * n + r) * 4);
    cs[2] = lds_ld<float>(L_MISC + 2048 + (40 * n + 20 + r) * 4);
    cs[3] = lds_ld<float>(L_MISC + 2048 + (80 + 40 * n + 20 + r) * 4);
    cs[4] = lds_ld<float>(L_MISC + 2048 + ((q4 & 1) * 80 + 40 * n + 16 + 20 * (q4 >> 1) + (r & 3)) * 4);
    u32x2 pk[4][3], vv[4][3];
    const unsigned vmask = 0u - (q4 & 1);
    f32x4 mm4[4], rr4[4];
#pragma unroll
    for (int i = 0; i < 4; ++i) {
      mm4[i] = lds_ld<f32x4>(L_MISC + (64 * m + 16 * i + 4 * g) * 4); rr4[i] = lds_ld<f32x4>(L_MISC + 1024 + (64 * m + 16 * i + 4 * g) * 4);
    }
    SB;
#pragma unroll
    for (int i = 0; i < 4; ++i) {
      const f32x4 mm = mm4[i], rr = rr4[i];
      const f32x4 rl = rr * 1.4426950408889634f, rh = rr * 0.5f;
      f32x4 val[5];
#pragma unroll
      for (int t = 0; t < 5; ++t) val[t] = acc[i][t] + mm * cs[t];
#pragma unroll
      for (int t = 0; t < 3; ++t) pk[i][t] = pk4(phi4(val[2 * t], rl, rh));
      vv[i][0] = pk4(val[1] * rr); vv[i][1] = pk4(val[3] * rr);
      const u32x2 v4 = pk4(val[4] * rr);
      vv[i][2] = u32x2{bsel(vmask, v4[0], 0x3f803f80u), bsel(vmask, v4[1], 0x3f803f80u)};
    }
    STAMP(25);
    f32x4 P[7] = {};
#pragma unroll
    for (int i = 0; i < 4; i += 2) {
      const bf16x8 k0 = frag(pk[i][0], pk[i + 1][0]), k2 = frag(pk[i][1], pk[i + 1][1]), k4 = frag(pk[i][2], pk[i + 1][2]);
      const bf16x8 v1 = frag(vv[i][0], vv[i + 1][0]), v3 = frag(vv[i][1], vv[i + 1][1]), v4 = frag(vv[i][2], vv[i + 1][2]);
      P[0] = mfma(k0, v1, P[0]); P[1] = mfma(k0, v4, P[1]); P[2] = mfma(k4, v1, P[2]);
      P[3] = mfma(k2, v3, P[3]); P[4] = mfma(k2, v4, P[4]); P[5] = mfma(k4, v3, P[5]); P[6] = mfma(k4, v4, P[6]);
    }
    lds_st<f32x4>(ka[0], P[0]); lds_st<f32x4>(ka[0] + 1680, P[3]); lds_st<f32x4>(ka[1], P[1]); lds_st<f32x4>(ka[2], P[4]);
    lds_st<f32x4>(ka[3], P[2]); lds_st<f32x4>(ka[4], P[5]); lds_st<f32x4>(ka[5], P[6]);
    lbar();
    if (tid < 210) {
      const unsigned a = 126976u + tid * 32;
      const f32x4 t0 = (lds_ld<f32x4>(a) + lds_ld<f32x4>(a + 6720)) + (lds_ld<f32x4>(a + 13440) + lds_ld<f32x4>(a + 20160));
      const f32x4 t1 = (lds_ld<f32x4>(a + 16) + lds_ld<f32x4>(a + 6736)) + (lds_ld<f32x4>(a + 13456) + lds_ld<f32x4>(a + 20176));
      st16(cat2(pk4(t0), pk4(t1)), rsWS, tid * 16, WS_KVP + (rb * 32 + 4 * jidx) * 840);
    }
    wait_lgkm0();
    STAMP(15);
    publish_flag(p, WS_FLAGKV + ((jidx >> 1) * 32 + (jidx & 1) * 8 + rb) * FS, tag, tid);
    STAMP(3);
  } else if (jtype == J_Q) {
    f32x4 acc[4][5];
    unsigned boff[5];
#pragma unroll
    for (int t = 0; t < 5; ++t) boff[t] = brow + n * 6144 + ((32 * t + 8 * p4) ^ bx);
    const unsigned ccol = 160 * jidx + (tid < 160 ? tid : 0);
    unsigned tch[3];
    auto warm = [&] {
      const unsigned gq = (rb * 4 + jidx) * 8 + w;
      asm volatile("buffer_load_dword %0, %3, %5, 0 offen\n\tbuffer_load_dword %1, %3, %5, %7 offen\n\tbuffer_load_dword %2, %4, %6, 0 offen"
                   : "=&v"(tch[0]), "=&v"(tch[1]), "=&v"(tch[2])
                   : "v"((gq * 34 + (lane < 34 ? lane : 0)) * 128), "v"((gq * 25 + (lane < 25 ? lane : 0)) * 128), "s"(mk_rsrc(p.wd, F * D * 2)),
                     "s"(mk_rsrc(p.wo, D * D * 2)), "s"(8576u * 128u) : "memory");
    };
    main_loop<5, true, false, false>(warm, rs_, p.ws, acc, boff, d, w, lane, tid, epoch, tag, rb, ccol, rsWd, rsWS, cb, rot, pre2 STAMP_PASS);
    relane();
    asm volatile("" :: "v"(tch[0]), "v"(tch[1]), "v"(tch[2]));
    u32x2 pq[4][5];
    {
      float mm[4], rr[4];
      f32x4 c[5];
#pragma unroll
      for (int i = 0; i < 4; ++i) { mm[i] = lds_ld<float>(L_MISC + (64 * m + 16 * i + r) * 4); rr[i] = lds_ld<float>(L_MISC + 1024 + (64 * m + 16 * i + r) * 4); }
#pragma unroll
      for (int t = 0; t < 5; ++t) c[t] = lds_ld<f32x4>(L_MISC + 2048 + (80 * n + 16 * t + 4 * g) * 4);
      SB;
#pragma unroll
      for (int i = 0; i < 4; ++i)
#pragma unroll
        for (int t = 0; t < 5; ++t) pq[i][t] = pk4(phi4(acc[i][t] + mm[i] * c[t], splat(rr[i] * 1.4426950408889634f), splat(rr[i] * 0.5f)));
#pragma unroll
      for (int i = 0; i < 4; ++i) asm volatile("" : "+v"(pq[i][0]), "+v"(pq[i][1]), "+v"(pq[i][2]), "+v"(pq[i][3]), "+v"(pq[i][4]));
    }
    constexpr unsigned KP = 208, KQ = 84 * KP;
    unsigned wa[2];
#pragma unroll
    for (int k = 0; k < 2; ++k) {
      const unsigned qd = 2 * (tid < 420 ? tid : 0) + k, hd = qd / 105, e = (qd % 105) / 5, dq = qd % 5, hl = hd & 3;
      wa[k] = (hd >> 2) * KQ + (e < 20 ? hl * 20 + e : 80 + hl) * KP + hl * 40 + dq * 8;
    }
    unsigned fk = n * KQ + r * KP + g * 8, fz = n * KQ + (80 + (r & 3)) * KP + g * 8;
    unsigned gm[3] = {g < 1 ? ~0u : 0u, g < 2 ? ~0u : 0u, g < 3 ? ~0u : 0u};
    u32x4 B[4][3];
    f32x4 eps4 = splat(1e-6f);
#pragma unroll
    for (int i = 0; i < 4; ++i) {
      B[i][0] = cat2(pq[i][0], pq[i][1]); B[i][1] = cat2(pq[i][2], pq[i][3]); B[i][2] = cat2(pq[i][4], u32x2{0u, 0u});
      asm volatile("" : "+v"(B[i][0]), "+v"(B[i][1]), "+v"(B[i][2]));
    }
    asm volatile("" : "+v"(wa[0]), "+v"(wa[1]), "+v"(fk), "+v"(fz), "+v"(gm[0]), "+v"(gm[1]), "+v"(gm[2]), "+v"(eps4));
    STAMP(25);
    wait_vm<0>();
    bar();
#pragma unroll
    for (int k = 0; k < 5; ++k) lds_st<u32x4>(k * 8192 + tid * 16, u32x4{0u, 0u, 0u, 0u});
    if (w == 0) poll_flags((gu32*)(p.ws + WS_FLAGKV + (jidx * 32 + (lane & 15)) * FS), true, tag);
    lbar();
    STAMP(4);
    if (w < 7) {
      u32x4 v[8];
#pragma unroll
      for (int j = 0; j < 8; ++j)
        v[j] = __builtin_amdgcn_raw_buffer_load_b128(rsWS, (tid < 420 ? tid : 0) * 16, WS_KVP + (j * 32 + 8 * jidx) * 840, 16);
      SB;
      f32x4 t0 = {0.f, 0.f, 0.f, 0.f}, t1 = t0;
#pragma unroll
      for (int j = 0; j < 8; ++j) {
        t0 += f32x4{bflo(v[j][0]), bfhi(v[j][0]), bflo(v[j][1]), bfhi(v[j][1])};
        t1 += f32x4{bflo(v[j][2]), bfhi(v[j][2]), bflo(v[j][3]), bfhi(v[j][3])};
      }
      lds_st<u32x2>(wa[0], pk4(t0)); lds_st<u32x2>(wa[1], pk4(t1));
    }
    lbar();
    STAMP(16);
    u32x4 A[12];
#pragma unroll
    for (int P = 0; P < 3; ++P) A[P] = cat2(lds_ld<u32x2>(fz + P * 64), lds_ld<u32x2>(fz + P * 64 + 32));
#pragma unroll
    for (int u = 0; u < 5; ++u) {
      const unsigned a0 = fk + u * 16 * KP + (u < 3 ? 0 : 64);
      A[3 + u] = cat2(lds_ld<u32x2>(a0), lds_ld<u32x2>(a0 + 32));
    }
#pragma unroll
    for (int u = 1; u < 5; ++u) {
      const unsigned a0 = fk + u * 16 * KP + (u < 3 ? 64 : 128);
      A[7 + u] = cat2(lds_ld<u32x2>(a0), lds_ld<u32x2>(a0 + 32));
    }
    SB;
    u32x2 sh[4];
    f32x4 den[4], num[2][5];
    static_for<0, 12>([&](auto E) {
      constexpr int P = E.v >> 2, i = E.v & 3;
      if constexpr (P == 0) mfma_c(den[i], A[0], B[i][0], eps4); else mfma_ip(den[i], A[P], B[i][P]);
    });
    u32x2 o[5];
    const unsigned oo = WS_HA + rb * RBSZ + 18 * 49152 + jidx * 81920 + m * 4 * 5120;
    auto fin = [&](auto I, auto K) {
      constexpr int i = I.v, k = K.v, u = k < 8 ? k >> 1 : 4;
      if constexpr (k == 8 || !(k & 1)) {
        asm volatile("" : "+v"(num[i & 1][u]));
        const float rr = u == 0 ? den[i][0] : (u == 4 ? den[i][3] : bself(gm[(u + 2) % 3], den[i][(u + 3) & 3], den[i][u & 3]));
#pragma unroll
        for (int j = 0; j < 4; ++j) num[i & 1][u][j] = smul(num[i & 1][u][j], rr);
      }
      if constexpr (k == 8 || (k & 1)) o[u] = pk4(num[i & 1][u]);
      if constexpr (k == 3) st_frag(rsWS, lane, oo + i * 5120 + n * 2048, o[0], o[1]);
      if constexpr (k == 7) st_frag(rsWS, lane, oo + i * 5120 + n * 2048 + 1024, o[2], o[3]);
      if constexpr (k == 8) { sh[i] = o[4]; asm volatile("" : "+v"(sh[i])); }
    };
    static_for<0, 4>([&](auto I) {
      constexpr int i = I.v;
      static_for<0, 9>([&](auto K) {
        constexpr int k = K.v, u = k < 5 ? k : k - 4;
        if constexpr (k < 5) mfma_z(num[i & 1][u], A[3 + u], B[i][u < 3 ? 0 : 1]); else mfma_ip(num[i & 1][u], A[7 + u], B[i][u < 3 ? 1 : 2]);
        SB;
        if constexpr (i == 0) {
          if constexpr (k >= 5) {
            asm volatile("" : "+v"(den[k - 5]));
#pragma unroll
            for (int j = 0; j < 4; ++j) den[k - 5][j] = __builtin_amdgcn_rcpf(den[k - 5][j]);
            asm volatile("" : "+v"(den[k - 5]));
          }
        } else {
          fin(IC<i - 1>{}, K);
        }
        SB;
      });
    });
    static_for<0, 9>([&](auto K) { fin(IC<3>{}, K); SB; });
    shared_send(w, lane, sh);
    shared_recv(rsWS, w, lane, oo + 4096, 5120, sh);
    STAMP(15);
    publish_flag(p, WS_FLAGO + (rb * 32 + jidx) * FS, tag, tid);
    STAMP(3);
    if (b == 224) {
      poll_flags((gu32*)(p.ws + WS_FLAGH + tid * FS), (tid & 31) < (tid < 256 ? 18u : 4u), tag);
      bar();
      if (tid == 0) __hip_atomic_store((gu32*)(p.ws + WS_EPOCH), tag, RLX_AGENT);
    }
  } else {
    const unsigned gw = spi * 8 + w;
    unsigned k10 = 0x00003f80u, k01 = 0x3f800000u;
    asm volatile("" : "+v"(k10), "+v"(k01));
    auto cs_src = [&](unsigned u, rsrc_t& rs, unsigned& cv, unsigned& st) {
      const unsigned cline = u / 3, third = u % 3;
      const void* cw = cline < 30 ? p.wqkv : (cline < 57 ? p.wg : p.wu);
      const unsigned cpitch = cline < 30 ? 3840u : 3456u;
      const unsigned clocal = cline < 30 ? cline : (cline < 57 ? cline - 30 : cline - 57);
      rs = mk_rsrc(cw, gw < 126 ? D * cpitch : 0);
      cv = (third * 216 + (lane >> 3)) * cpitch + clocal * 128 + (lane & 7) * 16;
      st = 8 * cpitch;
    };
    auto cs_publish = [&](unsigned u, float (&cs)[8]) {
#pragma unroll
      for (int k = 0; k < 4; ++k) { const bool hi = lane & 32; const float o = shfl_x(hi ? cs[k] : cs[k + 4], lane, 32); cs[k] = (hi ? cs[k + 4] : cs[k]) + o; }
#pragma unroll
      for (int k = 0; k < 2; ++k) { const bool hi = lane & 16; const float o = shfl_x(hi ? cs[k] : cs[k + 2], lane, 16); cs[k] = (hi ? cs[k + 2] : cs[k]) + o; }
      { const bool hi = lane & 8; const float o = shfl_x(hi ? cs[0] : cs[1], lane, 8); cs[0] = (hi ? cs[1] : cs[0]) + o; }
      if (gw < 126)
        __hip_atomic_store((gu64*)(p.ws + WS_CSUM) + (u % 3) * NCOL + (u / 3) * 64 + (lane & 7) * 8 + (lane >> 3),
                           ((unsigned long long)tag << 32) | __builtin_bit_cast(unsigned, cs[0]), RLX_AGENT);
    };
    row_load(rs_, rsX, lane);
    asm volatile("s_waitcnt vmcnt(0) lgkmcnt(0)\n\tv_readfirstlane_b32 %0, %1\n\ts_nop 3" : "=s"(tag) : "v"(epoch) : "memory");
    tag = rfl(tag) + 1;
    static_for<0, 14>([&](auto K) { row_step<K.v>(rs_, lane); });
    u32x4 c[27];
    rsrc_t rs0, rs1; unsigned cv0, cv1, st0, st1;
    cs_src(gw, rs0, cv0, st0); cs_src(gw + 126, rs1, cv1, st1);
#pragma unroll
    for (int j = 0; j < 27; ++j) c[j] = __builtin_amdgcn_raw_buffer_load_b128(rs0, cv0 + j * st0, 0, 0);
    lbar();
    row_publish(rs_, p.ws, tag, tid);
    float cs[8] = {};
#pragma unroll
    for (int j = 0; j < 27; ++j) {
#pragma unroll
      for (int k = 0; k < 4; ++k) { cs[2 * k] = dot2(c[j][k], k10, cs[2 * k]); cs[2 * k + 1] = dot2(c[j][k], k01, cs[2 * k + 1]); }
      if (j < 8) c[j] = __builtin_amdgcn_raw_buffer_load_b128(rs1, cv1 + j * st1, 0, 0);
      SB;
    }
    cs_publish(gw, cs);
    SB;
#pragma unroll
    for (int j = 8; j < 27; ++j) c[j] = __builtin_amdgcn_raw_buffer_load_b128(rs1, cv1 + j * st1, 0, 0);
    SB;
    STAMP(11);
#pragma unroll
    for (int k = 0; k < 8; ++k) cs[k] = 0.f;
#pragma unroll
    for (int j = 0; j < 27; ++j)
#pragma unroll
      for (int k = 0; k < 4; ++k) { cs[2 * k] = dot2(c[j][k], k10, cs[2 * k]); cs[2 * k + 1] = dot2(c[j][k], k01, cs[2 * k + 1]); }
    cs_publish(gw + 126, cs);
    STAMP(12);
#pragma unroll 1
    for (unsigned i = w; i < 144; i += 8) dma16(i * 1024, v2, rsWd, urow(i >> 2, rot) + (i & 3) * 10240u + cb * 128u);
    drain_vm();
    bar();
  }
  if (!has2) { STAMP_END; return; }
  relane();

  const unsigned rbA = (G * 6) >> 4, rbB = (G * 6 + 5 > 127 ? 127 : G * 6 + 5) >> 4;
  if (w >= 4) {
    __builtin_amdgcn_s_setprio(HPRIO);
    const unsigned lw = w - 4, ld0 = lw * 1024;
    gu32* fo = (gu32*)(p.ws + WS_FLAGO + ((lane < 32 ? rbA : rbB) * 32 + (lane & 3)) * FS);
    gu32* fh = (gu32*)(p.ws + WS_FLAGH + ((lane < 32 ? rbA : rbB) * 32 + ((lane & 31) < 18 ? lane & 31 : 17)) * FS);
    __builtin_amdgcn_s_sleep(HSLEEP);
    if (jtype == J_KV) {
#pragma unroll
      for (int u = 31; u < 36; ++u) dma16(u * 4096 + ld0, v2, rsWd, w2_off(W, u, lw));
    }
    if (jtype != J_MLP) {
#pragma unroll
      for (int u = 0; u < NPRE; ++u) { pre2[u] = w2_load(W, 36 + u, lw, v2); SB; }
    }
#pragma unroll
    for (int u = 36 + NPRE; u < PSPLIT; ++u) { pre2[u - 36] = w2_load(W, u, lw, v2); SB; }
    if (w == 7) {
      __builtin_amdgcn_s_setprio(3);
      const unsigned v = poll_flags(fh, (lane & 31) < 18 && (lane & 31) >= 10, tag);
      lds_set(L_GO);
      if (!all64((lane & 31) >= 18 || v == tag)) poll_flags(fh, (lane & 31) < 18, tag);
      lds_set(L_GO + 4);
      __builtin_amdgcn_s_setprio(HPRIO);
    }
#pragma unroll
    for (int u = PSPLIT; u < HSPLIT; ++u) { pre2[u - 36] = w2_load(W, u, lw, v2); SB; }
    STAMP(8);
    unsigned la[3], wb[3], xo[3], st[3];
    u32x4 xr[3];
    const unsigned ch = lane & 7;
#pragma unroll
    for (int i = 0; i < 3; ++i) {
      const unsigned row = lw * 24 + i * 8 + (lane >> 3), rl = row >> 4, rr = row & 15;
      la[i] = (rl >= 3 ? 24576u + (rl - 3) * 4096u : rl * 4096u) + (ch >> 1) * 1024 + (ch & 1) * 512 + ((rr + 2 * ch) & 15) * 16;
      xo[i] = (G * 96 + row) * 1280 + cb * 128 + ch * 16;
      wb[i] = ld0 + lane * 16 + i * 49152;
      asm volatile("" : "+v"(la[i]), "+v"(wb[i]), "+v"(xo[i]));
    }
    float ri[3], rl[3];
    unsigned of = 0;
    f32x4 xf[3][2], dd[3][2], d2[3][2];
#pragma unroll
    for (int k = 2; k <= 6; ++k) {
      if (k == O_BAR && w == 7 && !all64((lane & 31) >= 4 || of == tag)) poll_flags(fo, (lane & 31) < 4, tag);
      if (k == 6) {
        wait_vm<0>();
#pragma unroll
        for (int h = 0; h < 3; ++h) {
          ri[h] = 2.0f * bfhi(st[h]) * bfhi(st[h]);
          rl[h] = -1.4426950408889634f * __builtin_amdgcn_rcpf(ri[h]);
          xf[h][0] = f32x4{bflo(xr[h][0]), bfhi(xr[h][0]), bflo(xr[h][1]), bfhi(xr[h][1])};
          xf[h][1] = f32x4{bflo(xr[h][2]), bfhi(xr[h][2]), bflo(xr[h][3]), bfhi(xr[h][3])};
          asm volatile("" : "+v"(ri[h]), "+v"(rl[h]), "+v"(xf[h][0]), "+v"(xf[h][1]));
        }
      }
      lbar();
      if (k == O_BAR - 1) { of = __hip_atomic_load(fo, RLX_AGENT); SB; }
      if (k < 6) {
        const int kk = k - 1;
#pragma unroll
        for (int u = 9 * (kk - 1) + ((kk - 1) & 1); u < 9 * kk + (kk & 1); ++u) {
          lds_st<u32x4>(wb[u / 12] + (u % 12) * 4096, pre2[u]);
          if (WSLEEP == 1) __builtin_amdgcn_s_sleep(1);
          if (WSLEEP == 2) asm volatile("s_nop 7");
          SB;
        }
      }
      if (k == 2 || k == 3) {
        constexpr int mid = (HSPLIT + 74) / 2 < 72 ? (HSPLIT + 74) / 2 : 72;
#pragma unroll
        for (int u = (k == 2 ? HSPLIT : mid); u < (k == 2 ? mid : 72); ++u) { pre2[u - 36] = w2_load(W, u, lw, v2); SB; }
        if (k == 3) { dma16(36 * 4096 + ld0, v2, W.wo, w2_off(W, 72, lw)); dma16(37 * 4096 + ld0, v2, W.wo, w2_off(W, 73, lw)); }
      }
      if (k == XK) {
#pragma unroll
        for (int h = 0; h < 3; ++h) {
          const unsigned row = G * 96 + lw * 24 + h * 8 + (lane >> 3);
          st[h] = __builtin_amdgcn_raw_buffer_load_b32(rsWS, WS_STAT + (row < S ? row : 0) * 8, 0, 16);
          xr[h] = __builtin_amdgcn_raw_buffer_load_b128(rsX, xo[h], 0, 0);
        }
      }
    }
    lbar();
#pragma unroll
    for (int i = 0; i < 3; ++i) {
      dd[i][0] = lds_ld<f32x4>(la[i]); d2[i][0] = lds_ld<f32x4>(la[i] + 12288);
      dd[i][1] = lds_ld<f32x4>(la[i] + 256); d2[i][1] = lds_ld<f32x4>(la[i] + 12288 + 256);
      la[i] += 49152;
      asm volatile("" : "+v"(la[i]));
    }
    __builtin_amdgcn_s_setprio(GPRIO);
#pragma unroll
    for (int i = 0; i < 3; ++i) {
      float v[8], e[8];
#pragma unroll
      for (int j = 0; j < 8; ++j) v[j] = sadd(dd[i][j >> 2][j & 3], d2[i][j >> 2][j & 3]);
      SB;
#pragma unroll
      for (int j = 0; j < 8; ++j) e[j] = smul(v[j], rl[i]);
      SB;
#pragma unroll
      for (int j = 0; j < 8; ++j) e[j] = __builtin_amdgcn_exp2f(e[j]);
      SB;
#pragma unroll
      for (int j = 0; j < 8; ++j) e[j] = sfma(e[j], ri[i], ri[i]);
      SB;
#pragma unroll
      for (int j = 0; j < 8; ++j) e[j] = __builtin_amdgcn_rcpf(e[j]);
      SB;
#pragma unroll
      for (int j = 0; j < 8; ++j) dd[i][j >> 2][j & 3] = smul(v[j], e[j]);
      asm volatile("" : "+v"(dd[i][0]), "+v"(dd[i][1]));
      SB;
    }
    const rsrc_t rsO = mk_rsrc(p.out, S * D * 2);
    STAMP(9);
    if (PUTB) bar();
    bar();
    f32x4 ss[3][2];
#pragma unroll
    for (int i = 0; i < 3; ++i) {
      ss[i][0] = lds_ld<f32x4>(la[i]) + lds_ld<f32x4>(la[i] + 12288);
      ss[i][1] = lds_ld<f32x4>(la[i] + 256) + lds_ld<f32x4>(la[i] + 12288 + 256);
    }
#pragma unroll
    for (int i = 0; i < 3; ++i)
      __builtin_amdgcn_raw_buffer_store_b128(cat2(pk4(xf[i][0] + ss[i][0] * dd[i][0]), pk4(xf[i][1] + ss[i][1] * dd[i][1])), rsO, xo[i], 0, 0);
    STAMP(5);
    STAMP_END;
    return;
  }
  {
    const unsigned kh = w & 1, rt = G * 6 + 3 * (w >> 1);
    const rsrc_t rsH = mk_rsrc(p.ws + WS_HA, 8 * RBSZ);
    unsigned tb[3][4];
#pragma unroll
    for (int t = 0; t < 4; ++t) {
      tb[0][t] = (4 * g + q4) * 128 + ((t ^ ((2 * g + (q4 >> 1)) & 3)) << 5) + 8 * p4 + kh * 4096;
      tb[1][t] = tb[0][t] + 49152; tb[2][t] = tb[0][t] + 98304;
      asm volatile("" : "+v"(tb[0][t]), "+v"(tb[1][t]), "+v"(tb[2][t]));
    }
    auto rd1 = [&](int i, int t, int hf) {
      const int sl = i == 36 ? 36 : (2 * i) % 36, bi = sl / 12 > 2 ? 2 : sl / 12;
      return __builtin_bit_cast(u32x2, lds_tr(tb[bi][t] + (sl - 12 * bi) * 4096 + hf * 2048));
    };
    constexpr int NW = P2NW, W0 = P2W0;
    constexpr int OI = O_BAR == 4 ? 17 : (O_BAR == 5 ? 22 : 26);
    constexpr int OB0 = OI == 26 ? OBN : 0;
    f32x4 acc[2][3][4];
    u32x4 a[NW][3];
    u32x2 bw[2][4][2];
    unsigned va[2][3];
#pragma unroll
    for (int h = 0; h < 3; ++h) {
      va[0][h] = lane * 16 + ((rt + h) >> 4) * RBSZ + ((rt + h) & 15) * 3072;
      va[1][h] = va[0][h] + ((rt + h) & 15) * 2048;
      asm volatile("" : "+v"(va[0][h]), "+v"(va[1][h]));
    }
    unsigned xa[4];
#pragma unroll
    for (int t = 0; t < 4; ++t) {
      xa[t] = w * 12288u + t * 1024 + g * 256 + ((r + 4 * t + (g & 2)) & 15) * 16;
      asm volatile("" : "+v"(xa[t]));
    }
    auto put = [&](auto Z, auto E) {
      constexpr int z = Z.v, t = E.v / 3, h = 2 - E.v % 3;
      asm volatile("" : "+v"(acc[z][h][t]));
      lds_st<f32x4>(xa[t] + z * 49152 + h * 4096, acc[z][h][t]);
    };
    auto lda = [&](auto I, auto H) {
      constexpr int i = I.v, h = H.v;
      constexpr unsigned s0 = i < 27 ? (2 * i + rot) % 54 : 2 * i, s1 = i < 27 ? (2 * i + 1 + rot) % 54 : 2 * i + 1;
      a[i % NW][h] = __builtin_amdgcn_raw_buffer_load_b128(rsH, va[i >= 27][h], step_off(s0) + kh * (step_off(s1) - step_off(s0)), 16);
      SB;
    };
    lds_wait(L_GO);
    unsigned go2 = 0;
    STAMP(8);
    static_for<0, W0>([&](auto I) { lda(I, IC<0>{}); lda(I, IC<1>{}); lda(I, IC<2>{}); });
#pragma unroll
    for (int t = 0; t < 4; ++t) { bw[0][t][0] = rd1(0, t, 0); bw[0][t][1] = rd1(0, t, 1); }
    static_for<0, 37>([&](auto I) {
      constexpr int i = I.v;
      constexpr int lim = i + NW - 1;
      constexpr int nxt = i <= OI ? (W0 + 2 * i < lim ? W0 + 2 * i : lim) : (27 + OB0 + 2 * (i - OI - 1) < lim ? 27 + OB0 + 2 * (i - OI - 1) : lim);
      constexpr int end = i <= OI ? 27 : 37;
      constexpr int l0 = nxt < end ? nxt : -1, l1 = (nxt + 1 <= lim && nxt + 1 < end) ? nxt + 1 : -1;
      SB;
      static_for<0, 12>([&](auto E) {
        constexpr int e = E.v, t = e / 3, h = e % 3;
        const u32x4 bf = cat2(bw[i & 1][t][0], bw[i & 1][t][1]);
        if constexpr (i == 0 || i == 27) mfma_z(acc[i >= 27][2 - h][t], bf, a[i % NW][2 - h]); else mfma_ip(acc[i >= 27][2 - h][t], bf, a[i % NW][2 - h]);
        SB;
        if constexpr (i == 4 && e == 2) go2 = lds_get(L_GO + 4);
        if constexpr (!PUTB && (i == 27 || i == 28) && (e & 1)) put(IC<0>{}, IC<(i - 27) * 6 + e / 2>{});
        if constexpr (i == 36 && e >= 8) put(IC<1>{}, IC<e - 8>{});
        if constexpr (i < 36 && h < 2) bw[(i + 1) & 1][t][h] = rd1(i + 1, t, h);
        if constexpr (l1 < 0) { if constexpr (h == 2 && t < 3 && l0 >= 0) lda(IC<l0>{}, IC<t>{}); }
        else if constexpr (e & 1) lda(IC<(e < 6 ? l0 : l1)>{}, IC<(e >> 1) % 3>{});
        SB;
      });
      if constexpr (i == (PUTB ? 33 : 29)) { asm volatile("s_waitcnt lgkmcnt(8)" ::: "memory"); bar(); }
      if constexpr (i == 4) { if (__builtin_expect(!rfl(go2), 0)) lds_wait(L_GO + 4); asm volatile("" ::: "memory"); STAMP(17); }
      if constexpr (i > 4 && i < 27 && (i % 9 == 4 || i % 9 == 8)) {
        bar();
        if constexpr (i == 26 && OI == 26) static_for<27, 27 + OB0>([&](auto J) { lda(J, IC<0>{}); lda(J, IC<1>{}); lda(J, IC<2>{}); });
        if constexpr (PUTB && i == 26) { static_for<0, 12>([&](auto E) { put(IC<0>{}, E); SB; }); lbar(); }
        STAMP(17 + (2 * i) / 9);
      }
    });
    STAMP(24);
    static_for<4, 12>([&](auto E) { if constexpr (E.v == 8) asm volatile("s_nop 3" ::: "memory"); put(IC<1>{}, E); SB; });
    lbar();
    STAMP(5);
    STAMP_END;
  }
}

extern "C" void kernel_launch(void* const* d_in, const int* in_sizes, int n_in, void* d_out, int out_size, void* d_ws,
                              size_t ws_size, hipStream_t stream) {
  Params p;
  p.x = d_in[0]; p.wqkv = d_in[1]; p.wo = d_in[2]; p.wg = d_in[4]; p.wu = d_in[5]; p.wd = d_in[6];
  p.out = d_out; p.ws = (char*)d_ws;
  p.wsal = ws_size >= (64ull << 20) + WS_END ? (64ull << 20) - 1 : 0;
  fused_block<<<256, 512, 0, stream>>>(p);
}
```

```cpp
#include <hip/hip_runtime.h>
#include <stdint.h>

typedef __attribute__((ext_vector_type(8))) __bf16 bf16x8;
typedef __attribute__((ext_vector_type(4))) __bf16 bf16x4;
typedef __attribute__((ext_vector_type(2))) __bf16 bf16x2;
typedef __attribute__((ext_vector_type(4))) float f32x4;
typedef __attribute__((ext_vector_type(2))) float f32x2;
typedef __attribute__((ext_vector_type(4))) unsigned u32x4;
typedef __attribute__((ext_vector_type(2))) unsigned u32x2;
typedef __amdgpu_buffer_rsrc_t rsrc_t;
#define LAS __attribute__((address_space(3)))
#define GAS __attribute__((address_space(1)))
#define DI __device__ __forceinline__
#define RLX_AGENT __ATOMIC_RELAXED, __HIP_MEMORY_SCOPE_AGENT
typedef GAS unsigned gu32;
typedef GAS unsigned long long gu64;

constexpr int S = 2048, D = 640, F = 1728;
constexpr int NCOL = 5376;
constexpr int NSTEP = 74;
constexpr unsigned WS_EPOCH = 0;
#define FS 128
constexpr unsigned WS_FLAGH = 768u << 10;
constexpr unsigned WS_FLAGO = WS_FLAGH + 8 * 32 * FS;
constexpr unsigned WS_FLAGKV = WS_FLAGO + 8 * 32 * FS;
constexpr unsigned WS_STAT = 4096;
constexpr unsigned WS_CSUM = WS_STAT + S * 8;
constexpr unsigned WS_KVP = WS_CSUM + 3 * NCOL * 8;
constexpr unsigned WS_HA = 1u << 20;
constexpr unsigned RBSZ = 16 * NSTEP * 1024;
constexpr unsigned step_off(unsigned st) { return st < 54 ? (st / 3) * 49152 + (st % 3) * 1024 : 18 * 49152 + ((st - 54) / 5) * 81920 + ((st - 54) % 5) * 1024; }
constexpr unsigned WS_DBG = 12u << 20;
constexpr unsigned WS_END = 13u << 20;
constexpr unsigned L_B = 65536;
constexpr unsigned L_SCR = 147456;
constexpr unsigned L_MISC = 159744;
constexpr unsigned L_GO = L_MISC + 3072;
constexpr unsigned SPIN_CAP = 1u << 18;
#define HSLEEP 4
#define HSPLIT 64
#define PPIPE 0
#define STA 0
#define STB 2
#define SNAKE 0
#define ROTW 1
#define NPRE 12
#define PUTB 0
#define OBN 6
#define PSPLIT 48
#define WARM 1
#define CWIN 40
#define SDELAY 0
#define XK 2
#define GRP 1
#define GPRIO 0
#define HPRIO 0
#define PSLEEP 2
#define P2NW 7
#define P2W0 6
#define WSLEEP 0
#define ROTM 18
#define NA1 2
#define O_BAR 5
#define PRIO_MODE 0
#define DMA_SPACE_A T
#define DMA_SPACE_B (4 * T / 3)

struct Params {
  const void* x; const void* wqkv; const void* wo; const void* wg; const void* wu; const void* wd;
  void* out; char* wsraw;
  unsigned long long wsal;
};

__shared__ __attribute__((aligned(16))) char smem[163840];

DI float sadd(float a, float b) { float o; asm("v_add_f32 %0, %1, %2" : "=v"(o) : "v"(a), "v"(b)); return o; }
DI float smul(float a, float b) { float o; asm("v_mul_f32 %0, %1, %2" : "=v"(o) : "v"(a), "v"(b)); return o; }
DI float sfma(float a, float b, float c) { float o; asm("v_fma_f32 %0, %1, %2, %3" : "=v"(o) : "v"(a), "v"(b), "v"(c)); return o; }
DI unsigned bsel(unsigned m, unsigned a, unsigned b) { unsigned o; asm("v_bfi_b32 %0, %1, %2, %3" : "=v"(o) : "v"(m), "v"(a), "v"(b)); return o; }
DI float bself(unsigned m, float a, float b) { return __builtin_bit_cast(float, bsel(m, __builtin_bit_cast(unsigned, a), __builtin_bit_cast(unsigned, b))); }
DI bool all64(bool c) { return __builtin_amdgcn_ballot_w64(!c) == 0; }
DI float shfl_x(float v, unsigned lane, unsigned m) {
  return __builtin_bit_cast(float, __builtin_amdgcn_ds_bpermute((int)((lane ^ m) << 2), __builtin_bit_cast(int, v)));
}
DI unsigned umin(unsigned a, unsigned b) { return a < b ? a : b; }
DI unsigned rfl(unsigned v) { return __builtin_amdgcn_readfirstlane(v); }
DI rsrc_t mk_rsrc(const void* p, unsigned bytes) { return __builtin_amdgcn_make_buffer_rsrc((void*)p, (short)0, (int)bytes, 0x00020000); }
DI void dma16(unsigned ldsdst, unsigned voff, rsrc_t rs, unsigned soff) {
  asm volatile("s_mov_b32 m0, %0\n\ts_nop 0\n\tbuffer_load_dwordx4 %1, %2, %3 offen lds" ::"s"(ldsdst), "v"(voff), "s"(rs), "s"(soff) : "memory");
}
template <int N> DI void wait_vm() { asm volatile("s_waitcnt vmcnt(%0)" ::"i"(N) : "memory"); }
DI void drain_vm() { wait_vm<0>(); __builtin_amdgcn_s_waitcnt(0x0F70); }
DI void wait_lgkm0() { asm volatile("s_waitcnt lgkmcnt(0)" ::: "memory"); }
DI void bar() { asm volatile("" ::: "memory"); __builtin_amdgcn_s_barrier(); asm volatile("" ::: "memory"); }
DI void lbar() { wait_lgkm0(); bar(); }
template <class V> DI V lds_ld(unsigned a) { return *(LAS V*)((LAS char*)smem + a); }
template <class V> DI void lds_st(unsigned a, V v) { *(LAS V*)((LAS char*)smem + a) = v; }
DI bf16x4 lds_tr(unsigned a) { return __builtin_amdgcn_ds_read_tr16_b64_v4bf16((LAS bf16x4*)((LAS char*)smem + a)); }
DI bf16x8 cat(bf16x4 a, bf16x4 b) { return __builtin_shufflevector(a, b, 0, 1, 2, 3, 4, 5, 6, 7); }
DI f32x4 mfma(bf16x8 a, bf16x8 b, f32x4 c) { return __builtin_amdgcn_mfma_f32_16x16x32_bf16(a, b, c, 0, 0, 0); }
DI unsigned pk2(float a, float b) { f32x2 v = {a, b}; return __builtin_bit_cast(unsigned, __builtin_convertvector(v, bf16x2)); }
DI u32x2 pk4(f32x4 v) { u32x2 r = {pk2(v[0], v[1]), pk2(v[2], v[3])}; return r; }
DI bf16x8 frag(u32x2 lo, u32x2 hi) { u32x4 v = {lo[0], lo[1], hi[0], hi[1]}; return __builtin_bit_cast(bf16x8, v); }
DI float bflo(unsigned v) { return __builtin_bit_cast(float, v << 16); }
DI float bfhi(unsigned v) { return __builtin_bit_cast(float, v & 0xffff0000u); }
DI float rbf(float v) { return bflo(pk2(v, 0.f)); }
DI float relu2(float v) { float o; asm("v_add_f32 %0, %1, |%1|" : "=v"(o) : "v"(v)); return o; }
DI f32x4 phi4(f32x4 u, f32x4 rl, f32x4 rh) {
  const f32x4 l = u * rl;
  f32x4 e, a;
#pragma unroll
  for (int j = 0; j < 4; ++j) {
    e[j] = __builtin_amdgcn_fmed3f(__builtin_amdgcn_exp2f(l[j]), 0.f, 1.f);
    a[j] = relu2(u[j]);
  }
  return a * rh + e + 1e-6f;
}
DI f32x4 splat(float v) { return f32x4{v, v, v, v}; }
template <int CTRL> DI float dpp_add(float v) {
  return v + __builtin_bit_cast(float, __builtin_amdgcn_update_dpp(0, __builtin_bit_cast(int, v), CTRL, 0xf, 0xf, true));
}
DI float dot2(unsigned a, unsigned b, float c) {
  return __builtin_amdgcn_fdot2_f32_bf16(__builtin_bit_cast(bf16x2, a), __builtin_bit_cast(bf16x2, b), c, false);
}
DI u32x4 ldtr2(unsigned a, unsigned d) { return __builtin_bit_cast(u32x4, cat(lds_tr(a), lds_tr(a + d))); }
DI void mfma_ip(f32x4& c, const u32x4& a, const u32x4& b) { asm volatile("v_mfma_f32_16x16x32_bf16 %0, %1, %2, %0" : "+v"(c) : "v"(a), "v"(b)); }
DI void mfma_c(f32x4& d, const u32x4& a, const u32x4& b, const f32x4& c) { asm volatile("v_mfma_f32_16x16x32_bf16 %0, %1, %2, %3" : "=&v"(d) : "v"(a), "v"(b), "v"(c)); }
DI void mfma_z(f32x4& c, const u32x4& a, const u32x4& b) { asm volatile("v_mfma_f32_16x16x32_bf16 %0, %1, %2, 0" : "=&v"(c) : "v"(a), "v"(b)); }
template <int N> DI void acc_fence(f32x4* c) {
  asm volatile("s_nop 7" ::: "memory");
#pragma unroll
  for (int i = 0; i < N; ++i) asm volatile("" : "+v"(c[i]));
}
DI u32x4 cat2(u32x2 a, u32x2 b) { return u32x4{a[0], a[1], b[0], b[1]}; }
DI u32x2 ldtr1(unsigned a) { return __builtin_bit_cast(u32x2, lds_tr(a)); }
template <int IMM> DI void set_m0i(unsigned v) { asm volatile("s_add_i32 m0, %0, %1" ::"s"(v), "i"(IMM) : "memory", "scc"); }
template <int OFF> DI void dma_goi(unsigned voff, rsrc_t rs, unsigned soff) {
  asm volatile("buffer_load_dwordx4 %0, %1, %2 offen offset:%3 lds" ::"v"(voff), "s"(rs), "s"(soff), "i"(OFF) : "memory");
}
DI void set_m0(unsigned v) { asm volatile("s_mov_b32 m0, %0" ::"s"(v) : "memory"); }
DI void dma_go(unsigned voff, rsrc_t rs, unsigned soff) {
  asm volatile("buffer_load_dwordx4 %0, %1, %2 offen lds" ::"v"(voff), "s"(rs), "s"(soff) : "memory");
}
DI f32x4 mfma_u(u32x4 a, u32x4 b, f32x4 c) { return mfma(__builtin_bit_cast(bf16x8, a), __builtin_bit_cast(bf16x8, b), c); }
struct Stamps { unsigned long long t; };
#define STAMP_DECL Stamps stv_ = {0ull}
#define STAMP_ARG , Stamps& stv_
#define STAMP_PASS , stv_
#define STAMPX(c, i) do { if ((i) == 0) { asm volatile("s_memrealtime %0\n\ts_waitcnt lgkmcnt(0)" : "=s"(stv_.t)); } } while (0)
#define STAMP_END do { if (threadIdx.x == 0) __hip_atomic_store((gu64*)(ws + WS_DBG) + blockIdx.x, jtype == 0 ? stv_.t : 0ull, RLX_AGENT); } while (0)
#define STAMP(i) STAMPX(__builtin_amdgcn_workitem_id_x() == 0, i)

DI unsigned poll_flags(gu32* word, bool active, unsigned tag) {
  unsigned v = 0;
#pragma unroll 1
  for (unsigned spins = 0; spins < SPIN_CAP; ++spins) {
    v = __hip_atomic_load(word, RLX_AGENT);
    if (all64(!active || v == tag)) break;
    if (PSLEEP) __builtin_amdgcn_s_sleep(PSLEEP);
  }
  asm volatile("" ::: "memory");
  return v;
}

DI void lds_set(unsigned a) { __hip_atomic_store((LAS unsigned*)smem + a / 4, 1u, __ATOMIC_RELAXED, __HIP_MEMORY_SCOPE_WORKGROUP); }
DI unsigned lds_get(unsigned a) { return __hip_atomic_load((LAS unsigned*)smem + a / 4, __ATOMIC_RELAXED, __HIP_MEMORY_SCOPE_WORKGROUP); }
DI void lds_wait(unsigned a) {
#pragma unroll 1
  for (unsigned spins = 0; spins < (SPIN_CAP << 4); ++spins) {
    if (rfl(lds_get(a))) break;
    __builtin_amdgcn_s_sleep(1);
  }
  asm volatile("" ::: "memory");
}

template <int I> struct IC { static constexpr int v = I; };
template <int B, int E, class Fn> DI void static_for(Fn&& f) {
  if constexpr (B < E) { f(IC<B>{}); static_for<B + 1, E>(f); }
}

enum { J_MLP = 0, J_KV = 1, J_Q = 2, J_SPARE = 3 };

struct Dma {
  rsrc_t rsA, rsB;
  unsigned vA0, vA1, vB0, vB1, vB2;
  unsigned sA[4], sB, advB;
  unsigned wa, wq;
  unsigned mA, mB, mW;
};

constexpr unsigned a_slot(unsigned s3) { return s3 == 0 ? 126976u : (s3 - 1) * 32768u; }
#define SB __builtin_amdgcn_sched_barrier(0)

template <int T> DI unsigned bofs(const unsigned (&boff)[T], int t) { return T == 6 && t >= 3 ? boff[t - 3] + 6144 : boff[t]; }
template <int T, bool TR, int KS, class Extra>
DI void kstep(f32x4 (&acc)[4][T], const u32x4 (&ac)[4], const u32x2 (&bc)[T][2], u32x4 (&an)[4], u32x2 (&bn)[T][2],
              const unsigned (&aoff)[4], const unsigned (&boff)[T], const Dma& d, Extra&& extra) {
  constexpr int s = KS >> 1, KIND = KS & 1, NP = KIND == 0 ? 4 : 3, ST = KIND == 0 ? (STA ? STA : (4 * T - 1) / NP) : (STB ? STB : (4 * T - 1) / NP);
  constexpr int sn = (KS + 1) >> 1;
  constexpr unsigned asl = a_slot(sn % 3), bsl = ((KS + 1) % 5) * 12288u;
  static_for<0, 4 * T>([&](auto E) {
    constexpr int e = E.v, t = e >> 2, i = (SNAKE && (t & 1)) ? 3 - (e & 3) : (e & 3);
    const u32x4 bf = cat2(bc[t][0], bc[t][1]);
    if constexpr (KS == 0) { if (TR) mfma_z(acc[i][t], bf, ac[i]); else mfma_z(acc[i][t], ac[i], bf); }
    else { if (TR) mfma_ip(acc[i][t], bf, ac[i]); else mfma_ip(acc[i][t], ac[i], bf); }
    SB;
    if constexpr (KS < 19) {
      if constexpr (e < 4) an[e] = lds_ld<u32x4>(aoff[((KS + 1) & 1) + (sn % 3 == 0 ? 2 : 0)] + (sn % 3 == 0 ? 0u : asl) + e * 2048);
      else if constexpr (e < 4 + 2 * T) bn[(e - 4) >> 1][e & 1] = ldtr1(bofs<T>(boff, (e - 4) >> 1) + bsl + (e & 1) * 768);
    }
    constexpr int pc = e / ST;
    if constexpr (KIND == 0) {
      constexpr int adv = s < 8 ? (s + 2) * 128 : 0, dst = (int)a_slot((s + 2) % 3) + pc * 1024, off = dst >= adv ? adv : 0;
      if constexpr (pc < NP && e % ST == ST - 2) set_m0i<dst - off>(d.mA);
      if constexpr (pc < NP && e % ST == ST - 1) dma_goi<off>((pc & 1) ? d.vA1 : d.vA0, d.rsA, d.sA[pc] + (adv - off));
    } else {
      constexpr int h0 = (2 * s) % 5;
      if constexpr (pc < NP && e % ST == ST - 2) set_m0i<(h0 == 4 ? 0 : h0 * 12288) + pc * 1024>(h0 == 4 ? d.mW : d.mB);
      if constexpr (pc < NP && e % ST == ST - 1) dma_goi<0>(pc == 0 ? d.vB0 : (pc == 1 ? d.vB1 : d.vB2), d.rsB, d.sB);
    }
    extra(E);
    SB;
  });
}

struct RowStat { u32x4 xa; unsigned xb, row; float s, q; };
DI void row_load(RowStat& R, rsrc_t rsX, unsigned lane) {
  asm volatile("buffer_load_dwordx4 %0, %2, %4, %5 offen\n\tbuffer_load_dword %1, %3, %4, %5 offen offset:1024"
               : "=&v"(R.xa), "=&v"(R.xb) : "v"(lane * 16), "v"(lane * 4), "s"(rsX), "s"(R.row * 1280) : "memory");
}
template <int K> DI void row_step(RowStat& R, unsigned lane) {
  if constexpr (K < 5) {
    if constexpr (K == 0) { R.s = 0.f; R.q = 0.f; }
    const unsigned v = K < 4 ? R.xa[K & 3] : R.xb;
    asm volatile("v_dot2c_f32_bf16 %0, %2, %3\n\tv_dot2c_f32_bf16 %1, %2, %2" : "+v"(R.s), "+v"(R.q) : "v"(v), "v"(0x3f803f80u));
  } else if constexpr (K < 11) {
    constexpr int C = K == 5 ? 0xB1 : (K == 6 ? 0x4E : (K == 7 ? 0x141 : (K == 8 ? 0x140 : (K == 9 ? 0x142 : 0x143))));
    if constexpr (K == 5) asm volatile("s_nop 2" : "+v"(R.s), "+v"(R.q));
    R.s = dpp_add<C>(R.s); R.q = dpp_add<C>(R.q);
  } else if constexpr (K == 11) {
    R.s *= 1.0f / D;
    R.q = __builtin_fmaxf(R.q * (1.0f / D) - R.s * R.s, 0.f);
  } else if constexpr (K == 12) {
    R.q = __builtin_amdgcn_sqrtf(rbf(rbf(R.q) + 1e-5f));
  } else {
    lds_st<unsigned>(lane == 63 ? L_GO + 32 + (R.row & 7) * 4 : L_GO + 64, pk2(R.s, R.q));
  }
  if constexpr (K >= 5 && K < 13) asm volatile("" : "+v"(R.s), "+v"(R.q));
}
DI void row_publish(const RowStat& R, char* ws, unsigned tag, unsigned tid) {
  if (tid < 8) __hip_atomic_store((gu64*)(ws + WS_STAT) + R.row + tid, ((unsigned long long)tag << 32) | lds_ld<unsigned>(L_GO + 32 + tid * 4), RLX_AGENT);
}

struct Gran { u32x2 g0, g1, g2; unsigned off, step; };
DI void gran_issue(Gran& G, rsrc_t rs) {
  asm volatile("buffer_load_dwordx2 %0, %3, %4, 0 offen sc1\n\tbuffer_load_dwordx2 %1, %3, %4, %5 offen sc1\n\t"
               "buffer_load_dwordx2 %2, %3, %4, %6 offen sc1"
               : "=&v"(G.g0), "=&v"(G.g1), "=&v"(G.g2) : "v"(G.off), "s"(rs), "s"(G.step), "s"(2 * G.step) : "memory");
}
DI void gran_load(Gran& G, rsrc_t rs) {
  asm volatile("buffer_load_dwordx2 %0, %3, %4, 0 offen sc1\n\tbuffer_load_dwordx2 %1, %3, %4, %5 offen sc1\n\t"
               "buffer_load_dwordx2 %2, %3, %4, %6 offen sc1\n\ts_waitcnt vmcnt(0)"
               : "=&v"(G.g0), "=&v"(G.g1), "=&v"(G.g2) : "v"(G.off), "s"(rs), "s"(G.step), "s"(2 * G.step) : "memory");
}

DI unsigned urow(unsigned pos, unsigned rot) { const unsigned u = pos + rot; return (u >= 54 ? u - 54 : u) * 40960u; }

constexpr unsigned wo_tile(unsigned v) { return (v / 5) * 10 + (v % 5 == 4 ? 4 : (v % 5 < 2 ? 2 * (v % 5) : 2 * (v % 5) + 1)); }
struct W2 { rsrc_t wd, wo; unsigned rot, col; };
DI unsigned w2_off(const W2& W, int u, unsigned lw) {
  if (u < 54) return urow(u, W.rot) + lw * 10240u + W.col;
  return wo_tile(u - 54) * 20480u + (lw >> 1) * ((u - 54) % 5 == 4 ? 102400u : 20480u) + (lw & 1) * 10240u + W.col;
}
DI u32x4 w2_load(const W2& W, int u, unsigned lw, unsigned voff) {
  return __builtin_amdgcn_raw_buffer_load_b128(u < 54 ? W.wd : W.wo, voff, w2_off(W, u, lw), 0);
}

template <int T, bool TR, bool PF, bool LASTA, class Pre>
DI void main_loop(Pre&& pre, RowStat& R, char* ws, f32x4 (&acc)[4][T], unsigned (&boff)[T], Dma& d, unsigned w, unsigned lane, unsigned tid,
                  unsigned epoch, unsigned& tag, unsigned rb, unsigned ccol, rsrc_t rsB2, rsrc_t rsWS, unsigned cb, unsigned rot, u32x4 (&pre2)[36] STAMP_ARG) {
  const unsigned g = lane >> 4, r = lane & 15, m = w & 3;
  unsigned aoff[4];
  aoff[0] = (64 * m + r) * 128 + ((g ^ (r >> 1)) << 4);
  aoff[1] = aoff[0] ^ 64;
  aoff[2] = aoff[0] + a_slot(0); aoff[3] = aoff[1] + a_slot(0);
  const unsigned wq = d.wq, wa = d.wa;
  asm volatile("" : "+v"(aoff[0]), "+v"(aoff[1]), "+v"(aoff[2]), "+v"(aoff[3]), "+v"(boff[0]), "+v"(boff[1]), "+v"(boff[2]));
  if (T == 5) asm volatile("" : "+v"(boff[3]), "+v"(boff[4]));
  Gran G;
  unsigned v2, vb2, vpf, tA[3], tB[3];
  row_load(R, d.rsA, lane);
  {
    asm volatile("s_waitcnt vmcnt(%2) lgkmcnt(0)\n\tv_readfirstlane_b32 %0, %1\n\ts_nop 3" : "=s"(tag) : "v"(epoch), "i"(5 + NA1) : "memory");
    tag = rfl(tag) + 1;
    bar();
  }
  STAMP(1);
  u32x4 a0[4], a1[4];
  u32x2 b0[T][2], b1[T][2];
#pragma unroll
  for (int i = 0; i < 4; ++i) a0[i] = lds_ld<u32x4>(aoff[2] + i * 2048);
#pragma unroll
  for (int t = 0; t < T; ++t) { b0[t][0] = ldtr1(bofs<T>(boff, t)); b0[t][1] = ldtr1(bofs<T>(boff, t) + 768); }
  SB;
#pragma unroll
  for (int i = NA1; i < 4; ++i) dma16(a_slot(1) + d.mA + i * 1024, (i & 1) ? d.vA1 : d.vA0, d.rsA, d.sA[i] + 128);
  const bool y = w >= 4;
  bool ok = true;
  auto ln_store = [&] {
    const float v0 = y ? -bflo(G.g0[0]) : __builtin_bit_cast(float, G.g0[0]) + __builtin_bit_cast(float, G.g1[0]) + __builtin_bit_cast(float, G.g2[0]);
    const unsigned la = L_MISC + (y ? tid * 4 - 1024 : 2048 + tid * 4);
    lds_st<float>(la, v0);
    lds_st<float>(y ? la + 1024 : L_GO + 64 + (lane & 7) * 4, __builtin_amdgcn_rcpf(bfhi(G.g0[0])));
  };
  static_for<0, 10>([&](auto S_) {
    constexpr int s = S_.v, L = 4 * T;
    constexpr int NX = (PF && s >= 1 && 2 * s <= NPRE) ? 2 : 0;
    kstep<T, TR, 2 * s>(acc, a0, b0, a1, b1, aoff, boff, d, [&](auto E) {
      constexpr int e = E.v;
      if constexpr (s == 2) {
        if constexpr (e == 2) { vb2 = v2 + (3 * wq) * 10240u; asm volatile("" : "+v"(vb2)); }
        if constexpr (e == 5) { G.off = w < 4 ? WS_CSUM + ccol * 8 : WS_STAT + (rb * 256 + tid - 256) * 8; asm volatile("" : "+v"(G.off)); }
        if constexpr (e == 8) { G.step = w < 4 ? NCOL * 8 : 0; tA[0] = urow(wa, rot) + cb * 128u; asm volatile("" : "+s"(G.step), "+s"(tA[0])); }
        if constexpr (e == 10) { tA[1] = urow(8 + wa, rot) + cb * 128u; asm volatile("" : "+s"(tA[1])); }
        if constexpr (e == 12) { tA[2] = urow(31 + wa, rot) + cb * 128u; asm volatile("" : "+s"(tA[2])); }
        if constexpr (e == 14) {
          tB[0] = urow(16, rot) + cb * 128u; tB[1] = urow(w < 4 ? 19u : 22u, rot) + cb * 128u; tB[2] = urow(w < 4 ? 25u : 28u, rot) + cb * 128u;
          asm volatile("" : "+s"(tB[0]), "+s"(tB[1]), "+s"(tB[2]));
        }
      }
      if constexpr (s == (TR ? 1 : 3) && e == 2) pre();
      if constexpr (NX && e >= L - 2) {
        constexpr int u = 2 * (s - 1) + e - (L - 2);
        pre2[u] = __builtin_amdgcn_raw_buffer_load_b128(rsB2, vpf, urow(36 + u, rot) + (w & 3) * 10240u + cb * 128u, 0);
      }
      if constexpr (s < 7) { if constexpr (e == L - 3) d.sB += s == 0 ? d.advB * (1 + (w >> 2)) : d.advB; }
      else if constexpr (s == 7) {
        if constexpr (e == L - 6) d.vB0 = y ? vb2 : d.vB0;
        if constexpr (e == L - 4) d.vB1 = y ? vb2 + 10240u : d.vB1;
        if constexpr (e == L - 3) d.vB2 = y ? vb2 + 20480u : d.vB2;
        if constexpr (e == L - 2) d.sB = y ? tB[0] : d.sB + d.advB;
        if constexpr (e == L - 1) d.rsB = y ? rsB2 : d.rsB;
      } else {
        if constexpr (e == L - 3) { d.sB = tB[s - 7]; d.rsB = rsB2; d.vB0 = vb2; d.vB1 = vb2 + 10240u; d.vB2 = vb2 + 20480u; }
      }
      if constexpr (s == 9 && e == L - 4) {
        asm volatile("s_waitcnt vmcnt(7)" : "+v"(G.g0), "+v"(G.g1), "+v"(G.g2)::"memory");
        ok = all64(G.g0[1] == tag && G.g1[1] == tag && G.g2[1] == tag);
      }
      if constexpr (s == 9 && e == L - 3) ln_store();
    });
    if constexpr (s == 0) {
      wait_vm<8 - NA1>();
      static_for<0, 14>([&](auto K) { row_step<K.v>(R, lane); });
    }
    if constexpr (s == 9) {
      if (__builtin_expect(!ok, 0)) {
#pragma unroll 1
        for (unsigned spins = 0; spins < SPIN_CAP; ++spins) {
          __builtin_amdgcn_s_sleep(2);
          gran_load(G, rsWS);
          if (all64(G.g0[1] == tag && G.g1[1] == tag && G.g2[1] == tag)) break;
        }
        ln_store();
      }
    }
    wait_lgkm0();
    if constexpr (s < 9) wait_vm<4 + NX>();
    bar();
    if constexpr (s == 0) { STAMP(12); }
    SB;
    kstep<T, TR, 2 * s + 1>(acc, a1, b1, a0, b0, aoff, boff, d, [&](auto E) {
      constexpr int e = E.v;
      if constexpr (s == 0 && e == 6) {
        if (y) {
          const unsigned so = d.sB - d.advB;
          dma16(d.mW + 49152, d.vB0, d.rsB, so); dma16(d.mW + 49152 + 1024, d.vB1, d.rsB, so); dma16(d.mW + 49152 + 2048, d.vB2, d.rsB, so);
        }
      }
      if constexpr (s == 0 && e == L - 4) {
        v2 = (lane >> 3) * 1280 + (((lane & 7) ^ (((lane >> 4) & 3) << 1)) << 4);
        vpf = y ? v2 : 0x7ffffff0u;
        asm volatile("" : "+v"(v2), "+v"(vpf));
      }
      if constexpr (s == 0 && e == L - 6) row_publish(R, ws, tag, tid);
      if constexpr (s == 8 && e == 0) gran_issue(G, rsWS);
      if constexpr (s >= 7 && s < 9 && e >= L - 4) {
        d.rsA = rsB2; d.vA0 = v2; d.vA1 = v2;
        d.sA[e - (L - 4)] = tA[s - 7] + (e - (L - 4)) * 10240;
      }
      if constexpr (s == 9 && LASTA && e == 7) {
        if (wa < 5) {
#pragma unroll
          for (int i = 0; i < 4; ++i) dma16(a_slot(0) + d.mA + i * 1024, v2, rsB2, tA[2] + i * 10240);
        }
      }
    });
    if constexpr (s == 8) { STAMP(13); }
    if constexpr (s == 2) { STAMP(26); }
    if constexpr (s == 6) { STAMP(27); }
  });
  acc_fence<4 * T>(&acc[0][0]);
  STAMP(14);
}

DI void st16(u32x4 v, rsrc_t rs, unsigned voff, unsigned soff) {
  __builtin_amdgcn_raw_buffer_store_b128(v, rs, voff, soff, 16);
  asm volatile("s_nop 1" ::"v"(v) : "memory");
}
DI void publish_flag(char* ws, unsigned flag_off, unsigned tag, unsigned tid) {
  drain_vm();
  bar();
  if (tid == 0) __hip_atomic_store((gu32*)(ws + flag_off), tag, RLX_AGENT);
}
DI void st_frag(rsrc_t rs, unsigned lane, unsigned off, u32x2 lo, u32x2 hi) { st16(cat2(lo, hi), rs, lane * 16, off); }
DI void shared_send(unsigned w, unsigned lane, const u32x2 (&sh)[4]) {
  if (w >= 4) lds_st<u32x4>(L_SCR + w * 1024 + lane * 16, cat2(sh[0], sh[1]));
  else lds_st<u32x4>(L_SCR + w * 1024 + lane * 16, cat2(sh[2], sh[3]));
  asm volatile("" ::: "memory");
}
DI void shared_recv(rsrc_t rs, unsigned w, unsigned lane, unsigned off, unsigned str, const u32x2 (&sh)[4]) {
  lbar();
  u32x4 rcv = lds_ld<u32x4>(L_SCR + (w ^ 4) * 1024 + lane * 16);
  u32x2 r0 = {rcv[0], rcv[1]}, r1 = {rcv[2], rcv[3]};
  if (w >= 4) { st_frag(rs, lane, off + 2 * str, r0, sh[2]); st_frag(rs, lane, off + 3 * str, r1, sh[3]); }
  else        { st_frag(rs, lane, off, sh[0], r0); st_frag(rs, lane, off + str, sh[1], r1); }
}

__global__ __launch_bounds__(512, 2) void fused_block(Params p) {
  char* const ws = (char*)(((unsigned long long)p.wsraw + p.wsal) & ~p.wsal);
  unsigned tid = __builtin_amdgcn_workitem_id_x(), lane = tid & 63;
  const unsigned w = rfl(tid >> 6);
  const unsigned b = __builtin_amdgcn_workgroup_id_x(), xcd = b & 7, slot = b >> 3;
  const unsigned rb = 4 * (xcd & 1) + (slot & 3), jq = xcd >> 1, ji = slot >> 2;
  const unsigned spi = (xcd - 4) * 4 + (slot & 3);
  const unsigned long long jl0 = (jq & 1) ? 0x4123220908070605ull : 0x4021200403020100ull, jl1 = (jq & 1) ? 0x4327266011100f0eull : 0x422524600d0c0b0aull;
  const unsigned jb = (unsigned)(((jq & 2) ? jl1 : jl0) >> (8 * ji)) & 0xff, jtype = jb >> 5, jidx = jb & 31;
  STAMP_DECL;
  STAMP(0);

  Dma d;
  RowStat rs_;
  const bool mlp = jtype == J_MLP;
  const unsigned pitch = mlp ? 3456u : 3840u, wq = (w + (ROTW ? slot : 0)) & 3, wa = (w + (ROTW ? ji : 0)) & 7;
  d.wa = wa; d.wq = wq;
  const rsrc_t rsX = mk_rsrc(p.x, S * D * 2);
  d.vA0 = (lane >> 3) * 1280 + (((lane & 7) ^ (lane >> 4)) << 4); d.vA1 = d.vA0 ^ 64;
  d.rsA = rsX; d.mA = wa * 4096u;
#pragma unroll
  for (int i = 0; i < 4; ++i) d.sA[i] = (rb * 256 + 32 * wa + 8 * i) * 1280;
  unsigned epoch, tag;
  asm volatile("s_nop 4" :: "s"(rsX) : "memory");
  STAMP(6);
#pragma unroll
  for (int i = 0; i < 4; ++i) dma16(a_slot(0) + d.mA + i * 1024, (i & 1) ? d.vA1 : d.vA0, d.rsA, d.sA[i]);
  asm volatile("global_load_dword %0, %1, %2 sc1" : "=&v"(epoch) : "v"(0u), "s"(ws) : "memory");
  SB;
  {
    unsigned vb[3];
    const unsigned lim = mlp ? 176u : 144u;
    {
      const unsigned r0 = (lane * 43) >> 9, p0 = lane - 12 * r0;
      const bool w1 = p0 >= 8, w2 = p0 >= 4;
      const unsigned r1 = r0 + (w1 ? 6 : 5), p1 = p0 + (w1 ? -8 : 4), r2 = r0 + (w2 ? 11 : 10), p2 = p0 + (w2 ? -4 : 8);
      vb[0] = r0 * pitch + umin(p0 * 16, lim);
      vb[1] = r1 * pitch + umin((p1 * 16) ^ ((r1 & 8) << 2), lim);
      vb[2] = r2 * pitch + umin((p2 * 16) ^ 32, lim);
    }
    d.vB0 = vb[0]; d.vB1 = vb[1]; d.vB2 = vb[2];
    const unsigned mh = wq >> 1;
    const void* bw = mlp ? (mh ? p.wu : p.wg) : p.wqkv;
    const unsigned cj = mlp ? 192u : 160u * jtype, cm = mlp ? 0u : (jtype == J_KV ? 1280u : 160u), c0 = jtype == J_KV ? 1280u : 0u;
    d.rsB = mk_rsrc(bw, D * pitch);
    d.sB = (16 * (wq & 1) + (w < 4 ? 32 : 0)) * pitch + c0 + cj * jidx + cm * mh; d.advB = 64 * pitch;
    d.mB = L_B + wq * 3072u + (w < 4 ? 0u : 12288u); d.mW = L_B + wq * 3072u + (w < 4 ? 49152u : 0u);
    const unsigned bdst = L_B + (w < 4 ? 12288u : 0u) + wq * 3072u;
    rs_.row = rfl(b * 8 + w);
    if (jtype != J_SPARE) {
      dma16(bdst, d.vB0, d.rsB, d.sB); dma16(bdst + 1024, d.vB1, d.rsB, d.sB); dma16(bdst + 2048, d.vB2, d.rsB, d.sB);
      const unsigned so = d.sB + d.advB;
      dma16(bdst + 24576, d.vB0, d.rsB, so); dma16(bdst + 24576 + 1024, d.vB1, d.rsB, so); dma16(bdst + 24576 + 2048, d.vB2, d.rsB, so);
#pragma unroll
      for (int i = 0; i < NA1; ++i) dma16(a_slot(1) + d.mA + i * 1024, (i & 1) ? d.vA1 : d.vA0, d.rsA, d.sA[i] + 128);
    }
    d.sB += d.advB;
  }
  STAMP(10);
  lds_st<unsigned>(L_GO + (tid & 1) * 4, 0u);

  const unsigned t2 = 28 * xcd + slot;
  const bool has2 = slot < 28 && t2 < 220;
  const unsigned G = t2 / 10, cb = t2 % 10;
  constexpr unsigned rot = 32;
  unsigned g = lane >> 4, r = lane & 15, q4 = r >> 2, p4 = lane & 3;
  const rsrc_t rsWd = mk_rsrc(p.wd, has2 ? F * D * 2 : 0);
  const rsrc_t rsWS = mk_rsrc(ws, WS_END);
  unsigned v2 = (lane >> 3) * 1280 + (((lane & 7) ^ (((lane >> 4) & 3) << 1)) << 4);
  auto relane = [&] {
    asm volatile("" : "+v"(tid));
    lane = tid & 63; g = lane >> 4; r = lane & 15; q4 = r >> 2; p4 = lane & 3;
    v2 = (lane >> 3) * 1280 + (((lane & 7) ^ (((lane >> 4) & 3) << 1)) << 4);
  };
  const unsigned m = w & 3, n = w >> 2;
  const unsigned brow = L_B + (8 * g + q4) * 192;
  const unsigned bx = (g & 1) << 5;

  u32x4 pre2[36];
  W2 W;
  W.wd = rsWd; W.wo = mk_rsrc(p.wo, has2 ? D * D * 2 : 0); W.rot = rot; W.col = cb * 128u;
  if (jtype == J_MLP) {
    f32x4 acc[4][6];
    unsigned boff[6];
#pragma unroll
    for (int t = 0; t < 3; ++t) {
      const unsigned gt = n ? (t == 2 ? 3 : 4 + t) : t;
      boff[t] = brow + ((32 * gt + 8 * p4) ^ bx); boff[t + 3] = boff[t] + 6144;
    }
    const unsigned ccol = tid < 96 ? 1920 + 96 * jidx + tid : 3648 + 96 * jidx + (tid < 192 ? tid - 96 : 0);
    main_loop<6, true, true, true>([] {}, rs_, ws, acc, boff, d, w, lane, tid, epoch, tag, rb, ccol, rsWd, rsWS, cb, rot, pre2 STAMP_PASS);
    relane();
    float mm[4];
    f32x4 cg[3], cu[3];
#pragma unroll
    for (int i = 0; i < 4; ++i) mm[i] = lds_ld<float>(L_MISC + (64 * m + 16 * i + r) * 4);
#pragma unroll
    for (int t = 0; t < 3; ++t) {
      const unsigned gt = n ? (t == 2 ? 3 : 4 + t) : t;
      cg[t] = lds_ld<f32x4>(L_MISC + 2048 + (16 * gt + 4 * g) * 4); cu[t] = lds_ld<f32x4>(L_MISC + 2048 + (96 + 16 * gt + 4 * g) * 4);
    }
    auto hval = [&](int i, int t) {
      f32x4 gg = acc[i][t] + mm[i] * cg[t];
      const f32x4 uu = acc[i][t + 3] + mm[i] * cu[t];
#pragma unroll
      for (int j = 0; j < 4; ++j) gg[j] = relu2(gg[j]);
      return pk4(gg * uu);
    };
    const unsigned ho = WS_HA + rb * RBSZ + jidx * 49152 + m * 4 * 3072;
#pragma unroll
    for (int i = 0; i < 4; ++i) { st_frag(rsWS, lane, ho + i * 3072 + n * 2048, hval(i, 0), hval(i, 1)); SB; }
    const u32x2 sh[4] = {hval(0, 2), hval(1, 2), hval(2, 2), hval(3, 2)};
    shared_send(w, lane, sh);
    shared_recv(rsWS, w, lane, ho + 1024, 3072, sh);
    STAMP(15);
    publish_flag(ws, WS_FLAGH + (rb * 32 + jidx) * FS, tag, tid);
    STAMP(3);
  } else if (jtype == J_KV) {
    f32x4 acc[4][5];
    unsigned boff[5];
    boff[0] = brow + ((80 * n + 8 * p4) ^ bx); boff[1] = boff[0] + 6144;
    boff[2] = brow + ((80 * n + 40 + 8 * p4) ^ bx); boff[3] = boff[2] + 6144;
    boff[4] = brow + ((80 * n + 32 + 40 * (p4 >> 1)) ^ bx) + (p4 & 1) * 6144;
    const unsigned kb = 126976u + m * 6720 + n * 3360, dump = L_MISC - 2048 + lane * 16;
    const unsigned ea = (q4 == 1 || r == 0) ? kb + (r == 0 ? 20 : 12 + r) * 80 : 0u, eb = (q4 == 3 || r == 0) ? kb + 1680 + (r == 0 ? 20 : 4 + r) * 80 : 0u;
    unsigned ka[6] = {kb + r * 80 + g * 16, ea ? ea + g * 16 : dump, eb ? eb + g * 16 : dump, g == 0 ? kb + r * 80 + 64 : dump,
                      g == 2 ? kb + 1680 + r * 80 + 64 : dump, (g == 0 && ea) ? ea + 64 : ((g == 2 && eb) ? eb + 64 : dump)};
    auto pin = [&] { asm volatile("" : "+v"(ka[0]), "+v"(ka[1]), "+v"(ka[2]), "+v"(ka[3]), "+v"(ka[4]), "+v"(ka[5])); };
    const unsigned ccol = tid < 80 ? 640 + 80 * jidx + tid : 1280 + 80 * jidx + (tid < 160 ? tid - 80 : 0);
    main_loop<5, false, false, false>(pin, rs_, ws, acc, boff, d, w, lane, tid, epoch, tag, rb, ccol, rsWd, rsWS, cb, rot, pre2 STAMP_PASS);
    relane();
    float cs[5];
    cs[0] = lds_ld<float>(L_MISC + 2048 + (40 * n + r) * 4);
    cs[1] = lds_ld<float>(L_MISC + 2048 + (80 + 40 * n + r) * 4);
    cs[2] = lds_ld<float>(L_MISC + 2048 + (40 * n + 20 + r) * 4);
    cs[3] = lds_ld<float>(L_MISC + 2048 + (80 + 40 * n + 20 + r) * 4);
    cs[4] = lds_ld<float>(L_MISC + 2048 + ((q4 & 1) * 80 + 40 * n + 16 + 20 * (q4 >> 1) + (r & 3)) * 4);
    u32x2 pk[4][3], vv[4][3];
    const unsigned vmask = 0u - (q4 & 1);
    f32x4 mm4[4], rr4[4];
#pragma unroll
    for (int i = 0; i < 4; ++i) {
      mm4[i] = lds_ld<f32x4>(L_MISC + (64 * m + 16 * i + 4 * g) * 4); rr4[i] = lds_ld<f32x4>(L_MISC + 1024 + (64 * m + 16 * i + 4 * g) * 4);
    }
    SB;
#pragma unroll
    for (int i = 0; i < 4; ++i) {
      const f32x4 mm = mm4[i], rr = rr4[i];
      const f32x4 rl = rr * 1.4426950408889634f, rh = rr * 0.5f;
      f32x4 val[5];
#pragma unroll
      for (int t = 0; t < 5; ++t) val[t] = acc[i][t] + mm * cs[t];
#pragma unroll
      for (int t = 0; t < 3; ++t) pk[i][t] = pk4(phi4(val[2 * t], rl, rh));
      vv[i][0] = pk4(val[1] * rr); vv[i][1] = pk4(val[3] * rr);
      const u32x2 v4 = pk4(val[4] * rr);
      vv[i][2] = u32x2{bsel(vmask, v4[0], 0x3f803f80u), bsel(vmask, v4[1], 0x3f803f80u)};
    }
    STAMP(25);
    f32x4 P[7] = {};
#pragma unroll
    for (int i = 0; i < 4; i += 2) {
      const bf16x8 k0 = frag(pk[i][0], pk[i + 1][0]), k2 = frag(pk[i][1], pk[i + 1][1]), k4 = frag(pk[i][2], pk[i + 1][2]);
      const bf16x8 v1 = frag(vv[i][0], vv[i + 1][0]), v3 = frag(vv[i][1], vv[i + 1][1]), v4 = frag(vv[i][2], vv[i + 1][2]);
      P[0] = mfma(k0, v1, P[0]); P[1] = mfma(k0, v4, P[1]); P[2] = mfma(k4, v1, P[2]);
      P[3] = mfma(k2, v3, P[3]); P[4] = mfma(k2, v4, P[4]); P[5] = mfma(k4, v3, P[5]); P[6] = mfma(k4, v4, P[6]);
    }
    lds_st<f32x4>(ka[0], P[0]); lds_st<f32x4>(ka[0] + 1680, P[3]); lds_st<f32x4>(ka[1], P[1]); lds_st<f32x4>(ka[2], P[4]);
    lds_st<f32x4>(ka[3], P[2]); lds_st<f32x4>(ka[4], P[5]); lds_st<f32x4>(ka[5], P[6]);
    lbar();
    if (tid < 210) {
      const unsigned a = 126976u + tid * 32;
      const f32x4 t0 = (lds_ld<f32x4>(a) + lds_ld<f32x4>(a + 6720)) + (lds_ld<f32x4>(a + 13440) + lds_ld<f32x4>(a + 20160));
      const f32x4 t1 = (lds_ld<f32x4>(a + 16) + lds_ld<f32x4>(a + 6736)) + (lds_ld<f32x4>(a + 13456) + lds_ld<f32x4>(a + 20176));
      st16(cat2(pk4(t0), pk4(t1)), rsWS, tid * 16, WS_KVP + (rb * 32 + 4 * jidx) * 840);
    }
    wait_lgkm0();
    STAMP(15);
    publish_flag(ws, WS_FLAGKV + ((jidx >> 1) * 32 + (jidx & 1) * 8 + rb) * FS, tag, tid);
    STAMP(3);
  } else if (jtype == J_Q) {
    f32x4 acc[4][5];
    unsigned boff[5];
#pragma unroll
    for (int t = 0; t < 5; ++t) boff[t] = brow + n * 6144 + ((32 * t + 8 * p4) ^ bx);
    const unsigned ccol = 160 * jidx + (tid < 160 ? tid : 0);
    unsigned tch[3];
    auto warm = [&] {
      const unsigned gq = (rb * 4 + jidx) * 8 + w;
      asm volatile("buffer_load_dword %0, %3, %5, 0 offen\n\tbuffer_load_dword %1, %3, %5, %7 offen\n\tbuffer_load_dword %2, %4, %6, 0 offen"
                   : "=&v"(tch[0]), "=&v"(tch[1]), "=&v"(tch[2])
                   : "v"((gq * 34 + (lane < 34 ? lane : 0)) * 128), "v"((gq * 25 + (lane < 25 ? lane : 0)) * 128), "s"(mk_rsrc(p.wd, F * D * 2)),
                     "s"(mk_rsrc(p.wo, D * D * 2)), "s"(8576u * 128u) : "memory");
    };
    main_loop<5, true, false, false>(warm, rs_, ws, acc, boff, d, w, lane, tid, epoch, tag, rb, ccol, rsWd, rsWS, cb, rot, pre2 STAMP_PASS);
    relane();
    asm volatile("" :: "v"(tch[0]), "v"(tch[1]), "v"(tch[2]));
    u32x2 pq[4][5];
    {
      float mm[4], rr[4];
      f32x4 c[5];
#pragma unroll
      for (int i = 0; i < 4; ++i) { mm[i] = lds_ld<float>(L_MISC + (64 * m + 16 * i + r) * 4); rr[i] = lds_ld<float>(L_MISC + 1024 + (64 * m + 16 * i + r) * 4); }
#pragma unroll
      for (int t = 0; t < 5; ++t) c[t] = lds_ld<f32x4>(L_MISC + 2048 + (80 * n + 16 * t + 4 * g) * 4);
      SB;
#pragma unroll
      for (int i = 0; i < 4; ++i)
#pragma unroll
        for (int t = 0; t < 5; ++t) pq[i][t] = pk4(phi4(acc[i][t] + mm[i] * c[t], splat(rr[i] * 1.4426950408889634f), splat(rr[i] * 0.5f)));
#pragma unroll
      for (int i = 0; i < 4; ++i) asm volatile("" : "+v"(pq[i][0]), "+v"(pq[i][1]), "+v"(pq[i][2]), "+v"(pq[i][3]), "+v"(pq[i][4]));
    }
    constexpr unsigned KP = 208, KQ = 84 * KP;
    unsigned wa[2];
#pragma unroll
    for (int k = 0; k < 2; ++k) {
      const unsigned qd = 2 * (tid < 420 ? tid : 0) + k, hd = qd / 105, e = (qd % 105) / 5, dq = qd % 5, hl = hd & 3;
      wa[k] = (hd >> 2) * KQ + (e < 20 ? hl * 20 + e : 80 + hl) * KP + hl * 40 + dq * 8;
    }
    unsigned fk = n * KQ + r * KP + g * 8, fz = n * KQ + (80 + (r & 3)) * KP + g * 8;
    unsigned gm[3] = {g < 1 ? ~0u : 0u, g < 2 ? ~0u : 0u, g < 3 ? ~0u : 0u};
    u32x4 B[4][3];
    f32x4 eps4 = splat(1e-6f);
#pragma unroll
    for (int i = 0; i < 4; ++i) {
      B[i][0] = cat2(pq[i][0], pq[i][1]); B[i][1] = cat2(pq[i][2], pq[i][3]); B[i][2] = cat2(pq[i][4], u32x2{0u, 0u});
      asm volatile("" : "+v"(B[i][0]), "+v"(B[i][1]), "+v"(B[i][2]));
    }
    asm volatile("" : "+v"(wa[0]), "+v"(wa[1]), "+v"(fk), "+v"(fz), "+v"(gm[0]), "+v"(gm[1]), "+v"(gm[2]), "+v"(eps4));
    STAMP(25);
    wait_vm<0>();
    bar();
#pragma unroll
    for (int k = 0; k < 5; ++k) lds_st<u32x4>(k * 8192 + tid * 16, u32x4{0u, 0u, 0u, 0u});
    if (w == 0) poll_flags((gu32*)(ws + WS_FLAGKV + (jidx * 32 + (lane & 15)) * FS), true, tag);
    lbar();
    STAMP(4);
    if (w < 7) {
      u32x4 v[8];
#pragma unroll
      for (int j = 0; j < 8; ++j)
        v[j] = __builtin_amdgcn_raw_buffer_load_b128(rsWS, (tid < 420 ? tid : 0) * 16, WS_KVP + (j * 32 + 8 * jidx) * 840, 16);
      SB;
      f32x4 t0 = {0.f, 0.f, 0.f, 0.f}, t1 = t0;
#pragma unroll
      for (int j = 0; j < 8; ++j) {
        t0 += f32x4{bflo(v[j][0]), bfhi(v[j][0]), bflo(v[j][1]), bfhi(v[j][1])};
        t1 += f32x4{bflo(v[j][2]), bfhi(v[j][2]), bflo(v[j][3]), bfhi(v[j][3])};
      }
      lds_st<u32x2>(wa[0], pk4(t0)); lds_st<u32x2>(wa[1], pk4(t1));
    }
    lbar();
    STAMP(16);
    u32x4 A[12];
#pragma unroll
    for (int P = 0; P < 3; ++P) A[P] = cat2(lds_ld<u32x2>(fz + P * 64), lds_ld<u32x2>(fz + P * 64 + 32));
#pragma unroll
    for (int u = 0; u < 5; ++u) {
      const unsigned a0 = fk + u * 16 * KP + (u < 3 ? 0 : 64);
      A[3 + u] = cat2(lds_ld<u32x2>(a0), lds_ld<u32x2>(a0 + 32));
    }
#pragma unroll
    for (int u = 1; u < 5; ++u) {
      const unsigned a0 = fk + u * 16 * KP + (u < 3 ? 64 : 128);
      A[7 + u] = cat2(lds_ld<u32x2>(a0), lds_ld<u32x2>(a0 + 32));
    }
    SB;
    u32x2 sh[4];
    f32x4 den[4], num[2][5];
    static_for<0, 12>([&](auto E) {
      constexpr int P = E.v >> 2, i = E.v & 3;
      if constexpr (P == 0) mfma_c(den[i], A[0], B[i][0], eps4); else mfma_ip(den[i], A[P], B[i][P]);
    });
    u32x2 o[5];
    const unsigned oo = WS_HA + rb * RBSZ + 18 * 49152 + jidx * 81920 + m * 4 * 5120;
    auto fin = [&](auto I, auto K) {
      constexpr int i = I.v, k = K.v, u = k < 8 ? k >> 1 : 4;
      if constexpr (k == 8 || !(k & 1)) {
        asm volatile("" : "+v"(num[i & 1][u]));
        const float rr = u == 0 ? den[i][0] : (u == 4 ? den[i][3] : bself(gm[(u + 2) % 3], den[i][(u + 3) & 3], den[i][u & 3]));
#pragma unroll
        for (int j = 0; j < 4; ++j) num[i & 1][u][j] = smul(num[i & 1][u][j], rr);
      }
      if constexpr (k == 8 || (k & 1)) o[u] = pk4(num[i & 1][u]);
      if constexpr (k == 3) st_frag(rsWS, lane, oo + i * 5120 + n * 2048, o[0], o[1]);
      if constexpr (k == 7) st_frag(rsWS, lane, oo + i * 5120 + n * 2048 + 1024, o[2], o[3]);
      if constexpr (k == 8) { sh[i] = o[4]; asm volatile("" : "+v"(sh[i])); }
    };
    static_for<0, 4>([&](auto I) {
      constexpr int i = I.v;
      static_for<0, 9>([&](auto K) {
        constexpr int k = K.v, u = k < 5 ? k : k - 4;
        if constexpr (k < 5) mfma_z(num[i & 1][u], A[3 + u], B[i][u < 3 ? 0 : 1]); else mfma_ip(num[i & 1][u], A[7 + u], B[i][u < 3 ? 1 : 2]);
        SB;
        if constexpr (i == 0) {
          if constexpr (k >= 5) {
            asm volatile("" : "+v"(den[k - 5]));
#pragma unroll
            for (int j = 0; j < 4; ++j) den[k - 5][j] = __builtin_amdgcn_rcpf(den[k - 5][j]);
            asm volatile("" : "+v"(den[k - 5]));
          }
        } else {
          fin(IC<i - 1>{}, K);
        }
        SB;
      });
    });
    static_for<0, 9>([&](auto K) { fin(IC<3>{}, K); SB; });
    shared_send(w, lane, sh);
    shared_recv(rsWS, w, lane, oo + 4096, 5120, sh);
    STAMP(15);
    publish_flag(ws, WS_FLAGO + (rb * 32 + jidx) * FS, tag, tid);
    STAMP(3);
    if (b == 224) {
      poll_flags((gu32*)(ws + WS_FLAGH + tid * FS), (tid & 31) < (tid < 256 ? 18u : 4u), tag);
      bar();
      if (tid == 0) __hip_atomic_store((gu32*)(ws + WS_EPOCH), tag, RLX_AGENT);
    }
  } else {
    const unsigned rk = spi * 4 + (w & 3), p0 = w < 4 ? rk : 128 + rk, p1 = p0 + 64;
    auto unit = [](unsigned q) { return q < 60 ? q + 30 : (q < 90 ? q - 60 : q); };
    const unsigned u0 = unit(p0), u1 = unit(p1);
    unsigned k10 = 0x00003f80u, k01 = 0x3f800000u;
    asm volatile("" : "+v"(k10), "+v"(k01));
    auto cs_src = [&](unsigned u, rsrc_t& rs, unsigned& cv, unsigned& st) {
      const unsigned cline = u / 3, third = u % 3;
      const void* cw = cline < 30 ? p.wqkv : (cline < 57 ? p.wg : p.wu);
      const unsigned cpitch = cline < 30 ? 3840u : 3456u;
      const unsigned clocal = cline < 30 ? cline : (cline < 57 ? cline - 30 : cline - 57);
      rs = mk_rsrc(cw, u < 252 ? D * cpitch : 0);
      cv = (third * 216 + (lane >> 3)) * cpitch + clocal * 128 + (lane & 7) * 16;
      st = 8 * cpitch;
    };
    auto cs_publish = [&](unsigned u, float (&cs)[8]) {
#pragma unroll
      for (int k = 0; k < 4; ++k) { const bool hi = lane & 32; const float o = shfl_x(hi ? cs[k] : cs[k + 4], lane, 32); cs[k] = (hi ? cs[k + 4] : cs[k]) + o; }
#pragma unroll
      for (int k = 0; k < 2; ++k) { const bool hi = lane & 16; const float o = shfl_x(hi ? cs[k] : cs[k + 2], lane, 16); cs[k] = (hi ? cs[k + 2] : cs[k]) + o; }
      { const bool hi = lane & 8; const float o = shfl_x(hi ? cs[0] : cs[1], lane, 8); cs[0] = (hi ? cs[1] : cs[0]) + o; }
      if (u < 252)
        __hip_atomic_store((gu64*)(ws + WS_CSUM) + (u % 3) * NCOL + (u / 3) * 64 + (lane & 7) * 8 + (lane >> 3),
                           ((unsigned long long)tag << 32) | __builtin_bit_cast(unsigned, cs[0]), RLX_AGENT);
    };
    row_load(rs_, rsX, lane);
    constexpr int CW = CWIN;
    u32x4 c[CW];
    rsrc_t rs0, rs1; unsigned cv0, cv1, st0, st1;
    cs_src(u0, rs0, cv0, st0); cs_src(u1, rs1, cv1, st1);
    auto cld = [&](int j) { return j < 27 ? __builtin_amdgcn_raw_buffer_load_b128(rs0, cv0 + j * st0, 0, 0) : __builtin_amdgcn_raw_buffer_load_b128(rs1, cv1 + (j - 27) * st1, 0, 0); };
    if (SDELAY) __builtin_amdgcn_s_sleep(SDELAY);
#pragma unroll
    for (int j = 0; j < CW; ++j) c[j] = cld(j);
    SB;
    asm volatile("s_waitcnt vmcnt(%2) lgkmcnt(0)\n\tv_readfirstlane_b32 %0, %1\n\ts_nop 3" : "=s"(tag) : "v"(epoch), "i"(CW) : "memory");
    tag = rfl(tag) + 1;
    static_for<0, 14>([&](auto K) { row_step<K.v>(rs_, lane); });
    lbar();
    row_publish(rs_, ws, tag, tid);
    float cs[8] = {};
    static_for<0, 54>([&](auto J) {
      constexpr int j = J.v;
#pragma unroll
      for (int k = 0; k < 4; ++k) { cs[2 * k] = dot2(c[j % CW][k], k10, cs[2 * k]); cs[2 * k + 1] = dot2(c[j % CW][k], k01, cs[2 * k + 1]); }
      if constexpr (j + CW < 54) c[j % CW] = cld(j + CW);
      SB;
      if constexpr (j == 26) {
        cs_publish(u0, cs);
        STAMP(11);
#pragma unroll
        for (int k = 0; k < 8; ++k) cs[k] = 0.f;
      }
    });
    cs_publish(u1, cs);
    STAMP(12);
    bar();
#pragma unroll 1
    for (unsigned i = w; i < 144; i += 8) dma16(i * 1024, v2, rsWd, urow(i >> 2, rot) + (i & 3) * 10240u + cb * 128u);
    drain_vm();
    bar();
  }
  if (!has2) { STAMP_END; return; }
  relane();

  const unsigned rbA = (G * 6) >> 4, rbB = (G * 6 + 5 > 127 ? 127 : G * 6 + 5) >> 4;
  if (w >= 4) {
    __builtin_amdgcn_s_setprio(HPRIO);
    const unsigned lw = w - 4, ld0 = lw * 1024;
    gu32* fo = (gu32*)(ws + WS_FLAGO + ((lane < 32 ? rbA : rbB) * 32 + (lane & 3)) * FS);
    gu32* fh = (gu32*)(ws + WS_FLAGH + ((lane < 32 ? rbA : rbB) * 32 + ((lane & 31) < 18 ? lane & 31 : 17)) * FS);
    __builtin_amdgcn_s_sleep(HSLEEP);
    if (jtype == J_KV) {
#pragma unroll
      for (int u = 31; u < 36; ++u) dma16(u * 4096 + ld0, v2, rsWd, w2_off(W, u, lw));
    }
    if (jtype != J_MLP) {
#pragma unroll
      for (int u = 0; u < NPRE; ++u) { pre2[u] = w2_load(W, 36 + u, lw, v2); SB; }
    }
#pragma unroll
    for (int u = 36 + NPRE; u < PSPLIT; ++u) { pre2[u - 36] = w2_load(W, u, lw, v2); SB; }
    if (w == 7) {
      __builtin_amdgcn_s_setprio(3);
      const unsigned v = poll_flags(fh, (lane & 31) < 18 && (lane & 31) >= 10, tag);
      lds_set(L_GO);
      if (!all64((lane & 31) >= 18 || v == tag)) poll_flags(fh, (lane & 31) < 18, tag);
      lds_set(L_GO + 4);
      __builtin_amdgcn_s_setprio(HPRIO);
    }
#pragma unroll
    for (int u = PSPLIT; u < HSPLIT; ++u) { pre2[u - 36] = w2_load(W, u, lw, v2); SB; }
    STAMP(8);
    unsigned la[3], wb[3], xo[3], st[3];
    u32x4 xr[3];
    const unsigned ch = lane & 7;
#pragma unroll
    for (int i = 0; i < 3; ++i) {
      const unsigned row = lw * 24 + i * 8 + (lane >> 3), rl = row >> 4, rr = row & 15;
      la[i] = (rl >= 3 ? 24576u + (rl - 3) * 4096u : rl * 4096u) + (ch >> 1) * 1024 + (ch & 1) * 512 + ((rr + 2 * ch) & 15) * 16;
      xo[i] = (G * 96 + row) * 1280 + cb * 128 + ch * 16;
      wb[i] = ld0 + lane * 16 + i * 49152;
      asm volatile("" : "+v"(la[i]), "+v"(wb[i]), "+v"(xo[i]));
    }
    float ri[3], rl[3];
    unsigned of = 0;
    f32x4 xf[3][2], dd[3][2], d2[3][2];
#pragma unroll
    for (int k = 2; k <= 6; ++k) {
      if (k == O_BAR && w == 7 && !all64((lane & 31) >= 4 || of == tag)) poll_flags(fo, (lane & 31) < 4, tag);
      if (k == 6) {
        wait_vm<0>();
#pragma unroll
        for (int h = 0; h < 3; ++h) {
          ri[h] = 2.0f * bfhi(st[h]) * bfhi(st[h]);
          rl[h] = -1.4426950408889634f * __builtin_amdgcn_rcpf(ri[h]);
          xf[h][0] = f32x4{bflo(xr[h][0]), bfhi(xr[h][0]), bflo(xr[h][1]), bfhi(xr[h][1])};
          xf[h][1] = f32x4{bflo(xr[h][2]), bfhi(xr[h][2]), bflo(xr[h][3]), bfhi(xr[h][3])};
          asm volatile("" : "+v"(ri[h]), "+v"(rl[h]), "+v"(xf[h][0]), "+v"(xf[h][1]));
        }
      }
      lbar();
      if (k == O_BAR - 1) { of = __hip_atomic_load(fo, RLX_AGENT); SB; }
      if (k < 6) {
        const int kk = k - 1;
#pragma unroll
        for (int u = 9 * (kk - 1) + ((kk - 1) & 1); u < 9 * kk + (kk & 1); ++u) {
          lds_st<u32x4>(wb[u / 12] + (u % 12) * 4096, pre2[u]);
          if (WSLEEP == 1) __builtin_amdgcn_s_sleep(1);
          if (WSLEEP == 2) asm volatile("s_nop 7");
          SB;
        }
      }
      if (k == 2 || k == 3) {
        constexpr int mid = (HSPLIT + 74) / 2 < 72 ? (HSPLIT + 74) / 2 : 72;
#pragma unroll
        for (int u = (k == 2 ? HSPLIT : mid); u < (k == 2 ? mid : 72); ++u) { pre2[u - 36] = w2_load(W, u, lw, v2); SB; }
        if (k == 3) { dma16(36 * 4096 + ld0, v2, W.wo, w2_off(W, 72, lw)); dma16(37 * 4096 + ld0, v2, W.wo, w2_off(W, 73, lw)); }
      }
      if (k == XK) {
#pragma unroll
        for (int h = 0; h < 3; ++h) {
          const unsigned row = G * 96 + lw * 24 + h * 8 + (lane >> 3);
          st[h] = __builtin_amdgcn_raw_buffer_load_b32(rsWS, WS_STAT + (row < S ? row : 0) * 8, 0, 16);
          xr[h] = __builtin_amdgcn_raw_buffer_load_b128(rsX, xo[h], 0, 0);
        }
      }
    }
    lbar();
#pragma unroll
    for (int i = 0; i < 3; ++i) {
      dd[i][0] = lds_ld<f32x4>(la[i]); d2[i][0] = lds_ld<f32x4>(la[i] + 12288);
      dd[i][1] = lds_ld<f32x4>(la[i] + 256); d2[i][1] = lds_ld<f32x4>(la[i] + 12288 + 256);
      la[i] += 49152;
      asm volatile("" : "+v"(la[i]));
    }
    __builtin_amdgcn_s_setprio(GPRIO);
#pragma unroll
    for (int i = 0; i < 3; ++i) {
      float v[8], e[8];
#pragma unroll
      for (int j = 0; j < 8; ++j) v[j] = sadd(dd[i][j >> 2][j & 3], d2[i][j >> 2][j & 3]);
      SB;
#pragma unroll
      for (int j = 0; j < 8; ++j) e[j] = smul(v[j], rl[i]);
      SB;
#pragma unroll
      for (int j = 0; j < 8; ++j) e[j] = __builtin_amdgcn_exp2f(e[j]);
      SB;
#pragma unroll
      for (int j = 0; j < 8; ++j) e[j] = sfma(e[j], ri[i], ri[i]);
      SB;
#pragma unroll
      for (int j = 0; j < 8; ++j) e[j] = __builtin_amdgcn_rcpf(e[j]);
      SB;
#pragma unroll
      for (int j = 0; j < 8; ++j) dd[i][j >> 2][j & 3] = smul(v[j], e[j]);
      asm volatile("" : "+v"(dd[i][0]), "+v"(dd[i][1]));
      SB;
    }
    const rsrc_t rsO = mk_rsrc(p.out, S * D * 2);
    STAMP(9);
    if (PUTB) bar();
    bar();
    f32x4 ss[3][2];
#pragma unroll
    for (int i = 0; i < 3; ++i) {
      ss[i][0] = lds_ld<f32x4>(la[i]) + lds_ld<f32x4>(la[i] + 12288);
      ss[i][1] = lds_ld<f32x4>(la[i] + 256) + lds_ld<f32x4>(la[i] + 12288 + 256);
    }
#pragma unroll
    for (int i = 0; i < 3; ++i)
      __builtin_amdgcn_raw_buffer_store_b128(cat2(pk4(xf[i][0] + ss[i][0] * dd[i][0]), pk4(xf[i][1] + ss[i][1] * dd[i][1])), rsO, xo[i], 0, 0);
    STAMP(5);
    STAMP_END;
    return;
  }
  {
    const unsigned kh = w & 1, rt = G * 6 + 3 * (w >> 1);
    const rsrc_t rsH = mk_rsrc(ws + WS_HA, 8 * RBSZ);
    unsigned tb[3][4];
#pragma unroll
    for (int t = 0; t < 4; ++t) {
      tb[0][t] = (4 * g + q4) * 128 + ((t ^ ((2 * g + (q4 >> 1)) & 3)) << 5) + 8 * p4 + kh * 4096;
      tb[1][t] = tb[0][t] + 49152; tb[2][t] = tb[0][t] + 98304;
      asm volatile("" : "+v"(tb[0][t]), "+v"(tb[1][t]), "+v"(tb[2][t]));
    }
    auto rd1 = [&](int i, int t, int hf) {
      const int sl = i == 36 ? 36 : (2 * i) % 36, bi = sl / 12 > 2 ? 2 : sl / 12;
      return __builtin_bit_cast(u32x2, lds_tr(tb[bi][t] + (sl - 12 * bi) * 4096 + hf * 2048));
    };
    constexpr int NW = P2NW, W0 = P2W0;
    constexpr int OI = O_BAR == 4 ? 17 : (O_BAR == 5 ? 22 : 26);
    constexpr int OB0 = OI == 26 ? OBN : 0;
    f32x4 acc[2][3][4];
    u32x4 a[NW][3];
    u32x2 bw[2][4][2];
    unsigned va[2][3];
#pragma unroll
    for (int h = 0; h < 3; ++h) {
      va[0][h] = lane * 16 + ((rt + h) >> 4) * RBSZ + ((rt + h) & 15) * 3072;
      va[1][h] = va[0][h] + ((rt + h) & 15) * 2048;
      asm volatile("" : "+v"(va[0][h]), "+v"(va[1][h]));
    }
    unsigned xa[4];
#pragma unroll
    for (int t = 0; t < 4; ++t) {
      xa[t] = w * 12288u + t * 1024 + g * 256 + ((r + 4 * t + (g & 2)) & 15) * 16;
      asm volatile("" : "+v"(xa[t]));
    }
    auto put = [&](auto Z, auto E) {
      constexpr int z = Z.v, t = E.v / 3, h = 2 - E.v % 3;
      asm volatile("" : "+v"(acc[z][h][t]));
      lds_st<f32x4>(xa[t] + z * 49152 + h * 4096, acc[z][h][t]);
    };
    auto lda = [&](auto I, auto H) {
      constexpr int i = I.v, h = H.v;
      constexpr unsigned s0 = i < 27 ? (2 * i + rot) % 54 : 2 * i, s1 = i < 27 ? (2 * i + 1 + rot) % 54 : 2 * i + 1;
      a[i % NW][h] = __builtin_amdgcn_raw_buffer_load_b128(rsH, va[i >= 27][h], step_off(s0) + kh * (step_off(s1) - step_off(s0)), 16);
      SB;
    };
    lds_wait(L_GO);
    unsigned go2 = 0;
    STAMP(8);
    static_for<0, W0>([&](auto I) { lda(I, IC<0>{}); lda(I, IC<1>{}); lda(I, IC<2>{}); });
#pragma unroll
    for (int t = 0; t < 4; ++t) { bw[0][t][0] = rd1(0, t, 0); bw[0][t][1] = rd1(0, t, 1); }
    static_for<0, 37>([&](auto I) {
      constexpr int i = I.v;
      constexpr int lim = i + NW - 1;
      constexpr int nxt = i <= OI ? (W0 + 2 * i < lim ? W0 + 2 * i : lim) : (27 + OB0 + 2 * (i - OI - 1) < lim ? 27 + OB0 + 2 * (i - OI - 1) : lim);
      constexpr int end = i <= OI ? 27 : 37;
      constexpr int l0 = nxt < end ? nxt : -1, l1 = (nxt + 1 <= lim && nxt + 1 < end) ? nxt + 1 : -1;
      SB;
      static_for<0, 12>([&](auto E) {
        constexpr int e = E.v, t = e / 3, h = e % 3;
        const u32x4 bf = cat2(bw[i & 1][t][0], bw[i & 1][t][1]);
        if constexpr (i == 0 || i == 27) mfma_z(acc[i >= 27][2 - h][t], bf, a[i % NW][2 - h]); else mfma_ip(acc[i >= 27][2 - h][t], bf, a[i % NW][2 - h]);
        SB;
        if constexpr (i == 4 && e == 2) go2 = lds_get(L_GO + 4);
        if constexpr (!PUTB && (i == 27 || i == 28) && (e & 1)) put(IC<0>{}, IC<(i - 27) * 6 + e / 2>{});
        if constexpr (i == 36 && e >= 8) put(IC<1>{}, IC<e - 8>{});
        if constexpr (i < 36 && h < 2) bw[(i + 1) & 1][t][h] = rd1(i + 1, t, h);
        if constexpr (l1 < 0) { if constexpr (h == 2 && t < 3 && l0 >= 0) lda(IC<l0>{}, IC<t>{}); }
        else if constexpr (e & 1) lda(IC<(e < 6 ? l0 : l1)>{}, IC<(e >> 1) % 3>{});
        SB;
      });
      if constexpr (i == (PUTB ? 33 : 29)) { asm volatile("s_waitcnt lgkmcnt(8)" ::: "memory"); bar(); }
      if constexpr (i == 4) { if (__builtin_expect(!rfl(go2), 0)) lds_wait(L_GO + 4); asm volatile("" ::: "memory"); STAMP(17); }
      if constexpr (i > 4 && i < 27 && (i % 9 == 4 || i % 9 == 8)) {
        bar();
        if constexpr (i == 26 && OI == 26) static_for<27, 27 + OB0>([&](auto J) { lda(J, IC<0>{}); lda(J, IC<1>{}); lda(J, IC<2>{}); });
        if constexpr (PUTB && i == 26) { static_for<0, 12>([&](auto E) { put(IC<0>{}, E); SB; }); lbar(); }
        STAMP(17 + (2 * i) / 9);
      }
    });
    STAMP(24);
    static_for<4, 12>([&](auto E) { if constexpr (E.v == 8) asm volatile("s_nop 3" ::: "memory"); put(IC<1>{}, E); SB; });
    lbar();
    STAMP(5);
    STAMP_END;
  }
}

__global__ __launch_bounds__(64) void tailk(Params p) {
  char* const ws = (char*)(((unsigned long long)p.wsraw + p.wsal) & ~p.wsal);
  unsigned long long t0 = 0;
  for (int i = 0; i < 256; ++i) { const unsigned long long v = __hip_atomic_load((gu64*)(ws + WS_DBG) + i, RLX_AGENT); t0 = v > t0 ? v : t0; }
  for (unsigned i = 0; i < (1u << 22) && __builtin_amdgcn_s_memrealtime() - t0 < 30000; ++i) __builtin_amdgcn_s_sleep(1);
}

extern "C" void kernel_launch(void* const* d_in, const int* in_sizes, int n_in, void* d_out, int out_size, void* d_ws,
                              size_t ws_size, hipStream_t stream) {
  Params p;
  p.x = d_in[0]; p.wqkv = d_in[1]; p.wo = d_in[2]; p.wg = d_in[4]; p.wu = d_in[5]; p.wd = d_in[6];
  p.out = d_out; p.wsraw = (char*)d_ws;
  p.wsal = ws_size >= (64ull << 20) + WS_END ? (64ull << 20) - 1 : 0;
  fused_block<<<256, 512, 0, stream>>>(p);
  tailk<<<1, 64, 0, stream>>>(p);
}
```

```cpp
#include <hip/hip_runtime.h>
#include <stdint.h>

typedef __attribute__((ext_vector_type(8))) __bf16 bf16x8;
typedef __attribute__((ext_vector_type(4))) __bf16 bf16x4;
typedef __attribute__((ext_vector_type(2))) __bf16 bf16x2;
typedef __attribute__((ext_vector_type(4))) float f32x4;
typedef __attribute__((ext_vector_type(2))) float f32x2;
typedef __attribute__((ext_vector_type(4))) unsigned u32x4;
typedef __attribute__((ext_vector_type(2))) unsigned u32x2;
typedef __amdgpu_buffer_rsrc_t rsrc_t;
#define LAS __attribute__((address_space(3)))
#define GAS __attribute__((address_space(1)))
#define DI __device__ __forceinline__
#define RLX_AGENT __ATOMIC_RELAXED, __HIP_MEMORY_SCOPE_AGENT
typedef GAS unsigned gu32;
typedef GAS unsigned long long gu64;

constexpr int S = 2048, D = 640, F = 1728;
constexpr int NCOL = 5376;
constexpr int NSTEP = 74;
constexpr unsigned WS_EPOCH = 0;
#define FS 128
constexpr unsigned WS_FLAGH = 768u << 10;
constexpr unsigned WS_FLAGO = WS_FLAGH + 8 * 32 * FS;
constexpr unsigned WS_FLAGKV = WS_FLAGO + 8 * 32 * FS;
constexpr unsigned WS_STAT = 4096;
constexpr unsigned WS_CSUM = WS_STAT + S * 8;
constexpr unsigned WS_KVP = WS_CSUM + 3 * NCOL * 8;
constexpr unsigned WS_HA = 1u << 20;
constexpr unsigned RBSZ = 16 * NSTEP * 1024;
constexpr unsigned step_off(unsigned st) { return st < 54 ? (st / 3) * 49152 + (st % 3) * 1024 : 18 * 49152 + ((st - 54) / 5) * 81920 + ((st - 54) % 5) * 1024; }
constexpr unsigned WS_DBG = 12u << 20;
constexpr unsigned WS_END = 13u << 20;
constexpr unsigned L_B = 65536;
constexpr unsigned L_SCR = 147456;
constexpr unsigned L_MISC = 159744;
constexpr unsigned L_GO = L_MISC + 3072;
constexpr unsigned SPIN_CAP = 1u << 18;
#define HSLEEP 4
#define HSPLIT 64
#define PPIPE 0
#define STA 0
#define STB 2
#define SNAKE 0
#define ROTW 1
#define NPRE 12
#define PUTB 0
#define OBN 6
#define PSPLIT 48
#define WARM 1
#define CWIN 40
#define SDELAY 0
#define CSTG 6
#define CODE_BYTES 57344
#define XK 2
#define GRP 1
#define GPRIO 0
#define HPRIO 0
#define PSLEEP 2
#define P2NW 7
#define P2W0 6
#define WSLEEP 0
#define ROTM 18
#define NA1 2
#define O_BAR 5
#define PRIO_MODE 0
#define DMA_SPACE_A T
#define DMA_SPACE_B (4 * T / 3)

struct Params {
  const void* x; const void* wqkv; const void* wo; const void* wg; const void* wu; const void* wd;
  void* out; char* wsraw;
  unsigned long long wsal;
};

__shared__ __attribute__((aligned(16))) char smem[163840];

DI float sadd(float a, float b) { float o; asm("v_add_f32 %0, %1, %2" : "=v"(o) : "v"(a), "v"(b)); return o; }
DI float smul(float a, float b) { float o; asm("v_mul_f32 %0, %1, %2" : "=v"(o) : "v"(a), "v"(b)); return o; }
DI float sfma(float a, float b, float c) { float o; asm("v_fma_f32 %0, %1, %2, %3" : "=v"(o) : "v"(a), "v"(b), "v"(c)); return o; }
DI unsigned bsel(unsigned m, unsigned a, unsigned b) { unsigned o; asm("v_bfi_b32 %0, %1, %2, %3" : "=v"(o) : "v"(m), "v"(a), "v"(b)); return o; }
DI float bself(unsigned m, float a, float b) { return __builtin_bit_cast(float, bsel(m, __builtin_bit_cast(unsigned, a), __builtin_bit_cast(unsigned, b))); }
DI bool all64(bool c) { return __builtin_amdgcn_ballot_w64(!c) == 0; }
DI float shfl_x(float v, unsigned lane, unsigned m) {
  return __builtin_bit_cast(float, __builtin_amdgcn_ds_bpermute((int)((lane ^ m) << 2), __builtin_bit_cast(int, v)));
}
DI unsigned umin(unsigned a, unsigned b) { return a < b ? a : b; }
DI unsigned rfl(unsigned v) { return __builtin_amdgcn_readfirstlane(v); }
DI rsrc_t mk_rsrc(const void* p, unsigned bytes) { return __builtin_amdgcn_make_buffer_rsrc((void*)p, (short)0, (int)bytes, 0x00020000); }
DI void dma16(unsigned ldsdst, unsigned voff, rsrc_t rs, unsigned soff) {
  asm volatile("s_mov_b32 m0, %0\n\ts_nop 0\n\tbuffer_load_dwordx4 %1, %2, %3 offen lds" ::"s"(ldsdst), "v"(voff), "s"(rs), "s"(soff) : "memory");
}
template <int N> DI void wait_vm() { asm volatile("s_waitcnt vmcnt(%0)" ::"i"(N) : "memory"); }
DI void drain_vm() { wait_vm<0>(); __builtin_amdgcn_s_waitcnt(0x0F70); }
DI void wait_lgkm0() { asm volatile("s_waitcnt lgkmcnt(0)" ::: "memory"); }
DI void bar() { asm volatile("" ::: "memory"); __builtin_amdgcn_s_barrier(); asm volatile("" ::: "memory"); }
DI void lbar() { wait_lgkm0(); bar(); }
template <class V> DI V lds_ld(unsigned a) { return *(LAS V*)((LAS char*)smem + a); }
template <class V> DI void lds_st(unsigned a, V v) { *(LAS V*)((LAS char*)smem + a) = v; }
DI bf16x4 lds_tr(unsigned a) { return __builtin_amdgcn_ds_read_tr16_b64_v4bf16((LAS bf16x4*)((LAS char*)smem + a)); }
DI bf16x8 cat(bf16x4 a, bf16x4 b) { return __builtin_shufflevector(a, b, 0, 1, 2, 3, 4, 5, 6, 7); }
DI f32x4 mfma(bf16x8 a, bf16x8 b, f32x4 c) { return __builtin_amdgcn_mfma_f32_16x16x32_bf16(a, b, c, 0, 0, 0); }
DI unsigned pk2(float a, float b) { f32x2 v = {a, b}; return __builtin_bit_cast(unsigned, __builtin_convertvector(v, bf16x2)); }
DI u32x2 pk4(f32x4 v) { u32x2 r = {pk2(v[0], v[1]), pk2(v[2], v[3])}; return r; }
DI bf16x8 frag(u32x2 lo, u32x2 hi) { u32x4 v = {lo[0], lo[1], hi[0], hi[1]}; return __builtin_bit_cast(bf16x8, v); }
DI float bflo(unsigned v) { return __builtin_bit_cast(float, v << 16); }
DI float bfhi(unsigned v) { return __builtin_bit_cast(float, v & 0xffff0000u); }
DI float rbf(float v) { return bflo(pk2(v, 0.f)); }
DI float relu2(float v) { float o; asm("v_add_f32 %0, %1, |%1|" : "=v"(o) : "v"(v)); return o; }
DI f32x4 phi4(f32x4 u, f32x4 rl, f32x4 rh) {
  const f32x4 l = u * rl;
  f32x4 e, a;
#pragma unroll
  for (int j = 0; j < 4; ++j) {
    e[j] = __builtin_amdgcn_fmed3f(__builtin_amdgcn_exp2f(l[j]), 0.f, 1.f);
    a[j] = relu2(u[j]);
  }
  return a * rh + e + 1e-6f;
}
DI f32x4 splat(float v) { return f32x4{v, v, v, v}; }
template <int CTRL> DI float dpp_add(float v) {
  return v + __builtin_bit_cast(float, __builtin_amdgcn_update_dpp(0, __builtin_bit_cast(int, v), CTRL, 0xf, 0xf, true));
}
DI float dot2(unsigned a, unsigned b, float c) {
  return __builtin_amdgcn_fdot2_f32_bf16(__builtin_bit_cast(bf16x2, a), __builtin_bit_cast(bf16x2, b), c, false);
}
DI u32x4 ldtr2(unsigned a, unsigned d) { return __builtin_bit_cast(u32x4, cat(lds_tr(a), lds_tr(a + d))); }
DI void mfma_ip(f32x4& c, const u32x4& a, const u32x4& b) { asm volatile("v_mfma_f32_16x16x32_bf16 %0, %1, %2, %0" : "+v"(c) : "v"(a), "v"(b)); }
DI void mfma_c(f32x4& d, const u32x4& a, const u32x4& b, const f32x4& c) { asm volatile("v_mfma_f32_16x16x32_bf16 %0, %1, %2, %3" : "=&v"(d) : "v"(a), "v"(b), "v"(c)); }
DI void mfma_z(f32x4& c, const u32x4& a, const u32x4& b) { asm volatile("v_mfma_f32_16x16x32_bf16 %0, %1, %2, 0" : "=&v"(c) : "v"(a), "v"(b)); }
template <int N> DI void acc_fence(f32x4* c) {
  asm volatile("s_nop 7" ::: "memory");
#pragma unroll
  for (int i = 0; i < N; ++i) asm volatile("" : "+v"(c[i]));
}
DI u32x4 cat2(u32x2 a, u32x2 b) { return u32x4{a[0], a[1], b[0], b[1]}; }
DI u32x2 ldtr1(unsigned a) { return __builtin_bit_cast(u32x2, lds_tr(a)); }
template <int IMM> DI void set_m0i(unsigned v) { asm volatile("s_add_i32 m0, %0, %1" ::"s"(v), "i"(IMM) : "memory", "scc"); }
template <int OFF> DI void dma_goi(unsigned voff, rsrc_t rs, unsigned soff) {
  asm volatile("buffer_load_dwordx4 %0, %1, %2 offen offset:%3 lds" ::"v"(voff), "s"(rs), "s"(soff), "i"(OFF) : "memory");
}
DI void set_m0(unsigned v) { asm volatile("s_mov_b32 m0, %0" ::"s"(v) : "memory"); }
DI void dma_go(unsigned voff, rsrc_t rs, unsigned soff) {
  asm volatile("buffer_load_dwordx4 %0, %1, %2 offen lds" ::"v"(voff), "s"(rs), "s"(soff) : "memory");
}
DI f32x4 mfma_u(u32x4 a, u32x4 b, f32x4 c) { return mfma(__builtin_bit_cast(bf16x8, a), __builtin_bit_cast(bf16x8, b), c); }
#define STAMP_DECL
#define STAMP_ARG
#define STAMP_PASS
#define STAMPX(c, i)
#define STAMP_END
#define STAMP(i) STAMPX(__builtin_amdgcn_workitem_id_x() == 0, i)

DI unsigned poll_flags(gu32* word, bool active, unsigned tag) {
  unsigned v = 0;
#pragma unroll 1
  for (unsigned spins = 0; spins < SPIN_CAP; ++spins) {
    v = __hip_atomic_load(word, RLX_AGENT);
    if (all64(!active || v == tag)) break;
    if (PSLEEP) __builtin_amdgcn_s_sleep(PSLEEP);
  }
  asm volatile("" ::: "memory");
  return v;
}

DI void lds_set(unsigned a) { __hip_atomic_store((LAS unsigned*)smem + a / 4, 1u, __ATOMIC_RELAXED, __HIP_MEMORY_SCOPE_WORKGROUP); }
DI unsigned lds_get(unsigned a) { return __hip_atomic_load((LAS unsigned*)smem + a / 4, __ATOMIC_RELAXED, __HIP_MEMORY_SCOPE_WORKGROUP); }
DI void lds_wait(unsigned a) {
#pragma unroll 1
  for (unsigned spins = 0; spins < (SPIN_CAP << 4); ++spins) {
    if (rfl(lds_get(a))) break;
    __builtin_amdgcn_s_sleep(1);
  }
  asm volatile("" ::: "memory");
}

template <int I> struct IC { static constexpr int v = I; };
template <int B, int E, class Fn> DI void static_for(Fn&& f) {
  if constexpr (B < E) { f(IC<B>{}); static_for<B + 1, E>(f); }
}

enum { J_MLP = 0, J_KV = 1, J_Q = 2, J_SPARE = 3 };

struct Dma {
  rsrc_t rsA, rsB;
  unsigned vA0, vA1, vB0, vB1, vB2;
  unsigned sA[4], sB, advB;
  unsigned wa, wq;
  unsigned mA, mB, mW;
};

constexpr unsigned a_slot(unsigned s3) { return s3 == 0 ? 126976u : (s3 - 1) * 32768u; }
#define SB __builtin_amdgcn_sched_barrier(0)

template <int T> DI unsigned bofs(const unsigned (&boff)[T], int t) { return T == 6 && t >= 3 ? boff[t - 3] + 6144 : boff[t]; }
template <int T, bool TR, int KS, class Extra>
DI void kstep(f32x4 (&acc)[4][T], const u32x4 (&ac)[4], const u32x2 (&bc)[T][2], u32x4 (&an)[4], u32x2 (&bn)[T][2],
              const unsigned (&aoff)[4], const unsigned (&boff)[T], const Dma& d, Extra&& extra) {
  constexpr int s = KS >> 1, KIND = KS & 1, NP = KIND == 0 ? 4 : 3, ST = KIND == 0 ? (STA ? STA : (4 * T - 1) / NP) : (STB ? STB : (4 * T - 1) / NP);
  constexpr int sn = (KS + 1) >> 1;
  constexpr unsigned asl = a_slot(sn % 3), bsl = ((KS + 1) % 5) * 12288u;
  static_for<0, 4 * T>([&](auto E) {
    constexpr int e = E.v, t = e >> 2, i = (SNAKE && (t & 1)) ? 3 - (e & 3) : (e & 3);
    const u32x4 bf = cat2(bc[t][0], bc[t][1]);
    if constexpr (KS == 0) { if (TR) mfma_z(acc[i][t], bf, ac[i]); else mfma_z(acc[i][t], ac[i], bf); }
    else { if (TR) mfma_ip(acc[i][t], bf, ac[i]); else mfma_ip(acc[i][t], ac[i], bf); }
    SB;
    if constexpr (KS < 19) {
      if constexpr (e < 4) an[e] = lds_ld<u32x4>(aoff[((KS + 1) & 1) + (sn % 3 == 0 ? 2 : 0)] + (sn % 3 == 0 ? 0u : asl) + e * 2048);
      else if constexpr (e < 4 + 2 * T) bn[(e - 4) >> 1][e & 1] = ldtr1(bofs<T>(boff, (e - 4) >> 1) + bsl + (e & 1) * 768);
    }
    constexpr int pc = e / ST;
    if constexpr (KIND == 0) {
      constexpr int adv = s < 8 ? (s + 2) * 128 : 0, dst = (int)a_slot((s + 2) % 3) + pc * 1024, off = dst >= adv ? adv : 0;
      if constexpr (pc < NP && e % ST == ST - 2) set_m0i<dst - off>(d.mA);
      if constexpr (pc < NP && e % ST == ST - 1) dma_goi<off>((pc & 1) ? d.vA1 : d.vA0, d.rsA, d.sA[pc] + (adv - off));
    } else {
      constexpr int h0 = (2 * s) % 5;
      if constexpr (pc < NP && e % ST == ST - 2) set_m0i<(h0 == 4 ? 0 : h0 * 12288) + pc * 1024>(h0 == 4 ? d.mW : d.mB);
      if constexpr (pc < NP && e % ST == ST - 1) dma_goi<0>(pc == 0 ? d.vB0 : (pc == 1 ? d.vB1 : d.vB2), d.rsB, d.sB);
    }
    extra(E);
    SB;
  });
}

struct RowStat { u32x4 xa; unsigned xb, row; float s, q; };
DI void row_load(RowStat& R, rsrc_t rsX, unsigned lane) {
  asm volatile("buffer_load_dwordx4 %0, %2, %4, %5 offen\n\tbuffer_load_dword %1, %3, %4, %5 offen offset:1024"
               : "=&v"(R.xa), "=&v"(R.xb) : "v"(lane * 16), "v"(lane * 4), "s"(rsX), "s"(R.row * 1280) : "memory");
}
template <int K> DI void row_step(RowStat& R, unsigned lane) {
  if constexpr (K < 5) {
    if constexpr (K == 0) { R.s = 0.f; R.q = 0.f; }
    const unsigned v = K < 4 ? R.xa[K & 3] : R.xb;
    asm volatile("v_dot2c_f32_bf16 %0, %2, %3\n\tv_dot2c_f32_bf16 %1, %2, %2" : "+v"(R.s), "+v"(R.q) : "v"(v), "v"(0x3f803f80u));
  } else if constexpr (K < 11) {
    constexpr int C = K == 5 ? 0xB1 : (K == 6 ? 0x4E : (K == 7 ? 0x141 : (K == 8 ? 0x140 : (K == 9 ? 0x142 : 0x143))));
    if constexpr (K == 5) asm volatile("s_nop 2" : "+v"(R.s), "+v"(R.q));
    R.s = dpp_add<C>(R.s); R.q = dpp_add<C>(R.q);
  } else if constexpr (K == 11) {
    R.s *= 1.0f / D;
    R.q = __builtin_fmaxf(R.q * (1.0f / D) - R.s * R.s, 0.f);
  } else if constexpr (K == 12) {
    R.q = __builtin_amdgcn_sqrtf(rbf(rbf(R.q) + 1e-5f));
  } else {
    lds_st<unsigned>(lane == 63 ? L_GO + 32 + (R.row & 7) * 4 : L_GO + 64, pk2(R.s, R.q));
  }
  if constexpr (K >= 5 && K < 13) asm volatile("" : "+v"(R.s), "+v"(R.q));
}
DI void row_publish(const RowStat& R, char* ws, unsigned tag, unsigned tid) {
  if (tid < 8) __hip_atomic_store((gu64*)(ws + WS_STAT) + R.row + tid, ((unsigned long long)tag << 32) | lds_ld<unsigned>(L_GO + 32 + tid * 4), RLX_AGENT);
}

struct Gran { u32x2 g0, g1, g2; unsigned off, step; };
DI void gran_issue(Gran& G, rsrc_t rs) {
  asm volatile("buffer_load_dwordx2 %0, %3, %4, 0 offen sc1\n\tbuffer_load_dwordx2 %1, %3, %4, %5 offen sc1\n\t"
               "buffer_load_dwordx2 %2, %3, %4, %6 offen sc1"
               : "=&v"(G.g0), "=&v"(G.g1), "=&v"(G.g2) : "v"(G.off), "s"(rs), "s"(G.step), "s"(2 * G.step) : "memory");
}
DI void gran_load(Gran& G, rsrc_t rs) {
  asm volatile("buffer_load_dwordx2 %0, %3, %4, 0 offen sc1\n\tbuffer_load_dwordx2 %1, %3, %4, %5 offen sc1\n\t"
               "buffer_load_dwordx2 %2, %3, %4, %6 offen sc1\n\ts_waitcnt vmcnt(0)"
               : "=&v"(G.g0), "=&v"(G.g1), "=&v"(G.g2) : "v"(G.off), "s"(rs), "s"(G.step), "s"(2 * G.step) : "memory");
}

DI unsigned urow(unsigned pos, unsigned rot) { const unsigned u = pos + rot; return (u >= 54 ? u - 54 : u) * 40960u; }

constexpr unsigned wo_tile(unsigned v) { return (v / 5) * 10 + (v % 5 == 4 ? 4 : (v % 5 < 2 ? 2 * (v % 5) : 2 * (v % 5) + 1)); }
struct W2 { rsrc_t wd, wo; unsigned rot, col; };
DI unsigned w2_off(const W2& W, int u, unsigned lw) {
  if (u < 54) return urow(u, W.rot) + lw * 10240u + W.col;
  return wo_tile(u - 54) * 20480u + (lw >> 1) * ((u - 54) % 5 == 4 ? 102400u : 20480u) + (lw & 1) * 10240u + W.col;
}
DI u32x4 w2_load(const W2& W, int u, unsigned lw, unsigned voff) {
  return __builtin_amdgcn_raw_buffer_load_b128(u < 54 ? W.wd : W.wo, voff, w2_off(W, u, lw), 0);
}

template <int T, bool TR, bool PF, bool LASTA, class Pre>
DI void main_loop(Pre&& pre, RowStat& R, char* ws, f32x4 (&acc)[4][T], unsigned (&boff)[T], Dma& d, unsigned w, unsigned lane, unsigned tid,
                  unsigned epoch, unsigned& tag, unsigned rb, unsigned ccol, rsrc_t rsB2, rsrc_t rsWS, unsigned cb, unsigned rot, u32x4 (&pre2)[36] STAMP_ARG) {
  const unsigned g = lane >> 4, r = lane & 15, m = w & 3;
  unsigned aoff[4];
  aoff[0] = (64 * m + r) * 128 + ((g ^ (r >> 1)) << 4);
  aoff[1] = aoff[0] ^ 64;
  aoff[2] = aoff[0] + a_slot(0); aoff[3] = aoff[1] + a_slot(0);
  const unsigned wq = d.wq, wa = d.wa;
  asm volatile("" : "+v"(aoff[0]), "+v"(aoff[1]), "+v"(aoff[2]), "+v"(aoff[3]), "+v"(boff[0]), "+v"(boff[1]), "+v"(boff[2]));
  if (T == 5) asm volatile("" : "+v"(boff[3]), "+v"(boff[4]));
  Gran G;
  unsigned v2, vb2, vpf, tA[3], tB[3];
  row_load(R, d.rsA, lane);
  {
    asm volatile("s_waitcnt vmcnt(%2) lgkmcnt(0)\n\tv_readfirstlane_b32 %0, %1\n\ts_nop 3" : "=s"(tag) : "v"(epoch), "i"(5 + NA1) : "memory");
    tag = rfl(tag) + 1;
    bar();
  }
  STAMP(1);
  u32x4 a0[4], a1[4];
  u32x2 b0[T][2], b1[T][2];
#pragma unroll
  for (int i = 0; i < 4; ++i) a0[i] = lds_ld<u32x4>(aoff[2] + i * 2048);
#pragma unroll
  for (int t = 0; t < T; ++t) { b0[t][0] = ldtr1(bofs<T>(boff, t)); b0[t][1] = ldtr1(bofs<T>(boff, t) + 768); }
  SB;
#pragma unroll
  for (int i = NA1; i < 4; ++i) dma16(a_slot(1) + d.mA + i * 1024, (i & 1) ? d.vA1 : d.vA0, d.rsA, d.sA[i] + 128);
  const bool y = w >= 4;
  bool ok = true;
  auto ln_store = [&] {
    const float v0 = y ? -bflo(G.g0[0]) : __builtin_bit_cast(float, G.g0[0]) + __builtin_bit_cast(float, G.g1[0]) + __builtin_bit_cast(float, G.g2[0]);
    const unsigned la = L_MISC + (y ? tid * 4 - 1024 : 2048 + tid * 4);
    lds_st<float>(la, v0);
    lds_st<float>(y ? la + 1024 : L_GO + 64 + (lane & 7) * 4, __builtin_amdgcn_rcpf(bfhi(G.g0[0])));
  };
  static_for<0, 10>([&](auto S_) {
    constexpr int s = S_.v, L = 4 * T;
    constexpr int NX = (PF && s >= 1 && 2 * s <= NPRE) ? 2 : 0;
    kstep<T, TR, 2 * s>(acc, a0, b0, a1, b1, aoff, boff, d, [&](auto E) {
      constexpr int e = E.v;
      if constexpr (s == 2) {
        if constexpr (e == 2) { vb2 = v2 + (3 * wq) * 10240u; asm volatile("" : "+v"(vb2)); }
        if constexpr (e == 5) { G.off = w < 4 ? WS_CSUM + ccol * 8 : WS_STAT + (rb * 256 + tid - 256) * 8; asm volatile("" : "+v"(G.off)); }
        if constexpr (e == 8) { G.step = w < 4 ? NCOL * 8 : 0; tA[0] = urow(wa, rot) + cb * 128u; asm volatile("" : "+s"(G.step), "+s"(tA[0])); }
        if constexpr (e == 10) { tA[1] = urow(8 + wa, rot) + cb * 128u; asm volatile("" : "+s"(tA[1])); }
        if constexpr (e == 12) { tA[2] = urow(31 + wa, rot) + cb * 128u; asm volatile("" : "+s"(tA[2])); }
        if constexpr (e == 14) {
          tB[0] = urow(16, rot) + cb * 128u; tB[1] = urow(w < 4 ? 19u : 22u, rot) + cb * 128u; tB[2] = urow(w < 4 ? 25u : 28u, rot) + cb * 128u;
          asm volatile("" : "+s"(tB[0]), "+s"(tB[1]), "+s"(tB[2]));
        }
      }
      if constexpr (s == (TR ? 1 : 3) && e == 2) pre(0);
      if constexpr (s == CSTG && e == 2) pre(1);
      if constexpr (NX && e >= L - 2) {
        constexpr int u = 2 * (s - 1) + e - (L - 2);
        pre2[u] = __builtin_amdgcn_raw_buffer_load_b128(rsB2, vpf, urow(36 + u, rot) + (w & 3) * 10240u + cb * 128u, 0);
      }
      if constexpr (s < 7) { if constexpr (e == L - 3) d.sB += s == 0 ? d.advB * (1 + (w >> 2)) : d.advB; }
      else if constexpr (s == 7) {
        if constexpr (e == L - 6) d.vB0 = y ? vb2 : d.vB0;
        if constexpr (e == L - 4) d.vB1 = y ? vb2 + 10240u : d.vB1;
        if constexpr (e == L - 3) d.vB2 = y ? vb2 + 20480u : d.vB2;
        if constexpr (e == L - 2) d.sB = y ? tB[0] : d.sB + d.advB;
        if constexpr (e == L - 1) d.rsB = y ? rsB2 : d.rsB;
      } else {
        if constexpr (e == L - 3) { d.sB = tB[s - 7]; d.rsB = rsB2; d.vB0 = vb2; d.vB1 = vb2 + 10240u; d.vB2 = vb2 + 20480u; }
      }
      if constexpr (s == 9 && e == L - 4) {
        asm volatile("s_waitcnt vmcnt(7)" : "+v"(G.g0), "+v"(G.g1), "+v"(G.g2)::"memory");
        ok = all64(G.g0[1] == tag && G.g1[1] == tag && G.g2[1] == tag);
      }
      if constexpr (s == 9 && e == L - 3) ln_store();
    });
    if constexpr (s == 0) {
      wait_vm<8 - NA1>();
      static_for<0, 14>([&](auto K) { row_step<K.v>(R, lane); });
    }
    if constexpr (s == 9) {
      if (__builtin_expect(!ok, 0)) {
#pragma unroll 1
        for (unsigned spins = 0; spins < SPIN_CAP; ++spins) {
          __builtin_amdgcn_s_sleep(2);
          gran_load(G, rsWS);
          if (all64(G.g0[1] == tag && G.g1[1] == tag && G.g2[1] == tag)) break;
        }
        ln_store();
      }
    }
    wait_lgkm0();
    if constexpr (s < 9) wait_vm<4 + NX>();
    bar();
    if constexpr (s == 0) { STAMP(12); }
    SB;
    kstep<T, TR, 2 * s + 1>(acc, a1, b1, a0, b0, aoff, boff, d, [&](auto E) {
      constexpr int e = E.v;
      if constexpr (s == 0 && e == 6) {
        if (y) {
          const unsigned so = d.sB - d.advB;
          dma16(d.mW + 49152, d.vB0, d.rsB, so); dma16(d.mW + 49152 + 1024, d.vB1, d.rsB, so); dma16(d.mW + 49152 + 2048, d.vB2, d.rsB, so);
        }
      }
      if constexpr (s == 0 && e == L - 4) {
        v2 = (lane >> 3) * 1280 + (((lane & 7) ^ (((lane >> 4) & 3) << 1)) << 4);
        vpf = y ? v2 : 0x7ffffff0u;
        asm volatile("" : "+v"(v2), "+v"(vpf));
      }
      if constexpr (s == 0 && e == L - 6) row_publish(R, ws, tag, tid);
      if constexpr (s == 8 && e == 0) gran_issue(G, rsWS);
      if constexpr (s >= 7 && s < 9 && e >= L - 4) {
        d.rsA = rsB2; d.vA0 = v2; d.vA1 = v2;
        d.sA[e - (L - 4)] = tA[s - 7] + (e - (L - 4)) * 10240;
      }
      if constexpr (s == 9 && LASTA && e == 7) {
        if (wa < 5) {
#pragma unroll
          for (int i = 0; i < 4; ++i) dma16(a_slot(0) + d.mA + i * 1024, v2, rsB2, tA[2] + i * 10240);
        }
      }
    });
    if constexpr (s == 8) { STAMP(13); }
    if constexpr (s == 2) { STAMP(26); }
    if constexpr (s == 6) { STAMP(27); }
  });
  acc_fence<4 * T>(&acc[0][0]);
  STAMP(14);
}

DI void st16(u32x4 v, rsrc_t rs, unsigned voff, unsigned soff) {
  __builtin_amdgcn_raw_buffer_store_b128(v, rs, voff, soff, 16);
  asm volatile("s_nop 1" ::"v"(v) : "memory");
}
DI void publish_flag(char* ws, unsigned flag_off, unsigned tag, unsigned tid) {
  drain_vm();
  bar();
  if (tid == 0) __hip_atomic_store((gu32*)(ws + flag_off), tag, RLX_AGENT);
}
DI void st_frag(rsrc_t rs, unsigned lane, unsigned off, u32x2 lo, u32x2 hi) { st16(cat2(lo, hi), rs, lane * 16, off); }
DI void shared_send(unsigned w, unsigned lane, const u32x2 (&sh)[4]) {
  if (w >= 4) lds_st<u32x4>(L_SCR + w * 1024 + lane * 16, cat2(sh[0], sh[1]));
  else lds_st<u32x4>(L_SCR + w * 1024 + lane * 16, cat2(sh[2], sh[3]));
  asm volatile("" ::: "memory");
}
DI void shared_recv(rsrc_t rs, unsigned w, unsigned lane, unsigned off, unsigned str, const u32x2 (&sh)[4]) {
  lbar();
  u32x4 rcv = lds_ld<u32x4>(L_SCR + (w ^ 4) * 1024 + lane * 16);
  u32x2 r0 = {rcv[0], rcv[1]}, r1 = {rcv[2], rcv[3]};
  if (w >= 4) { st_frag(rs, lane, off + 2 * str, r0, sh[2]); st_frag(rs, lane, off + 3 * str, r1, sh[3]); }
  else        { st_frag(rs, lane, off, sh[0], r0); st_frag(rs, lane, off + str, sh[1], r1); }
}

__global__ __launch_bounds__(512, 2) void fused_block(Params p) {
  char* const ws = (char*)(((unsigned long long)p.wsraw + p.wsal) & ~p.wsal);
  unsigned tid = __builtin_amdgcn_workitem_id_x(), lane = tid & 63;
  const unsigned w = rfl(tid >> 6);
  const unsigned b = __builtin_amdgcn_workgroup_id_x(), xcd = b & 7, slot = b >> 3;
  const unsigned rb = 4 * (xcd & 1) + (slot & 3), jq = xcd >> 1, ji = slot >> 2;
  const unsigned spi = (xcd - 4) * 4 + (slot & 3);
  const unsigned long long jl0 = (jq & 1) ? 0x4123220908070605ull : 0x4021200403020100ull, jl1 = (jq & 1) ? 0x4327266011100f0eull : 0x422524600d0c0b0aull;
  const unsigned jb = (unsigned)(((jq & 2) ? jl1 : jl0) >> (8 * ji)) & 0xff, jtype = jb >> 5, jidx = jb & 31;
  unsigned long long pc0;
  asm volatile("s_getpc_b64 %0" : "=s"(pc0));
  STAMP_DECL;
  STAMP(0);

  Dma d;
  RowStat rs_;
  const bool mlp = jtype == J_MLP;
  const unsigned pitch = mlp ? 3456u : 3840u, wq = (w + (ROTW ? slot : 0)) & 3, wa = (w + (ROTW ? ji : 0)) & 7;
  d.wa = wa; d.wq = wq;
  const rsrc_t rsX = mk_rsrc(p.x, S * D * 2);
  d.vA0 = (lane >> 3) * 1280 + (((lane & 7) ^ (lane >> 4)) << 4); d.vA1 = d.vA0 ^ 64;
  d.rsA = rsX; d.mA = wa * 4096u;
#pragma unroll
  for (int i = 0; i < 4; ++i) d.sA[i] = (rb * 256 + 32 * wa + 8 * i) * 1280;
  unsigned epoch, tag;
  asm volatile("s_nop 4" :: "s"(rsX) : "memory");
  STAMP(6);
#pragma unroll
  for (int i = 0; i < 4; ++i) dma16(a_slot(0) + d.mA + i * 1024, (i & 1) ? d.vA1 : d.vA0, d.rsA, d.sA[i]);
  asm volatile("global_load_dword %0, %1, %2 sc1" : "=&v"(epoch) : "v"(0u), "s"(ws) : "memory");
  SB;
  {
    unsigned vb[3];
    const unsigned lim = mlp ? 176u : 144u;
    {
      const unsigned r0 = (lane * 43) >> 9, p0 = lane - 12 * r0;
      const bool w1 = p0 >= 8, w2 = p0 >= 4;
      const unsigned r1 = r0 + (w1 ? 6 : 5), p1 = p0 + (w1 ? -8 : 4), r2 = r0 + (w2 ? 11 : 10), p2 = p0 + (w2 ? -4 : 8);
      vb[0] = r0 * pitch + umin(p0 * 16, lim);
      vb[1] = r1 * pitch + umin((p1 * 16) ^ ((r1 & 8) << 2), lim);
      vb[2] = r2 * pitch + umin((p2 * 16) ^ 32, lim);
    }
    d.vB0 = vb[0]; d.vB1 = vb[1]; d.vB2 = vb[2];
    const unsigned mh = wq >> 1;
    const void* bw = mlp ? (mh ? p.wu : p.wg) : p.wqkv;
    const unsigned cj = mlp ? 192u : 160u * jtype, cm = mlp ? 0u : (jtype == J_KV ? 1280u : 160u), c0 = jtype == J_KV ? 1280u : 0u;
    d.rsB = mk_rsrc(bw, D * pitch);
    d.sB = (16 * (wq & 1) + (w < 4 ? 32 : 0)) * pitch + c0 + cj * jidx + cm * mh; d.advB = 64 * pitch;
    d.mB = L_B + wq * 3072u + (w < 4 ? 0u : 12288u); d.mW = L_B + wq * 3072u + (w < 4 ? 49152u : 0u);
    const unsigned bdst = L_B + (w < 4 ? 12288u : 0u) + wq * 3072u;
    rs_.row = rfl(b * 8 + w);
    if (jtype != J_SPARE) {
      dma16(bdst, d.vB0, d.rsB, d.sB); dma16(bdst + 1024, d.vB1, d.rsB, d.sB); dma16(bdst + 2048, d.vB2, d.rsB, d.sB);
      const unsigned so = d.sB + d.advB;
      dma16(bdst + 24576, d.vB0, d.rsB, so); dma16(bdst + 24576 + 1024, d.vB1, d.rsB, so); dma16(bdst + 24576 + 2048, d.vB2, d.rsB, so);
#pragma unroll
      for (int i = 0; i < NA1; ++i) dma16(a_slot(1) + d.mA + i * 1024, (i & 1) ? d.vA1 : d.vA0, d.rsA, d.sA[i] + 128);
    }
    d.sB += d.advB;
  }
  STAMP(10);
  lds_st<unsigned>(L_GO + (tid & 1) * 4, 0u);

  const unsigned t2 = 28 * xcd + slot;
  const bool has2 = slot < 28 && t2 < 220;
  const unsigned G = t2 / 10, cb = t2 % 10;
  constexpr unsigned rot = 32;
  unsigned g = lane >> 4, r = lane & 15, q4 = r >> 2, p4 = lane & 3;
  const rsrc_t rsWd = mk_rsrc(p.wd, has2 ? F * D * 2 : 0);
  const rsrc_t rsWS = mk_rsrc(ws, WS_END);
  unsigned v2 = (lane >> 3) * 1280 + (((lane & 7) ^ (((lane >> 4) & 3) << 1)) << 4);
  auto relane = [&] {
    asm volatile("" : "+v"(tid));
    lane = tid & 63; g = lane >> 4; r = lane & 15; q4 = r >> 2; p4 = lane & 3;
    v2 = (lane >> 3) * 1280 + (((lane & 7) ^ (((lane >> 4) & 3) << 1)) << 4);
  };
  const unsigned m = w & 3, n = w >> 2;
  const unsigned brow = L_B + (8 * g + q4) * 192;
  const unsigned bx = (g & 1) << 5;

  u32x4 pre2[36];
  W2 W;
  W.wd = rsWd; W.wo = mk_rsrc(p.wo, has2 ? D * D * 2 : 0); W.rot = rot; W.col = cb * 128u;
  if (jtype == J_MLP) {
    f32x4 acc[4][6];
    unsigned boff[6];
#pragma unroll
    for (int t = 0; t < 3; ++t) {
      const unsigned gt = n ? (t == 2 ? 3 : 4 + t) : t;
      boff[t] = brow + ((32 * gt + 8 * p4) ^ bx); boff[t + 3] = boff[t] + 6144;
    }
    const unsigned ccol = tid < 96 ? 1920 + 96 * jidx + tid : 3648 + 96 * jidx + (tid < 192 ? tid - 96 : 0);
    main_loop<6, true, true, true>([](int) {}, rs_, ws, acc, boff, d, w, lane, tid, epoch, tag, rb, ccol, rsWd, rsWS, cb, rot, pre2 STAMP_PASS);
    relane();
    float mm[4];
    f32x4 cg[3], cu[3];
#pragma unroll
    for (int i = 0; i < 4; ++i) mm[i] = lds_ld<float>(L_MISC + (64 * m + 16 * i + r) * 4);
#pragma unroll
    for (int t = 0; t < 3; ++t) {
      const unsigned gt = n ? (t == 2 ? 3 : 4 + t) : t;
      cg[t] = lds_ld<f32x4>(L_MISC + 2048 + (16 * gt + 4 * g) * 4); cu[t] = lds_ld<f32x4>(L_MISC + 2048 + (96 + 16 * gt + 4 * g) * 4);
    }
    auto hval = [&](int i, int t) {
      f32x4 gg = acc[i][t] + mm[i] * cg[t];
      const f32x4 uu = acc[i][t + 3] + mm[i] * cu[t];
#pragma unroll
      for (int j = 0; j < 4; ++j) gg[j] = relu2(gg[j]);
      return pk4(gg * uu);
    };
    const unsigned ho = WS_HA + rb * RBSZ + jidx * 49152 + m * 4 * 3072;
#pragma unroll
    for (int i = 0; i < 4; ++i) { st_frag(rsWS, lane, ho + i * 3072 + n * 2048, hval(i, 0), hval(i, 1)); SB; }
    const u32x2 sh[4] = {hval(0, 2), hval(1, 2), hval(2, 2), hval(3, 2)};
    shared_send(w, lane, sh);
    shared_recv(rsWS, w, lane, ho + 1024, 3072, sh);
    STAMP(15);
    publish_flag(ws, WS_FLAGH + (rb * 32 + jidx) * FS, tag, tid);
    STAMP(3);
  } else if (jtype == J_KV) {
    f32x4 acc[4][5];
    unsigned boff[5];
    boff[0] = brow + ((80 * n + 8 * p4) ^ bx); boff[1] = boff[0] + 6144;
    boff[2] = brow + ((80 * n + 40 + 8 * p4) ^ bx); boff[3] = boff[2] + 6144;
    boff[4] = brow + ((80 * n + 32 + 40 * (p4 >> 1)) ^ bx) + (p4 & 1) * 6144;
    const unsigned kb = 126976u + m * 6720 + n * 3360, dump = L_MISC - 2048 + lane * 16;
    const unsigned ea = (q4 == 1 || r == 0) ? kb + (r == 0 ? 20 : 12 + r) * 80 : 0u, eb = (q4 == 3 || r == 0) ? kb + 1680 + (r == 0 ? 20 : 4 + r) * 80 : 0u;
    unsigned ka[6] = {kb + r * 80 + g * 16, ea ? ea + g * 16 : dump, eb ? eb + g * 16 : dump, g == 0 ? kb + r * 80 + 64 : dump,
                      g == 2 ? kb + 1680 + r * 80 + 64 : dump, (g == 0 && ea) ? ea + 64 : ((g == 2 && eb) ? eb + 64 : dump)};
    auto pin = [&](int P) { if (P == 0) asm volatile("" : "+v"(ka[0]), "+v"(ka[1]), "+v"(ka[2]), "+v"(ka[3]), "+v"(ka[4]), "+v"(ka[5])); };
    const unsigned ccol = tid < 80 ? 640 + 80 * jidx + tid : 1280 + 80 * jidx + (tid < 160 ? tid - 80 : 0);
    main_loop<5, false, false, false>(pin, rs_, ws, acc, boff, d, w, lane, tid, epoch, tag, rb, ccol, rsWd, rsWS, cb, rot, pre2 STAMP_PASS);
    relane();
    float cs[5];
    cs[0] = lds_ld<float>(L_MISC + 2048 + (40 * n + r) * 4);
    cs[1] = lds_ld<float>(L_MISC + 2048 + (80 + 40 * n + r) * 4);
    cs[2] = lds_ld<float>(L_MISC + 2048 + (40 * n + 20 + r) * 4);
    cs[3] = lds_ld<float>(L_MISC + 2048 + (80 + 40 * n + 20 + r) * 4);
    cs[4] = lds_ld<float>(L_MISC + 2048 + ((q4 & 1) * 80 + 40 * n + 16 + 20 * (q4 >> 1) + (r & 3)) * 4);
    u32x2 pk[4][3], vv[4][3];
    const unsigned vmask = 0u - (q4 & 1);
    f32x4 mm4[4], rr4[4];
#pragma unroll
    for (int i = 0; i < 4; ++i) {
      mm4[i] = lds_ld<f32x4>(L_MISC + (64 * m + 16 * i + 4 * g) * 4); rr4[i] = lds_ld<f32x4>(L_MISC + 1024 + (64 * m + 16 * i + 4 * g) * 4);
    }
    SB;
#pragma unroll
    for (int i = 0; i < 4; ++i) {
      const f32x4 mm = mm4[i], rr = rr4[i];
      const f32x4 rl = rr * 1.4426950408889634f, rh = rr * 0.5f;
      f32x4 val[5];
#pragma unroll
      for (int t = 0; t < 5; ++t) val[t] = acc[i][t] + mm * cs[t];
#pragma unroll
      for (int t = 0; t < 3; ++t) pk[i][t] = pk4(phi4(val[2 * t], rl, rh));
      vv[i][0] = pk4(val[1] * rr); vv[i][1] = pk4(val[3] * rr);
      const u32x2 v4 = pk4(val[4] * rr);
      vv[i][2] = u32x2{bsel(vmask, v4[0], 0x3f803f80u), bsel(vmask, v4[1], 0x3f803f80u)};
    }
    STAMP(25);
    f32x4 P[7] = {};
#pragma unroll
    for (int i = 0; i < 4; i += 2) {
      const bf16x8 k0 = frag(pk[i][0], pk[i + 1][0]), k2 = frag(pk[i][1], pk[i + 1][1]), k4 = frag(pk[i][2], pk[i + 1][2]);
      const bf16x8 v1 = frag(vv[i][0], vv[i + 1][0]), v3 = frag(vv[i][1], vv[i + 1][1]), v4 = frag(vv[i][2], vv[i + 1][2]);
      P[0] = mfma(k0, v1, P[0]); P[1] = mfma(k0, v4, P[1]); P[2] = mfma(k4, v1, P[2]);
      P[3] = mfma(k2, v3, P[3]); P[4] = mfma(k2, v4, P[4]); P[5] = mfma(k4, v3, P[5]); P[6] = mfma(k4, v4, P[6]);
    }
    lds_st<f32x4>(ka[0], P[0]); lds_st<f32x4>(ka[0] + 1680, P[3]); lds_st<f32x4>(ka[1], P[1]); lds_st<f32x4>(ka[2], P[4]);
    lds_st<f32x4>(ka[3], P[2]); lds_st<f32x4>(ka[4], P[5]); lds_st<f32x4>(ka[5], P[6]);
    lbar();
    if (tid < 210) {
      const unsigned a = 126976u + tid * 32;
      const f32x4 t0 = (lds_ld<f32x4>(a) + lds_ld<f32x4>(a + 6720)) + (lds_ld<f32x4>(a + 13440) + lds_ld<f32x4>(a + 20160));
      const f32x4 t1 = (lds_ld<f32x4>(a + 16) + lds_ld<f32x4>(a + 6736)) + (lds_ld<f32x4>(a + 13456) + lds_ld<f32x4>(a + 20176));
      st16(cat2(pk4(t0), pk4(t1)), rsWS, tid * 16, WS_KVP + (rb * 32 + 4 * jidx) * 840);
    }
    wait_lgkm0();
    STAMP(15);
    publish_flag(ws, WS_FLAGKV + ((jidx >> 1) * 32 + (jidx & 1) * 8 + rb) * FS, tag, tid);
    STAMP(3);
  } else if (jtype == J_Q) {
    f32x4 acc[4][5];
    unsigned boff[5];
#pragma unroll
    for (int t = 0; t < 5; ++t) boff[t] = brow + n * 6144 + ((32 * t + 8 * p4) ^ bx);
    const unsigned ccol = 160 * jidx + (tid < 160 ? tid : 0);
    unsigned tch[4];
    auto warm = [&](int P) {
      if (P == 1) {
        asm volatile("buffer_load_dword %0, %1, %2, 0 offen" : "=&v"(tch[3]) : "v"((((rb & 3) * 8 + w) * 16 + (lane & 15)) * 128),
                     "s"(mk_rsrc((const void*)(pc0 & ~0x7full), CODE_BYTES)) : "memory");
        return;
      }
      const unsigned gq = (rb * 4 + jidx) * 8 + w;
      asm volatile("buffer_load_dword %0, %3, %5, 0 offen\n\tbuffer_load_dword %1, %3, %5, %7 offen\n\tbuffer_load_dword %2, %4, %6, 0 offen"
                   : "=&v"(tch[0]), "=&v"(tch[1]), "=&v"(tch[2])
                   : "v"((gq * 34 + (lane < 34 ? lane : 0)) * 128), "v"((gq * 25 + (lane < 25 ? lane : 0)) * 128), "s"(mk_rsrc(p.wd, F * D * 2)),
                     "s"(mk_rsrc(p.wo, D * D * 2)), "s"(8576u * 128u) : "memory");
    };
    main_loop<5, true, false, false>(warm, rs_, ws, acc, boff, d, w, lane, tid, epoch, tag, rb, ccol, rsWd, rsWS, cb, rot, pre2 STAMP_PASS);
    relane();
    asm volatile("" :: "v"(tch[0]), "v"(tch[1]), "v"(tch[2]), "v"(tch[3]));
    u32x2 pq[4][5];
    {
      float mm[4], rr[4];
      f32x4 c[5];
#pragma unroll
      for (int i = 0; i < 4; ++i) { mm[i] = lds_ld<float>(L_MISC + (64 * m + 16 * i + r) * 4); rr[i] = lds_ld<float>(L_MISC + 1024 + (64 * m + 16 * i + r) * 4); }
#pragma unroll
      for (int t = 0; t < 5; ++t) c[t] = lds_ld<f32x4>(L_MISC + 2048 + (80 * n + 16 * t + 4 * g) * 4);
      SB;
#pragma unroll
      for (int i = 0; i < 4; ++i)
#pragma unroll
        for (int t = 0; t < 5; ++t) pq[i][t] = pk4(phi4(acc[i][t] + mm[i] * c[t], splat(rr[i] * 1.4426950408889634f), splat(rr[i] * 0.5f)));
#pragma unroll
      for (int i = 0; i < 4; ++i) asm volatile("" : "+v"(pq[i][0]), "+v"(pq[i][1]), "+v"(pq[i][2]), "+v"(pq[i][3]), "+v"(pq[i][4]));
    }
    constexpr unsigned KP = 208, KQ = 84 * KP;
    unsigned wa[2];
#pragma unroll
    for (int k = 0; k < 2; ++k) {
      const unsigned qd = 2 * (tid < 420 ? tid : 0) + k, hd = qd / 105, e = (qd % 105) / 5, dq = qd % 5, hl = hd & 3;
      wa[k] = (hd >> 2) * KQ + (e < 20 ? hl * 20 + e : 80 + hl) * KP + hl * 40 + dq * 8;
    }
    unsigned fk = n * KQ + r * KP + g * 8, fz = n * KQ + (80 + (r & 3)) * KP + g * 8;
    unsigned gm[3] = {g < 1 ? ~0u : 0u, g < 2 ? ~0u : 0u, g < 3 ? ~0u : 0u};
    u32x4 B[4][3];
    f32x4 eps4 = splat(1e-6f);
#pragma unroll
    for (int i = 0; i < 4; ++i) {
      B[i][0] = cat2(pq[i][0], pq[i][1]); B[i][1] = cat2(pq[i][2], pq[i][3]); B[i][2] = cat2(pq[i][4], u32x2{0u, 0u});
      asm volatile("" : "+v"(B[i][0]), "+v"(B[i][1]), "+v"(B[i][2]));
    }
    asm volatile("" : "+v"(wa[0]), "+v"(wa[1]), "+v"(fk), "+v"(fz), "+v"(gm[0]), "+v"(gm[1]), "+v"(gm[2]), "+v"(eps4));
    STAMP(25);
    wait_vm<0>();
    bar();
#pragma unroll
    for (int k = 0; k < 5; ++k) lds_st<u32x4>(k * 8192 + tid * 16, u32x4{0u, 0u, 0u, 0u});
    if (w == 0) poll_flags((gu32*)(ws + WS_FLAGKV + (jidx * 32 + (lane & 15)) * FS), true, tag);
    lbar();
    STAMP(4);
    if (w < 7) {
      u32x4 v[8];
#pragma unroll
      for (int j = 0; j < 8; ++j)
        v[j] = __builtin_amdgcn_raw_buffer_load_b128(rsWS, (tid < 420 ? tid : 0) * 16, WS_KVP + (j * 32 + 8 * jidx) * 840, 16);
      SB;
      f32x4 t0 = {0.f, 0.f, 0.f, 0.f}, t1 = t0;
#pragma unroll
      for (int j = 0; j < 8; ++j) {
        t0 += f32x4{bflo(v[j][0]), bfhi(v[j][0]), bflo(v[j][1]), bfhi(v[j][1])};
        t1 += f32x4{bflo(v[j][2]), bfhi(v[j][2]), bflo(v[j][3]), bfhi(v[j][3])};
      }
      lds_st<u32x2>(wa[0], pk4(t0)); lds_st<u32x2>(wa[1], pk4(t1));
    }
    lbar();
    STAMP(16);
    u32x4 A[12];
#pragma unroll
    for (int P = 0; P < 3; ++P) A[P] = cat2(lds_ld<u32x2>(fz + P * 64), lds_ld<u32x2>(fz + P * 64 + 32));
#pragma unroll
    for (int u = 0; u < 5; ++u) {
      const unsigned a0 = fk + u * 16 * KP + (u < 3 ? 0 : 64);
      A[3 + u] = cat2(lds_ld<u32x2>(a0), lds_ld<u32x2>(a0 + 32));
    }
#pragma unroll
    for (int u = 1; u < 5; ++u) {
      const unsigned a0 = fk + u * 16 * KP + (u < 3 ? 64 : 128);
      A[7 + u] = cat2(lds_ld<u32x2>(a0), lds_ld<u32x2>(a0 + 32));
    }
    SB;
    u32x2 sh[4];
    f32x4 den[4], num[2][5];
    static_for<0, 12>([&](auto E) {
      constexpr int P = E.v >> 2, i = E.v & 3;
      if constexpr (P == 0) mfma_c(den[i], A[0], B[i][0], eps4); else mfma_ip(den[i], A[P], B[i][P]);
    });
    u32x2 o[5];
    const unsigned oo = WS_HA + rb * RBSZ + 18 * 49152 + jidx * 81920 + m * 4 * 5120;
    auto fin = [&](auto I, auto K) {
      constexpr int i = I.v, k = K.v, u = k < 8 ? k >> 1 : 4;
      if constexpr (k == 8 || !(k & 1)) {
        asm volatile("" : "+v"(num[i & 1][u]));
        const float rr = u == 0 ? den[i][0] : (u == 4 ? den[i][3] : bself(gm[(u + 2) % 3], den[i][(u + 3) & 3], den[i][u & 3]));
#pragma unroll
        for (int j = 0; j < 4; ++j) num[i & 1][u][j] = smul(num[i & 1][u][j], rr);
      }
      if constexpr (k == 8 || (k & 1)) o[u] = pk4(num[i & 1][u]);
      if constexpr (k == 3) st_frag(rsWS, lane, oo + i * 5120 + n * 2048, o[0], o[1]);
      if constexpr (k == 7) st_frag(rsWS, lane, oo + i * 5120 + n * 2048 + 1024, o[2], o[3]);
      if constexpr (k == 8) { sh[i] = o[4]; asm volatile("" : "+v"(sh[i])); }
    };
    static_for<0, 4>([&](auto I) {
      constexpr int i = I.v;
      static_for<0, 9>([&](auto K) {
        constexpr int k = K.v, u = k < 5 ? k : k - 4;
        if constexpr (k < 5) mfma_z(num[i & 1][u], A[3 + u], B[i][u < 3 ? 0 : 1]); else mfma_ip(num[i & 1][u], A[7 + u], B[i][u < 3 ? 1 : 2]);
        SB;
        if constexpr (i == 0) {
          if constexpr (k >= 5) {
            asm volatile("" : "+v"(den[k - 5]));
#pragma unroll
            for (int j = 0; j < 4; ++j) den[k - 5][j] = __builtin_amdgcn_rcpf(den[k - 5][j]);
            asm volatile("" : "+v"(den[k - 5]));
          }
        } else {
          fin(IC<i - 1>{}, K);
        }
        SB;
      });
    });
    static_for<0, 9>([&](auto K) { fin(IC<3>{}, K); SB; });
    shared_send(w, lane, sh);
    shared_recv(rsWS, w, lane, oo + 4096, 5120, sh);
    STAMP(15);
    publish_flag(ws, WS_FLAGO + (rb * 32 + jidx) * FS, tag, tid);
    STAMP(3);
    if (b == 224) {
      poll_flags((gu32*)(ws + WS_FLAGH + tid * FS), (tid & 31) < (tid < 256 ? 18u : 4u), tag);
      bar();
      if (tid == 0) __hip_atomic_store((gu32*)(ws + WS_EPOCH), tag, RLX_AGENT);
    }
  } else {
    const unsigned rk = spi * 4 + (w & 3), p0 = w < 4 ? rk : 128 + rk, p1 = p0 + 64;
    auto unit = [](unsigned q) { return q < 60 ? q + 30 : (q < 90 ? q - 60 : q); };
    const unsigned u0 = unit(p0), u1 = unit(p1);
    unsigned k10 = 0x00003f80u, k01 = 0x3f800000u;
    asm volatile("" : "+v"(k10), "+v"(k01));
    auto cs_src = [&](unsigned u, rsrc_t& rs, unsigned& cv, unsigned& st) {
      const unsigned cline = u / 3, third = u % 3;
      const void* cw = cline < 30 ? p.wqkv : (cline < 57 ? p.wg : p.wu);
      const unsigned cpitch = cline < 30 ? 3840u : 3456u;
      const unsigned clocal = cline < 30 ? cline : (cline < 57 ? cline - 30 : cline - 57);
      rs = mk_rsrc(cw, u < 252 ? D * cpitch : 0);
      cv = (third * 216 + (lane >> 3)) * cpitch + clocal * 128 + (lane & 7) * 16;
      st = 8 * cpitch;
    };
    auto cs_publish = [&](unsigned u, float (&cs)[8]) {
#pragma unroll
      for (int k = 0; k < 4; ++k) { const bool hi = lane & 32; const float o = shfl_x(hi ? cs[k] : cs[k + 4], lane, 32); cs[k] = (hi ? cs[k + 4] : cs[k]) + o; }
#pragma unroll
      for (int k = 0; k < 2; ++k) { const bool hi = lane & 16; const float o = shfl_x(hi ? cs[k] : cs[k + 2], lane, 16); cs[k] = (hi ? cs[k + 2] : cs[k]) + o; }
      { const bool hi = lane & 8; const float o = shfl_x(hi ? cs[0] : cs[1], lane, 8); cs[0] = (hi ? cs[1] : cs[0]) + o; }
      if (u < 252)
        __hip_atomic_store((gu64*)(ws + WS_CSUM) + (u % 3) * NCOL + (u / 3) * 64 + (lane & 7) * 8 + (lane >> 3),
                           ((unsigned long long)tag << 32) | __builtin_bit_cast(unsigned, cs[0]), RLX_AGENT);
    };
    row_load(rs_, rsX, lane);
    constexpr int CW = CWIN;
    u32x4 c[CW];
    rsrc_t rs0, rs1; unsigned cv0, cv1, st0, st1;
    cs_src(u0, rs0, cv0, st0); cs_src(u1, rs1, cv1, st1);
    auto cld = [&](int j) { return j < 27 ? __builtin_amdgcn_raw_buffer_load_b128(rs0, cv0 + j * st0, 0, 0) : __builtin_amdgcn_raw_buffer_load_b128(rs1, cv1 + (j - 27) * st1, 0, 0); };
    if (SDELAY) __builtin_amdgcn_s_sleep(SDELAY);
#pragma unroll
    for (int j = 0; j < CW; ++j) c[j] = cld(j);
    SB;
    asm volatile("s_waitcnt vmcnt(%2) lgkmcnt(0)\n\tv_readfirstlane_b32 %0, %1\n\ts_nop 3" : "=s"(tag) : "v"(epoch), "i"(CW) : "memory");
    tag = rfl(tag) + 1;
    static_for<0, 14>([&](auto K) { row_step<K.v>(rs_, lane); });
    lbar();
    row_publish(rs_, ws, tag, tid);
    float cs[8] = {};
    static_for<0, 54>([&](auto J) {
      constexpr int j = J.v;
#pragma unroll
      for (int k = 0; k < 4; ++k) { cs[2 * k] = dot2(c[j % CW][k], k10, cs[2 * k]); cs[2 * k + 1] = dot2(c[j % CW][k], k01, cs[2 * k + 1]); }
      if constexpr (j + CW < 54) c[j % CW] = cld(j + CW);
      SB;
      if constexpr (j == 26) {
        cs_publish(u0, cs);
        STAMP(11);
#pragma unroll
        for (int k = 0; k < 8; ++k) cs[k] = 0.f;
      }
    });
    cs_publish(u1, cs);
    STAMP(12);
    bar();
#pragma unroll 1
    for (unsigned i = w; i < 144; i += 8) dma16(i * 1024, v2, rsWd, urow(i >> 2, rot) + (i & 3) * 10240u + cb * 128u);
    drain_vm();
    bar();
  }
  if (!has2) { STAMP_END; return; }
  relane();

  const unsigned rbA = (G * 6) >> 4, rbB = (G * 6 + 5 > 127 ? 127 : G * 6 + 5) >> 4;
  if (w >= 4) {
    __builtin_amdgcn_s_setprio(HPRIO);
    const unsigned lw = w - 4, ld0 = lw * 1024;
    gu32* fo = (gu32*)(ws + WS_FLAGO + ((lane < 32 ? rbA : rbB) * 32 + (lane & 3)) * FS);
    gu32* fh = (gu32*)(ws + WS_FLAGH + ((lane < 32 ? rbA : rbB) * 32 + ((lane & 31) < 18 ? lane & 31 : 17)) * FS);
    __builtin_amdgcn_s_sleep(HSLEEP);
    if (jtype == J_KV) {
#pragma unroll
      for (int u = 31; u < 36; ++u) dma16(u * 4096 + ld0, v2, rsWd, w2_off(W, u, lw));
    }
    if (jtype != J_MLP) {
#pragma unroll
      for (int u = 0; u < NPRE; ++u) { pre2[u] = w2_load(W, 36 + u, lw, v2); SB; }
    }
#pragma unroll
    for (int u = 36 + NPRE; u < PSPLIT; ++u) { pre2[u - 36] = w2_load(W, u, lw, v2); SB; }
    if (w == 7) {
      __builtin_amdgcn_s_setprio(3);
      const unsigned v = poll_flags(fh, (lane & 31) < 18 && (lane & 31) >= 10, tag);
      lds_set(L_GO);
      if (!all64((lane & 31) >= 18 || v == tag)) poll_flags(fh, (lane & 31) < 18, tag);
      lds_set(L_GO + 4);
      __builtin_amdgcn_s_setprio(HPRIO);
    }
#pragma unroll
    for (int u = PSPLIT; u < HSPLIT; ++u) { pre2[u - 36] = w2_load(W, u, lw, v2); SB; }
    STAMP(8);
    unsigned la[3], wb[3], xo[3], st[3];
    u32x4 xr[3];
    const unsigned ch = lane & 7;
#pragma unroll
    for (int i = 0; i < 3; ++i) {
      const unsigned row = lw * 24 + i * 8 + (lane >> 3), rl = row >> 4, rr = row & 15;
      la[i] = (rl >= 3 ? 24576u + (rl - 3) * 4096u : rl * 4096u) + (ch >> 1) * 1024 + (ch & 1) * 512 + ((rr + 2 * ch) & 15) * 16;
      xo[i] = (G * 96 + row) * 1280 + cb * 128 + ch * 16;
      wb[i] = ld0 + lane * 16 + i * 49152;
      asm volatile("" : "+v"(la[i]), "+v"(wb[i]), "+v"(xo[i]));
    }
    float ri[3], rl[3];
    unsigned of = 0;
    f32x4 xf[3][2], dd[3][2], d2[3][2];
#pragma unroll
    for (int k = 2; k <= 6; ++k) {
      if (k == O_BAR && w == 7 && !all64((lane & 31) >= 4 || of == tag)) poll_flags(fo, (lane & 31) < 4, tag);
      if (k == 6) {
        wait_vm<0>();
#pragma unroll
        for (int h = 0; h < 3; ++h) {
          ri[h] = 2.0f * bfhi(st[h]) * bfhi(st[h]);
          rl[h] = -1.4426950408889634f * __builtin_amdgcn_rcpf(ri[h]);
          xf[h][0] = f32x4{bflo(xr[h][0]), bfhi(xr[h][0]), bflo(xr[h][1]), bfhi(xr[h][1])};
          xf[h][1] = f32x4{bflo(xr[h][2]), bfhi(xr[h][2]), bflo(xr[h][3]), bfhi(xr[h][3])};
          asm volatile("" : "+v"(ri[h]), "+v"(rl[h]), "+v"(xf[h][0]), "+v"(xf[h][1]));
        }
      }
      lbar();
      if (k == O_BAR - 1) { of = __hip_atomic_load(fo, RLX_AGENT); SB; }
      if (k < 6) {
        const int kk = k - 1;
#pragma unroll
        for (int u = 9 * (kk - 1) + ((kk - 1) & 1); u < 9 * kk + (kk & 1); ++u) {
          lds_st<u32x4>(wb[u / 12] + (u % 12) * 4096, pre2[u]);
          if (WSLEEP == 1) __builtin_amdgcn_s_sleep(1);
          if (WSLEEP == 2) asm volatile("s_nop 7");
          SB;
        }
      }
      if (k == 2 || k == 3) {
        constexpr int mid = (HSPLIT + 74) / 2 < 72 ? (HSPLIT + 74) / 2 : 72;
#pragma unroll
        for (int u = (k == 2 ? HSPLIT : mid); u < (k == 2 ? mid : 72); ++u) { pre2[u - 36] = w2_load(W, u, lw, v2); SB; }
        if (k == 3) { dma16(36 * 4096 + ld0, v2, W.wo, w2_off(W, 72, lw)); dma16(37 * 4096 + ld0, v2, W.wo, w2_off(W, 73, lw)); }
      }
      if (k == XK) {
#pragma unroll
        for (int h = 0; h < 3; ++h) {
          const unsigned row = G * 96 + lw * 24 + h * 8 + (lane >> 3);
          st[h] = __builtin_amdgcn_raw_buffer_load_b32(rsWS, WS_STAT + (row < S ? row : 0) * 8, 0, 16);
          xr[h] = __builtin_amdgcn_raw_buffer_load_b128(rsX, xo[h], 0, 0);
        }
      }
    }
    lbar();
#pragma unroll
    for (int i = 0; i < 3; ++i) {
      dd[i][0] = lds_ld<f32x4>(la[i]); d2[i][0] = lds_ld<f32x4>(la[i] + 12288);
      dd[i][1] = lds_ld<f32x4>(la[i] + 256); d2[i][1] = lds_ld<f32x4>(la[i] + 12288 + 256);
      la[i] += 49152;
      asm volatile("" : "+v"(la[i]));
    }
    __builtin_amdgcn_s_setprio(GPRIO);
#pragma unroll
    for (int i = 0; i < 3; ++i) {
      float v[8], e[8];
#pragma unroll
      for (int j = 0; j < 8; ++j) v[j] = sadd(dd[i][j >> 2][j & 3], d2[i][j >> 2][j & 3]);
      SB;
#pragma unroll
      for (int j = 0; j < 8; ++j) e[j] = smul(v[j], rl[i]);
      SB;
#pragma unroll
      for (int j = 0; j < 8; ++j) e[j] = __builtin_amdgcn_exp2f(e[j]);
      SB;
#pragma unroll
      for (int j = 0; j < 8; ++j) e[j] = sfma(e[j], ri[i], ri[i]);
      SB;
#pragma unroll
      for (int j = 0; j < 8; ++j) e[j] = __builtin_amdgcn_rcpf(e[j]);
      SB;
#pragma unroll
      for (int j = 0; j < 8; ++j) dd[i][j >> 2][j & 3] = smul(v[j], e[j]);
      asm volatile("" : "+v"(dd[i][0]), "+v"(dd[i][1]));
      SB;
    }
    const rsrc_t rsO = mk_rsrc(p.out, S * D * 2);
    STAMP(9);
    if (PUTB) bar();
    bar();
    f32x4 ss[3][2];
#pragma unroll
    for (int i = 0; i < 3; ++i) {
      ss[i][0] = lds_ld<f32x4>(la[i]) + lds_ld<f32x4>(la[i] + 12288);
      ss[i][1] = lds_ld<f32x4>(la[i] + 256) + lds_ld<f32x4>(la[i] + 12288 + 256);
    }
#pragma unroll
    for (int i = 0; i < 3; ++i)
      __builtin_amdgcn_raw_buffer_store_b128(cat2(pk4(xf[i][0] + ss[i][0] * dd[i][0]), pk4(xf[i][1] + ss[i][1] * dd[i][1])), rsO, xo[i], 0, 0);
    STAMP(5);
    STAMP_END;
    return;
  }
  {
    const unsigned kh = w & 1, rt = G * 6 + 3 * (w >> 1);
    const rsrc_t rsH = mk_rsrc(ws + WS_HA, 8 * RBSZ);
    unsigned tb[3][4];
#pragma unroll
    for (int t = 0; t < 4; ++t) {
      tb[0][t] = (4 * g + q4) * 128 + ((t ^ ((2 * g + (q4 >> 1)) & 3)) << 5) + 8 * p4 + kh * 4096;
      tb[1][t] = tb[0][t] + 49152; tb[2][t] = tb[0][t] + 98304;
      asm volatile("" : "+v"(tb[0][t]), "+v"(tb[1][t]), "+v"(tb[2][t]));
    }
    auto rd1 = [&](int i, int t, int hf) {
      const int sl = i == 36 ? 36 : (2 * i) % 36, bi = sl / 12 > 2 ? 2 : sl / 12;
      return __builtin_bit_cast(u32x2, lds_tr(tb[bi][t] + (sl - 12 * bi) * 4096 + hf * 2048));
    };
    constexpr int NW = P2NW, W0 = P2W0;
    constexpr int OI = O_BAR == 4 ? 17 : (O_BAR == 5 ? 22 : 26);
    constexpr int OB0 = OI == 26 ? OBN : 0;
    f32x4 acc[2][3][4];
    u32x4 a[NW][3];
    u32x2 bw[2][4][2];
    unsigned va[2][3];
#pragma unroll
    for (int h = 0; h < 3; ++h) {
      va[0][h] = lane * 16 + ((rt + h) >> 4) * RBSZ + ((rt + h) & 15) * 3072;
      va[1][h] = va[0][h] + ((rt + h) & 15) * 2048;
      asm volatile("" : "+v"(va[0][h]), "+v"(va[1][h]));
    }
    unsigned xa[4];
#pragma unroll
    for (int t = 0; t < 4; ++t) {
      xa[t] = w * 12288u + t * 1024 + g * 256 + ((r + 4 * t + (g & 2)) & 15) * 16;
      asm volatile("" : "+v"(xa[t]));
    }
    auto put = [&](auto Z, auto E) {
      constexpr int z = Z.v, t = E.v / 3, h = 2 - E.v % 3;
      asm volatile("" : "+v"(acc[z][h][t]));
      lds_st<f32x4>(xa[t] + z * 49152 + h * 4096, acc[z][h][t]);
    };
    auto lda = [&](auto I, auto H) {
      constexpr int i = I.v, h = H.v;
      constexpr unsigned s0 = i < 27 ? (2 * i + rot) % 54 : 2 * i, s1 = i < 27 ? (2 * i + 1 + rot) % 54 : 2 * i + 1;
      a[i % NW][h] = __builtin_amdgcn_raw_buffer_load_b128(rsH, va[i >= 27][h], step_off(s0) + kh * (step_off(s1) - step_off(s0)), 16);
      SB;
    };
    lds_wait(L_GO);
    unsigned go2 = 0;
    STAMP(8);
    static_for<0, W0>([&](auto I) { lda(I, IC<0>{}); lda(I, IC<1>{}); lda(I, IC<2>{}); });
#pragma unroll
    for (int t = 0; t < 4; ++t) { bw[0][t][0] = rd1(0, t, 0); bw[0][t][1] = rd1(0, t, 1); }
    static_for<0, 37>([&](auto I) {
      constexpr int i = I.v;
      constexpr int lim = i + NW - 1;
      constexpr int nxt = i <= OI ? (W0 + 2 * i < lim ? W0 + 2 * i : lim) : (27 + OB0 + 2 * (i - OI - 1) < lim ? 27 + OB0 + 2 * (i - OI - 1) : lim);
      constexpr int end = i <= OI ? 27 : 37;
      constexpr int l0 = nxt < end ? nxt : -1, l1 = (nxt + 1 <= lim && nxt + 1 < end) ? nxt + 1 : -1;
      SB;
      static_for<0, 12>([&](auto E) {
        constexpr int e = E.v, t = e / 3, h = e % 3;
        const u32x4 bf = cat2(bw[i & 1][t][0], bw[i & 1][t][1]);
        if constexpr (i == 0 || i == 27) mfma_z(acc[i >= 27][2 - h][t], bf, a[i % NW][2 - h]); else mfma_ip(acc[i >= 27][2 - h][t], bf, a[i % NW][2 - h]);
        SB;
        if constexpr (i == 4 && e == 2) go2 = lds_get(L_GO + 4);
        if constexpr (!PUTB && (i == 27 || i == 28) && (e & 1)) put(IC<0>{}, IC<(i - 27) * 6 + e / 2>{});
        if constexpr (i == 36 && e >= 8) put(IC<1>{}, IC<e - 8>{});
        if constexpr (i < 36 && h < 2) bw[(i + 1) & 1][t][h] = rd1(i + 1, t, h);
        if constexpr (l1 < 0) { if constexpr (h == 2 && t < 3 && l0 >= 0) lda(IC<l0>{}, IC<t>{}); }
        else if constexpr (e & 1) lda(IC<(e < 6 ? l0 : l1)>{}, IC<(e >> 1) % 3>{});
        SB;
      });
      if constexpr (i == (PUTB ? 33 : 29)) { asm volatile("s_waitcnt lgkmcnt(8)" ::: "memory"); bar(); }
      if constexpr (i == 4) { if (__builtin_expect(!rfl(go2), 0)) lds_wait(L_GO + 4); asm volatile("" ::: "memory"); STAMP(17); }
      if constexpr (i > 4 && i < 27 && (i % 9 == 4 || i % 9 == 8)) {
        bar();
        if constexpr (i == 26 && OI == 26) static_for<27, 27 + OB0>([&](auto J) { lda(J, IC<0>{}); lda(J, IC<1>{}); lda(J, IC<2>{}); });
        if constexpr (PUTB && i == 26) { static_for<0, 12>([&](auto E) { put(IC<0>{}, E); SB; }); lbar(); }
        STAMP(17 + (2 * i) / 9);
      }
    });
    STAMP(24);
    static_for<4, 12>([&](auto E) { if constexpr (E.v == 8) asm volatile("s_nop 3" ::: "memory"); put(IC<1>{}, E); SB; });
    lbar();
    STAMP(5);
    STAMP_END;
  }
}

extern "C" void kernel_launch(void* const* d_in, const int* in_sizes, int n_in, void* d_out, int out_size, void* d_ws,
                              size_t ws_size, hipStream_t stream) {
  Params p;
  p.x = d_in[0]; p.wqkv = d_in[1]; p.wo = d_in[2]; p.wg = d_in[4]; p.wu = d_in[5]; p.wd = d_in[6];
  p.out = d_out; p.wsraw = (char*)d_ws;
  p.wsal = ws_size >= (64ull << 20) + WS_END ? (64ull << 20) - 1 : 0;
  fused_block<<<256, 512, 0, stream>>>(p);
}
```

```cpp
#include <hip/hip_runtime.h>
#include <stdint.h>

typedef __attribute__((ext_vector_type(8))) __bf16 bf16x8;
typedef __attribute__((ext_vector_type(4))) __bf16 bf16x4;
typedef __attribute__((ext_vector_type(2))) __bf16 bf16x2;
typedef __attribute__((ext_vector_type(4))) float f32x4;
typedef __attribute__((ext_vector_type(2))) float f32x2;
typedef __attribute__((ext_vector_type(4))) unsigned u32x4;
typedef __attribute__((ext_vector_type(2))) unsigned u32x2;
typedef __amdgpu_buffer_rsrc_t rsrc_t;
#define LAS __attribute__((address_space(3)))
#define GAS __attribute__((address_space(1)))
#define DI __device__ __forceinline__
#define RLX_AGENT __ATOMIC_RELAXED, __HIP_MEMORY_SCOPE_AGENT
typedef GAS unsigned gu32;
typedef GAS unsigned long long gu64;

constexpr int S = 2048, D = 640, F = 1728;
constexpr int NCOL = 5376;
constexpr int NSTEP = 74;
constexpr unsigned WS_EPOCH = 0;
#define FS 128
constexpr unsigned WS_FLAGH = 768u << 10;
constexpr unsigned WS_FLAGO = WS_FLAGH + 8 * 32 * FS;
constexpr unsigned WS_FLAGKV = WS_FLAGO + 8 * 32 * FS;
constexpr unsigned WS_STAT = 4096;
constexpr unsigned WS_CSUM = WS_STAT + S * 8;
constexpr unsigned WS_KVP = WS_CSUM + 3 * NCOL * 8;
constexpr unsigned WS_HA = 1u << 20;
constexpr unsigned RBSZ = 16 * NSTEP * 1024;
constexpr unsigned step_off(unsigned st) { return st < 54 ? (st / 3) * 49152 + (st % 3) * 1024 : 18 * 49152 + ((st - 54) / 5) * 81920 + ((st - 54) % 5) * 1024; }
constexpr unsigned WS_DBG = 12u << 20;
constexpr unsigned WS_END = 13u << 20;
constexpr unsigned L_B = 65536;
constexpr unsigned L_SCR = 147456;
constexpr unsigned L_MISC = 159744;
constexpr unsigned L_GO = L_MISC + 3072;
constexpr unsigned SPIN_CAP = 1u << 18;
#define HSLEEP 4
#define HSPLIT 64
#define PPIPE 0
#define STA 0
#define STB 2
#define SNAKE 0
#define ROTW 1
#define NPRE 12
#define PUTB 0
#define OBN 6
#define PSPLIT 48
#define WARM 1
#define CWIN 40
#define SDELAY 0
#define CSTG 6
#define CEARLY 1
#define TCH0 tch0
#define XK 2
#define GRP 1
#define GPRIO 0
#define HPRIO 0
#define PSLEEP 2
#define P2NW 7
#define P2W0 6
#define WSLEEP 0
#define ROTM 18
#define NA1 2
#define O_BAR 5
#define PRIO_MODE 0
#define DMA_SPACE_A T
#define DMA_SPACE_B (4 * T / 3)

struct Params {
  const void* x; const void* wqkv; const void* wo; const void* wg; const void* wu; const void* wd;
  void* out; char* wsraw;
  unsigned long long wsal;
};

__shared__ __attribute__((aligned(16))) char smem[163840];

DI float sadd(float a, float b) { float o; asm("v_add_f32 %0, %1, %2" : "=v"(o) : "v"(a), "v"(b)); return o; }
DI float smul(float a, float b) { float o; asm("v_mul_f32 %0, %1, %2" : "=v"(o) : "v"(a), "v"(b)); return o; }
DI float sfma(float a, float b, float c) { float o; asm("v_fma_f32 %0, %1, %2, %3" : "=v"(o) : "v"(a), "v"(b), "v"(c)); return o; }
DI unsigned bsel(unsigned m, unsigned a, unsigned b) { unsigned o; asm("v_bfi_b32 %0, %1, %2, %3" : "=v"(o) : "v"(m), "v"(a), "v"(b)); return o; }
DI float bself(unsigned m, float a, float b) { return __builtin_bit_cast(float, bsel(m, __builtin_bit_cast(unsigned, a), __builtin_bit_cast(unsigned, b))); }
DI bool all64(bool c) { return __builtin_amdgcn_ballot_w64(!c) == 0; }
DI float shfl_x(float v, unsigned lane, unsigned m) {
  return __builtin_bit_cast(float, __builtin_amdgcn_ds_bpermute((int)((lane ^ m) << 2), __builtin_bit_cast(int, v)));
}
DI unsigned umin(unsigned a, unsigned b) { return a < b ? a : b; }
DI unsigned rfl(unsigned v) { return __builtin_amdgcn_readfirstlane(v); }
DI rsrc_t mk_rsrc(const void* p, unsigned bytes) { return __builtin_amdgcn_make_buffer_rsrc((void*)p, (short)0, (int)bytes, 0x00020000); }
DI void dma16(unsigned ldsdst, unsigned voff, rsrc_t rs, unsigned soff) {
  asm volatile("s_mov_b32 m0, %0\n\ts_nop 0\n\tbuffer_load_dwordx4 %1, %2, %3 offen lds" ::"s"(ldsdst), "v"(voff), "s"(rs), "s"(soff) : "memory");
}
template <int N> DI void wait_vm() { asm volatile("s_waitcnt vmcnt(%0)" ::"i"(N) : "memory"); }
DI void drain_vm() { wait_vm<0>(); __builtin_amdgcn_s_waitcnt(0x0F70); }
DI void wait_lgkm0() { asm volatile("s_waitcnt lgkmcnt(0)" ::: "memory"); }
DI void bar() { asm volatile("" ::: "memory"); __builtin_amdgcn_s_barrier(); asm volatile("" ::: "memory"); }
DI void lbar() { wait_lgkm0(); bar(); }
template <class V> DI V lds_ld(unsigned a) { return *(LAS V*)((LAS char*)smem + a); }
template <class V> DI void lds_st(unsigned a, V v) { *(LAS V*)((LAS char*)smem + a) = v; }
DI bf16x4 lds_tr(unsigned a) { return __builtin_amdgcn_ds_read_tr16_b64_v4bf16((LAS bf16x4*)((LAS char*)smem + a)); }
DI bf16x8 cat(bf16x4 a, bf16x4 b) { return __builtin_shufflevector(a, b, 0, 1, 2, 3, 4, 5, 6, 7); }
DI f32x4 mfma(bf16x8 a, bf16x8 b, f32x4 c) { return __builtin_amdgcn_mfma_f32_16x16x32_bf16(a, b, c, 0, 0, 0); }
DI unsigned pk2(float a, float b) { f32x2 v = {a, b}; return __builtin_bit_cast(unsigned, __builtin_convertvector(v, bf16x2)); }
DI u32x2 pk4(f32x4 v) { u32x2 r = {pk2(v[0], v[1]), pk2(v[2], v[3])}; return r; }
DI bf16x8 frag(u32x2 lo, u32x2 hi) { u32x4 v = {lo[0], lo[1], hi[0], hi[1]}; return __builtin_bit_cast(bf16x8, v); }
DI float bflo(unsigned v) { return __builtin_bit_cast(float, v << 16); }
DI float bfhi(unsigned v) { return __builtin_bit_cast(float, v & 0xffff0000u); }
DI float rbf(float v) { return bflo(pk2(v, 0.f)); }
DI float relu2(float v) { float o; asm("v_add_f32 %0, %1, |%1|" : "=v"(o) : "v"(v)); return o; }
DI f32x4 phi4(f32x4 u, f32x4 rl, f32x4 rh) {
  const f32x4 l = u * rl;
  f32x4 e, a;
#pragma unroll
  for (int j = 0; j < 4; ++j) {
    e[j] = __builtin_amdgcn_fmed3f(__builtin_amdgcn_exp2f(l[j]), 0.f, 1.f);
    a[j] = relu2(u[j]);
  }
  return a * rh + e + 1e-6f;
}
DI f32x4 splat(float v) { return f32x4{v, v, v, v}; }
template <int CTRL> DI float dpp_add(float v) {
  return v + __builtin_bit_cast(float, __builtin_amdgcn_update_dpp(0, __builtin_bit_cast(int, v), CTRL, 0xf, 0xf, true));
}
DI float dot2(unsigned a, unsigned b, float c) {
  return __builtin_amdgcn_fdot2_f32_bf16(__builtin_bit_cast(bf16x2, a), __builtin_bit_cast(bf16x2, b), c, false);
}
DI u32x4 ldtr2(unsigned a, unsigned d) { return __builtin_bit_cast(u32x4, cat(lds_tr(a), lds_tr(a + d))); }
DI void mfma_ip(f32x4& c, const u32x4& a, const u32x4& b) { asm volatile("v_mfma_f32_16x16x32_bf16 %0, %1, %2, %0" : "+v"(c) : "v"(a), "v"(b)); }
DI void mfma_c(f32x4& d, const u32x4& a, const u32x4& b, const f32x4& c) { asm volatile("v_mfma_f32_16x16x32_bf16 %0, %1, %2, %3" : "=&v"(d) : "v"(a), "v"(b), "v"(c)); }
DI void mfma_z(f32x4& c, const u32x4& a, const u32x4& b) { asm volatile("v_mfma_f32_16x16x32_bf16 %0, %1, %2, 0" : "=&v"(c) : "v"(a), "v"(b)); }
template <int N> DI void acc_fence(f32x4* c) {
  asm volatile("s_nop 7" ::: "memory");
#pragma unroll
  for (int i = 0; i < N; ++i) asm volatile("" : "+v"(c[i]));
}
DI u32x4 cat2(u32x2 a, u32x2 b) { return u32x4{a[0], a[1], b[0], b[1]}; }
DI u32x2 ldtr1(unsigned a) { return __builtin_bit_cast(u32x2, lds_tr(a)); }
template <int IMM> DI void set_m0i(unsigned v) { asm volatile("s_add_i32 m0, %0, %1" ::"s"(v), "i"(IMM) : "memory", "scc"); }
template <int OFF> DI void dma_goi(unsigned voff, rsrc_t rs, unsigned soff) {
  asm volatile("buffer_load_dwordx4 %0, %1, %2 offen offset:%3 lds" ::"v"(voff), "s"(rs), "s"(soff), "i"(OFF) : "memory");
}
DI void set_m0(unsigned v) { asm volatile("s_mov_b32 m0, %0" ::"s"(v) : "memory"); }
DI void dma_go(unsigned voff, rsrc_t rs, unsigned soff) {
  asm volatile("buffer_load_dwordx4 %0, %1, %2 offen lds" ::"v"(voff), "s"(rs), "s"(soff) : "memory");
}
DI f32x4 mfma_u(u32x4 a, u32x4 b, f32x4 c) { return mfma(__builtin_bit_cast(bf16x8, a), __builtin_bit_cast(bf16x8, b), c); }
#define STAMP_DECL
#define STAMP_ARG
#define STAMP_PASS
#define STAMPX(c, i)
#define STAMP_END
#define STAMP(i) STAMPX(__builtin_amdgcn_workitem_id_x() == 0, i)

DI unsigned poll_flags(gu32* word, bool active, unsigned tag) {
  unsigned v = 0;
#pragma unroll 1
  for (unsigned spins = 0; spins < SPIN_CAP; ++spins) {
    v = __hip_atomic_load(word, RLX_AGENT);
    if (all64(!active || v == tag)) break;
    if (PSLEEP) __builtin_amdgcn_s_sleep(PSLEEP);
  }
  asm volatile("" ::: "memory");
  return v;
}

DI void lds_set(unsigned a) { __hip_atomic_store((LAS unsigned*)smem + a / 4, 1u, __ATOMIC_RELAXED, __HIP_MEMORY_SCOPE_WORKGROUP); }
DI unsigned lds_get(unsigned a) { return __hip_atomic_load((LAS unsigned*)smem + a / 4, __ATOMIC_RELAXED, __HIP_MEMORY_SCOPE_WORKGROUP); }
DI void lds_wait(unsigned a) {
#pragma unroll 1
  for (unsigned spins = 0; spins < (SPIN_CAP << 4); ++spins) {
    if (rfl(lds_get(a))) break;
    __builtin_amdgcn_s_sleep(1);
  }
  asm volatile("" ::: "memory");
}

template <int I> struct IC { static constexpr int v = I; };
template <int B, int E, class Fn> DI void static_for(Fn&& f) {
  if constexpr (B < E) { f(IC<B>{}); static_for<B + 1, E>(f); }
}

enum { J_MLP = 0, J_KV = 1, J_Q = 2, J_SPARE = 3 };

struct Dma {
  rsrc_t rsA, rsB;
  unsigned vA0, vA1, vB0, vB1, vB2;
  unsigned sA[4], sB, advB;
  unsigned wa, wq;
  unsigned mA, mB, mW;
};

constexpr unsigned a_slot(unsigned s3) { return s3 == 0 ? 126976u : (s3 - 1) * 32768u; }
#define SB __builtin_amdgcn_sched_barrier(0)

template <int T> DI unsigned bofs(const unsigned (&boff)[T], int t) { return T == 6 && t >= 3 ? boff[t - 3] + 6144 : boff[t]; }
template <int T, bool TR, int KS, class Extra>
DI void kstep(f32x4 (&acc)[4][T], const u32x4 (&ac)[4], const u32x2 (&bc)[T][2], u32x4 (&an)[4], u32x2 (&bn)[T][2],
              const unsigned (&aoff)[4], const unsigned (&boff)[T], const Dma& d, Extra&& extra) {
  constexpr int s = KS >> 1, KIND = KS & 1, NP = KIND == 0 ? 4 : 3, ST = KIND == 0 ? (STA ? STA : (4 * T - 1) / NP) : (STB ? STB : (4 * T - 1) / NP);
  constexpr int sn = (KS + 1) >> 1;
  constexpr unsigned asl = a_slot(sn % 3), bsl = ((KS + 1) % 5) * 12288u;
  static_for<0, 4 * T>([&](auto E) {
    constexpr int e = E.v, t = e >> 2, i = (SNAKE && (t & 1)) ? 3 - (e & 3) : (e & 3);
    const u32x4 bf = cat2(bc[t][0], bc[t][1]);
    if constexpr (KS == 0) { if (TR) mfma_z(acc[i][t], bf, ac[i]); else mfma_z(acc[i][t], ac[i], bf); }
    else { if (TR) mfma_ip(acc[i][t], bf, ac[i]); else mfma_ip(acc[i][t], ac[i], bf); }
    SB;
    if constexpr (KS < 19) {
      if constexpr (e < 4) an[e] = lds_ld<u32x4>(aoff[((KS + 1) & 1) + (sn % 3 == 0 ? 2 : 0)] + (sn % 3 == 0 ? 0u : asl) + e * 2048);
      else if constexpr (e < 4 + 2 * T) bn[(e - 4) >> 1][e & 1] = ldtr1(bofs<T>(boff, (e - 4) >> 1) + bsl + (e & 1) * 768);
    }
    constexpr int pc = e / ST;
    if constexpr (KIND == 0) {
      constexpr int adv = s < 8 ? (s + 2) * 128 : 0, dst = (int)a_slot((s + 2) % 3) + pc * 1024, off = dst >= adv ? adv : 0;
      if constexpr (pc < NP && e % ST == ST - 2) set_m0i<dst - off>(d.mA);
      if constexpr (pc < NP && e % ST == ST - 1) dma_goi<off>((pc & 1) ? d.vA1 : d.vA0, d.rsA, d.sA[pc] + (adv - off));
    } else {
      constexpr int h0 = (2 * s) % 5;
      if constexpr (pc < NP && e % ST == ST - 2) set_m0i<(h0 == 4 ? 0 : h0 * 12288) + pc * 1024>(h0 == 4 ? d.mW : d.mB);
      if constexpr (pc < NP && e % ST == ST - 1) dma_goi<0>(pc == 0 ? d.vB0 : (pc == 1 ? d.vB1 : d.vB2), d.rsB, d.sB);
    }
    extra(E);
    SB;
  });
}

struct RowStat { u32x4 xa; unsigned xb, row; float s, q; };
DI void row_load(RowStat& R, rsrc_t rsX, unsigned lane) {
  asm volatile("buffer_load_dwordx4 %0, %2, %4, %5 offen\n\tbuffer_load_dword %1, %3, %4, %5 offen offset:1024"
               : "=&v"(R.xa), "=&v"(R.xb) : "v"(lane * 16), "v"(lane * 4), "s"(rsX), "s"(R.row * 1280) : "memory");
}
template <int K> DI void row_step(RowStat& R, unsigned lane) {
  if constexpr (K < 5) {
    if constexpr (K == 0) { R.s = 0.f; R.q = 0.f; }
    const unsigned v = K < 4 ? R.xa[K & 3] : R.xb;
    asm volatile("v_dot2c_f32_bf16 %0, %2, %3\n\tv_dot2c_f32_bf16 %1, %2, %2" : "+v"(R.s), "+v"(R.q) : "v"(v), "v"(0x3f803f80u));
  } else if constexpr (K < 11) {
    constexpr int C = K == 5 ? 0xB1 : (K == 6 ? 0x4E : (K == 7 ? 0x141 : (K == 8 ? 0x140 : (K == 9 ? 0x142 : 0x143))));
    if constexpr (K == 5) asm volatile("s_nop 2" : "+v"(R.s), "+v"(R.q));
    R.s = dpp_add<C>(R.s); R.q = dpp_add<C>(R.q);
  } else if constexpr (K == 11) {
    R.s *= 1.0f / D;
    R.q = __builtin_fmaxf(R.q * (1.0f / D) - R.s * R.s, 0.f);
  } else if constexpr (K == 12) {
    R.q = __builtin_amdgcn_sqrtf(rbf(rbf(R.q) + 1e-5f));
  } else {
    lds_st<unsigned>(lane == 63 ? L_GO + 32 + (R.row & 7) * 4 : L_GO + 64, pk2(R.s, R.q));
  }
  if constexpr (K >= 5 && K < 13) asm volatile("" : "+v"(R.s), "+v"(R.q));
}
DI void row_publish(const RowStat& R, char* ws, unsigned tag, unsigned tid) {
  if (tid < 8) __hip_atomic_store((gu64*)(ws + WS_STAT) + R.row + tid, ((unsigned long long)tag << 32) | lds_ld<unsigned>(L_GO + 32 + tid * 4), RLX_AGENT);
}

struct Gran { u32x2 g0, g1, g2; unsigned off, step; };
DI void gran_issue(Gran& G, rsrc_t rs) {
  asm volatile("buffer_load_dwordx2 %0, %3, %4, 0 offen sc1\n\tbuffer_load_dwordx2 %1, %3, %4, %5 offen sc1\n\t"
               "buffer_load_dwordx2 %2, %3, %4, %6 offen sc1"
               : "=&v"(G.g0), "=&v"(G.g1), "=&v"(G.g2) : "v"(G.off), "s"(rs), "s"(G.step), "s"(2 * G.step) : "memory");
}
DI void gran_load(Gran& G, rsrc_t rs) {
  asm volatile("buffer_load_dwordx2 %0, %3, %4, 0 offen sc1\n\tbuffer_load_dwordx2 %1, %3, %4, %5 offen sc1\n\t"
               "buffer_load_dwordx2 %2, %3, %4, %6 offen sc1\n\ts_waitcnt vmcnt(0)"
               : "=&v"(G.g0), "=&v"(G.g1), "=&v"(G.g2) : "v"(G.off), "s"(rs), "s"(G.step), "s"(2 * G.step) : "memory");
}

DI unsigned urow(unsigned pos, unsigned rot) { const unsigned u = pos + rot; return (u >= 54 ? u - 54 : u) * 40960u; }

constexpr unsigned wo_tile(unsigned v) { return (v / 5) * 10 + (v % 5 == 4 ? 4 : (v % 5 < 2 ? 2 * (v % 5) : 2 * (v % 5) + 1)); }
struct W2 { rsrc_t wd, wo; unsigned rot, col; };
DI unsigned w2_off(const W2& W, int u, unsigned lw) {
  if (u < 54) return urow(u, W.rot) + lw * 10240u + W.col;
  return wo_tile(u - 54) * 20480u + (lw >> 1) * ((u - 54) % 5 == 4 ? 102400u : 20480u) + (lw & 1) * 10240u + W.col;
}
DI u32x4 w2_load(const W2& W, int u, unsigned lw, unsigned voff) {
  return __builtin_amdgcn_raw_buffer_load_b128(u < 54 ? W.wd : W.wo, voff, w2_off(W, u, lw), 0);
}

template <int T, bool TR, bool PF, bool LASTA, class Pre>
DI void main_loop(Pre&& pre, RowStat& R, char* ws, f32x4 (&acc)[4][T], unsigned (&boff)[T], Dma& d, unsigned w, unsigned lane, unsigned tid,
                  unsigned epoch, unsigned keep, unsigned& tag, unsigned rb, unsigned ccol, rsrc_t rsB2, rsrc_t rsWS, unsigned cb, unsigned rot, u32x4 (&pre2)[36] STAMP_ARG) {
  const unsigned g = lane >> 4, r = lane & 15, m = w & 3;
  unsigned aoff[4];
  aoff[0] = (64 * m + r) * 128 + ((g ^ (r >> 1)) << 4);
  aoff[1] = aoff[0] ^ 64;
  aoff[2] = aoff[0] + a_slot(0); aoff[3] = aoff[1] + a_slot(0);
  const unsigned wq = d.wq, wa = d.wa;
  asm volatile("" : "+v"(aoff[0]), "+v"(aoff[1]), "+v"(aoff[2]), "+v"(aoff[3]), "+v"(boff[0]), "+v"(boff[1]), "+v"(boff[2]));
  if (T == 5) asm volatile("" : "+v"(boff[3]), "+v"(boff[4]));
  Gran G;
  unsigned v2, vb2, vpf, tA[3], tB[3];
  row_load(R, d.rsA, lane);
  {
    asm volatile("s_waitcnt vmcnt(%2) lgkmcnt(0)\n\tv_readfirstlane_b32 %0, %1\n\ts_nop 3" : "=s"(tag) : "v"(epoch), "i"(5 + NA1), "v"(keep) : "memory");
    tag = rfl(tag) + 1;
    bar();
  }
  STAMP(1);
  u32x4 a0[4], a1[4];
  u32x2 b0[T][2], b1[T][2];
#pragma unroll
  for (int i = 0; i < 4; ++i) a0[i] = lds_ld<u32x4>(aoff[2] + i * 2048);
#pragma unroll
  for (int t = 0; t < T; ++t) { b0[t][0] = ldtr1(bofs<T>(boff, t)); b0[t][1] = ldtr1(bofs<T>(boff, t) + 768); }
  SB;
#pragma unroll
  for (int i = NA1; i < 4; ++i) dma16(a_slot(1) + d.mA + i * 1024, (i & 1) ? d.vA1 : d.vA0, d.rsA, d.sA[i] + 128);
  const bool y = w >= 4;
  bool ok = true;
  auto ln_store = [&] {
    const float v0 = y ? -bflo(G.g0[0]) : __builtin_bit_cast(float, G.g0[0]) + __builtin_bit_cast(float, G.g1[0]) + __builtin_bit_cast(float, G.g2[0]);
    const unsigned la = L_MISC + (y ? tid * 4 - 1024 : 2048 + tid * 4);
    lds_st<float>(la, v0);
    lds_st<float>(y ? la + 1024 : L_GO + 64 + (lane & 7) * 4, __builtin_amdgcn_rcpf(bfhi(G.g0[0])));
  };
  static_for<0, 10>([&](auto S_) {
    constexpr int s = S_.v, L = 4 * T;
    constexpr int NX = (PF && s >= 1 && 2 * s <= NPRE) ? 2 : 0;
    kstep<T, TR, 2 * s>(acc, a0, b0, a1, b1, aoff, boff, d, [&](auto E) {
      constexpr int e = E.v;
      if constexpr (s == 2) {
        if constexpr (e == 2) { vb2 = v2 + (3 * wq) * 10240u; asm volatile("" : "+v"(vb2)); }
        if constexpr (e == 5) { G.off = w < 4 ? WS_CSUM + ccol * 8 : WS_STAT + (rb * 256 + tid - 256) * 8; asm volatile("" : "+v"(G.off)); }
        if constexpr (e == 8) { G.step = w < 4 ? NCOL * 8 : 0; tA[0] = urow(wa, rot) + cb * 128u; asm volatile("" : "+s"(G.step), "+s"(tA[0])); }
        if constexpr (e == 10) { tA[1] = urow(8 + wa, rot) + cb * 128u; asm volatile("" : "+s"(tA[1])); }
        if constexpr (e == 12) { tA[2] = urow(31 + wa, rot) + cb * 128u; asm volatile("" : "+s"(tA[2])); }
        if constexpr (e == 14) {
          tB[0] = urow(16, rot) + cb * 128u; tB[1] = urow(w < 4 ? 19u : 22u, rot) + cb * 128u; tB[2] = urow(w < 4 ? 25u : 28u, rot) + cb * 128u;
          asm volatile("" : "+s"(tB[0]), "+s"(tB[1]), "+s"(tB[2]));
        }
      }
      if constexpr (s == (TR ? 1 : 3) && e == 2) pre(0);
      if constexpr (s == CSTG && e == 2) pre(1);
      if constexpr (NX && e >= L - 2) {
        constexpr int u = 2 * (s - 1) + e - (L - 2);
        pre2[u] = __builtin_amdgcn_raw_buffer_load_b128(rsB2, vpf, urow(36 + u, rot) + (w & 3) * 10240u + cb * 128u, 0);
      }
      if constexpr (s < 7) { if constexpr (e == L - 3) d.sB += s == 0 ? d.advB * (1 + (w >> 2)) : d.advB; }
      else if constexpr (s == 7) {
        if constexpr (e == L - 6) d.vB0 = y ? vb2 : d.vB0;
        if constexpr (e == L - 4) d.vB1 = y ? vb2 + 10240u : d.vB1;
        if constexpr (e == L - 3) d.vB2 = y ? vb2 + 20480u : d.vB2;
        if constexpr (e == L - 2) d.sB = y ? tB[0] : d.sB + d.advB;
        if constexpr (e == L - 1) d.rsB = y ? rsB2 : d.rsB;
      } else {
        if constexpr (e == L - 3) { d.sB = tB[s - 7]; d.rsB = rsB2; d.vB0 = vb2; d.vB1 = vb2 + 10240u; d.vB2 = vb2 + 20480u; }
      }
      if constexpr (s == 9 && e == L - 4) {
        asm volatile("s_waitcnt vmcnt(7)" : "+v"(G.g0), "+v"(G.g1), "+v"(G.g2)::"memory");
        ok = all64(G.g0[1] == tag && G.g1[1] == tag && G.g2[1] == tag);
      }
      if constexpr (s == 9 && e == L - 3) ln_store();
    });
    if constexpr (s == 0) {
      wait_vm<8 - NA1>();
      static_for<0, 14>([&](auto K) { row_step<K.v>(R, lane); });
    }
    if constexpr (s == 9) {
      if (__builtin_expect(!ok, 0)) {
#pragma unroll 1
        for (unsigned spins = 0; spins < SPIN_CAP; ++spins) {
          __builtin_amdgcn_s_sleep(2);
          gran_load(G, rsWS);
          if (all64(G.g0[1] == tag && G.g1[1] == tag && G.g2[1] == tag)) break;
        }
        ln_store();
      }
    }
    wait_lgkm0();
    if constexpr (s < 9) wait_vm<4 + NX>();
    bar();
    if constexpr (s == 0) { STAMP(12); }
    SB;
    kstep<T, TR, 2 * s + 1>(acc, a1, b1, a0, b0, aoff, boff, d, [&](auto E) {
      constexpr int e = E.v;
      if constexpr (s == 0 && e == 6) {
        if (y) {
          const unsigned so = d.sB - d.advB;
          dma16(d.mW + 49152, d.vB0, d.rsB, so); dma16(d.mW + 49152 + 1024, d.vB1, d.rsB, so); dma16(d.mW + 49152 + 2048, d.vB2, d.rsB, so);
        }
      }
      if constexpr (s == 0 && e == L - 4) {
        v2 = (lane >> 3) * 1280 + (((lane & 7) ^ (((lane >> 4) & 3) << 1)) << 4);
        vpf = y ? v2 : 0x7ffffff0u;
        asm volatile("" : "+v"(v2), "+v"(vpf));
      }
      if constexpr (s == 0 && e == L - 6) row_publish(R, ws, tag, tid);
      if constexpr (s == 8 && e == 0) gran_issue(G, rsWS);
      if constexpr (s >= 7 && s < 9 && e >= L - 4) {
        d.rsA = rsB2; d.vA0 = v2; d.vA1 = v2;
        d.sA[e - (L - 4)] = tA[s - 7] + (e - (L - 4)) * 10240;
      }
      if constexpr (s == 9 && LASTA && e == 7) {
        if (wa < 5) {
#pragma unroll
          for (int i = 0; i < 4; ++i) dma16(a_slot(0) + d.mA + i * 1024, v2, rsB2, tA[2] + i * 10240);
        }
      }
    });
    if constexpr (s == 8) { STAMP(13); }
    if constexpr (s == 2) { STAMP(26); }
    if constexpr (s == 6) { STAMP(27); }
  });
  acc_fence<4 * T>(&acc[0][0]);
  STAMP(14);
}

DI void st16(u32x4 v, rsrc_t rs, unsigned voff, unsigned soff) {
  __builtin_amdgcn_raw_buffer_store_b128(v, rs, voff, soff, 16);
  asm volatile("s_nop 1" ::"v"(v) : "memory");
}
DI void publish_flag(char* ws, unsigned flag_off, unsigned tag, unsigned tid) {
  drain_vm();
  bar();
  if (tid == 0) __hip_atomic_store((gu32*)(ws + flag_off), tag, RLX_AGENT);
}
DI void st_frag(rsrc_t rs, unsigned lane, unsigned off, u32x2 lo, u32x2 hi) { st16(cat2(lo, hi), rs, lane * 16, off); }
DI void shared_send(unsigned w, unsigned lane, const u32x2 (&sh)[4]) {
  if (w >= 4) lds_st<u32x4>(L_SCR + w * 1024 + lane * 16, cat2(sh[0], sh[1]));
  else lds_st<u32x4>(L_SCR + w * 1024 + lane * 16, cat2(sh[2], sh[3]));
  asm volatile("" ::: "memory");
}
DI void shared_recv(rsrc_t rs, unsigned w, unsigned lane, unsigned off, unsigned str, const u32x2 (&sh)[4]) {
  lbar();
  u32x4 rcv = lds_ld<u32x4>(L_SCR + (w ^ 4) * 1024 + lane * 16);
  u32x2 r0 = {rcv[0], rcv[1]}, r1 = {rcv[2], rcv[3]};
  if (w >= 4) { st_frag(rs, lane, off + 2 * str, r0, sh[2]); st_frag(rs, lane, off + 3 * str, r1, sh[3]); }
  else        { st_frag(rs, lane, off, sh[0], r0); st_frag(rs, lane, off + str, sh[1], r1); }
}

__global__ __launch_bounds__(512, 2) void fused_block(Params p) {
  char* const ws = (char*)(((unsigned long long)p.wsraw + p.wsal) & ~p.wsal);
  unsigned tid = __builtin_amdgcn_workitem_id_x(), lane = tid & 63;
  const unsigned w = rfl(tid >> 6);
  const unsigned b = __builtin_amdgcn_workgroup_id_x(), xcd = b & 7, slot = b >> 3;
  const unsigned rb = 4 * (xcd & 1) + (slot & 3), jq = xcd >> 1, ji = slot >> 2;
  const unsigned spi = (xcd - 4) * 4 + (slot & 3);
  const unsigned long long jl0 = (jq & 1) ? 0x4123220908070605ull : 0x4021200403020100ull, jl1 = (jq & 1) ? 0x4327266011100f0eull : 0x422524600d0c0b0aull;
  const unsigned jb = (unsigned)(((jq & 2) ? jl1 : jl0) >> (8 * ji)) & 0xff, jtype = jb >> 5, jidx = jb & 31;
  unsigned long long pc0; unsigned codeb;
  asm volatile("s_getpc_b64 %0\n.Lfb_code_mark:\n\ts_mov_b32 %1, .Lfb_code_end0-.Lfb_code_mark\n\ts_max_u32 %1, %1, .Lfb_code_end1-.Lfb_code_mark"
               : "=s"(pc0), "=s"(codeb) :: "scc");
  STAMP_DECL;
  STAMP(0);

  Dma d;
  RowStat rs_;
  const bool mlp = jtype == J_MLP;
  const unsigned pitch = mlp ? 3456u : 3840u, wq = (w + (ROTW ? slot : 0)) & 3, wa = (w + (ROTW ? ji : 0)) & 7;
  d.wa = wa; d.wq = wq;
  const rsrc_t rsX = mk_rsrc(p.x, S * D * 2);
  d.vA0 = (lane >> 3) * 1280 + (((lane & 7) ^ (lane >> 4)) << 4); d.vA1 = d.vA0 ^ 64;
  d.rsA = rsX; d.mA = wa * 4096u;
#pragma unroll
  for (int i = 0; i < 4; ++i) d.sA[i] = (rb * 256 + 32 * wa + 8 * i) * 1280;
  unsigned epoch, tag;
  asm volatile("s_nop 4" :: "s"(rsX) : "memory");
  STAMP(6);
#pragma unroll
  for (int i = 0; i < 4; ++i) dma16(a_slot(0) + d.mA + i * 1024, (i & 1) ? d.vA1 : d.vA0, d.rsA, d.sA[i]);
  asm volatile("global_load_dword %0, %1, %2 sc1" : "=&v"(epoch) : "v"(0u), "s"(ws) : "memory");
  unsigned tch0;
  asm volatile("buffer_load_dword %0, %1, %2, 0 offen" : "=&v"(tch0) : "v"((((rb & 3) * 8 + w) * 16 + (lane & 15)) * 128),
               "s"(mk_rsrc((const void*)pc0, jtype == J_Q ? codeb : 0)) : "memory");
  SB;
  {
    unsigned vb[3];
    const unsigned lim = mlp ? 176u : 144u;
    {
      const unsigned r0 = (lane * 43) >> 9, p0 = lane - 12 * r0;
      const bool w1 = p0 >= 8, w2 = p0 >= 4;
      const unsigned r1 = r0 + (w1 ? 6 : 5), p1 = p0 + (w1 ? -8 : 4), r2 = r0 + (w2 ? 11 : 10), p2 = p0 + (w2 ? -4 : 8);
      vb[0] = r0 * pitch + umin(p0 * 16, lim);
      vb[1] = r1 * pitch + umin((p1 * 16) ^ ((r1 & 8) << 2), lim);
      vb[2] = r2 * pitch + umin((p2 * 16) ^ 32, lim);
    }
    d.vB0 = vb[0]; d.vB1 = vb[1]; d.vB2 = vb[2];
    const unsigned mh = wq >> 1;
    const void* bw = mlp ? (mh ? p.wu : p.wg) : p.wqkv;
    const unsigned cj = mlp ? 192u : 160u * jtype, cm = mlp ? 0u : (jtype == J_KV ? 1280u : 160u), c0 = jtype == J_KV ? 1280u : 0u;
    d.rsB = mk_rsrc(bw, D * pitch);
    d.sB = (16 * (wq & 1) + (w < 4 ? 32 : 0)) * pitch + c0 + cj * jidx + cm * mh; d.advB = 64 * pitch;
    d.mB = L_B + wq * 3072u + (w < 4 ? 0u : 12288u); d.mW = L_B + wq * 3072u + (w < 4 ? 49152u : 0u);
    const unsigned bdst = L_B + (w < 4 ? 12288u : 0u) + wq * 3072u;
    rs_.row = rfl(b * 8 + w);
    if (jtype != J_SPARE) {
      dma16(bdst, d.vB0, d.rsB, d.sB); dma16(bdst + 1024, d.vB1, d.rsB, d.sB); dma16(bdst + 2048, d.vB2, d.rsB, d.sB);
      const unsigned so = d.sB + d.advB;
      dma16(bdst + 24576, d.vB0, d.rsB, so); dma16(bdst + 24576 + 1024, d.vB1, d.rsB, so); dma16(bdst + 24576 + 2048, d.vB2, d.rsB, so);
#pragma unroll
      for (int i = 0; i < NA1; ++i) dma16(a_slot(1) + d.mA + i * 1024, (i & 1) ? d.vA1 : d.vA0, d.rsA, d.sA[i] + 128);
    }
    d.sB += d.advB;
  }
  STAMP(10);
  lds_st<unsigned>(L_GO + (tid & 1) * 4, 0u);

  const unsigned t2 = 28 * xcd + slot;
  const bool has2 = slot < 28 && t2 < 220;
  const unsigned G = t2 / 10, cb = t2 % 10;
  constexpr unsigned rot = 32;
  unsigned g = lane >> 4, r = lane & 15, q4 = r >> 2, p4 = lane & 3;
  const rsrc_t rsWd = mk_rsrc(p.wd, has2 ? F * D * 2 : 0);
  const rsrc_t rsWS = mk_rsrc(ws, WS_END);
  unsigned v2 = (lane >> 3) * 1280 + (((lane & 7) ^ (((lane >> 4) & 3) << 1)) << 4);
  auto relane = [&] {
    asm volatile("" : "+v"(tid));
    lane = tid & 63; g = lane >> 4; r = lane & 15; q4 = r >> 2; p4 = lane & 3;
    v2 = (lane >> 3) * 1280 + (((lane & 7) ^ (((lane >> 4) & 3) << 1)) << 4);
  };
  const unsigned m = w & 3, n = w >> 2;
  const unsigned brow = L_B + (8 * g + q4) * 192;
  const unsigned bx = (g & 1) << 5;

  u32x4 pre2[36];
  W2 W;
  W.wd = rsWd; W.wo = mk_rsrc(p.wo, has2 ? D * D * 2 : 0); W.rot = rot; W.col = cb * 128u;
  if (jtype == J_MLP) {
    f32x4 acc[4][6];
    unsigned boff[6];
#pragma unroll
    for (int t = 0; t < 3; ++t) {
      const unsigned gt = n ? (t == 2 ? 3 : 4 + t) : t;
      boff[t] = brow + ((32 * gt + 8 * p4) ^ bx); boff[t + 3] = boff[t] + 6144;
    }
    const unsigned ccol = tid < 96 ? 1920 + 96 * jidx + tid : 3648 + 96 * jidx + (tid < 192 ? tid - 96 : 0);
    main_loop<6, true, true, true>([](int) {}, rs_, ws, acc, boff, d, w, lane, tid, epoch, TCH0, tag, rb, ccol, rsWd, rsWS, cb, rot, pre2 STAMP_PASS);
    relane();
    float mm[4];
    f32x4 cg[3], cu[3];
#pragma unroll
    for (int i = 0; i < 4; ++i) mm[i] = lds_ld<float>(L_MISC + (64 * m + 16 * i + r) * 4);
#pragma unroll
    for (int t = 0; t < 3; ++t) {
      const unsigned gt = n ? (t == 2 ? 3 : 4 + t) : t;
      cg[t] = lds_ld<f32x4>(L_MISC + 2048 + (16 * gt + 4 * g) * 4); cu[t] = lds_ld<f32x4>(L_MISC + 2048 + (96 + 16 * gt + 4 * g) * 4);
    }
    auto hval = [&](int i, int t) {
      f32x4 gg = acc[i][t] + mm[i] * cg[t];
      const f32x4 uu = acc[i][t + 3] + mm[i] * cu[t];
#pragma unroll
      for (int j = 0; j < 4; ++j) gg[j] = relu2(gg[j]);
      return pk4(gg * uu);
    };
    const unsigned ho = WS_HA + rb * RBSZ + jidx * 49152 + m * 4 * 3072;
#pragma unroll
    for (int i = 0; i < 4; ++i) { st_frag(rsWS, lane, ho + i * 3072 + n * 2048, hval(i, 0), hval(i, 1)); SB; }
    const u32x2 sh[4] = {hval(0, 2), hval(1, 2), hval(2, 2), hval(3, 2)};
    shared_send(w, lane, sh);
    shared_recv(rsWS, w, lane, ho + 1024, 3072, sh);
    STAMP(15);
    publish_flag(ws, WS_FLAGH + (rb * 32 + jidx) * FS, tag, tid);
    STAMP(3);
  } else if (jtype == J_KV) {
    f32x4 acc[4][5];
    unsigned boff[5];
    boff[0] = brow + ((80 * n + 8 * p4) ^ bx); boff[1] = boff[0] + 6144;
    boff[2] = brow + ((80 * n + 40 + 8 * p4) ^ bx); boff[3] = boff[2] + 6144;
    boff[4] = brow + ((80 * n + 32 + 40 * (p4 >> 1)) ^ bx) + (p4 & 1) * 6144;
    const unsigned kb = 126976u + m * 6720 + n * 3360, dump = L_MISC - 2048 + lane * 16;
    const unsigned ea = (q4 == 1 || r == 0) ? kb + (r == 0 ? 20 : 12 + r) * 80 : 0u, eb = (q4 == 3 || r == 0) ? kb + 1680 + (r == 0 ? 20 : 4 + r) * 80 : 0u;
    unsigned ka[6] = {kb + r * 80 + g * 16, ea ? ea + g * 16 : dump, eb ? eb + g * 16 : dump, g == 0 ? kb + r * 80 + 64 : dump,
                      g == 2 ? kb + 1680 + r * 80 + 64 : dump, (g == 0 && ea) ? ea + 64 : ((g == 2 && eb) ? eb + 64 : dump)};
    auto pin = [&](int P) { if (P == 0) asm volatile("" : "+v"(ka[0]), "+v"(ka[1]), "+v"(ka[2]), "+v"(ka[3]), "+v"(ka[4]), "+v"(ka[5])); };
    const unsigned ccol = tid < 80 ? 640 + 80 * jidx + tid : 1280 + 80 * jidx + (tid < 160 ? tid - 80 : 0);
    main_loop<5, false, false, false>(pin, rs_, ws, acc, boff, d, w, lane, tid, epoch, TCH0, tag, rb, ccol, rsWd, rsWS, cb, rot, pre2 STAMP_PASS);
    relane();
    float cs[5];
    cs[0] = lds_ld<float>(L_MISC + 2048 + (40 * n + r) * 4);
    cs[1] = lds_ld<float>(L_MISC + 2048 + (80 + 40 * n + r) * 4);
    cs[2] = lds_ld<float>(L_MISC + 2048 + (40 * n + 20 + r) * 4);
    cs[3] = lds_ld<float>(L_MISC + 2048 + (80 + 40 * n + 20 + r) * 4);
    cs[4] = lds_ld<float>(L_MISC + 2048 + ((q4 & 1) * 80 + 40 * n + 16 + 20 * (q4 >> 1) + (r & 3)) * 4);
    u32x2 pk[4][3], vv[4][3];
    const unsigned vmask = 0u - (q4 & 1);
    f32x4 mm4[4], rr4[4];
#pragma unroll
    for (int i = 0; i < 4; ++i) {
      mm4[i] = lds_ld<f32x4>(L_MISC + (64 * m + 16 * i + 4 * g) * 4); rr4[i] = lds_ld<f32x4>(L_MISC + 1024 + (64 * m + 16 * i + 4 * g) * 4);
    }
    SB;
#pragma unroll
    for (int i = 0; i < 4; ++i) {
      const f32x4 mm = mm4[i], rr = rr4[i];
      const f32x4 rl = rr * 1.4426950408889634f, rh = rr * 0.5f;
      f32x4 val[5];
#pragma unroll
      for (int t = 0; t < 5; ++t) val[t] = acc[i][t] + mm * cs[t];
#pragma unroll
      for (int t = 0; t < 3; ++t) pk[i][t] = pk4(phi4(val[2 * t], rl, rh));
      vv[i][0] = pk4(val[1] * rr); vv[i][1] = pk4(val[3] * rr);
      const u32x2 v4 = pk4(val[4] * rr);
      vv[i][2] = u32x2{bsel(vmask, v4[0], 0x3f803f80u), bsel(vmask, v4[1], 0x3f803f80u)};
    }
    STAMP(25);
    f32x4 P[7] = {};
#pragma unroll
    for (int i = 0; i < 4; i += 2) {
      const bf16x8 k0 = frag(pk[i][0], pk[i + 1][0]), k2 = frag(pk[i][1], pk[i + 1][1]), k4 = frag(pk[i][2], pk[i + 1][2]);
      const bf16x8 v1 = frag(vv[i][0], vv[i + 1][0]), v3 = frag(vv[i][1], vv[i + 1][1]), v4 = frag(vv[i][2], vv[i + 1][2]);
      P[0] = mfma(k0, v1, P[0]); P[1] = mfma(k0, v4, P[1]); P[2] = mfma(k4, v1, P[2]);
      P[3] = mfma(k2, v3, P[3]); P[4] = mfma(k2, v4, P[4]); P[5] = mfma(k4, v3, P[5]); P[6] = mfma(k4, v4, P[6]);
    }
    lds_st<f32x4>(ka[0], P[0]); lds_st<f32x4>(ka[0] + 1680, P[3]); lds_st<f32x4>(ka[1], P[1]); lds_st<f32x4>(ka[2], P[4]);
    lds_st<f32x4>(ka[3], P[2]); lds_st<f32x4>(ka[4], P[5]); lds_st<f32x4>(ka[5], P[6]);
    lbar();
    if (tid < 210) {
      const unsigned a = 126976u + tid * 32;
      const f32x4 t0 = (lds_ld<f32x4>(a) + lds_ld<f32x4>(a + 6720)) + (lds_ld<f32x4>(a + 13440) + lds_ld<f32x4>(a + 20160));
      const f32x4 t1 = (lds_ld<f32x4>(a + 16) + lds_ld<f32x4>(a + 6736)) + (lds_ld<f32x4>(a + 13456) + lds_ld<f32x4>(a + 20176));
      st16(cat2(pk4(t0), pk4(t1)), rsWS, tid * 16, WS_KVP + (rb * 32 + 4 * jidx) * 840);
    }
    wait_lgkm0();
    STAMP(15);
    publish_flag(ws, WS_FLAGKV + ((jidx >> 1) * 32 + (jidx & 1) * 8 + rb) * FS, tag, tid);
    STAMP(3);
  } else if (jtype == J_Q) {
    f32x4 acc[4][5];
    unsigned boff[5];
#pragma unroll
    for (int t = 0; t < 5; ++t) boff[t] = brow + n * 6144 + ((32 * t + 8 * p4) ^ bx);
    const unsigned ccol = 160 * jidx + (tid < 160 ? tid : 0);
    unsigned tch[4];
    auto warm = [&](int P) {
      if (P == 1) {
        asm volatile("buffer_load_dword %0, %1, %2, 0 offen" : "=&v"(tch[3]) : "v"((((rb & 3) * 8 + w) * 16 + (lane & 15)) * 128),
                     "s"(mk_rsrc((const void*)pc0, codeb)) : "memory");
        return;
      }
      const unsigned gq = (rb * 4 + jidx) * 8 + w;
      asm volatile("buffer_load_dword %0, %3, %5, 0 offen\n\tbuffer_load_dword %1, %3, %5, %7 offen\n\tbuffer_load_dword %2, %4, %6, 0 offen"
                   : "=&v"(tch[0]), "=&v"(tch[1]), "=&v"(tch[2])
                   : "v"((gq * 34 + (lane < 34 ? lane : 0)) * 128), "v"((gq * 25 + (lane < 25 ? lane : 0)) * 128), "s"(mk_rsrc(p.wd, F * D * 2)),
                     "s"(mk_rsrc(p.wo, D * D * 2)), "s"(8576u * 128u) : "memory");
    };
    main_loop<5, true, false, false>(warm, rs_, ws, acc, boff, d, w, lane, tid, epoch, TCH0, tag, rb, ccol, rsWd, rsWS, cb, rot, pre2 STAMP_PASS);
    relane();
    asm volatile("" :: "v"(tch[0]), "v"(tch[1]), "v"(tch[2]), "v"(tch[3]));
    u32x2 pq[4][5];
    {
      float mm[4], rr[4];
      f32x4 c[5];
#pragma unroll
      for (int i = 0; i < 4; ++i) { mm[i] = lds_ld<float>(L_MISC + (64 * m + 16 * i + r) * 4); rr[i] = lds_ld<float>(L_MISC + 1024 + (64 * m + 16 * i + r) * 4); }
#pragma unroll
      for (int t = 0; t < 5; ++t) c[t] = lds_ld<f32x4>(L_MISC + 2048 + (80 * n + 16 * t + 4 * g) * 4);
      SB;
#pragma unroll
      for (int i = 0; i < 4; ++i)
#pragma unroll
        for (int t = 0; t < 5; ++t) pq[i][t] = pk4(phi4(acc[i][t] + mm[i] * c[t], splat(rr[i] * 1.4426950408889634f), splat(rr[i] * 0.5f)));
#pragma unroll
      for (int i = 0; i < 4; ++i) asm volatile("" : "+v"(pq[i][0]), "+v"(pq[i][1]), "+v"(pq[i][2]), "+v"(pq[i][3]), "+v"(pq[i][4]));
    }
    constexpr unsigned KP = 208, KQ = 84 * KP;
    unsigned wa[2];
#pragma unroll
    for (int k = 0; k < 2; ++k) {
      const unsigned qd = 2 * (tid < 420 ? tid : 0) + k, hd = qd / 105, e = (qd % 105) / 5, dq = qd % 5, hl = hd & 3;
      wa[k] = (hd >> 2) * KQ + (e < 20 ? hl * 20 + e : 80 + hl) * KP + hl * 40 + dq * 8;
    }
    unsigned fk = n * KQ + r * KP + g * 8, fz = n * KQ + (80 + (r & 3)) * KP + g * 8;
    unsigned gm[3] = {g < 1 ? ~0u : 0u, g < 2 ? ~0u : 0u, g < 3 ? ~0u : 0u};
    u32x4 B[4][3];
    f32x4 eps4 = splat(1e-6f);
#pragma unroll
    for (int i = 0; i < 4; ++i) {
      B[i][0] = cat2(pq[i][0], pq[i][1]); B[i][1] = cat2(pq[i][2], pq[i][3]); B[i][2] = cat2(pq[i][4], u32x2{0u, 0u});
      asm volatile("" : "+v"(B[i][0]), "+v"(B[i][1]), "+v"(B[i][2]));
    }
    asm volatile("" : "+v"(wa[0]), "+v"(wa[1]), "+v"(fk), "+v"(fz), "+v"(gm[0]), "+v"(gm[1]), "+v"(gm[2]), "+v"(eps4));
    STAMP(25);
    wait_vm<0>();
    bar();
#pragma unroll
    for (int k = 0; k < 5; ++k) lds_st<u32x4>(k * 8192 + tid * 16, u32x4{0u, 0u, 0u, 0u});
    if (w == 0) poll_flags((gu32*)(ws + WS_FLAGKV + (jidx * 32 + (lane & 15)) * FS), true, tag);
    lbar();
    STAMP(4);
    if (w < 7) {
      u32x4 v[8];
#pragma unroll
      for (int j = 0; j < 8; ++j)
        v[j] = __builtin_amdgcn_raw_buffer_load_b128(rsWS, (tid < 420 ? tid : 0) * 16, WS_KVP + (j * 32 + 8 * jidx) * 840, 16);
      SB;
      f32x4 t0 = {0.f, 0.f, 0.f, 0.f}, t1 = t0;
#pragma unroll
      for (int j = 0; j < 8; ++j) {
        t0 += f32x4{bflo(v[j][0]), bfhi(v[j][0]), bflo(v[j][1]), bfhi(v[j][1])};
        t1 += f32x4{bflo(v[j][2]), bfhi(v[j][2]), bflo(v[j][3]), bfhi(v[j][3])};
      }
      lds_st<u32x2>(wa[0], pk4(t0)); lds_st<u32x2>(wa[1], pk4(t1));
    }
    lbar();
    STAMP(16);
    u32x4 A[12];
#pragma unroll
    for (int P = 0; P < 3; ++P) A[P] = cat2(lds_ld<u32x2>(fz + P * 64), lds_ld<u32x2>(fz + P * 64 + 32));
#pragma unroll
    for (int u = 0; u < 5; ++u) {
      const unsigned a0 = fk + u * 16 * KP + (u < 3 ? 0 : 64);
      A[3 + u] = cat2(lds_ld<u32x2>(a0), lds_ld<u32x2>(a0 + 32));
    }
#pragma unroll
    for (int u = 1; u < 5; ++u) {
      const unsigned a0 = fk + u * 16 * KP + (u < 3 ? 64 : 128);
      A[7 + u] = cat2(lds_ld<u32x2>(a0), lds_ld<u32x2>(a0 + 32));
    }
    SB;
    u32x2 sh[4];
    f32x4 den[4], num[2][5];
    static_for<0, 12>([&](auto E) {
      constexpr int P = E.v >> 2, i = E.v & 3;
      if constexpr (P == 0) mfma_c(den[i], A[0], B[i][0], eps4); else mfma_ip(den[i], A[P], B[i][P]);
    });
    u32x2 o[5];
    const unsigned oo = WS_HA + rb * RBSZ + 18 * 49152 + jidx * 81920 + m * 4 * 5120;
    auto fin = [&](auto I, auto K) {
      constexpr int i = I.v, k = K.v, u = k < 8 ? k >> 1 : 4;
      if constexpr (k == 8 || !(k & 1)) {
        asm volatile("" : "+v"(num[i & 1][u]));
        const float rr = u == 0 ? den[i][0] : (u == 4 ? den[i][3] : bself(gm[(u + 2) % 3], den[i][(u + 3) & 3], den[i][u & 3]));
#pragma unroll
        for (int j = 0; j < 4; ++j) num[i & 1][u][j] = smul(num[i & 1][u][j], rr);
      }
      if constexpr (k == 8 || (k & 1)) o[u] = pk4(num[i & 1][u]);
      if constexpr (k == 3) st_frag(rsWS, lane, oo + i * 5120 + n * 2048, o[0], o[1]);
      if constexpr (k == 7) st_frag(rsWS, lane, oo + i * 5120 + n * 2048 + 1024, o[2], o[3]);
      if constexpr (k == 8) { sh[i] = o[4]; asm volatile("" : "+v"(sh[i])); }
    };
    static_for<0, 4>([&](auto I) {
      constexpr int i = I.v;
      static_for<0, 9>([&](auto K) {
        constexpr int k = K.v, u = k < 5 ? k : k - 4;
        if constexpr (k < 5) mfma_z(num[i & 1][u], A[3 + u], B[i][u < 3 ? 0 : 1]); else mfma_ip(num[i & 1][u], A[7 + u], B[i][u < 3 ? 1 : 2]);
        SB;
        if constexpr (i == 0) {
          if constexpr (k >= 5) {
            asm volatile("" : "+v"(den[k - 5]));
#pragma unroll
            for (int j = 0; j < 4; ++j) den[k - 5][j] = __builtin_amdgcn_rcpf(den[k - 5][j]);
            asm volatile("" : "+v"(den[k - 5]));
          }
        } else {
          fin(IC<i - 1>{}, K);
        }
        SB;
      });
    });
    static_for<0, 9>([&](auto K) { fin(IC<3>{}, K); SB; });
    shared_send(w, lane, sh);
    shared_recv(rsWS, w, lane, oo + 4096, 5120, sh);
    STAMP(15);
    publish_flag(ws, WS_FLAGO + (rb * 32 + jidx) * FS, tag, tid);
    STAMP(3);
    if (b == 224) {
      poll_flags((gu32*)(ws + WS_FLAGH + tid * FS), (tid & 31) < (tid < 256 ? 18u : 4u), tag);
      bar();
      if (tid == 0) __hip_atomic_store((gu32*)(ws + WS_EPOCH), tag, RLX_AGENT);
    }
  } else {
    const unsigned rk = spi * 4 + (w & 3), p0 = w < 4 ? rk : 128 + rk, p1 = p0 + 64;
    auto unit = [](unsigned q) { return q < 60 ? q + 30 : (q < 90 ? q - 60 : q); };
    const unsigned u0 = unit(p0), u1 = unit(p1);
    unsigned k10 = 0x00003f80u, k01 = 0x3f800000u;
    asm volatile("" : "+v"(k10), "+v"(k01));
    auto cs_src = [&](unsigned u, rsrc_t& rs, unsigned& cv, unsigned& st) {
      const unsigned cline = u / 3, third = u % 3;
      const void* cw = cline < 30 ? p.wqkv : (cline < 57 ? p.wg : p.wu);
      const unsigned cpitch = cline < 30 ? 3840u : 3456u;
      const unsigned clocal = cline < 30 ? cline : (cline < 57 ? cline - 30 : cline - 57);
      rs = mk_rsrc(cw, u < 252 ? D * cpitch : 0);
      cv = (third * 216 + (lane >> 3)) * cpitch + clocal * 128 + (lane & 7) * 16;
      st = 8 * cpitch;
    };
    auto cs_publish = [&](unsigned u, float (&cs)[8]) {
#pragma unroll
      for (int k = 0; k < 4; ++k) { const bool hi = lane & 32; const float o = shfl_x(hi ? cs[k] : cs[k + 4], lane, 32); cs[k] = (hi ? cs[k + 4] : cs[k]) + o; }
#pragma unroll
      for (int k = 0; k < 2; ++k) { const bool hi = lane & 16; const float o = shfl_x(hi ? cs[k] : cs[k + 2], lane, 16); cs[k] = (hi ? cs[k + 2] : cs[k]) + o; }
      { const bool hi = lane & 8; const float o = shfl_x(hi ? cs[0] : cs[1], lane, 8); cs[0] = (hi ? cs[1] : cs[0]) + o; }
      if (u < 252)
        __hip_atomic_store((gu64*)(ws + WS_CSUM) + (u % 3) * NCOL + (u / 3) * 64 + (lane & 7) * 8 + (lane >> 3),
                           ((unsigned long long)tag << 32) | __builtin_bit_cast(unsigned, cs[0]), RLX_AGENT);
    };
    row_load(rs_, rsX, lane);
    constexpr int CW = CWIN;
    u32x4 c[CW];
    rsrc_t rs0, rs1; unsigned cv0, cv1, st0, st1;
    cs_src(u0, rs0, cv0, st0); cs_src(u1, rs1, cv1, st1);
    auto cld = [&](int j) { return j < 27 ? __builtin_amdgcn_raw_buffer_load_b128(rs0, cv0 + j * st0, 0, 0) : __builtin_amdgcn_raw_buffer_load_b128(rs1, cv1 + (j - 27) * st1, 0, 0); };
    if (SDELAY) __builtin_amdgcn_s_sleep(SDELAY);
#pragma unroll
    for (int j = 0; j < CW; ++j) c[j] = cld(j);
    SB;
    asm volatile("s_waitcnt vmcnt(%2) lgkmcnt(0)\n\tv_readfirstlane_b32 %0, %1\n\ts_nop 3" : "=s"(tag) : "v"(epoch), "i"(CW), "v"(TCH0) : "memory");
    tag = rfl(tag) + 1;
    static_for<0, 14>([&](auto K) { row_step<K.v>(rs_, lane); });
    lbar();
    row_publish(rs_, ws, tag, tid);
    float cs[8] = {};
    static_for<0, 54>([&](auto J) {
      constexpr int j = J.v;
#pragma unroll
      for (int k = 0; k < 4; ++k) { cs[2 * k] = dot2(c[j % CW][k], k10, cs[2 * k]); cs[2 * k + 1] = dot2(c[j % CW][k], k01, cs[2 * k + 1]); }
      if constexpr (j + CW < 54) c[j % CW] = cld(j + CW);
      SB;
      if constexpr (j == 26) {
        cs_publish(u0, cs);
        STAMP(11);
#pragma unroll
        for (int k = 0; k < 8; ++k) cs[k] = 0.f;
      }
    });
    cs_publish(u1, cs);
    STAMP(12);
    bar();
#pragma unroll 1
    for (unsigned i = w; i < 144; i += 8) dma16(i * 1024, v2, rsWd, urow(i >> 2, rot) + (i & 3) * 10240u + cb * 128u);
    drain_vm();
    bar();
  }
  if (!has2) { STAMP_END; return; }
  relane();

  const unsigned rbA = (G * 6) >> 4, rbB = (G * 6 + 5 > 127 ? 127 : G * 6 + 5) >> 4;
  if (w >= 4) {
    __builtin_amdgcn_s_setprio(HPRIO);
    const unsigned lw = w - 4, ld0 = lw * 1024;
    gu32* fo = (gu32*)(ws + WS_FLAGO + ((lane < 32 ? rbA : rbB) * 32 + (lane & 3)) * FS);
    gu32* fh = (gu32*)(ws + WS_FLAGH + ((lane < 32 ? rbA : rbB) * 32 + ((lane & 31) < 18 ? lane & 31 : 17)) * FS);
    __builtin_amdgcn_s_sleep(HSLEEP);
    if (jtype == J_KV) {
#pragma unroll
      for (int u = 31; u < 36; ++u) dma16(u * 4096 + ld0, v2, rsWd, w2_off(W, u, lw));
    }
    if (jtype != J_MLP) {
#pragma unroll
      for (int u = 0; u < NPRE; ++u) { pre2[u] = w2_load(W, 36 + u, lw, v2); SB; }
    }
#pragma unroll
    for (int u = 36 + NPRE; u < PSPLIT; ++u) { pre2[u - 36] = w2_load(W, u, lw, v2); SB; }
    if (w == 7) {
      __builtin_amdgcn_s_setprio(3);
      const unsigned v = poll_flags(fh, (lane & 31) < 18 && (lane & 31) >= 10, tag);
      lds_set(L_GO);
      if (!all64((lane & 31) >= 18 || v == tag)) poll_flags(fh, (lane & 31) < 18, tag);
      lds_set(L_GO + 4);
      __builtin_amdgcn_s_setprio(HPRIO);
    }
#pragma unroll
    for (int u = PSPLIT; u < HSPLIT; ++u) { pre2[u - 36] = w2_load(W, u, lw, v2); SB; }
    STAMP(8);
    unsigned la[3], wb[3], xo[3], st[3];
    u32x4 xr[3];
    const unsigned ch = lane & 7;
#pragma unroll
    for (int i = 0; i < 3; ++i) {
      const unsigned row = lw * 24 + i * 8 + (lane >> 3), rl = row >> 4, rr = row & 15;
      la[i] = (rl >= 3 ? 24576u + (rl - 3) * 4096u : rl * 4096u) + (ch >> 1) * 1024 + (ch & 1) * 512 + ((rr + 2 * ch) & 15) * 16;
      xo[i] = (G * 96 + row) * 1280 + cb * 128 + ch * 16;
      wb[i] = ld0 + lane * 16 + i * 49152;
      asm volatile("" : "+v"(la[i]), "+v"(wb[i]), "+v"(xo[i]));
    }
    float ri[3], rl[3];
    unsigned of = 0;
    f32x4 xf[3][2], dd[3][2], d2[3][2];
#pragma unroll
    for (int k = 2; k <= 6; ++k) {
      if (k == O_BAR && w == 7 && !all64((lane & 31) >= 4 || of == tag)) poll_flags(fo, (lane & 31) < 4, tag);
      if (k == 6) {
        wait_vm<0>();
#pragma unroll
        for (int h = 0; h < 3; ++h) {
          ri[h] = 2.0f * bfhi(st[h]) * bfhi(st[h]);
          rl[h] = -1.4426950408889634f * __builtin_amdgcn_rcpf(ri[h]);
          xf[h][0] = f32x4{bflo(xr[h][0]), bfhi(xr[h][0]), bflo(xr[h][1]), bfhi(xr[h][1])};
          xf[h][1] = f32x4{bflo(xr[h][2]), bfhi(xr[h][2]), bflo(xr[h][3]), bfhi(xr[h][3])};
          asm volatile("" : "+v"(ri[h]), "+v"(rl[h]), "+v"(xf[h][0]), "+v"(xf[h][1]));
        }
      }
      lbar();
      if (k == O_BAR - 1) { of = __hip_atomic_load(fo, RLX_AGENT); SB; }
      if (k < 6) {
        const int kk = k - 1;
#pragma unroll
        for (int u = 9 * (kk - 1) + ((kk - 1) & 1); u < 9 * kk + (kk & 1); ++u) {
          lds_st<u32x4>(wb[u / 12] + (u % 12) * 4096, pre2[u]);
          if (WSLEEP == 1) __builtin_amdgcn_s_sleep(1);
          if (WSLEEP == 2) asm volatile("s_nop 7");
          SB;
        }
      }
      if (k == 2 || k == 3) {
        constexpr int mid = (HSPLIT + 74) / 2 < 72 ? (HSPLIT + 74) / 2 : 72;
#pragma unroll
        for (int u = (k == 2 ? HSPLIT : mid); u < (k == 2 ? mid : 72); ++u) { pre2[u - 36] = w2_load(W, u, lw, v2); SB; }
        if (k == 3) { dma16(36 * 4096 + ld0, v2, W.wo, w2_off(W, 72, lw)); dma16(37 * 4096 + ld0, v2, W.wo, w2_off(W, 73, lw)); }
      }
      if (k == XK) {
#pragma unroll
        for (int h = 0; h < 3; ++h) {
          const unsigned row = G * 96 + lw * 24 + h * 8 + (lane >> 3);
          st[h] = __builtin_amdgcn_raw_buffer_load_b32(rsWS, WS_STAT + (row < S ? row : 0) * 8, 0, 16);
          xr[h] = __builtin_amdgcn_raw_buffer_load_b128(rsX, xo[h], 0, 0);
        }
      }
    }
    lbar();
#pragma unroll
    for (int i = 0; i < 3; ++i) {
      dd[i][0] = lds_ld<f32x4>(la[i]); d2[i][0] = lds_ld<f32x4>(la[i] + 12288);
      dd[i][1] = lds_ld<f32x4>(la[i] + 256); d2[i][1] = lds_ld<f32x4>(la[i] + 12288 + 256);
      la[i] += 49152;
      asm volatile("" : "+v"(la[i]));
    }
    __builtin_amdgcn_s_setprio(GPRIO);
#pragma unroll
    for (int i = 0; i < 3; ++i) {
      float v[8], e[8];
#pragma unroll
      for (int j = 0; j < 8; ++j) v[j] = sadd(dd[i][j >> 2][j & 3], d2[i][j >> 2][j & 3]);
      SB;
#pragma unroll
      for (int j = 0; j < 8; ++j) e[j] = smul(v[j], rl[i]);
      SB;
#pragma unroll
      for (int j = 0; j < 8; ++j) e[j] = __builtin_amdgcn_exp2f(e[j]);
      SB;
#pragma unroll
      for (int j = 0; j < 8; ++j) e[j] = sfma(e[j], ri[i], ri[i]);
      SB;
#pragma unroll
      for (int j = 0; j < 8; ++j) e[j] = __builtin_amdgcn_rcpf(e[j]);
      SB;
#pragma unroll
      for (int j = 0; j < 8; ++j) dd[i][j >> 2][j & 3] = smul(v[j], e[j]);
      asm volatile("" : "+v"(dd[i][0]), "+v"(dd[i][1]));
      SB;
    }
    const rsrc_t rsO = mk_rsrc(p.out, S * D * 2);
    STAMP(9);
    if (PUTB) bar();
    bar();
    f32x4 ss[3][2];
#pragma unroll
    for (int i = 0; i < 3; ++i) {
      ss[i][0] = lds_ld<f32x4>(la[i]) + lds_ld<f32x4>(la[i] + 12288);
      ss[i][1] = lds_ld<f32x4>(la[i] + 256) + lds_ld<f32x4>(la[i] + 12288 + 256);
    }
#pragma unroll
    for (int i = 0; i < 3; ++i)
      __builtin_amdgcn_raw_buffer_store_b128(cat2(pk4(xf[i][0] + ss[i][0] * dd[i][0]), pk4(xf[i][1] + ss[i][1] * dd[i][1])), rsO, xo[i], 0, 0);
    STAMP(5);
    STAMP_END;
    asm volatile(".Lfb_code_end0:");
    return;
  }
  {
    const unsigned kh = w & 1, rt = G * 6 + 3 * (w >> 1);
    const rsrc_t rsH = mk_rsrc(ws + WS_HA, 8 * RBSZ);
    unsigned tb[3][4];
#pragma unroll
    for (int t = 0; t < 4; ++t) {
      tb[0][t] = (4 * g + q4) * 128 + ((t ^ ((2 * g + (q4 >> 1)) & 3)) << 5) + 8 * p4 + kh * 4096;
      tb[1][t] = tb[0][t] + 49152; tb[2][t] = tb[0][t] + 98304;
      asm volatile("" : "+v"(tb[0][t]), "+v"(tb[1][t]), "+v"(tb[2][t]));
    }
    auto rd1 = [&](int i, int t, int hf) {
      const int sl = i == 36 ? 36 : (2 * i) % 36, bi = sl / 12 > 2 ? 2 : sl / 12;
      return __builtin_bit_cast(u32x2, lds_tr(tb[bi][t] + (sl - 12 * bi) * 4096 + hf * 2048));
    };
    constexpr int NW = P2NW, W0 = P2W0;
    constexpr int OI = O_BAR == 4 ? 17 : (O_BAR == 5 ? 22 : 26);
    constexpr int OB0 = OI == 26 ? OBN : 0;
    f32x4 acc[2][3][4];
    u32x4 a[NW][3];
    u32x2 bw[2][4][2];
    unsigned va[2][3];
#pragma unroll
    for (int h = 0; h < 3; ++h) {
      va[0][h] = lane * 16 + ((rt + h) >> 4) * RBSZ + ((rt + h) & 15) * 3072;
      va[1][h] = va[0][h] + ((rt + h) & 15) * 2048;
      asm volatile("" : "+v"(va[0][h]), "+v"(va[1][h]));
    }
    unsigned xa[4];
#pragma unroll
    for (int t = 0; t < 4; ++t) {
      xa[t] = w * 12288u + t * 1024 + g * 256 + ((r + 4 * t + (g & 2)) & 15) * 16;
      asm volatile("" : "+v"(xa[t]));
    }
    auto put = [&](auto Z, auto E) {
      constexpr int z = Z.v, t = E.v / 3, h = 2 - E.v % 3;
      asm volatile("" : "+v"(acc[z][h][t]));
      lds_st<f32x4>(xa[t] + z * 49152 + h * 4096, acc[z][h][t]);
    };
    auto lda = [&](auto I, auto H) {
      constexpr int i = I.v, h = H.v;
      constexpr unsigned s0 = i < 27 ? (2 * i + rot) % 54 : 2 * i, s1 = i < 27 ? (2 * i + 1 + rot) % 54 : 2 * i + 1;
      a[i % NW][h] = __builtin_amdgcn_raw_buffer_load_b128(rsH, va[i >= 27][h], step_off(s0) + kh * (step_off(s1) - step_off(s0)), 16);
      SB;
    };
    lds_wait(L_GO);
    unsigned go2 = 0;
    STAMP(8);
    static_for<0, W0>([&](auto I) { lda(I, IC<0>{}); lda(I, IC<1>{}); lda(I, IC<2>{}); });
#pragma unroll
    for (int t = 0; t < 4; ++t) { bw[0][t][0] = rd1(0, t, 0); bw[0][t][1] = rd1(0, t, 1); }
    static_for<0, 37>([&](auto I) {
      constexpr int i = I.v;
      constexpr int lim = i + NW - 1;
      constexpr int nxt = i <= OI ? (W0 + 2 * i < lim ? W0 + 2 * i : lim) : (27 + OB0 + 2 * (i - OI - 1) < lim ? 27 + OB0 + 2 * (i - OI - 1) : lim);
      constexpr int end = i <= OI ? 27 : 37;
      constexpr int l0 = nxt < end ? nxt : -1, l1 = (nxt + 1 <= lim && nxt + 1 < end) ? nxt + 1 : -1;
      SB;
      static_for<0, 12>([&](auto E) {
        constexpr int e = E.v, t = e / 3, h = e % 3;
        const u32x4 bf = cat2(bw[i & 1][t][0], bw[i & 1][t][1]);
        if constexpr (i == 0 || i == 27) mfma_z(acc[i >= 27][2 - h][t], bf, a[i % NW][2 - h]); else mfma_ip(acc[i >= 27][2 - h][t], bf, a[i % NW][2 - h]);
        SB;
        if constexpr (i == 4 && e == 2) go2 = lds_get(L_GO + 4);
        if constexpr (!PUTB && (i == 27 || i == 28) && (e & 1)) put(IC<0>{}, IC<(i - 27) * 6 + e / 2>{});
        if constexpr (i == 36 && e >= 8) put(IC<1>{}, IC<e - 8>{});
        if constexpr (i < 36 && h < 2) bw[(i + 1) & 1][t][h] = rd1(i + 1, t, h);
        if constexpr (l1 < 0) { if constexpr (h == 2 && t < 3 && l0 >= 0) lda(IC<l0>{}, IC<t>{}); }
        else if constexpr (e & 1) lda(IC<(e < 6 ? l0 : l1)>{}, IC<(e >> 1) % 3>{});
        SB;
      });
      if constexpr (i == (PUTB ? 33 : 29)) { asm volatile("s_waitcnt lgkmcnt(8)" ::: "memory"); bar(); }
      if constexpr (i == 4) { if (__builtin_expect(!rfl(go2), 0)) lds_wait(L_GO + 4); asm volatile("" ::: "memory"); STAMP(17); }
      if constexpr (i > 4 && i < 27 && (i % 9 == 4 || i % 9 == 8)) {
        bar();
        if constexpr (i == 26 && OI == 26) static_for<27, 27 + OB0>([&](auto J) { lda(J, IC<0>{}); lda(J, IC<1>{}); lda(J, IC<2>{}); });
        if constexpr (PUTB && i == 26) { static_for<0, 12>([&](auto E) { put(IC<0>{}, E); SB; }); lbar(); }
        STAMP(17 + (2 * i) / 9);
      }
    });
    STAMP(24);
    static_for<4, 12>([&](auto E) { if constexpr (E.v == 8) asm volatile("s_nop 3" ::: "memory"); put(IC<1>{}, E); SB; });
    lbar();
    STAMP(5);
    STAMP_END;
    asm volatile(".Lfb_code_end1:");
  }
}

extern "C" void kernel_launch(void* const* d_in, const int* in_sizes, int n_in, void* d_out, int out_size, void* d_ws,
                              size_t ws_size, hipStream_t stream) {
  Params p;
  p.x = d_in[0]; p.wqkv = d_in[1]; p.wo = d_in[2]; p.wg = d_in[4]; p.wu = d_in[5]; p.wd = d_in[6];
  p.out = d_out; p.wsraw = (char*)d_ws;
  p.wsal = ws_size >= (64ull << 20) + WS_END ? (64ull << 20) - 1 : 0;
  fused_block<<<256, 512, 0, stream>>>(p);
}
```

```cpp
#include <hip/hip_runtime.h>
#include <stdint.h>

typedef __attribute__((ext_vector_type(8))) __bf16 bf16x8;
typedef __attribute__((ext_vector_type(4))) __bf16 bf16x4;
typedef __attribute__((ext_vector_type(2))) __bf16 bf16x2;
typedef __attribute__((ext_vector_type(4))) float f32x4;
typedef __attribute__((ext_vector_type(2))) float f32x2;
typedef __attribute__((ext_vector_type(4))) unsigned u32x4;
typedef __attribute__((ext_vector_type(2))) unsigned u32x2;
typedef __amdgpu_buffer_rsrc_t rsrc_t;
#define LAS __attribute__((address_space(3)))
#define GAS __attribute__((address_space(1)))
#define DI __device__ __forceinline__
#define RLX_AGENT __ATOMIC_RELAXED, __HIP_MEMORY_SCOPE_AGENT
typedef GAS unsigned gu32;
typedef GAS unsigned long long gu64;

constexpr int S = 2048, D = 640, F = 1728;
constexpr int NCOL = 5376;
constexpr int NSTEP = 74;
constexpr unsigned WS_EPOCH = 0;
#define FS 128
constexpr unsigned WS_FLAGH = 768u << 10;
constexpr unsigned WS_FLAGO = WS_FLAGH + 8 * 32 * FS;
constexpr unsigned WS_FLAGKV = WS_FLAGO + 8 * 32 * FS;
constexpr unsigned WS_STAT = 4096;
constexpr unsigned WS_CSUM = WS_STAT + S * 8;
constexpr unsigned WS_KVP = WS_CSUM + 3 * NCOL * 8;
constexpr unsigned WS_HA = 1u << 20;
constexpr unsigned RBSZ = 16 * NSTEP * 1024;
constexpr unsigned step_off(unsigned st) { return st < 54 ? (st / 3) * 49152 + (st % 3) * 1024 : 18 * 49152 + ((st - 54) / 5) * 81920 + ((st - 54) % 5) * 1024; }
constexpr unsigned WS_DBG = 12u << 20;
constexpr unsigned WS_END = 13u << 20;
constexpr unsigned L_B = 65536;
constexpr unsigned L_SCR = 147456;
constexpr unsigned L_MISC = 159744;
constexpr unsigned L_GO = L_MISC + 3072;
constexpr unsigned SPIN_CAP = 1u << 18;
#define HSLEEP 4
#define HSPLIT 64
#define PPIPE 0
#define STA 0
#define STB 2
#define SNAKE 0
#define ROTW 1
#define NPRE 12
#define PUTB 0
#define OBN 6
#define PSPLIT 48
#define WARM 1
#define CWIN 40
#define SDELAY 0
#define CSTG 0
#define CEARLY 0
#define TCH0 epoch
#define XK 2
#define GRP 1
#define GPRIO 0
#define HPRIO 0
#define PSLEEP 2
#define P2NW 7
#define P2W0 6
#define WSLEEP 0
#define ROTM 18
#define NA1 2
#define O_BAR 5
#define PRIO_MODE 0
#define DMA_SPACE_A T
#define DMA_SPACE_B (4 * T / 3)

struct Params {
  const void* x; const void* wqkv; const void* wo; const void* wg; const void* wu; const void* wd;
  void* out; char* wsraw;
  unsigned long long wsal;
};

__shared__ __attribute__((aligned(16))) char smem[163840];

DI float sadd(float a, float b) { float o; asm("v_add_f32 %0, %1, %2" : "=v"(o) : "v"(a), "v"(b)); return o; }
DI float smul(float a, float b) { float o; asm("v_mul_f32 %0, %1, %2" : "=v"(o) : "v"(a), "v"(b)); return o; }
DI float sfma(float a, float b, float c) { float o; asm("v_fma_f32 %0, %1, %2, %3" : "=v"(o) : "v"(a), "v"(b), "v"(c)); return o; }
DI unsigned bsel(unsigned m, unsigned a, unsigned b) { unsigned o; asm("v_bfi_b32 %0, %1, %2, %3" : "=v"(o) : "v"(m), "v"(a), "v"(b)); return o; }
DI float bself(unsigned m, float a, float b) { return __builtin_bit_cast(float, bsel(m, __builtin_bit_cast(unsigned, a), __builtin_bit_cast(unsigned, b))); }
DI bool all64(bool c) { return __builtin_amdgcn_ballot_w64(!c) == 0; }
DI float shfl_x(float v, unsigned lane, unsigned m) {
  return __builtin_bit_cast(float, __builtin_amdgcn_ds_bpermute((int)((lane ^ m) << 2), __builtin_bit_cast(int, v)));
}
DI unsigned umin(unsigned a, unsigned b) { return a < b ? a : b; }
DI unsigned rfl(unsigned v) { return __builtin_amdgcn_readfirstlane(v); }
DI rsrc_t mk_rsrc(const void* p, unsigned bytes) { return __builtin_amdgcn_make_buffer_rsrc((void*)p, (short)0, (int)bytes, 0x00020000); }
DI void dma16(unsigned ldsdst, unsigned voff, rsrc_t rs, unsigned soff) {
  asm volatile("s_mov_b32 m0, %0\n\ts_nop 0\n\tbuffer_load_dwordx4 %1, %2, %3 offen lds" ::"s"(ldsdst), "v"(voff), "s"(rs), "s"(soff) : "memory");
}
template <int N> DI void wait_vm() { asm volatile("s_waitcnt vmcnt(%0)" ::"i"(N) : "memory"); }
DI void drain_vm() { wait_vm<0>(); __builtin_amdgcn_s_waitcnt(0x0F70); }
DI void wait_lgkm0() { asm volatile("s_waitcnt lgkmcnt(0)" ::: "memory"); }
DI void bar() { asm volatile("" ::: "memory"); __builtin_amdgcn_s_barrier(); asm volatile("" ::: "memory"); }
DI void lbar() { wait_lgkm0(); bar(); }
template <class V> DI V lds_ld(unsigned a) { return *(LAS V*)((LAS char*)smem + a); }
template <class V> DI void lds_st(unsigned a, V v) { *(LAS V*)((LAS char*)smem + a) = v; }
DI bf16x4 lds_tr(unsigned a) { return __builtin_amdgcn_ds_read_tr16_b64_v4bf16((LAS bf16x4*)((LAS char*)smem + a)); }
DI bf16x8 cat(bf16x4 a, bf16x4 b) { return __builtin_shufflevector(a, b, 0, 1, 2, 3, 4, 5, 6, 7); }
DI f32x4 mfma(bf16x8 a, bf16x8 b, f32x4 c) { return __builtin_amdgcn_mfma_f32_16x16x32_bf16(a, b, c, 0, 0, 0); }
DI unsigned pk2(float a, float b) { f32x2 v = {a, b}; return __builtin_bit_cast(unsigned, __builtin_convertvector(v, bf16x2)); }
DI u32x2 pk4(f32x4 v) { u32x2 r = {pk2(v[0], v[1]), pk2(v[2], v[3])}; return r; }
DI bf16x8 frag(u32x2 lo, u32x2 hi) { u32x4 v = {lo[0], lo[1], hi[0], hi[1]}; return __builtin_bit_cast(bf16x8, v); }
DI float bflo(unsigned v) { return __builtin_bit_cast(float, v << 16); }
DI float bfhi(unsigned v) { return __builtin_bit_cast(float, v & 0xffff0000u); }
DI float rbf(float v) { return bflo(pk2(v, 0.f)); }
DI float relu2(float v) { float o; asm("v_add_f32 %0, %1, |%1|" : "=v"(o) : "v"(v)); return o; }
DI f32x4 phi4(f32x4 u, f32x4 rl, f32x4 rh) {
  const f32x4 l = u * rl;
  f32x4 e, a;
#pragma unroll
  for (int j = 0; j < 4; ++j) {
    e[j] = __builtin_amdgcn_fmed3f(__builtin_amdgcn_exp2f(l[j]), 0.f, 1.f);
    a[j] = relu2(u[j]);
  }
  return a * rh + e + 1e-6f;
}
DI f32x4 splat(float v) { return f32x4{v, v, v, v}; }
template <int CTRL> DI float dpp_add(float v) {
  return v + __builtin_bit_cast(float, __builtin_amdgcn_update_dpp(0, __builtin_bit_cast(int, v), CTRL, 0xf, 0xf, true));
}
DI float dot2(unsigned a, unsigned b, float c) {
  return __builtin_amdgcn_fdot2_f32_bf16(__builtin_bit_cast(bf16x2, a), __builtin_bit_cast(bf16x2, b), c, false);
}
DI u32x4 ldtr2(unsigned a, unsigned d) { return __builtin_bit_cast(u32x4, cat(lds_tr(a), lds_tr(a + d))); }
DI void mfma_ip(f32x4& c, const u32x4& a, const u32x4& b) { asm volatile("v_mfma_f32_16x16x32_bf16 %0, %1, %2, %0" : "+v"(c) : "v"(a), "v"(b)); }
DI void mfma_c(f32x4& d, const u32x4& a, const u32x4& b, const f32x4& c) { asm volatile("v_mfma_f32_16x16x32_bf16 %0, %1, %2, %3" : "=&v"(d) : "v"(a), "v"(b), "v"(c)); }
DI void mfma_z(f32x4& c, const u32x4& a, const u32x4& b) { asm volatile("v_mfma_f32_16x16x32_bf16 %0, %1, %2, 0" : "=&v"(c) : "v"(a), "v"(b)); }
template <int N> DI void acc_fence(f32x4* c) {
  asm volatile("s_nop 7" ::: "memory");
#pragma unroll
  for (int i = 0; i < N; ++i) asm volatile("" : "+v"(c[i]));
}
DI u32x4 cat2(u32x2 a, u32x2 b) { return u32x4{a[0], a[1], b[0], b[1]}; }
DI u32x2 ldtr1(unsigned a) { return __builtin_bit_cast(u32x2, lds_tr(a)); }
template <int IMM> DI void set_m0i(unsigned v) { asm volatile("s_add_i32 m0, %0, %1" ::"s"(v), "i"(IMM) : "memory", "scc"); }
template <int OFF> DI void dma_goi(unsigned voff, rsrc_t rs, unsigned soff) {
  asm volatile("buffer_load_dwordx4 %0, %1, %2 offen offset:%3 lds" ::"v"(voff), "s"(rs), "s"(soff), "i"(OFF) : "memory");
}
DI void set_m0(unsigned v) { asm volatile("s_mov_b32 m0, %0" ::"s"(v) : "memory"); }
DI void dma_go(unsigned voff, rsrc_t rs, unsigned soff) {
  asm volatile("buffer_load_dwordx4 %0, %1, %2 offen lds" ::"v"(voff), "s"(rs), "s"(soff) : "memory");
}
DI f32x4 mfma_u(u32x4 a, u32x4 b, f32x4 c) { return mfma(__builtin_bit_cast(bf16x8, a), __builtin_bit_cast(bf16x8, b), c); }
#define STAMP_DECL
#define STAMP_ARG
#define STAMP_PASS
#define STAMPX(c, i)
#define STAMP_END
#define STAMP(i) STAMPX(__builtin_amdgcn_workitem_id_x() == 0, i)

DI unsigned poll_flags(gu32* word, bool active, unsigned tag) {
  unsigned v = 0;
#pragma unroll 1
  for (unsigned spins = 0; spins < SPIN_CAP; ++spins) {
    v = __hip_atomic_load(word, RLX_AGENT);
    if (all64(!active || v == tag)) break;
    if (PSLEEP) __builtin_amdgcn_s_sleep(PSLEEP);
  }
  asm volatile("" ::: "memory");
  return v;
}

DI void lds_set(unsigned a) { __hip_atomic_store((LAS unsigned*)smem + a / 4, 1u, __ATOMIC_RELAXED, __HIP_MEMORY_SCOPE_WORKGROUP); }
DI unsigned lds_get(unsigned a) { return __hip_atomic_load((LAS unsigned*)smem + a / 4, __ATOMIC_RELAXED, __HIP_MEMORY_SCOPE_WORKGROUP); }
DI void lds_wait(unsigned a) {
#pragma unroll 1
  for (unsigned spins = 0; spins < (SPIN_CAP << 4); ++spins) {
    if (rfl(lds_get(a))) break;
    __builtin_amdgcn_s_sleep(1);
  }
  asm volatile("" ::: "memory");
}

template <int I> struct IC { static constexpr int v = I; };
template <int B, int E, class Fn> DI void static_for(Fn&& f) {
  if constexpr (B < E) { f(IC<B>{}); static_for<B + 1, E>(f); }
}

enum { J_MLP = 0, J_KV = 1, J_Q = 2, J_SPARE = 3 };

struct Dma {
  rsrc_t rsA, rsB;
  unsigned vA0, vA1, vB0, vB1, vB2;
  unsigned sA[4], sB, advB;
  unsigned wa, wq;
  unsigned mA, mB, mW;
};

constexpr unsigned a_slot(unsigned s3) { return s3 == 0 ? 126976u : (s3 - 1) * 32768u; }
#define SB __builtin_amdgcn_sched_barrier(0)

template <int T> DI unsigned bofs(const unsigned (&boff)[T], int t) { return T == 6 && t >= 3 ? boff[t - 3] + 6144 : boff[t]; }
template <int T, bool TR, int KS, class Extra>
DI void kstep(f32x4 (&acc)[4][T], const u32x4 (&ac)[4], const u32x2 (&bc)[T][2], u32x4 (&an)[4], u32x2 (&bn)[T][2],
              const unsigned (&aoff)[4], const unsigned (&boff)[T], const Dma& d, Extra&& extra) {
  constexpr int s = KS >> 1, KIND = KS & 1, NP = KIND == 0 ? 4 : 3, ST = KIND == 0 ? (STA ? STA : (4 * T - 1) / NP) : (STB ? STB : (4 * T - 1) / NP);
  constexpr int sn = (KS + 1) >> 1;
  constexpr unsigned asl = a_slot(sn % 3), bsl = ((KS + 1) % 5) * 12288u;
  static_for<0, 4 * T>([&](auto E) {
    constexpr int e = E.v, t = e >> 2, i = (SNAKE && (t & 1)) ? 3 - (e & 3) : (e & 3);
    const u32x4 bf = cat2(bc[t][0], bc[t][1]);
    if constexpr (KS == 0) { if (TR) mfma_z(acc[i][t], bf, ac[i]); else mfma_z(acc[i][t], ac[i], bf); }
    else { if (TR) mfma_ip(acc[i][t], bf, ac[i]); else mfma_ip(acc[i][t], ac[i], bf); }
    SB;
    if constexpr (KS < 19) {
      if constexpr (e < 4) an[e] = lds_ld<u32x4>(aoff[((KS + 1) & 1) + (sn % 3 == 0 ? 2 : 0)] + (sn % 3 == 0 ? 0u : asl) + e * 2048);
      else if constexpr (e < 4 + 2 * T) bn[(e - 4) >> 1][e & 1] = ldtr1(bofs<T>(boff, (e - 4) >> 1) + bsl + (e & 1) * 768);
    }
    constexpr int pc = e / ST;
    if constexpr (KIND == 0) {
      constexpr int adv = s < 8 ? (s + 2) * 128 : 0, dst = (int)a_slot((s + 2) % 3) + pc * 1024, off = dst >= adv ? adv : 0;
      if constexpr (pc < NP && e % ST == ST - 2) set_m0i<dst - off>(d.mA);
      if constexpr (pc < NP && e % ST == ST - 1) dma_goi<off>((pc & 1) ? d.vA1 : d.vA0, d.rsA, d.sA[pc] + (adv - off));
    } else {
      constexpr int h0 = (2 * s) % 5;
      if constexpr (pc < NP && e % ST == ST - 2) set_m0i<(h0 == 4 ? 0 : h0 * 12288) + pc * 1024>(h0 == 4 ? d.mW : d.mB);
      if constexpr (pc < NP && e % ST == ST - 1) dma_goi<0>(pc == 0 ? d.vB0 : (pc == 1 ? d.vB1 : d.vB2), d.rsB, d.sB);
    }
    extra(E);
    SB;
  });
}

struct RowStat { u32x4 xa; unsigned xb, row; float s, q; };
DI void row_load(RowStat& R, rsrc_t rsX, unsigned lane) {
  asm volatile("buffer_load_dwordx4 %0, %2, %4, %5 offen\n\tbuffer_load_dword %1, %3, %4, %5 offen offset:1024"
               : "=&v"(R.xa), "=&v"(R.xb) : "v"(lane * 16), "v"(lane * 4), "s"(rsX), "s"(R.row * 1280) : "memory");
}
template <int K> DI void row_step(RowStat& R, unsigned lane) {
  if constexpr (K < 5) {
    if constexpr (K == 0) { R.s = 0.f; R.q = 0.f; }
    const unsigned v = K < 4 ? R.xa[K & 3] : R.xb;
    asm volatile("v_dot2c_f32_bf16 %0, %2, %3\n\tv_dot2c_f32_bf16 %1, %2, %2" : "+v"(R.s), "+v"(R.q) : "v"(v), "v"(0x3f803f80u));
  } else if constexpr (K < 11) {
    constexpr int C = K == 5 ? 0xB1 : (K == 6 ? 0x4E : (K == 7 ? 0x141 : (K == 8 ? 0x140 : (K == 9 ? 0x142 : 0x143))));
    if constexpr (K == 5) asm volatile("s_nop 2" : "+v"(R.s), "+v"(R.q));
    R.s = dpp_add<C>(R.s); R.q = dpp_add<C>(R.q);
  } else if constexpr (K == 11) {
    R.s *= 1.0f / D;
    R.q = __builtin_fmaxf(R.q * (1.0f / D) - R.s * R.s, 0.f);
  } else if constexpr (K == 12) {
    R.q = __builtin_amdgcn_sqrtf(rbf(rbf(R.q) + 1e-5f));
  } else {
    lds_st<unsigned>(lane == 63 ? L_GO + 32 + (R.row & 7) * 4 : L_GO + 64, pk2(R.s, R.q));
  }
  if constexpr (K >= 5 && K < 13) asm volatile("" : "+v"(R.s), "+v"(R.q));
}
DI void row_publish(const RowStat& R, char* ws, unsigned tag, unsigned tid) {
  if (tid < 8) __hip_atomic_store((gu64*)(ws + WS_STAT) + R.row + tid, ((unsigned long long)tag << 32) | lds_ld<unsigned>(L_GO + 32 + tid * 4), RLX_AGENT);
}

struct Gran { u32x2 g0, g1, g2; unsigned off, step; };
DI void gran_issue(Gran& G, rsrc_t rs) {
  asm volatile("buffer_load_dwordx2 %0, %3, %4, 0 offen sc1\n\tbuffer_load_dwordx2 %1, %3, %4, %5 offen sc1\n\t"
               "buffer_load_dwordx2 %2, %3, %4, %6 offen sc1"
               : "=&v"(G.g0), "=&v"(G.g1), "=&v"(G.g2) : "v"(G.off), "s"(rs), "s"(G.step), "s"(2 * G.step) : "memory");
}
DI void gran_load(Gran& G, rsrc_t rs) {
  asm volatile("buffer_load_dwordx2 %0, %3, %4, 0 offen sc1\n\tbuffer_load_dwordx2 %1, %3, %4, %5 offen sc1\n\t"
               "buffer_load_dwordx2 %2, %3, %4, %6 offen sc1\n\ts_waitcnt vmcnt(0)"
               : "=&v"(G.g0), "=&v"(G.g1), "=&v"(G.g2) : "v"(G.off), "s"(rs), "s"(G.step), "s"(2 * G.step) : "memory");
}

DI unsigned urow(unsigned pos, unsigned rot) { const unsigned u = pos + rot; return (u >= 54 ? u - 54 : u) * 40960u; }

constexpr unsigned wo_tile(unsigned v) { return (v / 5) * 10 + (v % 5 == 4 ? 4 : (v % 5 < 2 ? 2 * (v % 5) : 2 * (v % 5) + 1)); }
struct W2 { rsrc_t wd, wo; unsigned rot, col; };
DI unsigned w2_off(const W2& W, int u, unsigned lw) {
  if (u < 54) return urow(u, W.rot) + lw * 10240u + W.col;
  return wo_tile(u - 54) * 20480u + (lw >> 1) * ((u - 54) % 5 == 4 ? 102400u : 20480u) + (lw & 1) * 10240u + W.col;
}
DI u32x4 w2_load(const W2& W, int u, unsigned lw, unsigned voff) {
  return __builtin_amdgcn_raw_buffer_load_b128(u < 54 ? W.wd : W.wo, voff, w2_off(W, u, lw), 0);
}

template <int T, bool TR, bool PF, bool LASTA, class Pre>
DI void main_loop(Pre&& pre, RowStat& R, char* ws, f32x4 (&acc)[4][T], unsigned (&boff)[T], Dma& d, unsigned w, unsigned lane, unsigned tid,
                  unsigned epoch, unsigned keep, unsigned& tag, unsigned rb, unsigned ccol, rsrc_t rsB2, rsrc_t rsWS, unsigned cb, unsigned rot, u32x4 (&pre2)[36] STAMP_ARG) {
  const unsigned g = lane >> 4, r = lane & 15, m = w & 3;
  unsigned aoff[4];
  aoff[0] = (64 * m + r) * 128 + ((g ^ (r >> 1)) << 4);
  aoff[1] = aoff[0] ^ 64;
  aoff[2] = aoff[0] + a_slot(0); aoff[3] = aoff[1] + a_slot(0);
  const unsigned wq = d.wq, wa = d.wa;
  asm volatile("" : "+v"(aoff[0]), "+v"(aoff[1]), "+v"(aoff[2]), "+v"(aoff[3]), "+v"(boff[0]), "+v"(boff[1]), "+v"(boff[2]));
  if (T == 5) asm volatile("" : "+v"(boff[3]), "+v"(boff[4]));
  Gran G;
  unsigned v2, vb2, vpf, tA[3], tB[3];
  row_load(R, d.rsA, lane);
  {
    asm volatile("s_waitcnt vmcnt(%2) lgkmcnt(0)\n\tv_readfirstlane_b32 %0, %1\n\ts_nop 3" : "=s"(tag) : "v"(epoch), "i"(5 + NA1), "v"(keep) : "memory");
    tag = rfl(tag) + 1;
    bar();
  }
  STAMP(1);
  u32x4 a0[4], a1[4];
  u32x2 b0[T][2], b1[T][2];
#pragma unroll
  for (int i = 0; i < 4; ++i) a0[i] = lds_ld<u32x4>(aoff[2] + i * 2048);
#pragma unroll
  for (int t = 0; t < T; ++t) { b0[t][0] = ldtr1(bofs<T>(boff, t)); b0[t][1] = ldtr1(bofs<T>(boff, t) + 768); }
  SB;
#pragma unroll
  for (int i = NA1; i < 4; ++i) dma16(a_slot(1) + d.mA + i * 1024, (i & 1) ? d.vA1 : d.vA0, d.rsA, d.sA[i] + 128);
  const bool y = w >= 4;
  bool ok = true;
  auto ln_store = [&] {
    const float v0 = y ? -bflo(G.g0[0]) : __builtin_bit_cast(float, G.g0[0]) + __builtin_bit_cast(float, G.g1[0]) + __builtin_bit_cast(float, G.g2[0]);
    const unsigned la = L_MISC + (y ? tid * 4 - 1024 : 2048 + tid * 4);
    lds_st<float>(la, v0);
    lds_st<float>(y ? la + 1024 : L_GO + 64 + (lane & 7) * 4, __builtin_amdgcn_rcpf(bfhi(G.g0[0])));
  };
  static_for<0, 10>([&](auto S_) {
    constexpr int s = S_.v, L = 4 * T;
    constexpr int NX = (PF && s >= 1 && 2 * s <= NPRE) ? 2 : 0;
    kstep<T, TR, 2 * s>(acc, a0, b0, a1, b1, aoff, boff, d, [&](auto E) {
      constexpr int e = E.v;
      if constexpr (s == 2) {
        if constexpr (e == 2) { vb2 = v2 + (3 * wq) * 10240u; asm volatile("" : "+v"(vb2)); }
        if constexpr (e == 5) { G.off = w < 4 ? WS_CSUM + ccol * 8 : WS_STAT + (rb * 256 + tid - 256) * 8; asm volatile("" : "+v"(G.off)); }
        if constexpr (e == 8) { G.step = w < 4 ? NCOL * 8 : 0; tA[0] = urow(wa, rot) + cb * 128u; asm volatile("" : "+s"(G.step), "+s"(tA[0])); }
        if constexpr (e == 10) { tA[1] = urow(8 + wa, rot) + cb * 128u; asm volatile("" : "+s"(tA[1])); }
        if constexpr (e == 12) { tA[2] = urow(31 + wa, rot) + cb * 128u; asm volatile("" : "+s"(tA[2])); }
        if constexpr (e == 14) {
          tB[0] = urow(16, rot) + cb * 128u; tB[1] = urow(w < 4 ? 19u : 22u, rot) + cb * 128u; tB[2] = urow(w < 4 ? 25u : 28u, rot) + cb * 128u;
          asm volatile("" : "+s"(tB[0]), "+s"(tB[1]), "+s"(tB[2]));
        }
      }
      if constexpr (s == (TR ? 1 : 3) && e == 2) pre(0);
      if constexpr (s == CSTG && e == 2) pre(1);
      if constexpr (NX && e >= L - 2) {
        constexpr int u = 2 * (s - 1) + e - (L - 2);
        pre2[u] = __builtin_amdgcn_raw_buffer_load_b128(rsB2, vpf, urow(36 + u, rot) + (w & 3) * 10240u + cb * 128u, 0);
      }
      if constexpr (s < 7) { if constexpr (e == L - 3) d.sB += s == 0 ? d.advB * (1 + (w >> 2)) : d.advB; }
      else if constexpr (s == 7) {
        if constexpr (e == L - 6) d.vB0 = y ? vb2 : d.vB0;
        if constexpr (e == L - 4) d.vB1 = y ? vb2 + 10240u : d.vB1;
        if constexpr (e == L - 3) d.vB2 = y ? vb2 + 20480u : d.vB2;
        if constexpr (e == L - 2) d.sB = y ? tB[0] : d.sB + d.advB;
        if constexpr (e == L - 1) d.rsB = y ? rsB2 : d.rsB;
      } else {
        if constexpr (e == L - 3) { d.sB = tB[s - 7]; d.rsB = rsB2; d.vB0 = vb2; d.vB1 = vb2 + 10240u; d.vB2 = vb2 + 20480u; }
      }
      if constexpr (s == 9 && e == L - 4) {
        asm volatile("s_waitcnt vmcnt(7)" : "+v"(G.g0), "+v"(G.g1), "+v"(G.g2)::"memory");
        ok = all64(G.g0[1] == tag && G.g1[1] == tag && G.g2[1] == tag);
      }
      if constexpr (s == 9 && e == L - 3) ln_store();
    });
    if constexpr (s == 0) {
      wait_vm<8 - NA1>();
      static_for<0, 14>([&](auto K) { row_step<K.v>(R, lane); });
    }
    if constexpr (s == 9) {
      if (__builtin_expect(!ok, 0)) {
#pragma unroll 1
        for (unsigned spins = 0; spins < SPIN_CAP; ++spins) {
          __builtin_amdgcn_s_sleep(2);
          gran_load(G, rsWS);
          if (all64(G.g0[1] == tag && G.g1[1] == tag && G.g2[1] == tag)) break;
        }
        ln_store();
      }
    }
    wait_lgkm0();
    if constexpr (s < 9) wait_vm<4 + NX>();
    bar();
    if constexpr (s == 0) { STAMP(12); }
    SB;
    kstep<T, TR, 2 * s + 1>(acc, a1, b1, a0, b0, aoff, boff, d, [&](auto E) {
      constexpr int e = E.v;
      if constexpr (s == 0 && e == 6) {
        if (y) {
          const unsigned so = d.sB - d.advB;
          dma16(d.mW + 49152, d.vB0, d.rsB, so); dma16(d.mW + 49152 + 1024, d.vB1, d.rsB, so); dma16(d.mW + 49152 + 2048, d.vB2, d.rsB, so);
        }
      }
      if constexpr (s == 0 && e == L - 4) {
        v2 = (lane >> 3) * 1280 + (((lane & 7) ^ (((lane >> 4) & 3) << 1)) << 4);
        vpf = y ? v2 : 0x7ffffff0u;
        asm volatile("" : "+v"(v2), "+v"(vpf));
      }
      if constexpr (s == 0 && e == L - 6) row_publish(R, ws, tag, tid);
      if constexpr (s == 8 && e == 0) gran_issue(G, rsWS);
      if constexpr (s >= 7 && s < 9 && e >= L - 4) {
        d.rsA = rsB2; d.vA0 = v2; d.vA1 = v2;
        d.sA[e - (L - 4)] = tA[s - 7] + (e - (L - 4)) * 10240;
      }
      if constexpr (s == 9 && LASTA && e == 7) {
        if (wa < 5) {
#pragma unroll
          for (int i = 0; i < 4; ++i) dma16(a_slot(0) + d.mA + i * 1024, v2, rsB2, tA[2] + i * 10240);
        }
      }
    });
    if constexpr (s == 8) { STAMP(13); }
    if constexpr (s == 2) { STAMP(26); }
    if constexpr (s == 6) { STAMP(27); }
  });
  acc_fence<4 * T>(&acc[0][0]);
  STAMP(14);
}

DI void st16(u32x4 v, rsrc_t rs, unsigned voff, unsigned soff) {
  __builtin_amdgcn_raw_buffer_store_b128(v, rs, voff, soff, 16);
  asm volatile("s_nop 1" ::"v"(v) : "memory");
}
DI void publish_flag(char* ws, unsigned flag_off, unsigned tag, unsigned tid) {
  drain_vm();
  bar();
  if (tid == 0) __hip_atomic_store((gu32*)(ws + flag_off), tag, RLX_AGENT);
}
DI void st_frag(rsrc_t rs, unsigned lane, unsigned off, u32x2 lo, u32x2 hi) { st16(cat2(lo, hi), rs, lane * 16, off); }
DI void shared_send(unsigned w, unsigned lane, const u32x2 (&sh)[4]) {
  if (w >= 4) lds_st<u32x4>(L_SCR + w * 1024 + lane * 16, cat2(sh[0], sh[1]));
  else lds_st<u32x4>(L_SCR + w * 1024 + lane * 16, cat2(sh[2], sh[3]));
  asm volatile("" ::: "memory");
}
DI void shared_recv(rsrc_t rs, unsigned w, unsigned lane, unsigned off, unsigned str, const u32x2 (&sh)[4]) {
  lbar();
  u32x4 rcv = lds_ld<u32x4>(L_SCR + (w ^ 4) * 1024 + lane * 16);
  u32x2 r0 = {rcv[0], rcv[1]}, r1 = {rcv[2], rcv[3]};
  if (w >= 4) { st_frag(rs, lane, off + 2 * str, r0, sh[2]); st_frag(rs, lane, off + 3 * str, r1, sh[3]); }
  else        { st_frag(rs, lane, off, sh[0], r0); st_frag(rs, lane, off + str, sh[1], r1); }
}

__global__ __launch_bounds__(512, 2) void fused_block(Params p) {
  char* const ws = (char*)(((unsigned long long)p.wsraw + p.wsal) & ~p.wsal);
  unsigned tid = __builtin_amdgcn_workitem_id_x(), lane = tid & 63;
  const unsigned w = rfl(tid >> 6);
  const unsigned b = __builtin_amdgcn_workgroup_id_x(), xcd = b & 7, slot = b >> 3;
  const unsigned rb = 4 * (xcd & 1) + (slot & 3), jq = xcd >> 1, ji = slot >> 2;
  const unsigned spi = (xcd - 4) * 4 + (slot & 3);
  const unsigned long long jl0 = (jq & 1) ? 0x4123220908070605ull : 0x4021200403020100ull, jl1 = (jq & 1) ? 0x4327266011100f0eull : 0x422524600d0c0b0aull;
  const unsigned jb = (unsigned)(((jq & 2) ? jl1 : jl0) >> (8 * ji)) & 0xff, jtype = jb >> 5, jidx = jb & 31;
  unsigned long long pc0; unsigned codeb;
  asm volatile("s_getpc_b64 %0\n.Lfb_code_mark:\n\ts_mov_b32 %1, .Lfb_code_end0-.Lfb_code_mark\n\ts_max_u32 %1, %1, .Lfb_code_end1-.Lfb_code_mark"
               : "=s"(pc0), "=s"(codeb) :: "scc");
  STAMP_DECL;
  STAMP(0);

  Dma d;
  RowStat rs_;
  const bool mlp = jtype == J_MLP;
  const unsigned pitch = mlp ? 3456u : 3840u, wq = (w + (ROTW ? slot : 0)) & 3, wa = (w + (ROTW ? ji : 0)) & 7;
  d.wa = wa; d.wq = wq;
  const rsrc_t rsX = mk_rsrc(p.x, S * D * 2);
  d.vA0 = (lane >> 3) * 1280 + (((lane & 7) ^ (lane >> 4)) << 4); d.vA1 = d.vA0 ^ 64;
  d.rsA = rsX; d.mA = wa * 4096u;
#pragma unroll
  for (int i = 0; i < 4; ++i) d.sA[i] = (rb * 256 + 32 * wa + 8 * i) * 1280;
  unsigned epoch, tag;
  asm volatile("s_nop 4" :: "s"(rsX) : "memory");
  STAMP(6);
#pragma unroll
  for (int i = 0; i < 4; ++i) dma16(a_slot(0) + d.mA + i * 1024, (i & 1) ? d.vA1 : d.vA0, d.rsA, d.sA[i]);
  asm volatile("global_load_dword %0, %1, %2 sc1" : "=&v"(epoch) : "v"(0u), "s"(ws) : "memory");
  SB;
  {
    unsigned vb[3];
    const unsigned lim = mlp ? 176u : 144u;
    {
      const unsigned r0 = (lane * 43) >> 9, p0 = lane - 12 * r0;
      const bool w1 = p0 >= 8, w2 = p0 >= 4;
      const unsigned r1 = r0 + (w1 ? 6 : 5), p1 = p0 + (w1 ? -8 : 4), r2 = r0 + (w2 ? 11 : 10), p2 = p0 + (w2 ? -4 : 8);
      vb[0] = r0 * pitch + umin(p0 * 16, lim);
      vb[1] = r1 * pitch + umin((p1 * 16) ^ ((r1 & 8) << 2), lim);
      vb[2] = r2 * pitch + umin((p2 * 16) ^ 32, lim);
    }
    d.vB0 = vb[0]; d.vB1 = vb[1]; d.vB2 = vb[2];
    const unsigned mh = wq >> 1;
    const void* bw = mlp ? (mh ? p.wu : p.wg) : p.wqkv;
    const unsigned cj = mlp ? 192u : 160u * jtype, cm = mlp ? 0u : (jtype == J_KV ? 1280u : 160u), c0 = jtype == J_KV ? 1280u : 0u;
    d.rsB = mk_rsrc(bw, D * pitch);
    d.sB = (16 * (wq & 1) + (w < 4 ? 32 : 0)) * pitch + c0 + cj * jidx + cm * mh; d.advB = 64 * pitch;
    d.mB = L_B + wq * 3072u + (w < 4 ? 0u : 12288u); d.mW = L_B + wq * 3072u + (w < 4 ? 49152u : 0u);
    const unsigned bdst = L_B + (w < 4 ? 12288u : 0u) + wq * 3072u;
    rs_.row = rfl(b * 8 + w);
    if (jtype != J_SPARE) {
      dma16(bdst, d.vB0, d.rsB, d.sB); dma16(bdst + 1024, d.vB1, d.rsB, d.sB); dma16(bdst + 2048, d.vB2, d.rsB, d.sB);
      const unsigned so = d.sB + d.advB;
      dma16(bdst + 24576, d.vB0, d.rsB, so); dma16(bdst + 24576 + 1024, d.vB1, d.rsB, so); dma16(bdst + 24576 + 2048, d.vB2, d.rsB, so);
#pragma unroll
      for (int i = 0; i < NA1; ++i) dma16(a_slot(1) + d.mA + i * 1024, (i & 1) ? d.vA1 : d.vA0, d.rsA, d.sA[i] + 128);
    }
    d.sB += d.advB;
  }
  STAMP(10);
  lds_st<unsigned>(L_GO + (tid & 1) * 4, 0u);

  const unsigned t2 = 28 * xcd + slot;
  const bool has2 = slot < 28 && t2 < 220;
  const unsigned G = t2 / 10, cb = t2 % 10;
  constexpr unsigned rot = 32;
  unsigned g = lane >> 4, r = lane & 15, q4 = r >> 2, p4 = lane & 3;
  const rsrc_t rsWd = mk_rsrc(p.wd, has2 ? F * D * 2 : 0);
  const rsrc_t rsWS = mk_rsrc(ws, WS_END);
  unsigned v2 = (lane >> 3) * 1280 + (((lane & 7) ^ (((lane >> 4) & 3) << 1)) << 4);
  auto relane = [&] {
    asm volatile("" : "+v"(tid));
    lane = tid & 63; g = lane >> 4; r = lane & 15; q4 = r >> 2; p4 = lane & 3;
    v2 = (lane >> 3) * 1280 + (((lane & 7) ^ (((lane >> 4) & 3) << 1)) << 4);
  };
  const unsigned m = w & 3, n = w >> 2;
  const unsigned brow = L_B + (8 * g + q4) * 192;
  const unsigned bx = (g & 1) << 5;

  u32x4 pre2[36];
  W2 W;
  W.wd = rsWd; W.wo = mk_rsrc(p.wo, has2 ? D * D * 2 : 0); W.rot = rot; W.col = cb * 128u;
  if (jtype == J_MLP) {
    f32x4 acc[4][6];
    unsigned boff[6];
#pragma unroll
    for (int t = 0; t < 3; ++t) {
      const unsigned gt = n ? (t == 2 ? 3 : 4 + t) : t;
      boff[t] = brow + ((32 * gt + 8 * p4) ^ bx); boff[t + 3] = boff[t] + 6144;
    }
    const unsigned ccol = tid < 96 ? 1920 + 96 * jidx + tid : 3648 + 96 * jidx + (tid < 192 ? tid - 96 : 0);
    main_loop<6, true, true, true>([](int) {}, rs_, ws, acc, boff, d, w, lane, tid, epoch, TCH0, tag, rb, ccol, rsWd, rsWS, cb, rot, pre2 STAMP_PASS);
    relane();
    float mm[4];
    f32x4 cg[3], cu[3];
#pragma unroll
    for (int i = 0; i < 4; ++i) mm[i] = lds_ld<float>(L_MISC + (64 * m + 16 * i + r) * 4);
#pragma unroll
    for (int t = 0; t < 3; ++t) {
      const unsigned gt = n ? (t == 2 ? 3 : 4 + t) : t;
      cg[t] = lds_ld<f32x4>(L_MISC + 2048 + (16 * gt + 4 * g) * 4); cu[t] = lds_ld<f32x4>(L_MISC + 2048 + (96 + 16 * gt + 4 * g) * 4);
    }
    auto hval = [&](int i, int t) {
      f32x4 gg = acc[i][t] + mm[i] * cg[t];
      const f32x4 uu = acc[i][t + 3] + mm[i] * cu[t];
#pragma unroll
      for (int j = 0; j < 4; ++j) gg[j] = relu2(gg[j]);
      return pk4(gg * uu);
    };
    const unsigned ho = WS_HA + rb * RBSZ + jidx * 49152 + m * 4 * 3072;
#pragma unroll
    for (int i = 0; i < 4; ++i) { st_frag(rsWS, lane, ho + i * 3072 + n * 2048, hval(i, 0), hval(i, 1)); SB; }
    const u32x2 sh[4] = {hval(0, 2), hval(1, 2), hval(2, 2), hval(3, 2)};
    shared_send(w, lane, sh);
    shared_recv(rsWS, w, lane, ho + 1024, 3072, sh);
    STAMP(15);
    publish_flag(ws, WS_FLAGH + (rb * 32 + jidx) * FS, tag, tid);
    STAMP(3);
  } else if (jtype == J_KV) {
    f32x4 acc[4][5];
    unsigned boff[5];
    boff[0] = brow + ((80 * n + 8 * p4) ^ bx); boff[1] = boff[0] + 6144;
    boff[2] = brow + ((80 * n + 40 + 8 * p4) ^ bx); boff[3] = boff[2] + 6144;
    boff[4] = brow + ((80 * n + 32 + 40 * (p4 >> 1)) ^ bx) + (p4 & 1) * 6144;
    const unsigned kb = 126976u + m * 6720 + n * 3360, dump = L_MISC - 2048 + lane * 16;
    const unsigned ea = (q4 == 1 || r == 0) ? kb + (r == 0 ? 20 : 12 + r) * 80 : 0u, eb = (q4 == 3 || r == 0) ? kb + 1680 + (r == 0 ? 20 : 4 + r) * 80 : 0u;
    unsigned ka[6] = {kb + r * 80 + g * 16, ea ? ea + g * 16 : dump, eb ? eb + g * 16 : dump, g == 0 ? kb + r * 80 + 64 : dump,
                      g == 2 ? kb + 1680 + r * 80 + 64 : dump, (g == 0 && ea) ? ea + 64 : ((g == 2 && eb) ? eb + 64 : dump)};
    auto pin = [&](int P) { if (P == 0) asm volatile("" : "+v"(ka[0]), "+v"(ka[1]), "+v"(ka[2]), "+v"(ka[3]), "+v"(ka[4]), "+v"(ka[5])); };
    const unsigned ccol = tid < 80 ? 640 + 80 * jidx + tid : 1280 + 80 * jidx + (tid < 160 ? tid - 80 : 0);
    main_loop<5, false, false, false>(pin, rs_, ws, acc, boff, d, w, lane, tid, epoch, TCH0, tag, rb, ccol, rsWd, rsWS, cb, rot, pre2 STAMP_PASS);
    relane();
    float cs[5];
    cs[0] = lds_ld<float>(L_MISC + 2048 + (40 * n + r) * 4);
    cs[1] = lds_ld<float>(L_MISC + 2048 + (80 + 40 * n + r) * 4);
    cs[2] = lds_ld<float>(L_MISC + 2048 + (40 * n + 20 + r) * 4);
    cs[3] = lds_ld<float>(L_MISC + 2048 + (80 + 40 * n + 20 + r) * 4);
    cs[4] = lds_ld<float>(L_MISC + 2048 + ((q4 & 1) * 80 + 40 * n + 16 + 20 * (q4 >> 1) + (r & 3)) * 4);
    u32x2 pk[4][3], vv[4][3];
    const unsigned vmask = 0u - (q4 & 1);
    f32x4 mm4[4], rr4[4];
#pragma unroll
    for (int i = 0; i < 4; ++i) {
      mm4[i] = lds_ld<f32x4>(L_MISC + (64 * m + 16 * i + 4 * g) * 4); rr4[i] = lds_ld<f32x4>(L_MISC + 1024 + (64 * m + 16 * i + 4 * g) * 4);
    }
    SB;
#pragma unroll
    for (int i = 0; i < 4; ++i) {
      const f32x4 mm = mm4[i], rr = rr4[i];
      const f32x4 rl = rr * 1.4426950408889634f, rh = rr * 0.5f;
      f32x4 val[5];
#pragma unroll
      for (int t = 0; t < 5; ++t) val[t] = acc[i][t] + mm * cs[t];
#pragma unroll
      for (int t = 0; t < 3; ++t) pk[i][t] = pk4(phi4(val[2 * t], rl, rh));
      vv[i][0] = pk4(val[1] * rr); vv[i][1] = pk4(val[3] * rr);
      const u32x2 v4 = pk4(val[4] * rr);
      vv[i][2] = u32x2{bsel(vmask, v4[0], 0x3f803f80u), bsel(vmask, v4[1], 0x3f803f80u)};
    }
    STAMP(25);
    f32x4 P[7] = {};
#pragma unroll
    for (int i = 0; i < 4; i += 2) {
      const bf16x8 k0 = frag(pk[i][0], pk[i + 1][0]), k2 = frag(pk[i][1], pk[i + 1][1]), k4 = frag(pk[i][2], pk[i + 1][2]);
      const bf16x8 v1 = frag(vv[i][0], vv[i + 1][0]), v3 = frag(vv[i][1], vv[i + 1][1]), v4 = frag(vv[i][2], vv[i + 1][2]);
      P[0] = mfma(k0, v1, P[0]); P[1] = mfma(k0, v4, P[1]); P[2] = mfma(k4, v1, P[2]);
      P[3] = mfma(k2, v3, P[3]); P[4] = mfma(k2, v4, P[4]); P[5] = mfma(k4, v3, P[5]); P[6] = mfma(k4, v4, P[6]);
    }
    lds_st<f32x4>(ka[0], P[0]); lds_st<f32x4>(ka[0] + 1680, P[3]); lds_st<f32x4>(ka[1], P[1]); lds_st<f32x4>(ka[2], P[4]);
    lds_st<f32x4>(ka[3], P[2]); lds_st<f32x4>(ka[4], P[5]); lds_st<f32x4>(ka[5], P[6]);
    lbar();
    if (tid < 210) {
      const unsigned a = 126976u + tid * 32;
      const f32x4 t0 = (lds_ld<f32x4>(a) + lds_ld<f32x4>(a + 6720)) + (lds_ld<f32x4>(a + 13440) + lds_ld<f32x4>(a + 20160));
      const f32x4 t1 = (lds_ld<f32x4>(a + 16) + lds_ld<f32x4>(a + 6736)) + (lds_ld<f32x4>(a + 13456) + lds_ld<f32x4>(a + 20176));
      st16(cat2(pk4(t0), pk4(t1)), rsWS, tid * 16, WS_KVP + (rb * 32 + 4 * jidx) * 840);
    }
    wait_lgkm0();
    STAMP(15);
    publish_flag(ws, WS_FLAGKV + ((jidx >> 1) * 32 + (jidx & 1) * 8 + rb) * FS, tag, tid);
    STAMP(3);
  } else if (jtype == J_Q) {
    f32x4 acc[4][5];
    unsigned boff[5];
#pragma unroll
    for (int t = 0; t < 5; ++t) boff[t] = brow + n * 6144 + ((32 * t + 8 * p4) ^ bx);
    const unsigned ccol = 160 * jidx + (tid < 160 ? tid : 0);
    unsigned tch[4];
    auto warm = [&](int P) {
      if (P == 1) {
        asm volatile("buffer_load_dword %0, %1, %2, 0 offen" : "=&v"(tch[3]) : "v"((((rb & 3) * 8 + w) * 16 + (lane & 15)) * 128),
                     "s"(mk_rsrc((const void*)pc0, codeb)) : "memory");
        return;
      }
      const unsigned gq = (rb * 4 + jidx) * 8 + w;
      asm volatile("buffer_load_dword %0, %3, %5, 0 offen\n\tbuffer_load_dword %1, %3, %5, %7 offen\n\tbuffer_load_dword %2, %4, %6, 0 offen"
                   : "=&v"(tch[0]), "=&v"(tch[1]), "=&v"(tch[2])
                   : "v"((gq * 34 + (lane < 34 ? lane : 0)) * 128), "v"((gq * 25 + (lane < 25 ? lane : 0)) * 128), "s"(mk_rsrc(p.wd, F * D * 2)),
                     "s"(mk_rsrc(p.wo, D * D * 2)), "s"(8576u * 128u) : "memory");
    };
    main_loop<5, true, false, false>(warm, rs_, ws, acc, boff, d, w, lane, tid, epoch, TCH0, tag, rb, ccol, rsWd, rsWS, cb, rot, pre2 STAMP_PASS);
    relane();
    asm volatile("" :: "v"(tch[0]), "v"(tch[1]), "v"(tch[2]), "v"(tch[3]));
    u32x2 pq[4][5];
    {
      float mm[4], rr[4];
      f32x4 c[5];
#pragma unroll
      for (int i = 0; i < 4; ++i) { mm[i] = lds_ld<float>(L_MISC + (64 * m + 16 * i + r) * 4); rr[i] = lds_ld<float>(L_MISC + 1024 + (64 * m + 16 * i + r) * 4); }
#pragma unroll
      for (int t = 0; t < 5; ++t) c[t] = lds_ld<f32x4>(L_MISC + 2048 + (80 * n + 16 * t + 4 * g) * 4);
      SB;
#pragma unroll
      for (int i = 0; i < 4; ++i)
#pragma unroll
        for (int t = 0; t < 5; ++t) pq[i][t] = pk4(phi4(acc[i][t] + mm[i] * c[t], splat(rr[i] * 1.4426950408889634f), splat(rr[i] * 0.5f)));
#pragma unroll
      for (int i = 0; i < 4; ++i) asm volatile("" : "+v"(pq[i][0]), "+v"(pq[i][1]), "+v"(pq[i][2]), "+v"(pq[i][3]), "+v"(pq[i][4]));
    }
    constexpr unsigned KP = 208, KQ = 84 * KP;
    unsigned wa[2];
#pragma unroll
    for (int k = 0; k < 2; ++k) {
      const unsigned qd = 2 * (tid < 420 ? tid : 0) + k, hd = qd / 105, e = (qd % 105) / 5, dq = qd % 5, hl = hd & 3;
      wa[k] = (hd >> 2) * KQ + (e < 20 ? hl * 20 + e : 80 + hl) * KP + hl * 40 + dq * 8;
    }
    unsigned fk = n * KQ + r * KP + g * 8, fz = n * KQ + (80 + (r & 3)) * KP + g * 8;
    unsigned gm[3] = {g < 1 ? ~0u : 0u, g < 2 ? ~0u : 0u, g < 3 ? ~0u : 0u};
    u32x4 B[4][3];
    f32x4 eps4 = splat(1e-6f);
#pragma unroll
    for (int i = 0; i < 4; ++i) {
      B[i][0] = cat2(pq[i][0], pq[i][1]); B[i][1] = cat2(pq[i][2], pq[i][3]); B[i][2] = cat2(pq[i][4], u32x2{0u, 0u});
      asm volatile("" : "+v"(B[i][0]), "+v"(B[i][1]), "+v"(B[i][2]));
    }
    asm volatile("" : "+v"(wa[0]), "+v"(wa[1]), "+v"(fk), "+v"(fz), "+v"(gm[0]), "+v"(gm[1]), "+v"(gm[2]), "+v"(eps4));
    STAMP(25);
    wait_vm<0>();
    bar();
#pragma unroll
    for (int k = 0; k < 5; ++k) lds_st<u32x4>(k * 8192 + tid * 16, u32x4{0u, 0u, 0u, 0u});
    if (w == 0) poll_flags((gu32*)(ws + WS_FLAGKV + (jidx * 32 + (lane & 15)) * FS), true, tag);
    lbar();
    STAMP(4);
    if (w < 7) {
      u32x4 v[8];
#pragma unroll
      for (int j = 0; j < 8; ++j)
        v[j] = __builtin_amdgcn_raw_buffer_load_b128(rsWS, (tid < 420 ? tid : 0) * 16, WS_KVP + (j * 32 + 8 * jidx) * 840, 16);
      SB;
      f32x4 t0 = {0.f, 0.f, 0.f, 0.f}, t1 = t0;
#pragma unroll
      for (int j = 0; j < 8; ++j) {
        t0 += f32x4{bflo(v[j][0]), bfhi(v[j][0]), bflo(v[j][1]), bfhi(v[j][1])};
        t1 += f32x4{bflo(v[j][2]), bfhi(v[j][2]), bflo(v[j][3]), bfhi(v[j][3])};
      }
      lds_st<u32x2>(wa[0], pk4(t0)); lds_st<u32x2>(wa[1], pk4(t1));
    }
    lbar();
    STAMP(16);
    u32x4 A[12];
#pragma unroll
    for (int P = 0; P < 3; ++P) A[P] = cat2(lds_ld<u32x2>(fz + P * 64), lds_ld<u32x2>(fz + P * 64 + 32));
#pragma unroll
    for (int u = 0; u < 5; ++u) {
      const unsigned a0 = fk + u * 16 * KP + (u < 3 ? 0 : 64);
      A[3 + u] = cat2(lds_ld<u32x2>(a0), lds_ld<u32x2>(a0 + 32));
    }
#pragma unroll
    for (int u = 1; u < 5; ++u) {
      const unsigned a0 = fk + u * 16 * KP + (u < 3 ? 64 : 128);
      A[7 + u] = cat2(lds_ld<u32x2>(a0), lds_ld<u32x2>(a0 + 32));
    }
    SB;
    u32x2 sh[4];
    f32x4 den[4], num[2][5];
    static_for<0, 12>([&](auto E) {
      constexpr int P = E.v >> 2, i = E.v & 3;
      if constexpr (P == 0) mfma_c(den[i], A[0], B[i][0], eps4); else mfma_ip(den[i], A[P], B[i][P]);
    });
    u32x2 o[5];
    const unsigned oo = WS_HA + rb * RBSZ + 18 * 49152 + jidx * 81920 + m * 4 * 5120;
    auto fin = [&](auto I, auto K) {
      constexpr int i = I.v, k = K.v, u = k < 8 ? k >> 1 : 4;
      if constexpr (k == 8 || !(k & 1)) {
        asm volatile("" : "+v"(num[i & 1][u]));
        const float rr = u == 0 ? den[i][0] : (u == 4 ? den[i][3] : bself(gm[(u + 2) % 3], den[i][(u + 3) & 3], den[i][u & 3]));
#pragma unroll
        for (int j = 0; j < 4; ++j) num[i & 1][u][j] = smul(num[i & 1][u][j], rr);
      }
      if constexpr (k == 8 || (k & 1)) o[u] = pk4(num[i & 1][u]);
      if constexpr (k == 3) st_frag(rsWS, lane, oo + i * 5120 + n * 2048, o[0], o[1]);
      if constexpr (k == 7) st_frag(rsWS, lane, oo + i * 5120 + n * 2048 + 1024, o[2], o[3]);
      if constexpr (k == 8) { sh[i] = o[4]; asm volatile("" : "+v"(sh[i])); }
    };
    static_for<0, 4>([&](auto I) {
      constexpr int i = I.v;
      static_for<0, 9>([&](auto K) {
        constexpr int k = K.v, u = k < 5 ? k : k - 4;
        if constexpr (k < 5) mfma_z(num[i & 1][u], A[3 + u], B[i][u < 3 ? 0 : 1]); else mfma_ip(num[i & 1][u], A[7 + u], B[i][u < 3 ? 1 : 2]);
        SB;
        if constexpr (i == 0) {
          if constexpr (k >= 5) {
            asm volatile("" : "+v"(den[k - 5]));
#pragma unroll
            for (int j = 0; j < 4; ++j) den[k - 5][j] = __builtin_amdgcn_rcpf(den[k - 5][j]);
            asm volatile("" : "+v"(den[k - 5]));
          }
        } else {
          fin(IC<i - 1>{}, K);
        }
        SB;
      });
    });
    static_for<0, 9>([&](auto K) { fin(IC<3>{}, K); SB; });
    shared_send(w, lane, sh);
    shared_recv(rsWS, w, lane, oo + 4096, 5120, sh);
    STAMP(15);
    publish_flag(ws, WS_FLAGO + (rb * 32 + jidx) * FS, tag, tid);
    STAMP(3);
    if (b == 224) {
      poll_flags((gu32*)(ws + WS_FLAGH + tid * FS), (tid & 31) < (tid < 256 ? 18u : 4u), tag);
      bar();
      if (tid == 0) __hip_atomic_store((gu32*)(ws + WS_EPOCH), tag, RLX_AGENT);
    }
  } else {
    const unsigned rk = spi * 4 + (w & 3), p0 = w < 4 ? rk : 128 + rk, p1 = p0 + 64;
    auto unit = [](unsigned q) { return q < 60 ? q + 30 : (q < 90 ? q - 60 : q); };
    const unsigned u0 = unit(p0), u1 = unit(p1);
    unsigned k10 = 0x00003f80u, k01 = 0x3f800000u;
    asm volatile("" : "+v"(k10), "+v"(k01));
    auto cs_src = [&](unsigned u, rsrc_t& rs, unsigned& cv, unsigned& st) {
      const unsigned cline = u / 3, third = u % 3;
      const void* cw = cline < 30 ? p.wqkv : (cline < 57 ? p.wg : p.wu);
      const unsigned cpitch = cline < 30 ? 3840u : 3456u;
      const unsigned clocal = cline < 30 ? cline : (cline < 57 ? cline - 30 : cline - 57);
      rs = mk_rsrc(cw, u < 252 ? D * cpitch : 0);
      cv = (third * 216 + (lane >> 3)) * cpitch + clocal * 128 + (lane & 7) * 16;
      st = 8 * cpitch;
    };
    auto cs_publish = [&](unsigned u, float (&cs)[8]) {
#pragma unroll
      for (int k = 0; k < 4; ++k) { const bool hi = lane & 32; const float o = shfl_x(hi ? cs[k] : cs[k + 4], lane, 32); cs[k] = (hi ? cs[k + 4] : cs[k]) + o; }
#pragma unroll
      for (int k = 0; k < 2; ++k) { const bool hi = lane & 16; const float o = shfl_x(hi ? cs[k] : cs[k + 2], lane, 16); cs[k] = (hi ? cs[k + 2] : cs[k]) + o; }
      { const bool hi = lane & 8; const float o = shfl_x(hi ? cs[0] : cs[1], lane, 8); cs[0] = (hi ? cs[1] : cs[0]) + o; }
      if (u < 252)
        __hip_atomic_store((gu64*)(ws + WS_CSUM) + (u % 3) * NCOL + (u / 3) * 64 + (lane & 7) * 8 + (lane >> 3),
                           ((unsigned long long)tag << 32) | __builtin_bit_cast(unsigned, cs[0]), RLX_AGENT);
    };
    row_load(rs_, rsX, lane);
    constexpr int CW = CWIN;
    u32x4 c[CW];
    rsrc_t rs0, rs1; unsigned cv0, cv1, st0, st1;
    cs_src(u0, rs0, cv0, st0); cs_src(u1, rs1, cv1, st1);
    auto cld = [&](int j) { return j < 27 ? __builtin_amdgcn_raw_buffer_load_b128(rs0, cv0 + j * st0, 0, 0) : __builtin_amdgcn_raw_buffer_load_b128(rs1, cv1 + (j - 27) * st1, 0, 0); };
    if (SDELAY) __builtin_amdgcn_s_sleep(SDELAY);
#pragma unroll
    for (int j = 0; j < CW; ++j) c[j] = cld(j);
    SB;
    asm volatile("s_waitcnt vmcnt(%2) lgkmcnt(0)\n\tv_readfirstlane_b32 %0, %1\n\ts_nop 3" : "=s"(tag) : "v"(epoch), "i"(CW), "v"(TCH0) : "memory");
    tag = rfl(tag) + 1;
    static_for<0, 14>([&](auto K) { row_step<K.v>(rs_, lane); });
    lbar();
    row_publish(rs_, ws, tag, tid);
    float cs[8] = {};
    static_for<0, 54>([&](auto J) {
      constexpr int j = J.v;
#pragma unroll
      for (int k = 0; k < 4; ++k) { cs[2 * k] = dot2(c[j % CW][k], k10, cs[2 * k]); cs[2 * k + 1] = dot2(c[j % CW][k], k01, cs[2 * k + 1]); }
      if constexpr (j + CW < 54) c[j % CW] = cld(j + CW);
      SB;
      if constexpr (j == 26) {
        cs_publish(u0, cs);
        STAMP(11);
#pragma unroll
        for (int k = 0; k < 8; ++k) cs[k] = 0.f;
      }
    });
    cs_publish(u1, cs);
    STAMP(12);
    bar();
#pragma unroll 1
    for (unsigned i = w; i < 144; i += 8) dma16(i * 1024, v2, rsWd, urow(i >> 2, rot) + (i & 3) * 10240u + cb * 128u);
    drain_vm();
    bar();
  }
  if (!has2) { STAMP_END; return; }
  relane();

  const unsigned rbA = (G * 6) >> 4, rbB = (G * 6 + 5 > 127 ? 127 : G * 6 + 5) >> 4;
  if (w >= 4) {
    __builtin_amdgcn_s_setprio(HPRIO);
    const unsigned lw = w - 4, ld0 = lw * 1024;
    gu32* fo = (gu32*)(ws + WS_FLAGO + ((lane < 32 ? rbA : rbB) * 32 + (lane & 3)) * FS);
    gu32* fh = (gu32*)(ws + WS_FLAGH + ((lane < 32 ? rbA : rbB) * 32 + ((lane & 31) < 18 ? lane & 31 : 17)) * FS);
    __builtin_amdgcn_s_sleep(HSLEEP);
    if (jtype == J_KV) {
#pragma unroll
      for (int u = 31; u < 36; ++u) dma16(u * 4096 + ld0, v2, rsWd, w2_off(W, u, lw));
    }
    if (jtype != J_MLP) {
#pragma unroll
      for (int u = 0; u < NPRE; ++u) { pre2[u] = w2_load(W, 36 + u, lw, v2); SB; }
    }
#pragma unroll
    for (int u = 36 + NPRE; u < PSPLIT; ++u) { pre2[u - 36] = w2_load(W, u, lw, v2); SB; }
    if (w == 7) {
      __builtin_amdgcn_s_setprio(3);
      const unsigned v = poll_flags(fh, (lane & 31) < 18 && (lane & 31) >= 10, tag);
      lds_set(L_GO);
      if (!all64((lane & 31) >= 18 || v == tag)) poll_flags(fh, (lane & 31) < 18, tag);
      lds_set(L_GO + 4);
      __builtin_amdgcn_s_setprio(HPRIO);
    }
#pragma unroll
    for (int u = PSPLIT; u < HSPLIT; ++u) { pre2[u - 36] = w2_load(W, u, lw, v2); SB; }
    STAMP(8);
    unsigned la[3], wb[3], xo[3], st[3];
    u32x4 xr[3];
    const unsigned ch = lane & 7;
#pragma unroll
    for (int i = 0; i < 3; ++i) {
      const unsigned row = lw * 24 + i * 8 + (lane >> 3), rl = row >> 4, rr = row & 15;
      la[i] = (rl >= 3 ? 24576u + (rl - 3) * 4096u : rl * 4096u) + (ch >> 1) * 1024 + (ch & 1) * 512 + ((rr + 2 * ch) & 15) * 16;
      xo[i] = (G * 96 + row) * 1280 + cb * 128 + ch * 16;
      wb[i] = ld0 + lane * 16 + i * 49152;
      asm volatile("" : "+v"(la[i]), "+v"(wb[i]), "+v"(xo[i]));
    }
    float ri[3], rl[3];
    unsigned of = 0;
    f32x4 xf[3][2], dd[3][2], d2[3][2];
#pragma unroll
    for (int k = 2; k <= 6; ++k) {
      if (k == O_BAR && w == 7 && !all64((lane & 31) >= 4 || of == tag)) poll_flags(fo, (lane & 31) < 4, tag);
      if (k == 6) {
        wait_vm<0>();
#pragma unroll
        for (int h = 0; h < 3; ++h) {
          ri[h] = 2.0f * bfhi(st[h]) * bfhi(st[h]);
          rl[h] = -1.4426950408889634f * __builtin_amdgcn_rcpf(ri[h]);
          xf[h][0] = f32x4{bflo(xr[h][0]), bfhi(xr[h][0]), bflo(xr[h][1]), bfhi(xr[h][1])};
          xf[h][1] = f32x4{bflo(xr[h][2]), bfhi(xr[h][2]), bflo(xr[h][3]), bfhi(xr[h][3])};
          asm volatile("" : "+v"(ri[h]), "+v"(rl[h]), "+v"(xf[h][0]), "+v"(xf[h][1]));
        }
      }
      lbar();
      if (k == O_BAR - 1) { of = __hip_atomic_load(fo, RLX_AGENT); SB; }
      if (k < 6) {
        const int kk = k - 1;
#pragma unroll
        for (int u = 9 * (kk - 1) + ((kk - 1) & 1); u < 9 * kk + (kk & 1); ++u) {
          lds_st<u32x4>(wb[u / 12] + (u % 12) * 4096, pre2[u]);
          if (WSLEEP == 1) __builtin_amdgcn_s_sleep(1);
          if (WSLEEP == 2) asm volatile("s_nop 7");
          SB;
        }
      }
      if (k == 2 || k == 3) {
        constexpr int mid = (HSPLIT + 74) / 2 < 72 ? (HSPLIT + 74) / 2 : 72;
#pragma unroll
        for (int u = (k == 2 ? HSPLIT : mid); u < (k == 2 ? mid : 72); ++u) { pre2[u - 36] = w2_load(W, u, lw, v2); SB; }
        if (k == 3) { dma16(36 * 4096 + ld0, v2, W.wo, w2_off(W, 72, lw)); dma16(37 * 4096 + ld0, v2, W.wo, w2_off(W, 73, lw)); }
      }
      if (k == XK) {
#pragma unroll
        for (int h = 0; h < 3; ++h) {
          const unsigned row = G * 96 + lw * 24 + h * 8 + (lane >> 3);
          st[h] = __builtin_amdgcn_raw_buffer_load_b32(rsWS, WS_STAT + (row < S ? row : 0) * 8, 0, 16);
          xr[h] = __builtin_amdgcn_raw_buffer_load_b128(rsX, xo[h], 0, 0);
        }
      }
    }
    lbar();
#pragma unroll
    for (int i = 0; i < 3; ++i) {
      dd[i][0] = lds_ld<f32x4>(la[i]); d2[i][0] = lds_ld<f32x4>(la[i] + 12288);
      dd[i][1] = lds_ld<f32x4>(la[i] + 256); d2[i][1] = lds_ld<f32x4>(la[i] + 12288 + 256);
      la[i] += 49152;
      asm volatile("" : "+v"(la[i]));
    }
    __builtin_amdgcn_s_setprio(GPRIO);
#pragma unroll
    for (int i = 0; i < 3; ++i) {
      float v[8], e[8];
#pragma unroll
      for (int j = 0; j < 8; ++j) v[j] = sadd(dd[i][j >> 2][j & 3], d2[i][j >> 2][j & 3]);
      SB;
#pragma unroll
      for (int j = 0; j < 8; ++j) e[j] = smul(v[j], rl[i]);
      SB;
#pragma unroll
      for (int j = 0; j < 8; ++j) e[j] = __builtin_amdgcn_exp2f(e[j]);
      SB;
#pragma unroll
      for (int j = 0; j < 8; ++j) e[j] = sfma(e[j], ri[i], ri[i]);
      SB;
#pragma unroll
      for (int j = 0; j < 8; ++j) e[j] = __builtin_amdgcn_rcpf(e[j]);
      SB;
#pragma unroll
      for (int j = 0; j < 8; ++j) dd[i][j >> 2][j & 3] = smul(v[j], e[j]);
      asm volatile("" : "+v"(dd[i][0]), "+v"(dd[i][1]));
      SB;
    }
    const rsrc_t rsO = mk_rsrc(p.out, S * D * 2);
    STAMP(9);
    if (PUTB) bar();
    bar();
    f32x4 ss[3][2];
#pragma unroll
    for (int i = 0; i < 3; ++i) {
      ss[i][0] = lds_ld<f32x4>(la[i]) + lds_ld<f32x4>(la[i] + 12288);
      ss[i][1] = lds_ld<f32x4>(la[i] + 256) + lds_ld<f32x4>(la[i] + 12288 + 256);
    }
#pragma unroll
    for (int i = 0; i < 3; ++i)
      __builtin_amdgcn_raw_buffer_store_b128(cat2(pk4(xf[i][0] + ss[i][0] * dd[i][0]), pk4(xf[i][1] + ss[i][1] * dd[i][1])), rsO, xo[i], 0, 0);
    STAMP(5);
    STAMP_END;
    asm volatile(".Lfb_code_end0:");
    return;
  }
  {
    const unsigned kh = w & 1, rt = G * 6 + 3 * (w >> 1);
    const rsrc_t rsH = mk_rsrc(ws + WS_HA, 8 * RBSZ);
    unsigned tb[3][4];
#pragma unroll
    for (int t = 0; t < 4; ++t) {
      tb[0][t] = (4 * g + q4) * 128 + ((t ^ ((2 * g + (q4 >> 1)) & 3)) << 5) + 8 * p4 + kh * 4096;
      tb[1][t] = tb[0][t] + 49152; tb[2][t] = tb[0][t] + 98304;
      asm volatile("" : "+v"(tb[0][t]), "+v"(tb[1][t]), "+v"(tb[2][t]));
    }
    auto rd1 = [&](int i, int t, int hf) {
      const int sl = i == 36 ? 36 : (2 * i) % 36, bi = sl / 12 > 2 ? 2 : sl / 12;
      return __builtin_bit_cast(u32x2, lds_tr(tb[bi][t] + (sl - 12 * bi) * 4096 + hf * 2048));
    };
    constexpr int NW = P2NW, W0 = P2W0;
    constexpr int OI = O_BAR == 4 ? 17 : (O_BAR == 5 ? 22 : 26);
    constexpr int OB0 = OI == 26 ? OBN : 0;
    f32x4 acc[2][3][4];
    u32x4 a[NW][3];
    u32x2 bw[2][4][2];
    unsigned va[2][3];
#pragma unroll
    for (int h = 0; h < 3; ++h) {
      va[0][h] = lane * 16 + ((rt + h) >> 4) * RBSZ + ((rt + h) & 15) * 3072;
      va[1][h] = va[0][h] + ((rt + h) & 15) * 2048;
      asm volatile("" : "+v"(va[0][h]), "+v"(va[1][h]));
    }
    unsigned xa[4];
#pragma unroll
    for (int t = 0; t < 4; ++t) {
      xa[t] = w * 12288u + t * 1024 + g * 256 + ((r + 4 * t + (g & 2)) & 15) * 16;
      asm volatile("" : "+v"(xa[t]));
    }
    auto put = [&](auto Z, auto E) {
      constexpr int z = Z.v, t = E.v / 3, h = 2 - E.v % 3;
      asm volatile("" : "+v"(acc[z][h][t]));
      lds_st<f32x4>(xa[t] + z * 49152 + h * 4096, acc[z][h][t]);
    };
    auto lda = [&](auto I, auto H) {
      constexpr int i = I.v, h = H.v;
      constexpr unsigned s0 = i < 27 ? (2 * i + rot) % 54 : 2 * i, s1 = i < 27 ? (2 * i + 1 + rot) % 54 : 2 * i + 1;
      a[i % NW][h] = __builtin_amdgcn_raw_buffer_load_b128(rsH, va[i >= 27][h], step_off(s0) + kh * (step_off(s1) - step_off(s0)), 16);
      SB;
    };
    lds_wait(L_GO);
    unsigned go2 = 0;
    STAMP(8);
    static_for<0, W0>([&](auto I) { lda(I, IC<0>{}); lda(I, IC<1>{}); lda(I, IC<2>{}); });
#pragma unroll
    for (int t = 0; t < 4; ++t) { bw[0][t][0] = rd1(0, t, 0); bw[0][t][1] = rd1(0, t, 1); }
    static_for<0, 37>([&](auto I) {
      constexpr int i = I.v;
      constexpr int lim = i + NW - 1;
      constexpr int nxt = i <= OI ? (W0 + 2 * i < lim ? W0 + 2 * i : lim) : (27 + OB0 + 2 * (i - OI - 1) < lim ? 27 + OB0 + 2 * (i - OI - 1) : lim);
      constexpr int end = i <= OI ? 27 : 37;
      constexpr int l0 = nxt < end ? nxt : -1, l1 = (nxt + 1 <= lim && nxt + 1 < end) ? nxt + 1 : -1;
      SB;
      static_for<0, 12>([&](auto E) {
        constexpr int e = E.v, t = e / 3, h = e % 3;
        const u32x4 bf = cat2(bw[i & 1][t][0], bw[i & 1][t][1]);
        if constexpr (i == 0 || i == 27) mfma_z(acc[i >= 27][2 - h][t], bf, a[i % NW][2 - h]); else mfma_ip(acc[i >= 27][2 - h][t], bf, a[i % NW][2 - h]);
        SB;
        if constexpr (i == 4 && e == 2) go2 = lds_get(L_GO + 4);
        if constexpr (!PUTB && (i == 27 || i == 28) && (e & 1)) put(IC<0>{}, IC<(i - 27) * 6 + e / 2>{});
        if constexpr (i == 36 && e >= 8) put(IC<1>{}, IC<e - 8>{});
        if constexpr (i < 36 && h < 2) bw[(i + 1) & 1][t][h] = rd1(i + 1, t, h);
        if constexpr (l1 < 0) { if constexpr (h == 2 && t < 3 && l0 >= 0) lda(IC<l0>{}, IC<t>{}); }
        else if constexpr (e & 1) lda(IC<(e < 6 ? l0 : l1)>{}, IC<(e >> 1) % 3>{});
        SB;
      });
      if constexpr (i == (PUTB ? 33 : 29)) { asm volatile("s_waitcnt lgkmcnt(8)" ::: "memory"); bar(); }
      if constexpr (i == 4) { if (__builtin_expect(!rfl(go2), 0)) lds_wait(L_GO + 4); asm volatile("" ::: "memory"); STAMP(17); }
      if constexpr (i > 4 && i < 27 && (i % 9 == 4 || i % 9 == 8)) {
        bar();
        if constexpr (i == 26 && OI == 26) static_for<27, 27 + OB0>([&](auto J) { lda(J, IC<0>{}); lda(J, IC<1>{}); lda(J, IC<2>{}); });
        if constexpr (PUTB && i == 26) { static_for<0, 12>([&](auto E) { put(IC<0>{}, E); SB; }); lbar(); }
        STAMP(17 + (2 * i) / 9);
      }
    });
    STAMP(24);
    static_for<4, 12>([&](auto E) { if constexpr (E.v == 8) asm volatile("s_nop 3" ::: "memory"); put(IC<1>{}, E); SB; });
    lbar();
    STAMP(5);
    STAMP_END;
    asm volatile(".Lfb_code_end1:");
  }
}

extern "C" void kernel_launch(void* const* d_in, const int* in_sizes, int n_in, void* d_out, int out_size, void* d_ws,
                              size_t ws_size, hipStream_t stream) {
  Params p;
  p.x = d_in[0]; p.wqkv = d_in[1]; p.wo = d_in[2]; p.wg = d_in[4]; p.wu = d_in[5]; p.wd = d_in[6];
  p.out = d_out; p.wsraw = (char*)d_ws;
  p.wsal = ws_size >= (64ull << 20) + WS_END ? (64ull << 20) - 1 : 0;
  fused_block<<<256, 512, 0, stream>>>(p);
}
```

```cpp
#include <hip/hip_runtime.h>
#include <stdint.h>

typedef __attribute__((ext_vector_type(8))) __bf16 bf16x8;
typedef __attribute__((ext_vector_type(4))) __bf16 bf16x4;
typedef __attribute__((ext_vector_type(2))) __bf16 bf16x2;
typedef __attribute__((ext_vector_type(4))) float f32x4;
typedef __attribute__((ext_vector_type(2))) float f32x2;
typedef __attribute__((ext_vector_type(4))) unsigned u32x4;
typedef __attribute__((ext_vector_type(2))) unsigned u32x2;
typedef __amdgpu_buffer_rsrc_t rsrc_t;
#define LAS __attribute__((address_space(3)))
#define GAS __attribute__((address_space(1)))
#define DI __device__ __forceinline__
#define RLX_AGENT __ATOMIC_RELAXED, __HIP_MEMORY_SCOPE_AGENT
typedef GAS unsigned gu32;
typedef GAS unsigned long long gu64;

constexpr int S = 2048, D = 640, F = 1728;
constexpr int NCOL = 5376;
constexpr int NSTEP = 74;
constexpr unsigned WS_EPOCH = 0;
constexpr unsigned FS = 128;
constexpr unsigned WS_FLAGH = 768u << 10;
constexpr unsigned WS_FLAGO = WS_FLAGH + 8 * 32 * FS;
constexpr unsigned WS_FLAGKV = WS_FLAGO + 8 * 32 * FS;
constexpr unsigned WS_STAT = 4096;
constexpr unsigned WS_CSUM = WS_STAT + S * 8;
constexpr unsigned WS_KVP = WS_CSUM + 3 * NCOL * 8;
constexpr unsigned WS_HA = 1u << 20;
constexpr unsigned RBSZ = 16 * NSTEP * 1024;
constexpr unsigned step_off(unsigned st) { return st < 54 ? (st / 3) * 49152 + (st % 3) * 1024 : 18 * 49152 + ((st - 54) / 5) * 81920 + ((st - 54) % 5) * 1024; }
constexpr unsigned WS_DBG = 12u << 20;
constexpr unsigned WS_END = 13u << 20;
constexpr unsigned L_B = 65536;
constexpr unsigned L_SCR = 147456;
constexpr unsigned L_MISC = 159744;
constexpr unsigned L_GO = L_MISC + 3072;
constexpr unsigned SPIN_CAP = 1u << 18;
constexpr int NA1 = 2;
constexpr int NPRE = 12;
constexpr int PSPLIT = 48;
constexpr int HSPLIT = 64;
constexpr int CWIN = 40;
constexpr unsigned ROT = 32;

struct Params {
  const void* x; const void* wqkv; const void* wo; const void* wg; const void* wu; const void* wd;
  void* out; char* wsraw;
  unsigned long long wsal;
};

__shared__ __attribute__((aligned(16))) char smem[163840];

DI float sadd(float a, float b) { float o; asm("v_add_f32 %0, %1, %2" : "=v"(o) : "v"(a), "v"(b)); return o; }
DI float smul(float a, float b) { float o; asm("v_mul_f32 %0, %1, %2" : "=v"(o) : "v"(a), "v"(b)); return o; }
DI float sfma(float a, float b, float c) { float o; asm("v_fma_f32 %0, %1, %2, %3" : "=v"(o) : "v"(a), "v"(b), "v"(c)); return o; }
DI unsigned bsel(unsigned m, unsigned a, unsigned b) { return (m & a) | (~m & b); }
DI float bself(unsigned m, float a, float b) { return __builtin_bit_cast(float, bsel(m, __builtin_bit_cast(unsigned, a), __builtin_bit_cast(unsigned, b))); }
DI bool all64(bool c) { return __builtin_amdgcn_ballot_w64(!c) == 0; }
DI float shfl_x(float v, unsigned lane, unsigned m) {
  return __builtin_bit_cast(float, __builtin_amdgcn_ds_bpermute((int)((lane ^ m) << 2), __builtin_bit_cast(int, v)));
}
DI unsigned umin(unsigned a, unsigned b) { return a < b ? a : b; }
DI unsigned rfl(unsigned v) { return __builtin_amdgcn_readfirstlane(v); }
DI rsrc_t mk_rsrc(const void* p, unsigned bytes) { return __builtin_amdgcn_make_buffer_rsrc((void*)p, (short)0, (int)bytes, 0x00020000); }
DI void dma16(unsigned ldsdst, unsigned voff, rsrc_t rs, unsigned soff) {
  asm volatile("s_mov_b32 m0, %0\n\ts_nop 0\n\tbuffer_load_dwordx4 %1, %2, %3 offen lds" ::"s"(ldsdst), "v"(voff), "s"(rs), "s"(soff) : "memory");
}
template <int N> DI void wait_vm() { asm volatile("s_waitcnt vmcnt(%0)" ::"i"(N) : "memory"); }
DI void drain_vm() { wait_vm<0>(); __builtin_amdgcn_s_waitcnt(0x0F70); }
DI void wait_lgkm0() { asm volatile("s_waitcnt lgkmcnt(0)" ::: "memory"); }
DI void bar() { asm volatile("" ::: "memory"); __builtin_amdgcn_s_barrier(); asm volatile("" ::: "memory"); }
DI void lbar() { wait_lgkm0(); bar(); }
template <class V> DI V lds_ld(unsigned a) { return *(LAS V*)((LAS char*)smem + a); }
template <class V> DI void lds_st(unsigned a, V v) { *(LAS V*)((LAS char*)smem + a) = v; }
DI bf16x4 lds_tr(unsigned a) { return __builtin_amdgcn_ds_read_tr16_b64_v4bf16((LAS bf16x4*)((LAS char*)smem + a)); }
DI f32x4 mfma(bf16x8 a, bf16x8 b, f32x4 c) { return __builtin_amdgcn_mfma_f32_16x16x32_bf16(a, b, c, 0, 0, 0); }
DI unsigned pk2(float a, float b) { f32x2 v = {a, b}; return __builtin_bit_cast(unsigned, __builtin_convertvector(v, bf16x2)); }
DI u32x2 pk4(f32x4 v) { u32x2 r = {pk2(v[0], v[1]), pk2(v[2], v[3])}; return r; }
DI bf16x8 frag(u32x2 lo, u32x2 hi) { u32x4 v = {lo[0], lo[1], hi[0], hi[1]}; return __builtin_bit_cast(bf16x8, v); }
DI float bflo(unsigned v) { return __builtin_bit_cast(float, v << 16); }
DI float bfhi(unsigned v) { return __builtin_bit_cast(float, v & 0xffff0000u); }
DI float rbf(float v) { return bflo(pk2(v, 0.f)); }
DI float relu2(float v) { float o; asm("v_add_f32 %0, %1, |%1|" : "=v"(o) : "v"(v)); return o; }
DI f32x4 phi4(f32x4 u, f32x4 rl, f32x4 rh) {
  const f32x4 l = u * rl;
  f32x4 e, a;
#pragma unroll
  for (int j = 0; j < 4; ++j) {
    e[j] = __builtin_amdgcn_fmed3f(__builtin_amdgcn_exp2f(l[j]), 0.f, 1.f);
    a[j] = relu2(u[j]);
  }
  return a * rh + e + 1e-6f;
}
DI f32x4 splat(float v) { return f32x4{v, v, v, v}; }
template <int CTRL> DI float dpp_add(float v) {
  return v + __builtin_bit_cast(float, __builtin_amdgcn_update_dpp(0, __builtin_bit_cast(int, v), CTRL, 0xf, 0xf, true));
}
DI float dot2(unsigned a, unsigned b, float c) {
  return __builtin_amdgcn_fdot2_f32_bf16(__builtin_bit_cast(bf16x2, a), __builtin_bit_cast(bf16x2, b), c, false);
}
DI void mfma_ip(f32x4& c, const u32x4& a, const u32x4& b) { asm volatile("v_mfma_f32_16x16x32_bf16 %0, %1, %2, %0" : "+v"(c) : "v"(a), "v"(b)); }
DI void mfma_c(f32x4& d, const u32x4& a, const u32x4& b, const f32x4& c) { asm volatile("v_mfma_f32_16x16x32_bf16 %0, %1, %2, %3" : "=&v"(d) : "v"(a), "v"(b), "v"(c)); }
DI void mfma_z(f32x4& c, const u32x4& a, const u32x4& b) { asm volatile("v_mfma_f32_16x16x32_bf16 %0, %1, %2, 0" : "=&v"(c) : "v"(a), "v"(b)); }
template <int N> DI void acc_fence(f32x4* c) {
  asm volatile("s_nop 7" ::: "memory");
#pragma unroll
  for (int i = 0; i < N; ++i) asm volatile("" : "+v"(c[i]));
}
DI u32x4 cat2(u32x2 a, u32x2 b) { return u32x4{a[0], a[1], b[0], b[1]}; }
DI u32x2 ldtr1(unsigned a) { return __builtin_bit_cast(u32x2, lds_tr(a)); }
template <int IMM> DI void set_m0i(unsigned v) { asm volatile("s_add_i32 m0, %0, %1" ::"s"(v), "i"(IMM) : "memory", "scc"); }
template <int OFF> DI void dma_goi(unsigned voff, rsrc_t rs, unsigned soff) {
  asm volatile("buffer_load_dwordx4 %0, %1, %2 offen offset:%3 lds" ::"v"(voff), "s"(rs), "s"(soff), "i"(OFF) : "memory");
}
#define STAMP_DECL
#define STAMP_ARG
#define STAMP_PASS
#define STAMPX(c, i)
#define STAMP_END
#define STAMP(i) STAMPX(__builtin_amdgcn_workitem_id_x() == 0, i)

DI unsigned poll_flags(gu32* word, bool active, unsigned tag) {
  unsigned v = 0;
#pragma unroll 1
  for (unsigned spins = 0; spins < SPIN_CAP; ++spins) {
    v = __hip_atomic_load(word, RLX_AGENT);
    if (all64(!active || v == tag)) break;
    __builtin_amdgcn_s_sleep(2);
  }
  asm volatile("" ::: "memory");
  return v;
}

DI void lds_set(unsigned a) { __hip_atomic_store((LAS unsigned*)smem + a / 4, 1u, __ATOMIC_RELAXED, __HIP_MEMORY_SCOPE_WORKGROUP); }
DI unsigned lds_get(unsigned a) { return __hip_atomic_load((LAS unsigned*)smem + a / 4, __ATOMIC_RELAXED, __HIP_MEMORY_SCOPE_WORKGROUP); }
DI void lds_wait(unsigned a) {
#pragma unroll 1
  for (unsigned spins = 0; spins < (SPIN_CAP << 4); ++spins) {
    if (rfl(lds_get(a))) break;
    __builtin_amdgcn_s_sleep(1);
  }
  asm volatile("" ::: "memory");
}

template <int I> struct IC { static constexpr int v = I; };
template <int B, int E, class Fn> DI void static_for(Fn&& f) {
  if constexpr (B < E) { f(IC<B>{}); static_for<B + 1, E>(f); }
}

enum { J_MLP = 0, J_KV = 1, J_Q = 2, J_SPARE = 3 };

struct Dma {
  rsrc_t rsA, rsB;
  unsigned vA0, vA1, vB0, vB1, vB2;
  unsigned sA[4], sB, advB;
  unsigned wa, wq;
  unsigned mA, mB, mW;
};

constexpr unsigned a_slot(unsigned s3) { return s3 == 0 ? 126976u : (s3 - 1) * 32768u; }
#define SB __builtin_amdgcn_sched_barrier(0)

template <int T> DI unsigned bofs(const unsigned (&boff)[T], int t) { return T == 6 && t >= 3 ? boff[t - 3] + 6144 : boff[t]; }
template <int T, bool TR, int KS, class Extra>
DI void kstep(f32x4 (&acc)[4][T], const u32x4 (&ac)[4], const u32x2 (&bc)[T][2], u32x4 (&an)[4], u32x2 (&bn)[T][2],
              const unsigned (&aoff)[4], const unsigned (&boff)[T], const Dma& d, Extra&& extra) {
  constexpr int s = KS >> 1, KIND = KS & 1, NP = KIND == 0 ? 4 : 3, ST = KIND == 0 ? (4 * T - 1) / NP : 2;
  constexpr int sn = (KS + 1) >> 1;
  constexpr unsigned asl = a_slot(sn % 3), bsl = ((KS + 1) % 5) * 12288u;
  static_for<0, 4 * T>([&](auto E) {
    constexpr int e = E.v, t = e >> 2, i = e & 3;
    const u32x4 bf = cat2(bc[t][0], bc[t][1]);
    if constexpr (KS == 0) { if (TR) mfma_z(acc[i][t], bf, ac[i]); else mfma_z(acc[i][t], ac[i], bf); }
    else { if (TR) mfma_ip(acc[i][t], bf, ac[i]); else mfma_ip(acc[i][t], ac[i], bf); }
    SB;
    if constexpr (KS < 19) {
      if constexpr (e < 4) an[e] = lds_ld<u32x4>(aoff[((KS + 1) & 1) + (sn % 3 == 0 ? 2 : 0)] + (sn % 3 == 0 ? 0u : asl) + e * 2048);
      else if constexpr (e < 4 + 2 * T) bn[(e - 4) >> 1][e & 1] = ldtr1(bofs<T>(boff, (e - 4) >> 1) + bsl + (e & 1) * 768);
    }
    constexpr int pc = e / ST;
    if constexpr (KIND == 0) {
      constexpr int adv = s < 8 ? (s + 2) * 128 : 0, dst = (int)a_slot((s + 2) % 3) + pc * 1024, off = dst >= adv ? adv : 0;
      if constexpr (pc < NP && e % ST == ST - 2) set_m0i<dst - off>(d.mA);
      if constexpr (pc < NP && e % ST == ST - 1) dma_goi<off>((pc & 1) ? d.vA1 : d.vA0, d.rsA, d.sA[pc] + (adv - off));
    } else {
      constexpr int h0 = (2 * s) % 5;
      if constexpr (pc < NP && e % ST == ST - 2) set_m0i<(h0 == 4 ? 0 : h0 * 12288) + pc * 1024>(h0 == 4 ? d.mW : d.mB);
      if constexpr (pc < NP && e % ST == ST - 1) dma_goi<0>(pc == 0 ? d.vB0 : (pc == 1 ? d.vB1 : d.vB2), d.rsB, d.sB);
    }
    extra(E);
    SB;
  });
}

struct RowStat { u32x4 xa; unsigned xb, ep, row; float s, q; };
DI void row_load(RowStat& R, rsrc_t rsX, char* ws, unsigned lane) {
  asm volatile("buffer_load_dwordx4 %0, %3, %5, %6 offen\n\tbuffer_load_dword %1, %4, %5, %6 offen offset:1024\n\tglobal_load_dword %2, %7, %8 sc1"
               : "=&v"(R.xa), "=&v"(R.xb), "=&v"(R.ep) : "v"(lane * 16), "v"(lane * 4), "s"(rsX), "s"(R.row * 1280), "v"(0u), "s"(ws) : "memory");
}
template <int NY> DI unsigned row_wait(const RowStat& R) {
  unsigned e;
  asm volatile("s_waitcnt vmcnt(%2)\n\tv_readfirstlane_b32 %0, %1\n\ts_nop 3" : "=s"(e) : "v"(R.ep), "i"(NY) : "memory");
  return rfl(e) + 1;
}
template <int K> DI void row_step(RowStat& R, unsigned lane) {
  if constexpr (K < 5) {
    if constexpr (K == 0) { R.s = 0.f; R.q = 0.f; }
    const unsigned v = K < 4 ? R.xa[K & 3] : R.xb;
    asm volatile("v_dot2c_f32_bf16 %0, %2, %3\n\tv_dot2c_f32_bf16 %1, %2, %2" : "+v"(R.s), "+v"(R.q) : "v"(v), "v"(0x3f803f80u));
  } else if constexpr (K < 11) {
    constexpr int C = K == 5 ? 0xB1 : (K == 6 ? 0x4E : (K == 7 ? 0x141 : (K == 8 ? 0x140 : (K == 9 ? 0x142 : 0x143))));
    if constexpr (K == 5) asm volatile("s_nop 2" : "+v"(R.s), "+v"(R.q));
    R.s = dpp_add<C>(R.s); R.q = dpp_add<C>(R.q);
  } else if constexpr (K == 11) {
    R.s *= 1.0f / D;
    R.q = __builtin_fmaxf(R.q * (1.0f / D) - R.s * R.s, 0.f);
  } else if constexpr (K == 12) {
    R.q = __builtin_amdgcn_sqrtf(rbf(rbf(R.q) + 1e-5f));
  } else {
    lds_st<unsigned>(lane == 63 ? L_GO + 32 + (R.row & 7) * 4 : L_GO + 64, pk2(R.s, R.q));
  }
  if constexpr (K >= 5 && K < 13) asm volatile("" : "+v"(R.s), "+v"(R.q));
}
DI void row_publish(const RowStat& R, char* ws, unsigned tag, unsigned tid) {
  if (tid < 8) __hip_atomic_store((gu64*)(ws + WS_STAT) + R.row + tid, ((unsigned long long)tag << 32) | lds_ld<unsigned>(L_GO + 32 + tid * 4), RLX_AGENT);
}

struct Gran { u32x2 g0, g1, g2; unsigned off, step; };
DI void gran_issue(Gran& G, rsrc_t rs) {
  asm volatile("buffer_load_dwordx2 %0, %3, %4, 0 offen sc1\n\tbuffer_load_dwordx2 %1, %3, %4, %5 offen sc1\n\t"
               "buffer_load_dwordx2 %2, %3, %4, %6 offen sc1"
               : "=&v"(G.g0), "=&v"(G.g1), "=&v"(G.g2) : "v"(G.off), "s"(rs), "s"(G.step), "s"(2 * G.step) : "memory");
}
DI void gran_load(Gran& G, rsrc_t rs) {
  asm volatile("buffer_load_dwordx2 %0, %3, %4, 0 offen sc1\n\tbuffer_load_dwordx2 %1, %3, %4, %5 offen sc1\n\t"
               "buffer_load_dwordx2 %2, %3, %4, %6 offen sc1\n\ts_waitcnt vmcnt(0)"
               : "=&v"(G.g0), "=&v"(G.g1), "=&v"(G.g2) : "v"(G.off), "s"(rs), "s"(G.step), "s"(2 * G.step) : "memory");
}

DI unsigned urow(unsigned pos) { const unsigned u = pos + ROT; return (u >= 54 ? u - 54 : u) * 40960u; }

constexpr unsigned wo_tile(unsigned v) { return (v / 5) * 10 + (v % 5 == 4 ? 4 : (v % 5 < 2 ? 2 * (v % 5) : 2 * (v % 5) + 1)); }
struct W2 { rsrc_t wd, wo; unsigned col; };
DI unsigned w2_off(const W2& W, int u, unsigned lw) {
  if (u < 54) return urow(u) + lw * 10240u + W.col;
  return wo_tile(u - 54) * 20480u + (lw >> 1) * ((u - 54) % 5 == 4 ? 102400u : 20480u) + (lw & 1) * 10240u + W.col;
}
DI u32x4 w2_load(const W2& W, int u, unsigned lw, unsigned voff) {
  return __builtin_amdgcn_raw_buffer_load_b128(u < 54 ? W.wd : W.wo, voff, w2_off(W, u, lw), 0);
}

template <int T, bool TR, bool PF, bool LASTA, class Pre>
DI void main_loop(Pre&& pre, RowStat& R, char* ws, f32x4 (&acc)[4][T], unsigned (&boff)[T], Dma& d, unsigned w, unsigned lane, unsigned tid,
                  unsigned& tag, unsigned rb, unsigned ccol, rsrc_t rsB2, rsrc_t rsWS, unsigned cb, u32x4 (&pre2)[36] STAMP_ARG) {
  const unsigned g = lane >> 4, r = lane & 15, m = w & 3;
  unsigned aoff[4];
  aoff[0] = (64 * m + r) * 128 + ((g ^ (r >> 1)) << 4);
  aoff[1] = aoff[0] ^ 64;
  aoff[2] = aoff[0] + a_slot(0); aoff[3] = aoff[1] + a_slot(0);
  const unsigned wq = d.wq, wa = d.wa;
  asm volatile("" : "+v"(aoff[0]), "+v"(aoff[1]), "+v"(aoff[2]), "+v"(aoff[3]), "+v"(boff[0]), "+v"(boff[1]), "+v"(boff[2]));
  if (T == 5) asm volatile("" : "+v"(boff[3]), "+v"(boff[4]));
  Gran G;
  unsigned v2, vb2, vpf, tA[3], tB[3];
  row_load(R, d.rsA, ws, lane);
  wait_vm<6 + NA1>();
  lbar();
  STAMP(1);
  u32x4 a0[4], a1[4];
  u32x2 b0[T][2], b1[T][2];
#pragma unroll
  for (int i = 0; i < 4; ++i) a0[i] = lds_ld<u32x4>(aoff[2] + i * 2048);
#pragma unroll
  for (int t = 0; t < T; ++t) { b0[t][0] = ldtr1(bofs<T>(boff, t)); b0[t][1] = ldtr1(bofs<T>(boff, t) + 768); }
  SB;
#pragma unroll
  for (int i = NA1; i < 4; ++i) dma16(a_slot(1) + d.mA + i * 1024, (i & 1) ? d.vA1 : d.vA0, d.rsA, d.sA[i] + 128);
  const bool y = w >= 4;
  bool ok = true;
  auto ln_store = [&] {
    const float v0 = y ? -bflo(G.g0[0]) : __builtin_bit_cast(float, G.g0[0]) + __builtin_bit_cast(float, G.g1[0]) + __builtin_bit_cast(float, G.g2[0]);
    const unsigned la = L_MISC + (y ? tid * 4 - 1024 : 2048 + tid * 4);
    lds_st<float>(la, v0);
    lds_st<float>(y ? la + 1024 : L_GO + 64 + (lane & 7) * 4, __builtin_amdgcn_rcpf(bfhi(G.g0[0])));
  };
  static_for<0, 10>([&](auto S_) {
    constexpr int s = S_.v, L = 4 * T;
    constexpr int NX = (PF && s >= 1 && 2 * s <= NPRE) ? 2 : 0;
    kstep<T, TR, 2 * s>(acc, a0, b0, a1, b1, aoff, boff, d, [&](auto E) {
      constexpr int e = E.v;
      if constexpr (s == 2) {
        if constexpr (e == 2) { vb2 = v2 + (3 * wq) * 10240u; asm volatile("" : "+v"(vb2)); }
        if constexpr (e == 5) { G.off = w < 4 ? WS_CSUM + ccol * 8 : WS_STAT + (rb * 256 + tid - 256) * 8; asm volatile("" : "+v"(G.off)); }
        if constexpr (e == 8) { G.step = w < 4 ? NCOL * 8 : 0; tA[0] = urow(wa) + cb * 128u; asm volatile("" : "+s"(G.step), "+s"(tA[0])); }
        if constexpr (e == 10) { tA[1] = urow(8 + wa) + cb * 128u; asm volatile("" : "+s"(tA[1])); }
        if constexpr (e == 12) { tA[2] = urow(31 + wa) + cb * 128u; asm volatile("" : "+s"(tA[2])); }
        if constexpr (e == 14) {
          tB[0] = urow(16) + cb * 128u; tB[1] = urow(w < 4 ? 19u : 22u) + cb * 128u; tB[2] = urow(w < 4 ? 25u : 28u) + cb * 128u;
          asm volatile("" : "+s"(tB[0]), "+s"(tB[1]), "+s"(tB[2]));
        }
      }
      if constexpr (s == (TR ? 1 : 3) && e == 2) pre(0);
      if constexpr (s == 0 && e == 2) pre(1);
      if constexpr (NX && e >= L - 2) {
        constexpr int u = 2 * (s - 1) + e - (L - 2);
        pre2[u] = __builtin_amdgcn_raw_buffer_load_b128(rsB2, vpf, urow(36 + u) + (w & 3) * 10240u + cb * 128u, 0);
      }
      if constexpr (s < 7) { if constexpr (e == L - 3) d.sB += s == 0 ? d.advB * (1 + (w >> 2)) : d.advB; }
      else if constexpr (s == 7) {
        if constexpr (e == L - 6) d.vB0 = y ? vb2 : d.vB0;
        if constexpr (e == L - 4) d.vB1 = y ? vb2 + 10240u : d.vB1;
        if constexpr (e == L - 3) d.vB2 = y ? vb2 + 20480u : d.vB2;
        if constexpr (e == L - 2) d.sB = y ? tB[0] : d.sB + d.advB;
        if constexpr (e == L - 1) d.rsB = y ? rsB2 : d.rsB;
      } else {
        if constexpr (e == L - 3) { d.sB = tB[s - 7]; d.rsB = rsB2; d.vB0 = vb2; d.vB1 = vb2 + 10240u; d.vB2 = vb2 + 20480u; }
      }
      if constexpr (s == 9 && e == L - 4) {
        asm volatile("s_waitcnt vmcnt(7)" : "+v"(G.g0), "+v"(G.g1), "+v"(G.g2)::"memory");
        ok = all64(G.g0[1] == tag && G.g1[1] == tag && G.g2[1] == tag);
      }
      if constexpr (s == 9 && e == L - 3) ln_store();
    });
    if constexpr (s == 0) {
      tag = row_wait<8 - NA1>(R);
      static_for<0, 14>([&](auto K) { row_step<K.v>(R, lane); });
    }
    if constexpr (s == 9) {
      if (__builtin_expect(!ok, 0)) {
#pragma unroll 1
        for (unsigned spins = 0; spins < SPIN_CAP; ++spins) {
          __builtin_amdgcn_s_sleep(2);
          gran_load(G, rsWS);
          if (all64(G.g0[1] == tag && G.g1[1] == tag && G.g2[1] == tag)) break;
        }
        ln_store();
      }
    }
    wait_lgkm0();
    if constexpr (s < 9) wait_vm<4 + NX>();
    bar();
    if constexpr (s == 0) { STAMP(12); }
    SB;
    kstep<T, TR, 2 * s + 1>(acc, a1, b1, a0, b0, aoff, boff, d, [&](auto E) {
      constexpr int e = E.v;
      if constexpr (s == 0 && e == 6) {
        if (y) {
          const unsigned so = d.sB - d.advB;
          dma16(d.mW + 49152, d.vB0, d.rsB, so); dma16(d.mW + 49152 + 1024, d.vB1, d.rsB, so); dma16(d.mW + 49152 + 2048, d.vB2, d.rsB, so);
        }
      }
      if constexpr (s == 0 && e == L - 4) {
        v2 = (lane >> 3) * 1280 + (((lane & 7) ^ (((lane >> 4) & 3) << 1)) << 4);
        vpf = y ? v2 : 0x7ffffff0u;
        asm volatile("" : "+v"(v2), "+v"(vpf));
      }
      if constexpr (s == 0 && e == L - 6) row_publish(R, ws, tag, tid);
      if constexpr (s == 8 && e == 0) gran_issue(G, rsWS);
      if constexpr (s >= 7 && s < 9 && e >= L - 4) {
        d.rsA = rsB2; d.vA0 = v2; d.vA1 = v2;
        d.sA[e - (L - 4)] = tA[s - 7] + (e - (L - 4)) * 10240;
      }
      if constexpr (s == 9 && LASTA && e == 7) {
        if (wa < 5) {
#pragma unroll
          for (int i = 0; i < 4; ++i) dma16(a_slot(0) + d.mA + i * 1024, v2, rsB2, tA[2] + i * 10240);
        }
      }
    });
    if constexpr (s == 8) { STAMP(13); }
    if constexpr (s == 2) { STAMP(26); }
    if constexpr (s == 6) { STAMP(27); }
  });
  acc_fence<4 * T>(&acc[0][0]);
  STAMP(14);
}

DI void st16(u32x4 v, rsrc_t rs, unsigned voff, unsigned soff) {
  __builtin_amdgcn_raw_buffer_store_b128(v, rs, voff, soff, 16);
  asm volatile("s_nop 1" ::"v"(v) : "memory");
}
DI void publish_flag(char* ws, unsigned flag_off, unsigned tag, unsigned tid) {
  drain_vm();
  bar();
  if (tid == 0) __hip_atomic_store((gu32*)(ws + flag_off), tag, RLX_AGENT);
}
DI void st_frag(rsrc_t rs, unsigned lane, unsigned off, u32x2 lo, u32x2 hi) { st16(cat2(lo, hi), rs, lane * 16, off); }
DI void shared_send(unsigned w, unsigned lane, const u32x2 (&sh)[4]) {
  if (w >= 4) lds_st<u32x4>(L_SCR + w * 1024 + lane * 16, cat2(sh[0], sh[1]));
  else lds_st<u32x4>(L_SCR + w * 1024 + lane * 16, cat2(sh[2], sh[3]));
  asm volatile("" ::: "memory");
}
DI void shared_recv(rsrc_t rs, unsigned w, unsigned lane, unsigned off, unsigned str, const u32x2 (&sh)[4]) {
  lbar();
  u32x4 rcv = lds_ld<u32x4>(L_SCR + (w ^ 4) * 1024 + lane * 16);
  u32x2 r0 = {rcv[0], rcv[1]}, r1 = {rcv[2], rcv[3]};
  if (w >= 4) { st_frag(rs, lane, off + 2 * str, r0, sh[2]); st_frag(rs, lane, off + 3 * str, r1, sh[3]); }
  else        { st_frag(rs, lane, off, sh[0], r0); st_frag(rs, lane, off + str, sh[1], r1); }
}

__global__ __launch_bounds__(512, 2) void fused_block(Params p) {
  char* const ws = (char*)(((unsigned long long)p.wsraw + p.wsal) & ~p.wsal);
  unsigned tid = __builtin_amdgcn_workitem_id_x(), lane = tid & 63;
  const unsigned w = rfl(tid >> 6);
  const unsigned b = __builtin_amdgcn_workgroup_id_x(), xcd = b & 7, slot = b >> 3;
  const unsigned rb = 4 * (xcd & 1) + (slot & 3), jq = xcd >> 1, ji = slot >> 2;
  const unsigned spi = (xcd - 4) * 4 + (slot & 3);
  const unsigned long long jl0 = (jq & 1) ? 0x4123220908070605ull : 0x4021200403020100ull, jl1 = (jq & 1) ? 0x4327266011100f0eull : 0x422524600d0c0b0aull;
  const unsigned jb = (unsigned)(((jq & 2) ? jl1 : jl0) >> (8 * ji)) & 0xff, jtype = jb >> 5, jidx = jb & 31;
  unsigned long long pc0; unsigned codeb;
  asm volatile("s_getpc_b64 %0\n.Lfb_code_mark:\n\ts_mov_b32 %1, .Lfb_code_end0-.Lfb_code_mark\n\ts_max_u32 %1, %1, .Lfb_code_end1-.Lfb_code_mark"
               : "=s"(pc0), "=s"(codeb) :: "scc");
  STAMP_DECL;
  STAMP(0);

  Dma d;
  RowStat rs_;
  const bool mlp = jtype == J_MLP;
  const unsigned pitch = mlp ? 3456u : 3840u, wq = (w + slot) & 3, wa = (w + ji) & 7;
  d.wa = wa; d.wq = wq;
  const rsrc_t rsX = mk_rsrc(p.x, S * D * 2);
  d.vA0 = (lane >> 3) * 1280 + (((lane & 7) ^ (lane >> 4)) << 4); d.vA1 = d.vA0 ^ 64;
  d.rsA = rsX; d.mA = wa * 4096u;
#pragma unroll
  for (int i = 0; i < 4; ++i) d.sA[i] = (rb * 256 + 32 * wa + 8 * i) * 1280;
  unsigned tag;
  asm volatile("s_nop 4" :: "s"(rsX) : "memory");
  STAMP(6);
#pragma unroll
  for (int i = 0; i < 4; ++i) dma16(a_slot(0) + d.mA + i * 1024, (i & 1) ? d.vA1 : d.vA0, d.rsA, d.sA[i]);
  SB;
  {
    unsigned vb[3];
    const unsigned lim = mlp ? 176u : 144u;
    {
      const unsigned r0 = (lane * 43) >> 9, p0 = lane - 12 * r0;
      const bool w1 = p0 >= 8, w2 = p0 >= 4;
      const unsigned r1 = r0 + (w1 ? 6 : 5), p1 = p0 + (w1 ? -8 : 4), r2 = r0 + (w2 ? 11 : 10), p2 = p0 + (w2 ? -4 : 8);
      vb[0] = r0 * pitch + umin(p0 * 16, lim);
      vb[1] = r1 * pitch + umin((p1 * 16) ^ ((r1 & 8) << 2), lim);
      vb[2] = r2 * pitch + umin((p2 * 16) ^ 32, lim);
    }
    d.vB0 = vb[0]; d.vB1 = vb[1]; d.vB2 = vb[2];
    const unsigned mh = wq >> 1;
    const void* bw = mlp ? (mh ? p.wu : p.wg) : p.wqkv;
    const unsigned cj = mlp ? 192u : 160u * jtype, cm = mlp ? 0u : (jtype == J_KV ? 1280u : 160u), c0 = jtype == J_KV ? 1280u : 0u;
    d.rsB = mk_rsrc(bw, D * pitch);
    d.sB = (16 * (wq & 1) + (w < 4 ? 32 : 0)) * pitch + c0 + cj * jidx + cm * mh; d.advB = 64 * pitch;
    d.mB = L_B + wq * 3072u + (w < 4 ? 0u : 12288u); d.mW = L_B + wq * 3072u + (w < 4 ? 49152u : 0u);
    const unsigned bdst = L_B + (w < 4 ? 12288u : 0u) + wq * 3072u;
    rs_.row = rfl(b * 8 + w);
    if (jtype != J_SPARE) {
      dma16(bdst, d.vB0, d.rsB, d.sB); dma16(bdst + 1024, d.vB1, d.rsB, d.sB); dma16(bdst + 2048, d.vB2, d.rsB, d.sB);
      const unsigned so = d.sB + d.advB;
      dma16(bdst + 24576, d.vB0, d.rsB, so); dma16(bdst + 24576 + 1024, d.vB1, d.rsB, so); dma16(bdst + 24576 + 2048, d.vB2, d.rsB, so);
#pragma unroll
      for (int i = 0; i < NA1; ++i) dma16(a_slot(1) + d.mA + i * 1024, (i & 1) ? d.vA1 : d.vA0, d.rsA, d.sA[i] + 128);
    }
    d.sB += d.advB;
  }
  STAMP(10);
  lds_st<unsigned>(L_GO + (tid & 1) * 4, 0u);

  const unsigned t2 = 28 * xcd + slot;
  const bool has2 = slot < 28 && t2 < 220;
  const unsigned G = t2 / 10, cb = t2 % 10;
  unsigned g = lane >> 4, r = lane & 15, q4 = r >> 2, p4 = lane & 3;
  const rsrc_t rsWd = mk_rsrc(p.wd, has2 ? F * D * 2 : 0);
  const rsrc_t rsWS = mk_rsrc(ws, WS_END);
  unsigned v2 = (lane >> 3) * 1280 + (((lane & 7) ^ (((lane >> 4) & 3) << 1)) << 4);
  auto relane = [&] {
    asm volatile("" : "+v"(tid));
    lane = tid & 63; g = lane >> 4; r = lane & 15; q4 = r >> 2; p4 = lane & 3;
    v2 = (lane >> 3) * 1280 + (((lane & 7) ^ (((lane >> 4) & 3) << 1)) << 4);
  };
  const unsigned m = w & 3, n = w >> 2;
  const unsigned brow = L_B + (8 * g + q4) * 192;
  const unsigned bx = (g & 1) << 5;

  u32x4 pre2[36];
  W2 W;
  W.wd = rsWd; W.wo = mk_rsrc(p.wo, has2 ? D * D * 2 : 0); W.col = cb * 128u;
  if (jtype == J_MLP) {
    f32x4 acc[4][6];
    unsigned boff[6];
#pragma unroll
    for (int t = 0; t < 3; ++t) {
      const unsigned gt = n ? (t == 2 ? 3 : 4 + t) : t;
      boff[t] = brow + ((32 * gt + 8 * p4) ^ bx); boff[t + 3] = boff[t] + 6144;
    }
    const unsigned ccol = tid < 96 ? 1920 + 96 * jidx + tid : 3648 + 96 * jidx + (tid < 192 ? tid - 96 : 0);
    main_loop<6, true, true, true>([](int) {}, rs_, ws, acc, boff, d, w, lane, tid, tag, rb, ccol, rsWd, rsWS, cb, pre2 STAMP_PASS);
    relane();
    float mm[4];
    f32x4 cg[3], cu[3];
#pragma unroll
    for (int i = 0; i < 4; ++i) mm[i] = lds_ld<float>(L_MISC + (64 * m + 16 * i + r) * 4);
#pragma unroll
    for (int t = 0; t < 3; ++t) {
      const unsigned gt = n ? (t == 2 ? 3 : 4 + t) : t;
      cg[t] = lds_ld<f32x4>(L_MISC + 2048 + (16 * gt + 4 * g) * 4); cu[t] = lds_ld<f32x4>(L_MISC + 2048 + (96 + 16 * gt + 4 * g) * 4);
    }
    auto hval = [&](int i, int t) {
      f32x4 gg = acc[i][t] + mm[i] * cg[t];
      const f32x4 uu = acc[i][t + 3] + mm[i] * cu[t];
#pragma unroll
      for (int j = 0; j < 4; ++j) gg[j] = relu2(gg[j]);
      return pk4(gg * uu);
    };
    const unsigned ho = WS_HA + rb * RBSZ + jidx * 49152 + m * 4 * 3072;
#pragma unroll
    for (int i = 0; i < 4; ++i) { st_frag(rsWS, lane, ho + i * 3072 + n * 2048, hval(i, 0), hval(i, 1)); SB; }
    const u32x2 sh[4] = {hval(0, 2), hval(1, 2), hval(2, 2), hval(3, 2)};
    shared_send(w, lane, sh);
    shared_recv(rsWS, w, lane, ho + 1024, 3072, sh);
    STAMP(15);
    publish_flag(ws, WS_FLAGH + (rb * 32 + jidx) * FS, tag, tid);
    STAMP(3);
  } else if (jtype == J_KV) {
    f32x4 acc[4][5];
    unsigned boff[5];
    boff[0] = brow + ((80 * n + 8 * p4) ^ bx); boff[1] = boff[0] + 6144;
    boff[2] = brow + ((80 * n + 40 + 8 * p4) ^ bx); boff[3] = boff[2] + 6144;
    boff[4] = brow + ((80 * n + 32 + 40 * (p4 >> 1)) ^ bx) + (p4 & 1) * 6144;
    const unsigned kb = 126976u + m * 6720 + n * 3360, dump = L_MISC - 2048 + lane * 16;
    const unsigned ea = (q4 == 1 || r == 0) ? kb + (r == 0 ? 20 : 12 + r) * 80 : 0u, eb = (q4 == 3 || r == 0) ? kb + 1680 + (r == 0 ? 20 : 4 + r) * 80 : 0u;
    unsigned ka[6] = {kb + r * 80 + g * 16, ea ? ea + g * 16 : dump, eb ? eb + g * 16 : dump, g == 0 ? kb + r * 80 + 64 : dump,
                      g == 2 ? kb + 1680 + r * 80 + 64 : dump, (g == 0 && ea) ? ea + 64 : ((g == 2 && eb) ? eb + 64 : dump)};
    auto pin = [&](int P) { if (P == 0) asm volatile("" : "+v"(ka[0]), "+v"(ka[1]), "+v"(ka[2]), "+v"(ka[3]), "+v"(ka[4]), "+v"(ka[5])); };
    const unsigned ccol = tid < 80 ? 640 + 80 * jidx + tid : 1280 + 80 * jidx + (tid < 160 ? tid - 80 : 0);
    main_loop<5, false, false, false>(pin, rs_, ws, acc, boff, d, w, lane, tid, tag, rb, ccol, rsWd, rsWS, cb, pre2 STAMP_PASS);
    relane();
    float cs[5];
    cs[0] = lds_ld<float>(L_MISC + 2048 + (40 * n + r) * 4);
    cs[1] = lds_ld<float>(L_MISC + 2048 + (80 + 40 * n + r) * 4);
    cs[2] = lds_ld<float>(L_MISC + 2048 + (40 * n + 20 + r) * 4);
    cs[3] = lds_ld<float>(L_MISC + 2048 + (80 + 40 * n + 20 + r) * 4);
    cs[4] = lds_ld<float>(L_MISC + 2048 + ((q4 & 1) * 80 + 40 * n + 16 + 20 * (q4 >> 1) + (r & 3)) * 4);
    u32x2 pk[4][3], vv[4][3];
    unsigned vmask = 0u - (q4 & 1);
    asm volatile("" : "+v"(vmask));
    f32x4 mm4[4], rr4[4];
#pragma unroll
    for (int i = 0; i < 4; ++i) {
      mm4[i] = lds_ld<f32x4>(L_MISC + (64 * m + 16 * i + 4 * g) * 4); rr4[i] = lds_ld<f32x4>(L_MISC + 1024 + (64 * m + 16 * i + 4 * g) * 4);
    }
    SB;
#pragma unroll
    for (int i = 0; i < 4; ++i) {
      const f32x4 mm = mm4[i], rr = rr4[i];
      const f32x4 rl = rr * 1.4426950408889634f, rh = rr * 0.5f;
      f32x4 val[5];
#pragma unroll
      for (int t = 0; t < 5; ++t) val[t] = acc[i][t] + mm * cs[t];
#pragma unroll
      for (int t = 0; t < 3; ++t) pk[i][t] = pk4(phi4(val[2 * t], rl, rh));
      vv[i][0] = pk4(val[1] * rr); vv[i][1] = pk4(val[3] * rr);
      const u32x2 v4 = pk4(val[4] * rr);
      vv[i][2] = u32x2{bsel(vmask, v4[0], 0x3f803f80u), bsel(vmask, v4[1], 0x3f803f80u)};
    }
    STAMP(25);
    f32x4 P[7] = {};
#pragma unroll
    for (int i = 0; i < 4; i += 2) {
      const bf16x8 k0 = frag(pk[i][0], pk[i + 1][0]), k2 = frag(pk[i][1], pk[i + 1][1]), k4 = frag(pk[i][2], pk[i + 1][2]);
      const bf16x8 v1 = frag(vv[i][0], vv[i + 1][0]), v3 = frag(vv[i][1], vv[i + 1][1]), v4 = frag(vv[i][2], vv[i + 1][2]);
      P[0] = mfma(k0, v1, P[0]); P[1] = mfma(k0, v4, P[1]); P[2] = mfma(k4, v1, P[2]);
      P[3] = mfma(k2, v3, P[3]); P[4] = mfma(k2, v4, P[4]); P[5] = mfma(k4, v3, P[5]); P[6] = mfma(k4, v4, P[6]);
    }
    lds_st<f32x4>(ka[0], P[0]); lds_st<f32x4>(ka[0] + 1680, P[3]); lds_st<f32x4>(ka[1], P[1]); lds_st<f32x4>(ka[2], P[4]);
    lds_st<f32x4>(ka[3], P[2]); lds_st<f32x4>(ka[4], P[5]); lds_st<f32x4>(ka[5], P[6]);
    lbar();
    if (tid < 210) {
      const unsigned a = 126976u + tid * 32;
      const f32x4 t0 = (lds_ld<f32x4>(a) + lds_ld<f32x4>(a + 6720)) + (lds_ld<f32x4>(a + 13440) + lds_ld<f32x4>(a + 20160));
      const f32x4 t1 = (lds_ld<f32x4>(a + 16) + lds_ld<f32x4>(a + 6736)) + (lds_ld<f32x4>(a + 13456) + lds_ld<f32x4>(a + 20176));
      st16(cat2(pk4(t0), pk4(t1)), rsWS, tid * 16, WS_KVP + (rb * 32 + 4 * jidx) * 840);
    }
    wait_lgkm0();
    STAMP(15);
    publish_flag(ws, WS_FLAGKV + ((jidx >> 1) * 32 + (jidx & 1) * 8 + rb) * FS, tag, tid);
    STAMP(3);
  } else if (jtype == J_Q) {
    f32x4 acc[4][5];
    unsigned boff[5];
#pragma unroll
    for (int t = 0; t < 5; ++t) boff[t] = brow + n * 6144 + ((32 * t + 8 * p4) ^ bx);
    const unsigned ccol = 160 * jidx + (tid < 160 ? tid : 0);
    unsigned tch[4];
    auto warm = [&](int P) {
      if (P == 1) {
        asm volatile("buffer_load_dword %0, %1, %2, 0 offen" : "=&v"(tch[3]) : "v"((((rb & 3) * 8 + w) * 16 + (lane & 15)) * 128),
                     "s"(mk_rsrc((const void*)pc0, codeb)) : "memory");
        return;
      }
      const unsigned gq = (rb * 4 + jidx) * 8 + w;
      asm volatile("buffer_load_dword %0, %3, %5, 0 offen\n\tbuffer_load_dword %1, %3, %5, %7 offen\n\tbuffer_load_dword %2, %4, %6, 0 offen"
                   : "=&v"(tch[0]), "=&v"(tch[1]), "=&v"(tch[2])
                   : "v"((gq * 34 + (lane < 34 ? lane : 0)) * 128), "v"((gq * 25 + (lane < 25 ? lane : 0)) * 128), "s"(mk_rsrc(p.wd, F * D * 2)),
                     "s"(mk_rsrc(p.wo, D * D * 2)), "s"(8576u * 128u) : "memory");
    };
    main_loop<5, true, false, false>(warm, rs_, ws, acc, boff, d, w, lane, tid, tag, rb, ccol, rsWd, rsWS, cb, pre2 STAMP_PASS);
    relane();
    asm volatile("" :: "v"(tch[0]), "v"(tch[1]), "v"(tch[2]), "v"(tch[3]));
    u32x2 pq[4][5];
    {
      float mm[4], rr[4];
      f32x4 c[5];
#pragma unroll
      for (int i = 0; i < 4; ++i) { mm[i] = lds_ld<float>(L_MISC + (64 * m + 16 * i + r) * 4); rr[i] = lds_ld<float>(L_MISC + 1024 + (64 * m + 16 * i + r) * 4); }
#pragma unroll
      for (int t = 0; t < 5; ++t) c[t] = lds_ld<f32x4>(L_MISC + 2048 + (80 * n + 16 * t + 4 * g) * 4);
      SB;
#pragma unroll
      for (int i = 0; i < 4; ++i)
#pragma unroll
        for (int t = 0; t < 5; ++t) pq[i][t] = pk4(phi4(acc[i][t] + mm[i] * c[t], splat(rr[i] * 1.4426950408889634f), splat(rr[i] * 0.5f)));
#pragma unroll
      for (int i = 0; i < 4; ++i) asm volatile("" : "+v"(pq[i][0]), "+v"(pq[i][1]), "+v"(pq[i][2]), "+v"(pq[i][3]), "+v"(pq[i][4]));
    }
    constexpr unsigned KP = 208, KQ = 84 * KP;
    unsigned wa[2];
#pragma unroll
    for (int k = 0; k < 2; ++k) {
      const unsigned qd = 2 * (tid < 420 ? tid : 0) + k, hd = qd / 105, e = (qd % 105) / 5, dq = qd % 5, hl = hd & 3;
      wa[k] = (hd >> 2) * KQ + (e < 20 ? hl * 20 + e : 80 + hl) * KP + hl * 40 + dq * 8;
    }
    unsigned fk = n * KQ + r * KP + g * 8, fz = n * KQ + (80 + (r & 3)) * KP + g * 8;
    unsigned gm[3] = {g < 1 ? ~0u : 0u, g < 2 ? ~0u : 0u, g < 3 ? ~0u : 0u};
    u32x4 B[4][3];
    f32x4 eps4 = splat(1e-6f);
#pragma unroll
    for (int i = 0; i < 4; ++i) {
      B[i][0] = cat2(pq[i][0], pq[i][1]); B[i][1] = cat2(pq[i][2], pq[i][3]); B[i][2] = cat2(pq[i][4], u32x2{0u, 0u});
      asm volatile("" : "+v"(B[i][0]), "+v"(B[i][1]), "+v"(B[i][2]));
    }
    asm volatile("" : "+v"(wa[0]), "+v"(wa[1]), "+v"(fk), "+v"(fz), "+v"(gm[0]), "+v"(gm[1]), "+v"(gm[2]), "+v"(eps4));
    STAMP(25);
    wait_vm<0>();
    bar();
#pragma unroll
    for (int k = 0; k < 5; ++k) lds_st<u32x4>(k * 8192 + tid * 16, u32x4{0u, 0u, 0u, 0u});
    if (w == 0) poll_flags((gu32*)(ws + WS_FLAGKV + (jidx * 32 + (lane & 15)) * FS), true, tag);
    lbar();
    STAMP(4);
    if (w < 7) {
      u32x4 v[8];
#pragma unroll
      for (int j = 0; j < 8; ++j)
        v[j] = __builtin_amdgcn_raw_buffer_load_b128(rsWS, (tid < 420 ? tid : 0) * 16, WS_KVP + (j * 32 + 8 * jidx) * 840, 16);
      SB;
      f32x4 t0 = {0.f, 0.f, 0.f, 0.f}, t1 = t0;
#pragma unroll
      for (int j = 0; j < 8; ++j) {
        t0 += f32x4{bflo(v[j][0]), bfhi(v[j][0]), bflo(v[j][1]), bfhi(v[j][1])};
        t1 += f32x4{bflo(v[j][2]), bfhi(v[j][2]), bflo(v[j][3]), bfhi(v[j][3])};
      }
      lds_st<u32x2>(wa[0], pk4(t0)); lds_st<u32x2>(wa[1], pk4(t1));
    }
    lbar();
    STAMP(16);
    u32x4 A[12];
#pragma unroll
    for (int P = 0; P < 3; ++P) A[P] = cat2(lds_ld<u32x2>(fz + P * 64), lds_ld<u32x2>(fz + P * 64 + 32));
#pragma unroll
    for (int u = 0; u < 5; ++u) {
      const unsigned a0 = fk + u * 16 * KP + (u < 3 ? 0 : 64);
      A[3 + u] = cat2(lds_ld<u32x2>(a0), lds_ld<u32x2>(a0 + 32));
    }
#pragma unroll
    for (int u = 1; u < 5; ++u) {
      const unsigned a0 = fk + u * 16 * KP + (u < 3 ? 64 : 128);
      A[7 + u] = cat2(lds_ld<u32x2>(a0), lds_ld<u32x2>(a0 + 32));
    }
    SB;
    u32x2 sh[4];
    f32x4 den[4], num[2][5];
    static_for<0, 12>([&](auto E) {
      constexpr int P = E.v >> 2, i = E.v & 3;
      if constexpr (P == 0) mfma_c(den[i], A[0], B[i][0], eps4); else mfma_ip(den[i], A[P], B[i][P]);
    });
    u32x2 o[5];
    const unsigned oo = WS_HA + rb * RBSZ + 18 * 49152 + jidx * 81920 + m * 4 * 5120;
    auto fin = [&](auto I, auto K) {
      constexpr int i = I.v, k = K.v, u = k < 8 ? k >> 1 : 4;
      if constexpr (k == 8 || !(k & 1)) {
        asm volatile("" : "+v"(num[i & 1][u]));
        const float rr = u == 0 ? den[i][0] : (u == 4 ? den[i][3] : bself(gm[(u + 2) % 3], den[i][(u + 3) & 3], den[i][u & 3]));
#pragma unroll
        for (int j = 0; j < 4; ++j) num[i & 1][u][j] = smul(num[i & 1][u][j], rr);
      }
      if constexpr (k == 8 || (k & 1)) o[u] = pk4(num[i & 1][u]);
      if constexpr (k == 3) st_frag(rsWS, lane, oo + i * 5120 + n * 2048, o[0], o[1]);
      if constexpr (k == 7) st_frag(rsWS, lane, oo + i * 5120 + n * 2048 + 1024, o[2], o[3]);
      if constexpr (k == 8) { sh[i] = o[4]; asm volatile("" : "+v"(sh[i])); }
    };
    static_for<0, 4>([&](auto I) {
      constexpr int i = I.v;
      static_for<0, 9>([&](auto K) {
        constexpr int k = K.v, u = k < 5 ? k : k - 4;
        if constexpr (k < 5) mfma_z(num[i & 1][u], A[3 + u], B[i][u < 3 ? 0 : 1]); else mfma_ip(num[i & 1][u], A[7 + u], B[i][u < 3 ? 1 : 2]);
        SB;
        if constexpr (i == 0) {
          if constexpr (k >= 5) {
            asm volatile("" : "+v"(den[k - 5]));
#pragma unroll
            for (int j = 0; j < 4; ++j) den[k - 5][j] = __builtin_amdgcn_rcpf(den[k - 5][j]);
            asm volatile("" : "+v"(den[k - 5]));
          }
        } else {
          fin(IC<i - 1>{}, K);
        }
        SB;
      });
    });
    static_for<0, 9>([&](auto K) { fin(IC<3>{}, K); SB; });
    shared_send(w, lane, sh);
    shared_recv(rsWS, w, lane, oo + 4096, 5120, sh);
    STAMP(15);
    publish_flag(ws, WS_FLAGO + (rb * 32 + jidx) * FS, tag, tid);
    STAMP(3);
    if (b == 224) {
      poll_flags((gu32*)(ws + WS_FLAGH + tid * FS), (tid & 31) < (tid < 256 ? 18u : 4u), tag);
      bar();
      if (tid == 0) __hip_atomic_store((gu32*)(ws + WS_EPOCH), tag, RLX_AGENT);
    }
  } else {
    const unsigned rk = spi * 4 + (w & 3), p0 = w < 4 ? rk : 128 + rk, p1 = p0 + 64;
    auto unit = [](unsigned q) { return q < 60 ? q + 30 : (q < 90 ? q - 60 : q); };
    const unsigned u0 = unit(p0), u1 = unit(p1);
    unsigned k10 = 0x00003f80u, k01 = 0x3f800000u;
    asm volatile("" : "+v"(k10), "+v"(k01));
    auto cs_src = [&](unsigned u, rsrc_t& rs, unsigned& cv, unsigned& st) {
      const unsigned cline = u / 3, third = u % 3;
      const void* cw = cline < 30 ? p.wqkv : (cline < 57 ? p.wg : p.wu);
      const unsigned cpitch = cline < 30 ? 3840u : 3456u;
      const unsigned clocal = cline < 30 ? cline : (cline < 57 ? cline - 30 : cline - 57);
      rs = mk_rsrc(cw, u < 252 ? D * cpitch : 0);
      cv = (third * 216 + (lane >> 3)) * cpitch + clocal * 128 + (lane & 7) * 16;
      st = 8 * cpitch;
    };
    auto cs_publish = [&](unsigned u, float (&cs)[8]) {
#pragma unroll
      for (int k = 0; k < 4; ++k) { const bool hi = lane & 32; const float o = shfl_x(hi ? cs[k] : cs[k + 4], lane, 32); cs[k] = (hi ? cs[k + 4] : cs[k]) + o; }
#pragma unroll
      for (int k = 0; k < 2; ++k) { const bool hi = lane & 16; const float o = shfl_x(hi ? cs[k] : cs[k + 2], lane, 16); cs[k] = (hi ? cs[k + 2] : cs[k]) + o; }
      { const bool hi = lane & 8; const float o = shfl_x(hi ? cs[0] : cs[1], lane, 8); cs[0] = (hi ? cs[1] : cs[0]) + o; }
      if (u < 252)
        __hip_atomic_store((gu64*)(ws + WS_CSUM) + (u % 3) * NCOL + (u / 3) * 64 + (lane & 7) * 8 + (lane >> 3),
                           ((unsigned long long)tag << 32) | __builtin_bit_cast(unsigned, cs[0]), RLX_AGENT);
    };
    row_load(rs_, rsX, ws, lane);
    constexpr int CW = CWIN;
    u32x4 c[CW];
    rsrc_t rs0, rs1; unsigned cv0, cv1, st0, st1;
    cs_src(u0, rs0, cv0, st0); cs_src(u1, rs1, cv1, st1);
    auto cld = [&](int j) { return j < 27 ? __builtin_amdgcn_raw_buffer_load_b128(rs0, cv0 + j * st0, 0, 0) : __builtin_amdgcn_raw_buffer_load_b128(rs1, cv1 + (j - 27) * st1, 0, 0); };
#pragma unroll
    for (int j = 0; j < CW; ++j) c[j] = cld(j);
    SB;
    tag = row_wait<CW>(rs_);
    static_for<0, 14>([&](auto K) { row_step<K.v>(rs_, lane); });
    lbar();
    row_publish(rs_, ws, tag, tid);
    float cs[8] = {};
    static_for<0, 54>([&](auto J) {
      constexpr int j = J.v;
#pragma unroll
      for (int k = 0; k < 4; ++k) { cs[2 * k] = dot2(c[j % CW][k], k10, cs[2 * k]); cs[2 * k + 1] = dot2(c[j % CW][k], k01, cs[2 * k + 1]); }
      if constexpr (j + CW < 54) c[j % CW] = cld(j + CW);
      SB;
      if constexpr (j == 26) {
        cs_publish(u0, cs);
        STAMP(11);
#pragma unroll
        for (int k = 0; k < 8; ++k) cs[k] = 0.f;
      }
    });
    cs_publish(u1, cs);
    STAMP(12);
    bar();
#pragma unroll 1
    for (unsigned i = w; i < 144; i += 8) dma16(i * 1024, v2, rsWd, urow(i >> 2) + (i & 3) * 10240u + cb * 128u);
    drain_vm();
    bar();
  }
  if (!has2) { STAMP_END; return; }
  relane();

  const unsigned rbA = (G * 6) >> 4, rbB = (G * 6 + 5 > 127 ? 127 : G * 6 + 5) >> 4;
  if (w >= 4) {
    const unsigned lw = w - 4, ld0 = lw * 1024;
    gu32* fo = (gu32*)(ws + WS_FLAGO + ((lane < 32 ? rbA : rbB) * 32 + (lane & 3)) * FS);
    gu32* fh = (gu32*)(ws + WS_FLAGH + ((lane < 32 ? rbA : rbB) * 32 + ((lane & 31) < 18 ? lane & 31 : 17)) * FS);
    __builtin_amdgcn_s_sleep(4);
    if (jtype == J_KV) {
#pragma unroll
      for (int u = 31; u < 36; ++u) dma16(u * 4096 + ld0, v2, rsWd, w2_off(W, u, lw));
    }
    if (jtype != J_MLP) {
#pragma unroll
      for (int u = 0; u < NPRE; ++u) { pre2[u] = w2_load(W, 36 + u, lw, v2); SB; }
    }
#pragma unroll
    for (int u = 36 + NPRE; u < PSPLIT; ++u) { pre2[u - 36] = w2_load(W, u, lw, v2); SB; }
    if (w == 7) {
      __builtin_amdgcn_s_setprio(3);
      const unsigned v = poll_flags(fh, (lane & 31) < 18 && (lane & 31) >= 10, tag);
      lds_set(L_GO);
      if (!all64((lane & 31) >= 18 || v == tag)) poll_flags(fh, (lane & 31) < 18, tag);
      lds_set(L_GO + 4);
      __builtin_amdgcn_s_setprio(0);
    }
#pragma unroll
    for (int u = PSPLIT; u < HSPLIT; ++u) { pre2[u - 36] = w2_load(W, u, lw, v2); SB; }
    STAMP(8);
    unsigned la[3], wb[3], xo[3], sto[3];
    u32x2 st[3];
    u32x4 xr[3];
    const unsigned ch = lane & 7;
#pragma unroll
    for (int i = 0; i < 3; ++i) {
      const unsigned row = lw * 24 + i * 8 + (lane >> 3), rl = row >> 4, rr = row & 15;
      la[i] = (rl >= 3 ? 24576u + (rl - 3) * 4096u : rl * 4096u) + (ch >> 1) * 1024 + (ch & 1) * 512 + ((rr + 2 * ch) & 15) * 16;
      xo[i] = (G * 96 + row) * 1280 + cb * 128 + ch * 16;
      wb[i] = ld0 + lane * 16 + i * 49152;
      asm volatile("" : "+v"(la[i]), "+v"(wb[i]), "+v"(xo[i]));
    }
    float ri[3], rl[3];
    unsigned of = 0;
    f32x4 xf[3][2], dd[3][2], d2[3][2];
#pragma unroll
    for (int k = 2; k <= 6; ++k) {
      if (k == 5 && w == 7 && !all64((lane & 31) >= 4 || of == tag)) poll_flags(fo, (lane & 31) < 4, tag);
      if (k == 6) {
        wait_vm<0>();
#pragma unroll 1
        for (unsigned spins = 0; spins < SPIN_CAP && !all64(st[0][1] == tag && st[1][1] == tag && st[2][1] == tag); ++spins) {
#pragma unroll
          for (int h = 0; h < 3; ++h) st[h] = __builtin_amdgcn_raw_buffer_load_b64(rsWS, sto[h], 0, 16);
          asm volatile("" ::: "memory");
        }
#pragma unroll
        for (int h = 0; h < 3; ++h) {
          ri[h] = 2.0f * bfhi(st[h][0]) * bfhi(st[h][0]);
          rl[h] = -1.4426950408889634f * __builtin_amdgcn_rcpf(ri[h]);
          xf[h][0] = f32x4{bflo(xr[h][0]), bfhi(xr[h][0]), bflo(xr[h][1]), bfhi(xr[h][1])};
          xf[h][1] = f32x4{bflo(xr[h][2]), bfhi(xr[h][2]), bflo(xr[h][3]), bfhi(xr[h][3])};
          asm volatile("" : "+v"(ri[h]), "+v"(rl[h]), "+v"(xf[h][0]), "+v"(xf[h][1]));
        }
      }
      lbar();
      if (k == 4) { of = __hip_atomic_load(fo, RLX_AGENT); SB; }
      if (k < 6) {
        const int kk = k - 1;
#pragma unroll
        for (int u = 9 * (kk - 1) + ((kk - 1) & 1); u < 9 * kk + (kk & 1); ++u) {
          lds_st<u32x4>(wb[u / 12] + (u % 12) * 4096, pre2[u]);
          SB;
        }
      }
      if (k == 2 || k == 3) {
        constexpr int mid = (HSPLIT + 74) / 2 < 72 ? (HSPLIT + 74) / 2 : 72;
#pragma unroll
        for (int u = (k == 2 ? HSPLIT : mid); u < (k == 2 ? mid : 72); ++u) { pre2[u - 36] = w2_load(W, u, lw, v2); SB; }
        if (k == 3) { dma16(36 * 4096 + ld0, v2, W.wo, w2_off(W, 72, lw)); dma16(37 * 4096 + ld0, v2, W.wo, w2_off(W, 73, lw)); }
      }
      if (k == 2) {
#pragma unroll
        for (int h = 0; h < 3; ++h) {
          const unsigned row = G * 96 + lw * 24 + h * 8 + (lane >> 3);
          sto[h] = WS_STAT + (row < S ? row : 0) * 8;
          st[h] = __builtin_amdgcn_raw_buffer_load_b64(rsWS, sto[h], 0, 16);
          xr[h] = __builtin_amdgcn_raw_buffer_load_b128(rsX, xo[h], 0, 0);
        }
      }
    }
    lbar();
#pragma unroll
    for (int i = 0; i < 3; ++i) {
      dd[i][0] = lds_ld<f32x4>(la[i]); d2[i][0] = lds_ld<f32x4>(la[i] + 12288);
      dd[i][1] = lds_ld<f32x4>(la[i] + 256); d2[i][1] = lds_ld<f32x4>(la[i] + 12288 + 256);
      la[i] += 49152;
      asm volatile("" : "+v"(la[i]));
    }
#pragma unroll
    for (int i = 0; i < 3; ++i) {
      float v[8], e[8];
#pragma unroll
      for (int j = 0; j < 8; ++j) v[j] = sadd(dd[i][j >> 2][j & 3], d2[i][j >> 2][j & 3]);
      SB;
#pragma unroll
      for (int j = 0; j < 8; ++j) e[j] = smul(v[j], rl[i]);
      SB;
#pragma unroll
      for (int j = 0; j < 8; ++j) e[j] = __builtin_amdgcn_exp2f(e[j]);
      SB; asm volatile("s_nop 0"); SB;
#pragma unroll
      for (int j = 0; j < 8; ++j) e[j] = sfma(e[j], ri[i], ri[i]);
      SB;
#pragma unroll
      for (int j = 0; j < 8; ++j) e[j] = __builtin_amdgcn_rcpf(e[j]);
      SB; asm volatile("s_nop 0"); SB;
#pragma unroll
      for (int j = 0; j < 8; ++j) dd[i][j >> 2][j & 3] = smul(v[j], e[j]);
      asm volatile("" : "+v"(dd[i][0]), "+v"(dd[i][1]));
      SB;
    }
    const rsrc_t rsO = mk_rsrc(p.out, S * D * 2);
    STAMP(9);
    bar();
    f32x4 ss[3][2];
#pragma unroll
    for (int i = 0; i < 3; ++i) {
      ss[i][0] = lds_ld<f32x4>(la[i]) + lds_ld<f32x4>(la[i] + 12288);
      ss[i][1] = lds_ld<f32x4>(la[i] + 256) + lds_ld<f32x4>(la[i] + 12288 + 256);
    }
#pragma unroll
    for (int i = 0; i < 3; ++i)
      __builtin_amdgcn_raw_buffer_store_b128(cat2(pk4(xf[i][0] + ss[i][0] * dd[i][0]), pk4(xf[i][1] + ss[i][1] * dd[i][1])), rsO, xo[i], 0, 0);
    STAMP(5);
    STAMP_END;
    asm volatile(".Lfb_code_end0:");
    return;
  }
  {
    const unsigned kh = w & 1, rt = G * 6 + 3 * (w >> 1);
    const rsrc_t rsH = mk_rsrc(ws + WS_HA, 8 * RBSZ);
    unsigned tb[3][4];
#pragma unroll
    for (int t = 0; t < 4; ++t) {
      tb[0][t] = (4 * g + q4) * 128 + ((t ^ ((2 * g + (q4 >> 1)) & 3)) << 5) + 8 * p4 + kh * 4096;
      tb[1][t] = tb[0][t] + 49152; tb[2][t] = tb[0][t] + 98304;
      asm volatile("" : "+v"(tb[0][t]), "+v"(tb[1][t]), "+v"(tb[2][t]));
    }
    auto rd1 = [&](int i, int t, int hf) {
      const int sl = i == 36 ? 36 : (2 * i) % 36, bi = sl / 12 > 2 ? 2 : sl / 12;
      return __builtin_bit_cast(u32x2, lds_tr(tb[bi][t] + (sl - 12 * bi) * 4096 + hf * 2048));
    };
    constexpr int NW = 7, W0 = 6;
    constexpr int OI = 22;
    f32x4 acc[2][3][4];
    u32x4 a[NW][3];
    u32x2 bw[2][4][2];
    unsigned va[2][3];
#pragma unroll
    for (int h = 0; h < 3; ++h) {
      va[0][h] = lane * 16 + ((rt + h) >> 4) * RBSZ + ((rt + h) & 15) * 3072;
      va[1][h] = va[0][h] + ((rt + h) & 15) * 2048;
      asm volatile("" : "+v"(va[0][h]), "+v"(va[1][h]));
    }
    unsigned xa[4];
#pragma unroll
    for (int t = 0; t < 4; ++t) {
      xa[t] = w * 12288u + t * 1024 + g * 256 + ((r + 4 * t + (g & 2)) & 15) * 16;
      asm volatile("" : "+v"(xa[t]));
    }
    auto put = [&](auto Z, auto E) {
      constexpr int z = Z.v, t = E.v / 3, h = 2 - E.v % 3;
      asm volatile("" : "+v"(acc[z][h][t]));
      lds_st<f32x4>(xa[t] + z * 49152 + h * 4096, acc[z][h][t]);
    };
    auto lda = [&](auto I, auto H) {
      constexpr int i = I.v, h = H.v;
      constexpr unsigned s0 = i < 27 ? (2 * i + ROT) % 54 : 2 * i, s1 = i < 27 ? (2 * i + 1 + ROT) % 54 : 2 * i + 1;
      a[i % NW][h] = __builtin_amdgcn_raw_buffer_load_b128(rsH, va[i >= 27][h], step_off(s0) + kh * (step_off(s1) - step_off(s0)), 16);
      SB;
    };
    lds_wait(L_GO);
    unsigned go2 = 0;
    STAMP(8);
    static_for<0, W0>([&](auto I) { lda(I, IC<0>{}); lda(I, IC<1>{}); lda(I, IC<2>{}); });
#pragma unroll
    for (int t = 0; t < 4; ++t) { bw[0][t][0] = rd1(0, t, 0); bw[0][t][1] = rd1(0, t, 1); }
    static_for<0, 37>([&](auto I) {
      constexpr int i = I.v;
      constexpr int lim = i + NW - 1;
      constexpr int nxt = i <= OI ? (W0 + 2 * i < lim ? W0 + 2 * i : lim) : (27 + 2 * (i - OI - 1) < lim ? 27 + 2 * (i - OI - 1) : lim);
      constexpr int end = i <= OI ? 27 : 37;
      constexpr int l0 = nxt < end ? nxt : -1, l1 = (nxt + 1 <= lim && nxt + 1 < end) ? nxt + 1 : -1;
      SB;
      static_for<0, 12>([&](auto E) {
        constexpr int e = E.v, t = e / 3, h = e % 3;
        const u32x4 bf = cat2(bw[i & 1][t][0], bw[i & 1][t][1]);
        if constexpr (i == 0 || i == 27) mfma_z(acc[i >= 27][2 - h][t], bf, a[i % NW][2 - h]); else mfma_ip(acc[i >= 27][2 - h][t], bf, a[i % NW][2 - h]);
        SB;
        if constexpr (i == 4 && e == 2) go2 = lds_get(L_GO + 4);
        if constexpr ((i == 27 || i == 28) && (e & 1)) put(IC<0>{}, IC<(i - 27) * 6 + e / 2>{});
        if constexpr (i == 36 && e >= 8) put(IC<1>{}, IC<e - 8>{});
        if constexpr (i < 36 && h < 2) bw[(i + 1) & 1][t][h] = rd1(i + 1, t, h);
        if constexpr (l1 < 0) { if constexpr (h == 2 && t < 3 && l0 >= 0) lda(IC<l0>{}, IC<t>{}); }
        else if constexpr (e & 1) lda(IC<(e < 6 ? l0 : l1)>{}, IC<(e >> 1) % 3>{});
        SB;
      });
      if constexpr (i == 29) { asm volatile("s_waitcnt lgkmcnt(8)" ::: "memory"); bar(); }
      if constexpr (i == 4) { if (__builtin_expect(!rfl(go2), 0)) lds_wait(L_GO + 4); asm volatile("" ::: "memory"); STAMP(17); }
      if constexpr (i > 4 && i < 27 && (i % 9 == 4 || i % 9 == 8)) {
        bar();
        STAMP(17 + (2 * i) / 9);
      }
    });
    STAMP(24);
    static_for<4, 12>([&](auto E) { if constexpr (E.v == 8) asm volatile("s_nop 3" ::: "memory"); put(IC<1>{}, E); SB; });
    lbar();
    STAMP(5);
    STAMP_END;
    asm volatile(".Lfb_code_end1:");
  }
}

extern "C" void kernel_launch(void* const* d_in, const int* in_sizes, int n_in, void* d_out, int out_size, void* d_ws,
                              size_t ws_size, hipStream_t stream) {
  Params p;
  p.x = d_in[0]; p.wqkv = d_in[1]; p.wo = d_in[2]; p.wg = d_in[4]; p.wu = d_in[5]; p.wd = d_in[6];
  p.out = d_out; p.wsraw = (char*)d_ws;
  p.wsal = ws_size >= (64ull << 20) + WS_END ? (64ull << 20) - 1 : 0;
  fused_block<<<256, 512, 0, stream>>>(p);
}
```

```cpp
#include <hip/hip_runtime.h>
#include <stdint.h>

typedef __attribute__((ext_vector_type(8))) __bf16 bf16x8;
typedef __attribute__((ext_vector_type(4))) __bf16 bf16x4;
typedef __attribute__((ext_vector_type(2))) __bf16 bf16x2;
typedef __attribute__((ext_vector_type(4))) float f32x4;
typedef __attribute__((ext_vector_type(2))) float f32x2;
typedef __attribute__((ext_vector_type(4))) unsigned u32x4;
typedef __attribute__((ext_vector_type(2))) unsigned u32x2;
typedef __amdgpu_buffer_rsrc_t rsrc_t;
#define LAS __attribute__((address_space(3)))
#define GAS __attribute__((address_space(1)))
#define DI __device__ __forceinline__
#define RLX_AGENT __ATOMIC_RELAXED, __HIP_MEMORY_SCOPE_AGENT
typedef GAS unsigned gu32;
typedef GAS unsigned long long gu64;

constexpr int S = 2048, D = 640, F = 1728;
constexpr int NCOL = 5376;
constexpr int NSTEP = 74;
constexpr unsigned WS_EPOCH = 0;
constexpr unsigned FS = 128;
constexpr unsigned WS_FLAGH = 768u << 10;
constexpr unsigned WS_FLAGO = WS_FLAGH + 8 * 32 * FS;
constexpr unsigned WS_FLAGKV = WS_FLAGO + 8 * 32 * FS;
constexpr unsigned WS_STAT = 4096;
constexpr unsigned WS_CSUM = WS_STAT + S * 8;
constexpr unsigned WS_KVP = WS_CSUM + 3 * NCOL * 8;
constexpr unsigned WS_HA = 1u << 20;
constexpr unsigned RBSZ = 16 * NSTEP * 1024;
constexpr unsigned step_off(unsigned st) { return st < 54 ? (st / 3) * 49152 + (st % 3) * 1024 : 18 * 49152 + ((st - 54) / 5) * 81920 + ((st - 54) % 5) * 1024; }
constexpr unsigned WS_DBG = 12u << 20;
constexpr unsigned WS_END = 13u << 20;
constexpr unsigned long long WS_ALIGN = 64ull << 20;
constexpr unsigned L_B = 65536;
constexpr unsigned L_SCR = 147456;
constexpr unsigned L_MISC = 159744;
constexpr unsigned L_GO = L_MISC + 3072;
constexpr unsigned SPIN_CAP = 1u << 18;
constexpr int NA1 = 2;
constexpr int NPRE = 12;
constexpr int PSPLIT = 48;
constexpr int HSPLIT = 64;
constexpr int CWIN = 40;
constexpr unsigned ROT = 32;

struct Params {
  const void* x; const void* wqkv; const void* wo; const void* wg; const void* wu; const void* wd;
  void* out; char* wsraw;
};

__shared__ __attribute__((aligned(16))) char smem[163840];

DI float sadd(float a, float b) { float o; asm("v_add_f32 %0, %1, %2" : "=v"(o) : "v"(a), "v"(b)); return o; }
DI float smul(float a, float b) { float o; asm("v_mul_f32 %0, %1, %2" : "=v"(o) : "v"(a), "v"(b)); return o; }
DI float sfma(float a, float b, float c) { float o; asm("v_fma_f32 %0, %1, %2, %3" : "=v"(o) : "v"(a), "v"(b), "v"(c)); return o; }
DI unsigned bsel(unsigned m, unsigned a, unsigned b) { return (m & a) | (~m & b); }
DI float bself(unsigned m, float a, float b) { return __builtin_bit_cast(float, bsel(m, __builtin_bit_cast(unsigned, a), __builtin_bit_cast(unsigned, b))); }
DI bool all64(bool c) { return __builtin_amdgcn_ballot_w64(!c) == 0; }
DI float shfl_x(float v, unsigned lane, unsigned m) {
  return __builtin_bit_cast(float, __builtin_amdgcn_ds_bpermute((int)((lane ^ m) << 2), __builtin_bit_cast(int, v)));
}
DI unsigned umin(unsigned a, unsigned b) { return a < b ? a : b; }
DI unsigned rfl(unsigned v) { return __builtin_amdgcn_readfirstlane(v); }
DI rsrc_t mk_rsrc(const void* p, unsigned bytes) { return __builtin_amdgcn_make_buffer_rsrc((void*)p, (short)0, (int)bytes, 0x00020000); }
DI void dma16(unsigned ldsdst, unsigned voff, rsrc_t rs, unsigned soff) {
  asm volatile("s_mov_b32 m0, %0\n\ts_nop 0\n\tbuffer_load_dwordx4 %1, %2, %3 offen lds" ::"s"(ldsdst), "v"(voff), "s"(rs), "s"(soff) : "memory");
}
template <int N> DI void wait_vm() { asm volatile("s_waitcnt vmcnt(%0)" ::"i"(N) : "memory"); }
DI void drain_vm() { wait_vm<0>(); __builtin_amdgcn_s_waitcnt(0x0F70); }
DI void wait_lgkm0() { asm volatile("s_waitcnt lgkmcnt(0)" ::: "memory"); }
DI void bar() { asm volatile("" ::: "memory"); __builtin_amdgcn_s_barrier(); asm volatile("" ::: "memory"); }
DI void lbar() { wait_lgkm0(); bar(); }
template <class V> DI V lds_ld(unsigned a) { return *(LAS V*)((LAS char*)smem + a); }
template <class V> DI void lds_st(unsigned a, V v) { *(LAS V*)((LAS char*)smem + a) = v; }
DI bf16x4 lds_tr(unsigned a) { return __builtin_amdgcn_ds_read_tr16_b64_v4bf16((LAS bf16x4*)((LAS char*)smem + a)); }
DI f32x4 mfma(bf16x8 a, bf16x8 b, f32x4 c) { return __builtin_amdgcn_mfma_f32_16x16x32_bf16(a, b, c, 0, 0, 0); }
DI unsigned pk2(float a, float b) { f32x2 v = {a, b}; return __builtin_bit_cast(unsigned, __builtin_convertvector(v, bf16x2)); }
DI u32x2 pk4(f32x4 v) { u32x2 r = {pk2(v[0], v[1]), pk2(v[2], v[3])}; return r; }
DI bf16x8 frag(u32x2 lo, u32x2 hi) { u32x4 v = {lo[0], lo[1], hi[0], hi[1]}; return __builtin_bit_cast(bf16x8, v); }
DI float bflo(unsigned v) { return __builtin_bit_cast(float, v << 16); }
DI float bfhi(unsigned v) { return __builtin_bit_cast(float, v & 0xffff0000u); }
DI float rbf(float v) { return bflo(pk2(v, 0.f)); }
DI float relu2(float v) { float o; asm("v_add_f32 %0, %1, |%1|" : "=v"(o) : "v"(v)); return o; }
DI f32x4 phi4(f32x4 u, f32x4 rl, f32x4 rh) {
  const f32x4 l = u * rl;
  f32x4 e, a;
#pragma unroll
  for (int j = 0; j < 4; ++j) {
    e[j] = __builtin_amdgcn_fmed3f(__builtin_amdgcn_exp2f(l[j]), 0.f, 1.f);
    a[j] = relu2(u[j]);
  }
  return a * rh + e + 1e-6f;
}
DI f32x4 splat(float v) { return f32x4{v, v, v, v}; }
template <int CTRL> DI float dpp_add(float v) {
  return v + __builtin_bit_cast(float, __builtin_amdgcn_update_dpp(0, __builtin_bit_cast(int, v), CTRL, 0xf, 0xf, true));
}
DI float dot2(unsigned a, unsigned b, float c) {
  return __builtin_amdgcn_fdot2_f32_bf16(__builtin_bit_cast(bf16x2, a), __builtin_bit_cast(bf16x2, b), c, false);
}
DI void mfma_ip(f32x4& c, const u32x4& a, const u32x4& b) { asm volatile("v_mfma_f32_16x16x32_bf16 %0, %1, %2, %0" : "+v"(c) : "v"(a), "v"(b)); }
DI void mfma_c(f32x4& d, const u32x4& a, const u32x4& b, const f32x4& c) { asm volatile("v_mfma_f32_16x16x32_bf16 %0, %1, %2, %3" : "=&v"(d) : "v"(a), "v"(b), "v"(c)); }
DI void mfma_z(f32x4& c, const u32x4& a, const u32x4& b) { asm volatile("v_mfma_f32_16x16x32_bf16 %0, %1, %2, 0" : "=&v"(c) : "v"(a), "v"(b)); }
template <int N> DI void acc_fence(f32x4* c) {
  asm volatile("s_nop 7" ::: "memory");
#pragma unroll
  for (int i = 0; i < N; ++i) asm volatile("" : "+v"(c[i]));
}
DI u32x4 cat2(u32x2 a, u32x2 b) { return u32x4{a[0], a[1], b[0], b[1]}; }
DI u32x2 ldtr1(unsigned a) { return __builtin_bit_cast(u32x2, lds_tr(a)); }
template <int IMM> DI void set_m0i(unsigned v) { asm volatile("s_add_i32 m0, %0, %1" ::"s"(v), "i"(IMM) : "memory", "scc"); }
template <int OFF> DI void dma_goi(unsigned voff, rsrc_t rs, unsigned soff) {
  asm volatile("buffer_load_dwordx4 %0, %1, %2 offen offset:%3 lds" ::"v"(voff), "s"(rs), "s"(soff), "i"(OFF) : "memory");
}
#define STAMP_DECL
#define STAMP_ARG
#define STAMP_PASS
#define STAMPX(c, i)
#define STAMP_END
#define STAMP(i) STAMPX(__builtin_amdgcn_workitem_id_x() == 0, i)

DI unsigned poll_flags(gu32* word, bool active, unsigned tag) {
  unsigned v = 0;
#pragma unroll 1
  for (unsigned spins = 0; spins < SPIN_CAP; ++spins) {
    v = __hip_atomic_load(word, RLX_AGENT);
    if (all64(!active || v == tag)) break;
    __builtin_amdgcn_s_sleep(2);
  }
  asm volatile("" ::: "memory");
  return v;
}

DI void lds_set(unsigned a) { __hip_atomic_store((LAS unsigned*)smem + a / 4, 1u, __ATOMIC_RELAXED, __HIP_MEMORY_SCOPE_WORKGROUP); }
DI unsigned lds_get(unsigned a) { return __hip_atomic_load((LAS unsigned*)smem + a / 4, __ATOMIC_RELAXED, __HIP_MEMORY_SCOPE_WORKGROUP); }
DI void lds_wait(unsigned a) {
#pragma unroll 1
  for (unsigned spins = 0; spins < (SPIN_CAP << 4); ++spins) {
    if (rfl(lds_get(a))) break;
    __builtin_amdgcn_s_sleep(1);
  }
  asm volatile("" ::: "memory");
}

template <int I> struct IC { static constexpr int v = I; };
template <int B, int E, class Fn> DI void static_for(Fn&& f) {
  if constexpr (B < E) { f(IC<B>{}); static_for<B + 1, E>(f); }
}

enum { J_MLP = 0, J_KV = 1, J_Q = 2, J_SPARE = 3 };

struct Dma {
  rsrc_t rsA, rsB;
  unsigned vA0, vA1, vB0, vB1, vB2;
  unsigned sA[4], sB, advB;
  unsigned wa, wq;
  unsigned mA, mB, mW;
};

constexpr unsigned a_slot(unsigned s3) { return s3 == 0 ? 126976u : (s3 - 1) * 32768u; }
#define SB __builtin_amdgcn_sched_barrier(0)

template <int T> DI unsigned bofs(const unsigned (&boff)[T], int t) { return T == 6 && t >= 3 ? boff[t - 3] + 6144 : boff[t]; }
template <int T, bool TR, int KS, class Extra>
DI void kstep(f32x4 (&acc)[4][T], const u32x4 (&ac)[4], const u32x2 (&bc)[T][2], u32x4 (&an)[4], u32x2 (&bn)[T][2],
              const unsigned (&aoff)[4], const unsigned (&boff)[T], const Dma& d, Extra&& extra) {
  constexpr int s = KS >> 1, KIND = KS & 1, NP = KIND == 0 ? 4 : 3, ST = KIND == 0 ? (4 * T - 1) / NP : 2;
  constexpr int sn = (KS + 1) >> 1;
  constexpr unsigned asl = a_slot(sn % 3), bsl = ((KS + 1) % 5) * 12288u;
  static_for<0, 4 * T>([&](auto E) {
    constexpr int e = E.v, t = e >> 2, i = e & 3;
    const u32x4 bf = cat2(bc[t][0], bc[t][1]);
    if constexpr (KS == 0) { if (TR) mfma_z(acc[i][t], bf, ac[i]); else mfma_z(acc[i][t], ac[i], bf); }
    else { if (TR) mfma_ip(acc[i][t], bf, ac[i]); else mfma_ip(acc[i][t], ac[i], bf); }
    SB;
    if constexpr (KS < 19) {
      if constexpr (e < 4) an[e] = lds_ld<u32x4>(aoff[((KS + 1) & 1) + (sn % 3 == 0 ? 2 : 0)] + (sn % 3 == 0 ? 0u : asl) + e * 2048);
      else if constexpr (e < 4 + 2 * T) bn[(e - 4) >> 1][e & 1] = ldtr1(bofs<T>(boff, (e - 4) >> 1) + bsl + (e & 1) * 768);
    }
    constexpr int pc = e / ST;
    if constexpr (KIND == 0) {
      constexpr int adv = s < 8 ? (s + 2) * 128 : 0, dst = (int)a_slot((s + 2) % 3) + pc * 1024, off = dst >= adv ? adv : 0;
      if constexpr (pc < NP && e % ST == ST - 2) set_m0i<dst - off>(d.mA);
      if constexpr (pc < NP && e % ST == ST - 1) dma_goi<off>((pc & 1) ? d.vA1 : d.vA0, d.rsA, d.sA[pc] + (adv - off));
    } else {
      constexpr int h0 = (2 * s) % 5;
      if constexpr (pc < NP && e % ST == ST - 2) set_m0i<(h0 == 4 ? 0 : h0 * 12288) + pc * 1024>(h0 == 4 ? d.mW : d.mB);
      if constexpr (pc < NP && e % ST == ST - 1) dma_goi<0>(pc == 0 ? d.vB0 : (pc == 1 ? d.vB1 : d.vB2), d.rsB, d.sB);
    }
    extra(E);
    SB;
  });
}

struct RowStat { u32x4 xa; unsigned xb, ep, row; float s, q; };
DI void row_load(RowStat& R, rsrc_t rsX, char* ws, unsigned lane) {
  asm volatile("buffer_load_dwordx4 %0, %3, %5, %6 offen\n\tbuffer_load_dword %1, %4, %5, %6 offen offset:1024\n\tglobal_load_dword %2, %7, %8 sc1"
               : "=&v"(R.xa), "=&v"(R.xb), "=&v"(R.ep) : "v"(lane * 16), "v"(lane * 4), "s"(rsX), "s"(R.row * 1280), "v"(0u), "s"(ws) : "memory");
}
template <int NY> DI unsigned row_wait(const RowStat& R) {
  unsigned e;
  asm volatile("s_waitcnt vmcnt(%2)\n\tv_readfirstlane_b32 %0, %1\n\ts_nop 3" : "=s"(e) : "v"(R.ep), "i"(NY) : "memory");
  return rfl(e) + 1;
}
template <int K> DI void row_step(RowStat& R, unsigned lane) {
  if constexpr (K < 5) {
    if constexpr (K == 0) { R.s = 0.f; R.q = 0.f; }
    const unsigned v = K < 4 ? R.xa[K & 3] : R.xb;
    asm volatile("v_dot2c_f32_bf16 %0, %2, %3\n\tv_dot2c_f32_bf16 %1, %2, %2" : "+v"(R.s), "+v"(R.q) : "v"(v), "v"(0x3f803f80u));
  } else if constexpr (K < 11) {
    constexpr int C = K == 5 ? 0xB1 : (K == 6 ? 0x4E : (K == 7 ? 0x141 : (K == 8 ? 0x140 : (K == 9 ? 0x142 : 0x143))));
    if constexpr (K == 5) asm volatile("s_nop 2" : "+v"(R.s), "+v"(R.q));
    R.s = dpp_add<C>(R.s); R.q = dpp_add<C>(R.q);
  } else if constexpr (K == 11) {
    R.s *= 1.0f / D;
    R.q = __builtin_fmaxf(R.q * (1.0f / D) - R.s * R.s, 0.f);
  } else if constexpr (K == 12) {
    R.q = __builtin_amdgcn_sqrtf(rbf(rbf(R.q) + 1e-5f));
  } else {
    lds_st<unsigned>(lane == 63 ? L_GO + 32 + (R.row & 7) * 4 : L_GO + 64, pk2(R.s, R.q));
  }
  if constexpr (K >= 5 && K < 13) asm volatile("" : "+v"(R.s), "+v"(R.q));
}
DI void row_publish(const RowStat& R, char* ws, unsigned tag, unsigned tid) {
  if (tid < 8) __hip_atomic_store((gu64*)(ws + WS_STAT) + R.row + tid, ((unsigned long long)tag << 32) | lds_ld<unsigned>(L_GO + 32 + tid * 4), RLX_AGENT);
}

struct Gran { u32x2 g0, g1, g2; unsigned off, step; };
DI void gran_issue(Gran& G, rsrc_t rs) {
  asm volatile("buffer_load_dwordx2 %0, %3, %4, 0 offen sc1\n\tbuffer_load_dwordx2 %1, %3, %4, %5 offen sc1\n\t"
               "buffer_load_dwordx2 %2, %3, %4, %6 offen sc1"
               : "=&v"(G.g0), "=&v"(G.g1), "=&v"(G.g2) : "v"(G.off), "s"(rs), "s"(G.step), "s"(2 * G.step) : "memory");
}
DI void gran_load(Gran& G, rsrc_t rs) {
  asm volatile("buffer_load_dwordx2 %0, %3, %4, 0 offen sc1\n\tbuffer_load_dwordx2 %1, %3, %4, %5 offen sc1\n\t"
               "buffer_load_dwordx2 %2, %3, %4, %6 offen sc1\n\ts_waitcnt vmcnt(0)"
               : "=&v"(G.g0), "=&v"(G.g1), "=&v"(G.g2) : "v"(G.off), "s"(rs), "s"(G.step), "s"(2 * G.step) : "memory");
}

DI unsigned urow(unsigned pos) { const unsigned u = pos + ROT; return (u >= 54 ? u - 54 : u) * 40960u; }

constexpr unsigned wo_tile(unsigned v) { return (v / 5) * 10 + (v % 5 == 4 ? 4 : (v % 5 < 2 ? 2 * (v % 5) : 2 * (v % 5) + 1)); }
struct W2 { rsrc_t wd, wo; unsigned col; };
DI unsigned w2_off(const W2& W, int u, unsigned lw) {
  if (u < 54) return urow(u) + lw * 10240u + W.col;
  return wo_tile(u - 54) * 20480u + (lw >> 1) * ((u - 54) % 5 == 4 ? 102400u : 20480u) + (lw & 1) * 10240u + W.col;
}
DI u32x4 w2_load(const W2& W, int u, unsigned lw, unsigned voff) {
  return __builtin_amdgcn_raw_buffer_load_b128(u < 54 ? W.wd : W.wo, voff, w2_off(W, u, lw), 0);
}

template <int T, bool TR, bool PF, bool LASTA, class Pre>
DI void main_loop(Pre&& pre, RowStat& R, char* ws, f32x4 (&acc)[4][T], unsigned (&boff)[T], Dma& d, unsigned w, unsigned lane, unsigned tid,
                  unsigned& tag, unsigned rb, unsigned ccol, rsrc_t rsB2, rsrc_t rsWS, unsigned cb, u32x4 (&pre2)[36] STAMP_ARG) {
  const unsigned g = lane >> 4, r = lane & 15, m = w & 3;
  unsigned aoff[4];
  aoff[0] = (64 * m + r) * 128 + ((g ^ (r >> 1)) << 4);
  aoff[1] = aoff[0] ^ 64;
  aoff[2] = aoff[0] + a_slot(0); aoff[3] = aoff[1] + a_slot(0);
  const unsigned wq = d.wq, wa = d.wa;
  asm volatile("" : "+v"(aoff[0]), "+v"(aoff[1]), "+v"(aoff[2]), "+v"(aoff[3]), "+v"(boff[0]), "+v"(boff[1]), "+v"(boff[2]));
  if (T == 5) asm volatile("" : "+v"(boff[3]), "+v"(boff[4]));
  Gran G;
  unsigned v2, vb2, vpf, tA[3], tB[3];
  row_load(R, d.rsA, ws, lane);
  wait_vm<6 + NA1>();
  lbar();
  STAMP(1);
  u32x4 a0[4], a1[4];
  u32x2 b0[T][2], b1[T][2];
#pragma unroll
  for (int i = 0; i < 4; ++i) a0[i] = lds_ld<u32x4>(aoff[2] + i * 2048);
#pragma unroll
  for (int t = 0; t < T; ++t) { b0[t][0] = ldtr1(bofs<T>(boff, t)); b0[t][1] = ldtr1(bofs<T>(boff, t) + 768); }
  SB;
#pragma unroll
  for (int i = NA1; i < 4; ++i) dma16(a_slot(1) + d.mA + i * 1024, (i & 1) ? d.vA1 : d.vA0, d.rsA, d.sA[i] + 128);
  const bool y = w >= 4;
  bool ok = true;
  auto ln_store = [&] {
    const float v0 = y ? -bflo(G.g0[0]) : __builtin_bit_cast(float, G.g0[0]) + __builtin_bit_cast(float, G.g1[0]) + __builtin_bit_cast(float, G.g2[0]);
    const unsigned la = L_MISC + (y ? tid * 4 - 1024 : 2048 + tid * 4);
    lds_st<float>(la, v0);
    lds_st<float>(y ? la + 1024 : L_GO + 64 + (lane & 7) * 4, __builtin_amdgcn_rcpf(bfhi(G.g0[0])));
  };
  static_for<0, 10>([&](auto S_) {
    constexpr int s = S_.v, L = 4 * T;
    constexpr int NX = (PF && s >= 1 && 2 * s <= NPRE) ? 2 : 0;
    kstep<T, TR, 2 * s>(acc, a0, b0, a1, b1, aoff, boff, d, [&](auto E) {
      constexpr int e = E.v;
      if constexpr (s == 2) {
        if constexpr (e == 2) { vb2 = v2 + (3 * wq) * 10240u; asm volatile("" : "+v"(vb2)); }
        if constexpr (e == 5) { G.off = w < 4 ? WS_CSUM + ccol * 8 : WS_STAT + (rb * 256 + tid - 256) * 8; asm volatile("" : "+v"(G.off)); }
        if constexpr (e == 8) { G.step = w < 4 ? NCOL * 8 : 0; tA[0] = urow(wa) + cb * 128u; asm volatile("" : "+s"(G.step), "+s"(tA[0])); }
        if constexpr (e == 10) { tA[1] = urow(8 + wa) + cb * 128u; asm volatile("" : "+s"(tA[1])); }
        if constexpr (e == 12) { tA[2] = urow(31 + wa) + cb * 128u; asm volatile("" : "+s"(tA[2])); }
        if constexpr (e == 14) {
          tB[0] = urow(16) + cb * 128u; tB[1] = urow(w < 4 ? 19u : 22u) + cb * 128u; tB[2] = urow(w < 4 ? 25u : 28u) + cb * 128u;
          asm volatile("" : "+s"(tB[0]), "+s"(tB[1]), "+s"(tB[2]));
        }
      }
      if constexpr (s == (TR ? 1 : 3) && e == 2) pre(0);
      if constexpr (s == 0 && e == 2) pre(1);
      if constexpr (NX && e >= L - 2) {
        constexpr int u = 2 * (s - 1) + e - (L - 2);
        pre2[u] = __builtin_amdgcn_raw_buffer_load_b128(rsB2, vpf, urow(36 + u) + (w & 3) * 10240u + cb * 128u, 0);
      }
      if constexpr (s < 7) { if constexpr (e == L - 3) d.sB += s == 0 ? d.advB * (1 + (w >> 2)) : d.advB; }
      else if constexpr (s == 7) {
        if constexpr (e == L - 6) d.vB0 = y ? vb2 : d.vB0;
        if constexpr (e == L - 4) d.vB1 = y ? vb2 + 10240u : d.vB1;
        if constexpr (e == L - 3) d.vB2 = y ? vb2 + 20480u : d.vB2;
        if constexpr (e == L - 2) d.sB = y ? tB[0] : d.sB + d.advB;
        if constexpr (e == L - 1) d.rsB = y ? rsB2 : d.rsB;
      } else {
        if constexpr (e == L - 3) { d.sB = tB[s - 7]; d.rsB = rsB2; d.vB0 = vb2; d.vB1 = vb2 + 10240u; d.vB2 = vb2 + 20480u; }
      }
      if constexpr (s == 9 && e == L - 4) {
        asm volatile("s_waitcnt vmcnt(7)" : "+v"(G.g0), "+v"(G.g1), "+v"(G.g2)::"memory");
        ok = all64(G.g0[1] == tag && G.g1[1] == tag && G.g2[1] == tag);
      }
      if constexpr (s == 9 && e == L - 3) ln_store();
    });
    if constexpr (s == 0) {
      tag = row_wait<8 - NA1>(R);
      static_for<0, 14>([&](auto K) { row_step<K.v>(R, lane); });
    }
    if constexpr (s == 9) {
      if (__builtin_expect(!ok, 0)) {
#pragma unroll 1
        for (unsigned spins = 0; spins < SPIN_CAP; ++spins) {
          __builtin_amdgcn_s_sleep(2);
          gran_load(G, rsWS);
          if (all64(G.g0[1] == tag && G.g1[1] == tag && G.g2[1] == tag)) break;
        }
        ln_store();
      }
    }
    wait_lgkm0();
    if constexpr (s < 9) wait_vm<4 + NX>();
    bar();
    if constexpr (s == 0) { STAMP(12); }
    SB;
    kstep<T, TR, 2 * s + 1>(acc, a1, b1, a0, b0, aoff, boff, d, [&](auto E) {
      constexpr int e = E.v;
      if constexpr (s == 0 && e == 6) {
        if (y) {
          const unsigned so = d.sB - d.advB;
          dma16(d.mW + 49152, d.vB0, d.rsB, so); dma16(d.mW + 49152 + 1024, d.vB1, d.rsB, so); dma16(d.mW + 49152 + 2048, d.vB2, d.rsB, so);
        }
      }
      if constexpr (s == 0 && e == L - 4) {
        v2 = (lane >> 3) * 1280 + (((lane & 7) ^ (((lane >> 4) & 3) << 1)) << 4);
        vpf = y ? v2 : 0x7ffffff0u;
        asm volatile("" : "+v"(v2), "+v"(vpf));
      }
      if constexpr (s == 0 && e == L - 6) row_publish(R, ws, tag, tid);
      if constexpr (s == 8 && e == 0) gran_issue(G, rsWS);
      if constexpr (s >= 7 && s < 9 && e >= L - 4) {
        d.rsA = rsB2; d.vA0 = v2; d.vA1 = v2;
        d.sA[e - (L - 4)] = tA[s - 7] + (e - (L - 4)) * 10240;
      }
      if constexpr (s == 9 && LASTA && e == 7) {
        if (wa < 5) {
#pragma unroll
          for (int i = 0; i < 4; ++i) dma16(a_slot(0) + d.mA + i * 1024, v2, rsB2, tA[2] + i * 10240);
        }
      }
    });
    if constexpr (s == 8) { STAMP(13); }
    if constexpr (s == 2) { STAMP(26); }
    if constexpr (s == 6) { STAMP(27); }
  });
  acc_fence<4 * T>(&acc[0][0]);
  STAMP(14);
}

DI void st16(u32x4 v, rsrc_t rs, unsigned voff, unsigned soff) {
  __builtin_amdgcn_raw_buffer_store_b128(v, rs, voff, soff, 16);
  asm volatile("s_nop 1" ::"v"(v) : "memory");
}
DI void publish_flag(char* ws, unsigned flag_off, unsigned tag, unsigned tid) {
  drain_vm();
  bar();
  if (tid == 0) __hip_atomic_store((gu32*)(ws + flag_off), tag, RLX_AGENT);
}
DI void st_frag(rsrc_t rs, unsigned lane, unsigned off, u32x2 lo, u32x2 hi) { st16(cat2(lo, hi), rs, lane * 16, off); }
DI void shared_send(unsigned w, unsigned lane, const u32x2 (&sh)[4]) {
  if (w >= 4) lds_st<u32x4>(L_SCR + w * 1024 + lane * 16, cat2(sh[0], sh[1]));
  else lds_st<u32x4>(L_SCR + w * 1024 + lane * 16, cat2(sh[2], sh[3]));
  asm volatile("" ::: "memory");
}
DI void shared_recv(rsrc_t rs, unsigned w, unsigned lane, unsigned off, unsigned str, const u32x2 (&sh)[4]) {
  lbar();
  u32x4 rcv = lds_ld<u32x4>(L_SCR + (w ^ 4) * 1024 + lane * 16);
  u32x2 r0 = {rcv[0], rcv[1]}, r1 = {rcv[2], rcv[3]};
  if (w >= 4) { st_frag(rs, lane, off + 2 * str, r0, sh[2]); st_frag(rs, lane, off + 3 * str, r1, sh[3]); }
  else        { st_frag(rs, lane, off, sh[0], r0); st_frag(rs, lane, off + str, sh[1], r1); }
}

__global__ __launch_bounds__(512, 2) void fused_block(Params p) {
  const unsigned long long wsr = (unsigned long long)p.wsraw;
  char* const ws = (char*)((wsr & 1) ? wsr - 1 : (wsr + WS_ALIGN - 1) & ~(WS_ALIGN - 1));
  unsigned tid = __builtin_amdgcn_workitem_id_x(), lane = tid & 63;
  const unsigned w = rfl(tid >> 6);
  const unsigned b = __builtin_amdgcn_workgroup_id_x(), xcd = b & 7, slot = b >> 3;
  const unsigned rb = 4 * (xcd & 1) + (slot & 3), jq = xcd >> 1, ji = slot >> 2;
  const unsigned spi = (xcd - 4) * 4 + (slot & 3);
  const unsigned long long jl0 = (jq & 1) ? 0x4123220908070605ull : 0x4021200403020100ull, jl1 = (jq & 1) ? 0x4327266011100f0eull : 0x422524600d0c0b0aull;
  const unsigned jb = (unsigned)(((jq & 2) ? jl1 : jl0) >> (8 * ji)) & 0xff, jtype = jb >> 5, jidx = jb & 31;
  STAMP_DECL;
  STAMP(0);

  Dma d;
  RowStat rs_;
  const bool mlp = jtype == J_MLP;
  const unsigned pitch = mlp ? 3456u : 3840u, wq = (w + slot) & 3, wa = (w + ji) & 7;
  d.wa = wa; d.wq = wq;
  const rsrc_t rsX = mk_rsrc(p.x, S * D * 2);
  d.vA0 = (lane >> 3) * 1280 + (((lane & 7) ^ (lane >> 4)) << 4); d.vA1 = d.vA0 ^ 64;
  d.rsA = rsX; d.mA = wa * 4096u;
#pragma unroll
  for (int i = 0; i < 4; ++i) d.sA[i] = (rb * 256 + 32 * wa + 8 * i) * 1280;
  unsigned tag;
  asm volatile("s_nop 4" :: "s"(rsX) : "memory");
  STAMP(6);
#pragma unroll
  for (int i = 0; i < 4; ++i) dma16(a_slot(0) + d.mA + i * 1024, (i & 1) ? d.vA1 : d.vA0, d.rsA, d.sA[i]);
  SB;
  {
    unsigned vb[3];
    const unsigned lim = mlp ? 176u : 144u;
    {
      const unsigned r0 = (lane * 43) >> 9, p0 = lane - 12 * r0;
      const bool w1 = p0 >= 8, w2 = p0 >= 4;
      const unsigned r1 = r0 + (w1 ? 6 : 5), p1 = p0 + (w1 ? -8 : 4), r2 = r0 + (w2 ? 11 : 10), p2 = p0 + (w2 ? -4 : 8);
      vb[0] = r0 * pitch + umin(p0 * 16, lim);
      vb[1] = r1 * pitch + umin((p1 * 16) ^ ((r1 & 8) << 2), lim);
      vb[2] = r2 * pitch + umin((p2 * 16) ^ 32, lim);
    }
    d.vB0 = vb[0]; d.vB1 = vb[1]; d.vB2 = vb[2];
    const unsigned mh = wq >> 1;
    const void* bw = mlp ? (mh ? p.wu : p.wg) : p.wqkv;
    const unsigned cj = mlp ? 192u : 160u * jtype, cm = mlp ? 0u : (jtype == J_KV ? 1280u : 160u), c0 = jtype == J_KV ? 1280u : 0u;
    d.rsB = mk_rsrc(bw, D * pitch);
    d.sB = (16 * (wq & 1) + (w < 4 ? 32 : 0)) * pitch + c0 + cj * jidx + cm * mh; d.advB = 64 * pitch;
    d.mB = L_B + wq * 3072u + (w < 4 ? 0u : 12288u); d.mW = L_B + wq * 3072u + (w < 4 ? 49152u : 0u);
    const unsigned bdst = L_B + (w < 4 ? 12288u : 0u) + wq * 3072u;
    rs_.row = rfl(b * 8 + w);
    if (jtype != J_SPARE) {
      dma16(bdst, d.vB0, d.rsB, d.sB); dma16(bdst + 1024, d.vB1, d.rsB, d.sB); dma16(bdst + 2048, d.vB2, d.rsB, d.sB);
      const unsigned so = d.sB + d.advB;
      dma16(bdst + 24576, d.vB0, d.rsB, so); dma16(bdst + 24576 + 1024, d.vB1, d.rsB, so); dma16(bdst + 24576 + 2048, d.vB2, d.rsB, so);
#pragma unroll
      for (int i = 0; i < NA1; ++i) dma16(a_slot(1) + d.mA + i * 1024, (i & 1) ? d.vA1 : d.vA0, d.rsA, d.sA[i] + 128);
    }
    d.sB += d.advB;
  }
  unsigned long long pc0; unsigned codeb;
  asm volatile("s_getpc_b64 %0\n.Lfb_code_mark:\n\ts_mov_b32 %1, .Lfb_code_end0-.Lfb_code_mark\n\ts_max_u32 %1, %1, .Lfb_code_end1-.Lfb_code_mark"
               : "=s"(pc0), "=s"(codeb) :: "scc");
  STAMP(10);
  lds_st<unsigned>(L_GO + (tid & 1) * 4, 0u);

  const unsigned t2 = 28 * xcd + slot;
  const bool has2 = slot < 28 && t2 < 220;
  const unsigned G = t2 / 10, cb = t2 % 10;
  unsigned g = lane >> 4, r = lane & 15, q4 = r >> 2, p4 = lane & 3;
  const rsrc_t rsWd = mk_rsrc(p.wd, has2 ? F * D * 2 : 0);
  const rsrc_t rsWS = mk_rsrc(ws, WS_END);
  unsigned v2 = (lane >> 3) * 1280 + (((lane & 7) ^ (((lane >> 4) & 3) << 1)) << 4);
  auto relane = [&] {
    asm volatile("" : "+v"(tid));
    lane = tid & 63; g = lane >> 4; r = lane & 15; q4 = r >> 2; p4 = lane & 3;
    v2 = (lane >> 3) * 1280 + (((lane & 7) ^ (((lane >> 4) & 3) << 1)) << 4);
  };
  const unsigned m = w & 3, n = w >> 2;
  const unsigned brow = L_B + (8 * g + q4) * 192;
  const unsigned bx = (g & 1) << 5;

  u32x4 pre2[36];
  W2 W;
  W.wd = rsWd; W.wo = mk_rsrc(p.wo, has2 ? D * D * 2 : 0); W.col = cb * 128u;
  if (jtype == J_MLP) {
    f32x4 acc[4][6];
    unsigned boff[6];
#pragma unroll
    for (int t = 0; t < 3; ++t) {
      const unsigned gt = n ? (t == 2 ? 3 : 4 + t) : t;
      boff[t] = brow + ((32 * gt + 8 * p4) ^ bx); boff[t + 3] = boff[t] + 6144;
    }
    const unsigned ccol = tid < 96 ? 1920 + 96 * jidx + tid : 3648 + 96 * jidx + (tid < 192 ? tid - 96 : 0);
    main_loop<6, true, true, true>([](int) {}, rs_, ws, acc, boff, d, w, lane, tid, tag, rb, ccol, rsWd, rsWS, cb, pre2 STAMP_PASS);
    relane();
    float mm[4];
    f32x4 cg[3], cu[3];
#pragma unroll
    for (int i = 0; i < 4; ++i) mm[i] = lds_ld<float>(L_MISC + (64 * m + 16 * i + r) * 4);
#pragma unroll
    for (int t = 0; t < 3; ++t) {
      const unsigned gt = n ? (t == 2 ? 3 : 4 + t) : t;
      cg[t] = lds_ld<f32x4>(L_MISC + 2048 + (16 * gt + 4 * g) * 4); cu[t] = lds_ld<f32x4>(L_MISC + 2048 + (96 + 16 * gt + 4 * g) * 4);
    }
    auto hval = [&](int i, int t) {
      f32x4 gg = acc[i][t] + mm[i] * cg[t];
      const f32x4 uu = acc[i][t + 3] + mm[i] * cu[t];
#pragma unroll
      for (int j = 0; j < 4; ++j) gg[j] = relu2(gg[j]);
      return pk4(gg * uu);
    };
    const unsigned ho = WS_HA + rb * RBSZ + jidx * 49152 + m * 4 * 3072;
#pragma unroll
    for (int i = 0; i < 4; ++i) { st_frag(rsWS, lane, ho + i * 3072 + n * 2048, hval(i, 0), hval(i, 1)); SB; }
    const u32x2 sh[4] = {hval(0, 2), hval(1, 2), hval(2, 2), hval(3, 2)};
    shared_send(w, lane, sh);
    shared_recv(rsWS, w, lane, ho + 1024, 3072, sh);
    STAMP(15);
    publish_flag(ws, WS_FLAGH + (rb * 32 + jidx) * FS, tag, tid);
    STAMP(3);
  } else if (jtype == J_KV) {
    f32x4 acc[4][5];
    unsigned boff[5];
    boff[0] = brow + ((80 * n + 8 * p4) ^ bx); boff[1] = boff[0] + 6144;
    boff[2] = brow + ((80 * n + 40 + 8 * p4) ^ bx); boff[3] = boff[2] + 6144;
    boff[4] = brow + ((80 * n + 32 + 40 * (p4 >> 1)) ^ bx) + (p4 & 1) * 6144;
    const unsigned kb = 126976u + m * 6720 + n * 3360, dump = L_MISC - 2048 + lane * 16;
    const unsigned ea = (q4 == 1 || r == 0) ? kb + (r == 0 ? 20 : 12 + r) * 80 : 0u, eb = (q4 == 3 || r == 0) ? kb + 1680 + (r == 0 ? 20 : 4 + r) * 80 : 0u;
    unsigned ka[6] = {kb + r * 80 + g * 16, ea ? ea + g * 16 : dump, eb ? eb + g * 16 : dump, g == 0 ? kb + r * 80 + 64 : dump,
                      g == 2 ? kb + 1680 + r * 80 + 64 : dump, (g == 0 && ea) ? ea + 64 : ((g == 2 && eb) ? eb + 64 : dump)};
    auto pin = [&](int P) { if (P == 0) asm volatile("" : "+v"(ka[0]), "+v"(ka[1]), "+v"(ka[2]), "+v"(ka[3]), "+v"(ka[4]), "+v"(ka[5])); };
    const unsigned ccol = tid < 80 ? 640 + 80 * jidx + tid : 1280 + 80 * jidx + (tid < 160 ? tid - 80 : 0);
    main_loop<5, false, false, false>(pin, rs_, ws, acc, boff, d, w, lane, tid, tag, rb, ccol, rsWd, rsWS, cb, pre2 STAMP_PASS);
    relane();
    float cs[5];
    cs[0] = lds_ld<float>(L_MISC + 2048 + (40 * n + r) * 4);
    cs[1] = lds_ld<float>(L_MISC + 2048 + (80 + 40 * n + r) * 4);
    cs[2] = lds_ld<float>(L_MISC + 2048 + (40 * n + 20 + r) * 4);
    cs[3] = lds_ld<float>(L_MISC + 2048 + (80 + 40 * n + 20 + r) * 4);
    cs[4] = lds_ld<float>(L_MISC + 2048 + ((q4 & 1) * 80 + 40 * n + 16 + 20 * (q4 >> 1) + (r & 3)) * 4);
    u32x2 pk[4][3], vv[4][3];
    unsigned vmask = 0u - (q4 & 1);
    asm volatile("" : "+v"(vmask));
    f32x4 mm4[4], rr4[4];
#pragma unroll
    for (int i = 0; i < 4; ++i) {
      mm4[i] = lds_ld<f32x4>(L_MISC + (64 * m + 16 * i + 4 * g) * 4); rr4[i] = lds_ld<f32x4>(L_MISC + 1024 + (64 * m + 16 * i + 4 * g) * 4);
    }
    SB;
#pragma unroll
    for (int i = 0; i < 4; ++i) {
      const f32x4 mm = mm4[i], rr = rr4[i];
      const f32x4 rl = rr * 1.4426950408889634f, rh = rr * 0.5f;
      f32x4 val[5];
#pragma unroll
      for (int t = 0; t < 5; ++t) val[t] = acc[i][t] + mm * cs[t];
#pragma unroll
      for (int t = 0; t < 3; ++t) pk[i][t] = pk4(phi4(val[2 * t], rl, rh));
      vv[i][0] = pk4(val[1] * rr); vv[i][1] = pk4(val[3] * rr);
      const u32x2 v4 = pk4(val[4] * rr);
      vv[i][2] = u32x2{bsel(vmask, v4[0], 0x3f803f80u), bsel(vmask, v4[1], 0x3f803f80u)};
    }
    STAMP(25);
    f32x4 P[7] = {};
#pragma unroll
    for (int i = 0; i < 4; i += 2) {
      const bf16x8 k0 = frag(pk[i][0], pk[i + 1][0]), k2 = frag(pk[i][1], pk[i + 1][1]), k4 = frag(pk[i][2], pk[i + 1][2]);
      const bf16x8 v1 = frag(vv[i][0], vv[i + 1][0]), v3 = frag(vv[i][1], vv[i + 1][1]), v4 = frag(vv[i][2], vv[i + 1][2]);
      P[0] = mfma(k0, v1, P[0]); P[1] = mfma(k0, v4, P[1]); P[2] = mfma(k4, v1, P[2]);
      P[3] = mfma(k2, v3, P[3]); P[4] = mfma(k2, v4, P[4]); P[5] = mfma(k4, v3, P[5]); P[6] = mfma(k4, v4, P[6]);
    }
    lds_st<f32x4>(ka[0], P[0]); lds_st<f32x4>(ka[0] + 1680, P[3]); lds_st<f32x4>(ka[1], P[1]); lds_st<f32x4>(ka[2], P[4]);
    lds_st<f32x4>(ka[3], P[2]); lds_st<f32x4>(ka[4], P[5]); lds_st<f32x4>(ka[5], P[6]);
    lbar();
    if (tid < 210) {
      const unsigned a = 126976u + tid * 32;
      const f32x4 t0 = (lds_ld<f32x4>(a) + lds_ld<f32x4>(a + 6720)) + (lds_ld<f32x4>(a + 13440) + lds_ld<f32x4>(a + 20160));
      const f32x4 t1 = (lds_ld<f32x4>(a + 16) + lds_ld<f32x4>(a + 6736)) + (lds_ld<f32x4>(a + 13456) + lds_ld<f32x4>(a + 20176));
      st16(cat2(pk4(t0), pk4(t1)), rsWS, tid * 16, WS_KVP + (rb * 32 + 4 * jidx) * 840);
    }
    wait_lgkm0();
    STAMP(15);
    publish_flag(ws, WS_FLAGKV + ((jidx >> 1) * 32 + (jidx & 1) * 8 + rb) * FS, tag, tid);
    STAMP(3);
  } else if (jtype == J_Q) {
    f32x4 acc[4][5];
    unsigned boff[5];
#pragma unroll
    for (int t = 0; t < 5; ++t) boff[t] = brow + n * 6144 + ((32 * t + 8 * p4) ^ bx);
    const unsigned ccol = 160 * jidx + (tid < 160 ? tid : 0);
    unsigned tch[4];
    auto warm = [&](int P) {
      if (P == 1) {
        asm volatile("buffer_load_dword %0, %1, %2, 0 offen" : "=&v"(tch[3]) : "v"((((rb & 3) * 8 + w) * 16 + (lane & 15)) * 128),
                     "s"(mk_rsrc((const void*)pc0, codeb)) : "memory");
        return;
      }
      const unsigned gq = (rb * 4 + jidx) * 8 + w;
      asm volatile("buffer_load_dword %0, %3, %5, 0 offen\n\tbuffer_load_dword %1, %3, %5, %7 offen\n\tbuffer_load_dword %2, %4, %6, 0 offen"
                   : "=&v"(tch[0]), "=&v"(tch[1]), "=&v"(tch[2])
                   : "v"((gq * 34 + (lane < 34 ? lane : 0)) * 128), "v"((gq * 25 + (lane < 25 ? lane : 0)) * 128), "s"(mk_rsrc(p.wd, F * D * 2)),
                     "s"(mk_rsrc(p.wo, D * D * 2)), "s"(8576u * 128u) : "memory");
    };
    main_loop<5, true, false, false>(warm, rs_, ws, acc, boff, d, w, lane, tid, tag, rb, ccol, rsWd, rsWS, cb, pre2 STAMP_PASS);
    relane();
    asm volatile("" :: "v"(tch[0]), "v"(tch[1]), "v"(tch[2]), "v"(tch[3]));
    u32x2 pq[4][5];
    {
      float mm[4], rr[4];
      f32x4 c[5];
#pragma unroll
      for (int i = 0; i < 4; ++i) { mm[i] = lds_ld<float>(L_MISC + (64 * m + 16 * i + r) * 4); rr[i] = lds_ld<float>(L_MISC + 1024 + (64 * m + 16 * i + r) * 4); }
#pragma unroll
      for (int t = 0; t < 5; ++t) c[t] = lds_ld<f32x4>(L_MISC + 2048 + (80 * n + 16 * t + 4 * g) * 4);
      SB;
#pragma unroll
      for (int i = 0; i < 4; ++i)
#pragma unroll
        for (int t = 0; t < 5; ++t) pq[i][t] = pk4(phi4(acc[i][t] + mm[i] * c[t], splat(rr[i] * 1.4426950408889634f), splat(rr[i] * 0.5f)));
#pragma unroll
      for (int i = 0; i < 4; ++i) asm volatile("" : "+v"(pq[i][0]), "+v"(pq[i][1]), "+v"(pq[i][2]), "+v"(pq[i][3]), "+v"(pq[i][4]));
    }
    constexpr unsigned KP = 208, KQ = 84 * KP;
    unsigned wa[2];
#pragma unroll
    for (int k = 0; k < 2; ++k) {
      const unsigned qd = 2 * (tid < 420 ? tid : 0) + k, hd = qd / 105, e = (qd % 105) / 5, dq = qd % 5, hl = hd & 3;
      wa[k] = (hd >> 2) * KQ + (e < 20 ? hl * 20 + e : 80 + hl) * KP + hl * 40 + dq * 8;
    }
    unsigned fk = n * KQ + r * KP + g * 8, fz = n * KQ + (80 + (r & 3)) * KP + g * 8;
    unsigned gm[3] = {g < 1 ? ~0u : 0u, g < 2 ? ~0u : 0u, g < 3 ? ~0u : 0u};
    u32x4 B[4][3];
    f32x4 eps4 = splat(1e-6f);
#pragma unroll
    for (int i = 0; i < 4; ++i) {
      B[i][0] = cat2(pq[i][0], pq[i][1]); B[i][1] = cat2(pq[i][2], pq[i][3]); B[i][2] = cat2(pq[i][4], u32x2{0u, 0u});
      asm volatile("" : "+v"(B[i][0]), "+v"(B[i][1]), "+v"(B[i][2]));
    }
    asm volatile("" : "+v"(wa[0]), "+v"(wa[1]), "+v"(fk), "+v"(fz), "+v"(gm[0]), "+v"(gm[1]), "+v"(gm[2]), "+v"(eps4));
    STAMP(25);
    wait_vm<0>();
    bar();
#pragma unroll
    for (int k = 0; k < 5; ++k) lds_st<u32x4>(k * 8192 + tid * 16, u32x4{0u, 0u, 0u, 0u});
    if (w == 0) poll_flags((gu32*)(ws + WS_FLAGKV + (jidx * 32 + (lane & 15)) * FS), true, tag);
    lbar();
    STAMP(4);
    if (w < 7) {
      u32x4 v[8];
#pragma unroll
      for (int j = 0; j < 8; ++j)
        v[j] = __builtin_amdgcn_raw_buffer_load_b128(rsWS, (tid < 420 ? tid : 0) * 16, WS_KVP + (j * 32 + 8 * jidx) * 840, 16);
      SB;
      f32x4 t0 = {0.f, 0.f, 0.f, 0.f}, t1 = t0;
#pragma unroll
      for (int j = 0; j < 8; ++j) {
        t0 += f32x4{bflo(v[j][0]), bfhi(v[j][0]), bflo(v[j][1]), bfhi(v[j][1])};
        t1 += f32x4{bflo(v[j][2]), bfhi(v[j][2]), bflo(v[j][3]), bfhi(v[j][3])};
      }
      lds_st<u32x2>(wa[0], pk4(t0)); lds_st<u32x2>(wa[1], pk4(t1));
    }
    lbar();
    STAMP(16);
    u32x4 A[12];
#pragma unroll
    for (int P = 0; P < 3; ++P) A[P] = cat2(lds_ld<u32x2>(fz + P * 64), lds_ld<u32x2>(fz + P * 64 + 32));
#pragma unroll
    for (int u = 0; u < 5; ++u) {
      const unsigned a0 = fk + u * 16 * KP + (u < 3 ? 0 : 64);
      A[3 + u] = cat2(lds_ld<u32x2>(a0), lds_ld<u32x2>(a0 + 32));
    }
#pragma unroll
    for (int u = 1; u < 5; ++u) {
      const unsigned a0 = fk + u * 16 * KP + (u < 3 ? 64 : 128);
      A[7 + u] = cat2(lds_ld<u32x2>(a0), lds_ld<u32x2>(a0 + 32));
    }
    SB;
    u32x2 sh[4];
    f32x4 den[4], num[2][5];
    static_for<0, 12>([&](auto E) {
      constexpr int P = E.v >> 2, i = E.v & 3;
      if constexpr (P == 0) mfma_c(den[i], A[0], B[i][0], eps4); else mfma_ip(den[i], A[P], B[i][P]);
    });
    u32x2 o[5];
    const unsigned oo = WS_HA + rb * RBSZ + 18 * 49152 + jidx * 81920 + m * 4 * 5120;
    auto fin = [&](auto I, auto K) {
      constexpr int i = I.v, k = K.v, u = k < 8 ? k >> 1 : 4;
      if constexpr (k == 8 || !(k & 1)) {
        asm volatile("" : "+v"(num[i & 1][u]));
        const float rr = u == 0 ? den[i][0] : (u == 4 ? den[i][3] : bself(gm[(u + 2) % 3], den[i][(u + 3) & 3], den[i][u & 3]));
#pragma unroll
        for (int j = 0; j < 4; ++j) num[i & 1][u][j] = smul(num[i & 1][u][j], rr);
      }
      if constexpr (k == 8 || (k & 1)) o[u] = pk4(num[i & 1][u]);
      if constexpr (k == 3) st_frag(rsWS, lane, oo + i * 5120 + n * 2048, o[0], o[1]);
      if constexpr (k == 7) st_frag(rsWS, lane, oo + i * 5120 + n * 2048 + 1024, o[2], o[3]);
      if constexpr (k == 8) { sh[i] = o[4]; asm volatile("" : "+v"(sh[i])); }
    };
    static_for<0, 4>([&](auto I) {
      constexpr int i = I.v;
      static_for<0, 9>([&](auto K) {
        constexpr int k = K.v, u = k < 5 ? k : k - 4;
        if constexpr (k < 5) mfma_z(num[i & 1][u], A[3 + u], B[i][u < 3 ? 0 : 1]); else mfma_ip(num[i & 1][u], A[7 + u], B[i][u < 3 ? 1 : 2]);
        SB;
        if constexpr (i == 0) {
          if constexpr (k >= 5) {
            asm volatile("" : "+v"(den[k - 5]));
#pragma unroll
            for (int j = 0; j < 4; ++j) den[k - 5][j] = __builtin_amdgcn_rcpf(den[k - 5][j]);
            asm volatile("" : "+v"(den[k - 5]));
          }
        } else {
          fin(IC<i - 1>{}, K);
        }
        SB;
      });
    });
    static_for<0, 9>([&](auto K) { fin(IC<3>{}, K); SB; });
    shared_send(w, lane, sh);
    shared_recv(rsWS, w, lane, oo + 4096, 5120, sh);
    STAMP(15);
    publish_flag(ws, WS_FLAGO + (rb * 32 + jidx) * FS, tag, tid);
    STAMP(3);
    if (b == 224) {
      poll_flags((gu32*)(ws + WS_FLAGH + tid * FS), (tid & 31) < (tid < 256 ? 18u : 4u), tag);
      bar();
      if (tid == 0) __hip_atomic_store((gu32*)(ws + WS_EPOCH), tag, RLX_AGENT);
    }
  } else {
    const unsigned rk = spi * 4 + (w & 3), p0 = w < 4 ? rk : 128 + rk, p1 = p0 + 64;
    auto unit = [](unsigned q) { return q < 60 ? q + 30 : (q < 90 ? q - 60 : q); };
    const unsigned u0 = unit(p0), u1 = unit(p1);
    unsigned k10 = 0x00003f80u, k01 = 0x3f800000u;
    asm volatile("" : "+v"(k10), "+v"(k01));
    auto cs_src = [&](unsigned u, rsrc_t& rs, unsigned& cv, unsigned& st) {
      const unsigned cline = u / 3, third = u % 3;
      const void* cw = cline < 30 ? p.wqkv : (cline < 57 ? p.wg : p.wu);
      const unsigned cpitch = cline < 30 ? 3840u : 3456u;
      const unsigned clocal = cline < 30 ? cline : (cline < 57 ? cline - 30 : cline - 57);
      rs = mk_rsrc(cw, u < 252 ? D * cpitch : 0);
      cv = (third * 216 + (lane >> 3)) * cpitch + clocal * 128 + (lane & 7) * 16;
      st = 8 * cpitch;
    };
    auto cs_publish = [&](unsigned u, float (&cs)[8]) {
#pragma unroll
      for (int k = 0; k < 4; ++k) { const bool hi = lane & 32; const float o = shfl_x(hi ? cs[k] : cs[k + 4], lane, 32); cs[k] = (hi ? cs[k + 4] : cs[k]) + o; }
#pragma unroll
      for (int k = 0; k < 2; ++k) { const bool hi = lane & 16; const float o = shfl_x(hi ? cs[k] : cs[k + 2], lane, 16); cs[k] = (hi ? cs[k + 2] : cs[k]) + o; }
      { const bool hi = lane & 8; const float o = shfl_x(hi ? cs[0] : cs[1], lane, 8); cs[0] = (hi ? cs[1] : cs[0]) + o; }
      if (u < 252)
        __hip_atomic_store((gu64*)(ws + WS_CSUM) + (u % 3) * NCOL + (u / 3) * 64 + (lane & 7) * 8 + (lane >> 3),
                           ((unsigned long long)tag << 32) | __builtin_bit_cast(unsigned, cs[0]), RLX_AGENT);
    };
    row_load(rs_, rsX, ws, lane);
    constexpr int CW = CWIN;
    u32x4 c[CW];
    rsrc_t rs0, rs1; unsigned cv0, cv1, st0, st1;
    cs_src(u0, rs0, cv0, st0); cs_src(u1, rs1, cv1, st1);
    auto cld = [&](int j) { return j < 27 ? __builtin_amdgcn_raw_buffer_load_b128(rs0, cv0 + j * st0, 0, 0) : __builtin_amdgcn_raw_buffer_load_b128(rs1, cv1 + (j - 27) * st1, 0, 0); };
#pragma unroll
    for (int j = 0; j < CW; ++j) c[j] = cld(j);
    SB;
    tag = row_wait<CW>(rs_);
    static_for<0, 14>([&](auto K) { row_step<K.v>(rs_, lane); });
    lbar();
    row_publish(rs_, ws, tag, tid);
    float cs[8] = {};
    static_for<0, 54>([&](auto J) {
      constexpr int j = J.v;
#pragma unroll
      for (int k = 0; k < 4; ++k) { cs[2 * k] = dot2(c[j % CW][k], k10, cs[2 * k]); cs[2 * k + 1] = dot2(c[j % CW][k], k01, cs[2 * k + 1]); }
      if constexpr (j + CW < 54) c[j % CW] = cld(j + CW);
      SB;
      if constexpr (j == 26) {
        cs_publish(u0, cs);
        STAMP(11);
#pragma unroll
        for (int k = 0; k < 8; ++k) cs[k] = 0.f;
      }
    });
    cs_publish(u1, cs);
    STAMP(12);
    bar();
#pragma unroll 1
    for (unsigned i = w; i < 144; i += 8) dma16(i * 1024, v2, rsWd, urow(i >> 2) + (i & 3) * 10240u + cb * 128u);
    drain_vm();
    bar();
  }
  if (!has2) { STAMP_END; return; }
  relane();

  const unsigned rbA = (G * 6) >> 4, rbB = (G * 6 + 5 > 127 ? 127 : G * 6 + 5) >> 4;
  if (w >= 4) {
    const unsigned lw = w - 4, ld0 = lw * 1024;
    gu32* fo = (gu32*)(ws + WS_FLAGO + ((lane < 32 ? rbA : rbB) * 32 + (lane & 3)) * FS);
    gu32* fh = (gu32*)(ws + WS_FLAGH + ((lane < 32 ? rbA : rbB) * 32 + ((lane & 31) < 18 ? lane & 31 : 17)) * FS);
    __builtin_amdgcn_s_sleep(4);
    if (jtype == J_KV) {
#pragma unroll
      for (int u = 31; u < 36; ++u) dma16(u * 4096 + ld0, v2, rsWd, w2_off(W, u, lw));
    }
    if (jtype != J_MLP) {
#pragma unroll
      for (int u = 0; u < NPRE; ++u) { pre2[u] = w2_load(W, 36 + u, lw, v2); SB; }
    }
#pragma unroll
    for (int u = 36 + NPRE; u < PSPLIT; ++u) { pre2[u - 36] = w2_load(W, u, lw, v2); SB; }
    if (w == 7) {
      __builtin_amdgcn_s_setprio(3);
      const unsigned v = poll_flags(fh, (lane & 31) < 18 && (lane & 31) >= 10, tag);
      lds_set(L_GO);
      if (!all64((lane & 31) >= 18 || v == tag)) poll_flags(fh, (lane & 31) < 18, tag);
      lds_set(L_GO + 4);
      __builtin_amdgcn_s_setprio(0);
    }
#pragma unroll
    for (int u = PSPLIT; u < HSPLIT; ++u) { pre2[u - 36] = w2_load(W, u, lw, v2); SB; }
    STAMP(8);
    unsigned la[3], wb[3], xo[3], sto[3];
    u32x2 st[3];
    u32x4 xr[3];
    const unsigned ch = lane & 7;
#pragma unroll
    for (int i = 0; i < 3; ++i) {
      const unsigned row = lw * 24 + i * 8 + (lane >> 3), rl = row >> 4, rr = row & 15;
      la[i] = (rl >= 3 ? 24576u + (rl - 3) * 4096u : rl * 4096u) + (ch >> 1) * 1024 + (ch & 1) * 512 + ((rr + 2 * ch) & 15) * 16;
      xo[i] = (G * 96 + row) * 1280 + cb * 128 + ch * 16;
      wb[i] = ld0 + lane * 16 + i * 49152;
      asm volatile("" : "+v"(la[i]), "+v"(wb[i]), "+v"(xo[i]));
    }
    float ri[3], rl[3];
    unsigned of = 0;
    f32x4 xf[3][2], dd[3][2], d2[3][2];
#pragma unroll
    for (int k = 2; k <= 6; ++k) {
      if (k == 5 && w == 7 && !all64((lane & 31) >= 4 || of == tag)) poll_flags(fo, (lane & 31) < 4, tag);
      if (k == 6) {
        wait_vm<0>();
#pragma unroll 1
        for (unsigned spins = 0; spins < SPIN_CAP && !all64(st[0][1] == tag && st[1][1] == tag && st[2][1] == tag); ++spins) {
#pragma unroll
          for (int h = 0; h < 3; ++h) st[h] = __builtin_amdgcn_raw_buffer_load_b64(rsWS, sto[h], 0, 16);
          asm volatile("" ::: "memory");
        }
#pragma unroll
        for (int h = 0; h < 3; ++h) {
          ri[h] = 2.0f * bfhi(st[h][0]) * bfhi(st[h][0]);
          rl[h] = -1.4426950408889634f * __builtin_amdgcn_rcpf(ri[h]);
          xf[h][0] = f32x4{bflo(xr[h][0]), bfhi(xr[h][0]), bflo(xr[h][1]), bfhi(xr[h][1])};
          xf[h][1] = f32x4{bflo(xr[h][2]), bfhi(xr[h][2]), bflo(xr[h][3]), bfhi(xr[h][3])};
          asm volatile("" : "+v"(ri[h]), "+v"(rl[h]), "+v"(xf[h][0]), "+v"(xf[h][1]));
        }
      }
      lbar();
      if (k == 4) { of = __hip_atomic_load(fo, RLX_AGENT); SB; }
      if (k < 6) {
        const int kk = k - 1;
#pragma unroll
        for (int u = 9 * (kk - 1) + ((kk - 1) & 1); u < 9 * kk + (kk & 1); ++u) {
          lds_st<u32x4>(wb[u / 12] + (u % 12) * 4096, pre2[u]);
          SB;
        }
      }
      if (k == 2 || k == 3) {
        constexpr int mid = (HSPLIT + 74) / 2 < 72 ? (HSPLIT + 74) / 2 : 72;
#pragma unroll
        for (int u = (k == 2 ? HSPLIT : mid); u < (k == 2 ? mid : 72); ++u) { pre2[u - 36] = w2_load(W, u, lw, v2); SB; }
        if (k == 3) { dma16(36 * 4096 + ld0, v2, W.wo, w2_off(W, 72, lw)); dma16(37 * 4096 + ld0, v2, W.wo, w2_off(W, 73, lw)); }
      }
      if (k == 2) {
#pragma unroll
        for (int h = 0; h < 3; ++h) {
          const unsigned row = G * 96 + lw * 24 + h * 8 + (lane >> 3);
          sto[h] = WS_STAT + (row < S ? row : 0) * 8;
          st[h] = __builtin_amdgcn_raw_buffer_load_b64(rsWS, sto[h], 0, 16);
          xr[h] = __builtin_amdgcn_raw_buffer_load_b128(rsX, xo[h], 0, 0);
        }
      }
    }
    lbar();
#pragma unroll
    for (int i = 0; i < 3; ++i) {
      dd[i][0] = lds_ld<f32x4>(la[i]); d2[i][0] = lds_ld<f32x4>(la[i] + 12288);
      dd[i][1] = lds_ld<f32x4>(la[i] + 256); d2[i][1] = lds_ld<f32x4>(la[i] + 12288 + 256);
      la[i] += 49152;
      asm volatile("" : "+v"(la[i]));
    }
#pragma unroll
    for (int i = 0; i < 3; ++i) {
      float v[8], e[8];
#pragma unroll
      for (int j = 0; j < 8; ++j) v[j] = sadd(dd[i][j >> 2][j & 3], d2[i][j >> 2][j & 3]);
      SB;
#pragma unroll
      for (int j = 0; j < 8; ++j) e[j] = smul(v[j], rl[i]);
      SB;
#pragma unroll
      for (int j = 0; j < 8; ++j) e[j] = __builtin_amdgcn_exp2f(e[j]);
      SB; asm volatile("s_nop 0"); SB;
#pragma unroll
      for (int j = 0; j < 8; ++j) e[j] = sfma(e[j], ri[i], ri[i]);
      SB;
#pragma unroll
      for (int j = 0; j < 8; ++j) e[j] = __builtin_amdgcn_rcpf(e[j]);
      SB; asm volatile("s_nop 0"); SB;
#pragma unroll
      for (int j = 0; j < 8; ++j) dd[i][j >> 2][j & 3] = smul(v[j], e[j]);
      asm volatile("" : "+v"(dd[i][0]), "+v"(dd[i][1]));
      SB;
    }
    const rsrc_t rsO = mk_rsrc(p.out, S * D * 2);
    STAMP(9);
    bar();
    f32x4 ss[3][2];
#pragma unroll
    for (int i = 0; i < 3; ++i) {
      ss[i][0] = lds_ld<f32x4>(la[i]) + lds_ld<f32x4>(la[i] + 12288);
      ss[i][1] = lds_ld<f32x4>(la[i] + 256) + lds_ld<f32x4>(la[i] + 12288 + 256);
    }
#pragma unroll
    for (int i = 0; i < 3; ++i)
      __builtin_amdgcn_raw_buffer_store_b128(cat2(pk4(xf[i][0] + ss[i][0] * dd[i][0]), pk4(xf[i][1] + ss[i][1] * dd[i][1])), rsO, xo[i], 0, 0);
    STAMP(5);
    STAMP_END;
    asm volatile(".Lfb_code_end0:");
    return;
  }
  {
    const unsigned kh = w & 1, rt = G * 6 + 3 * (w >> 1);
    const rsrc_t rsH = mk_rsrc(ws + WS_HA, 8 * RBSZ);
    unsigned tb[3][4];
#pragma unroll
    for (int t = 0; t < 4; ++t) {
      tb[0][t] = (4 * g + q4) * 128 + ((t ^ ((2 * g + (q4 >> 1)) & 3)) << 5) + 8 * p4 + kh * 4096;
      tb[1][t] = tb[0][t] + 49152; tb[2][t] = tb[0][t] + 98304;
      asm volatile("" : "+v"(tb[0][t]), "+v"(tb[1][t]), "+v"(tb[2][t]));
    }
    auto rd1 = [&](int i, int t, int hf) {
      const int sl = i == 36 ? 36 : (2 * i) % 36, bi = sl / 12 > 2 ? 2 : sl / 12;
      return __builtin_bit_cast(u32x2, lds_tr(tb[bi][t] + (sl - 12 * bi) * 4096 + hf * 2048));
    };
    constexpr int NW = 7, W0 = 6;
    constexpr int OI = 22;
    f32x4 acc[2][3][4];
    u32x4 a[NW][3];
    u32x2 bw[2][4][2];
    unsigned va[2][3];
#pragma unroll
    for (int h = 0; h < 3; ++h) {
      va[0][h] = lane * 16 + ((rt + h) >> 4) * RBSZ + ((rt + h) & 15) * 3072;
      va[1][h] = va[0][h] + ((rt + h) & 15) * 2048;
      asm volatile("" : "+v"(va[0][h]), "+v"(va[1][h]));
    }
    unsigned xa[4];
#pragma unroll
    for (int t = 0; t < 4; ++t) {
      xa[t] = w * 12288u + t * 1024 + g * 256 + ((r + 4 * t + (g & 2)) & 15) * 16;
      asm volatile("" : "+v"(xa[t]));
    }
    auto put = [&](auto Z, auto E) {
      constexpr int z = Z.v, t = E.v / 3, h = 2 - E.v % 3;
      asm volatile("" : "+v"(acc[z][h][t]));
      lds_st<f32x4>(xa[t] + z * 49152 + h * 4096, acc[z][h][t]);
    };
    auto lda = [&](auto I, auto H) {
      constexpr int i = I.v, h = H.v;
      constexpr unsigned s0 = i < 27 ? (2 * i + ROT) % 54 : 2 * i, s1 = i < 27 ? (2 * i + 1 + ROT) % 54 : 2 * i + 1;
      a[i % NW][h] = __builtin_amdgcn_raw_buffer_load_b128(rsH, va[i >= 27][h], step_off(s0) + kh * (step_off(s1) - step_off(s0)), 16);
      SB;
    };
    lds_wait(L_GO);
    unsigned go2 = 0;
    STAMP(8);
    static_for<0, W0>([&](auto I) { lda(I, IC<0>{}); lda(I, IC<1>{}); lda(I, IC<2>{}); });
#pragma unroll
    for (int t = 0; t < 4; ++t) { bw[0][t][0] = rd1(0, t, 0); bw[0][t][1] = rd1(0, t, 1); }
    static_for<0, 37>([&](auto I) {
      constexpr int i = I.v;
      constexpr int lim = i + NW - 1;
      constexpr int nxt = i <= OI ? (W0 + 2 * i < lim ? W0 + 2 * i : lim) : (27 + 2 * (i - OI - 1) < lim ? 27 + 2 * (i - OI - 1) : lim);
      constexpr int end = i <= OI ? 27 : 37;
      constexpr int l0 = nxt < end ? nxt : -1, l1 = (nxt + 1 <= lim && nxt + 1 < end) ? nxt + 1 : -1;
      SB;
      static_for<0, 12>([&](auto E) {
        constexpr int e = E.v, t = e / 3, h = e % 3;
        const u32x4 bf = cat2(bw[i & 1][t][0], bw[i & 1][t][1]);
        if constexpr (i == 0 || i == 27) mfma_z(acc[i >= 27][2 - h][t], bf, a[i % NW][2 - h]); else mfma_ip(acc[i >= 27][2 - h][t], bf, a[i % NW][2 - h]);
        SB;
        if constexpr (i == 4 && e == 2) go2 = lds_get(L_GO + 4);
        if constexpr ((i == 27 || i == 28) && (e & 1)) put(IC<0>{}, IC<(i - 27) * 6 + e / 2>{});
        if constexpr (i == 36 && e >= 8) put(IC<1>{}, IC<e - 8>{});
        if constexpr (i < 36 && h < 2) bw[(i + 1) & 1][t][h] = rd1(i + 1, t, h);
        if constexpr (l1 < 0) { if constexpr (h == 2 && t < 3 && l0 >= 0) lda(IC<l0>{}, IC<t>{}); }
        else if constexpr (e & 1) lda(IC<(e < 6 ? l0 : l1)>{}, IC<(e >> 1) % 3>{});
        SB;
      });
      if constexpr (i == 29) { asm volatile("s_waitcnt lgkmcnt(8)" ::: "memory"); bar(); }
      if constexpr (i == 4) { if (__builtin_expect(!rfl(go2), 0)) lds_wait(L_GO + 4); asm volatile("" ::: "memory"); STAMP(17); }
      if constexpr (i > 4 && i < 27 && (i % 9 == 4 || i % 9 == 8)) {
        bar();
        STAMP(17 + (2 * i) / 9);
      }
    });
    STAMP(24);
    static_for<4, 12>([&](auto E) { if constexpr (E.v == 8) asm volatile("s_nop 3" ::: "memory"); put(IC<1>{}, E); SB; });
    lbar();
    STAMP(5);
    STAMP_END;
    asm volatile(".Lfb_code_end1:");
  }
}

extern "C" void kernel_launch(void* const* d_in, const int* in_sizes, int n_in, void* d_out, int out_size, void* d_ws,
                              size_t ws_size, hipStream_t stream) {
  Params p;
  p.x = d_in[0]; p.wqkv = d_in[1]; p.wo = d_in[2]; p.wg = d_in[4]; p.wu = d_in[5]; p.wd = d_in[6];
  p.out = d_out; p.wsraw = (char*)d_ws + (ws_size >= WS_ALIGN + WS_END ? 0 : 1);
  fused_block<<<256, 512, 0, stream>>>(p);
}
```

```cpp
#include <hip/hip_runtime.h>
#include <stdint.h>

typedef __attribute__((ext_vector_type(8))) __bf16 bf16x8;
typedef __attribute__((ext_vector_type(4))) __bf16 bf16x4;
typedef __attribute__((ext_vector_type(2))) __bf16 bf16x2;
typedef __attribute__((ext_vector_type(4))) float f32x4;
typedef __attribute__((ext_vector_type(2))) float f32x2;
typedef __attribute__((ext_vector_type(4))) unsigned u32x4;
typedef __attribute__((ext_vector_type(2))) unsigned u32x2;
typedef __amdgpu_buffer_rsrc_t rsrc_t;
#define LAS __attribute__((address_space(3)))
#define GAS __attribute__((address_space(1)))
#define DI __device__ __forceinline__
#define RLX_AGENT __ATOMIC_RELAXED, __HIP_MEMORY_SCOPE_AGENT
typedef GAS unsigned gu32;
typedef GAS unsigned long long gu64;

constexpr int S = 2048, D = 640, F = 1728;
constexpr int NCOL = 5376;
constexpr int NSTEP = 74;
constexpr unsigned WS_EPOCH = 0;
constexpr unsigned FS = 128;
constexpr unsigned WS_FLAGH = 768u << 10;
constexpr unsigned WS_FLAGO = WS_FLAGH + 8 * 32 * FS;
constexpr unsigned WS_FLAGKV = WS_FLAGO + 8 * 32 * FS;
constexpr unsigned WS_STAT = 4096;
constexpr unsigned WS_CSUM = WS_STAT + S * 8;
constexpr unsigned WS_KVP = WS_CSUM + 3 * NCOL * 8;
constexpr unsigned WS_HA = 1u << 20;
constexpr unsigned RBSZ = 16 * NSTEP * 1024;
constexpr unsigned step_off(unsigned st) { return st < 54 ? (st / 3) * 49152 + (st % 3) * 1024 : 18 * 49152 + ((st - 54) / 5) * 81920 + ((st - 54) % 5) * 1024; }
constexpr unsigned WS_DBG = 12u << 20;
constexpr unsigned WS_END = 13u << 20;
constexpr unsigned long long WS_ALIGN = 64ull << 20;
constexpr unsigned L_B = 65536;
constexpr unsigned L_SCR = 147456;
constexpr unsigned L_MISC = 159744;
constexpr unsigned L_GO = L_MISC + 3072;
constexpr unsigned SPIN_CAP = 1u << 18;
constexpr int NA1 = 2;
constexpr int NPRE = 12;
constexpr int PSPLIT = 48;
constexpr int HSPLIT = 64;
constexpr int CWIN = 40;
constexpr unsigned ROT = 32;

__shared__ __attribute__((aligned(16))) char smem[163840];

DI float sadd(float a, float b) { float o; asm("v_add_f32 %0, %1, %2" : "=v"(o) : "v"(a), "v"(b)); return o; }
DI float smul(float a, float b) { float o; asm("v_mul_f32 %0, %1, %2" : "=v"(o) : "v"(a), "v"(b)); return o; }
DI float sfma(float a, float b, float c) { float o; asm("v_fma_f32 %0, %1, %2, %3" : "=v"(o) : "v"(a), "v"(b), "v"(c)); return o; }
DI unsigned bsel(unsigned m, unsigned a, unsigned b) { return (m & a) | (~m & b); }
DI float bself(unsigned m, float a, float b) { return __builtin_bit_cast(float, bsel(m, __builtin_bit_cast(unsigned, a), __builtin_bit_cast(unsigned, b))); }
DI bool all64(bool c) { return __builtin_amdgcn_ballot_w64(!c) == 0; }
DI float shfl_x(float v, unsigned lane, unsigned m) {
  return __builtin_bit_cast(float, __builtin_amdgcn_ds_bpermute((int)((lane ^ m) << 2), __builtin_bit_cast(int, v)));
}
DI unsigned umin(unsigned a, unsigned b) { return a < b ? a : b; }
DI unsigned rfl(unsigned v) { return __builtin_amdgcn_readfirstlane(v); }
DI rsrc_t mk_rsrc(const void* p, unsigned bytes) { return __builtin_amdgcn_make_buffer_rsrc((void*)p, (short)0, (int)bytes, 0x00020000); }
DI void dma16(unsigned ldsdst, unsigned voff, rsrc_t rs, unsigned soff) {
  asm volatile("s_mov_b32 m0, %0\n\ts_nop 0\n\tbuffer_load_dwordx4 %1, %2, %3 offen lds" ::"s"(ldsdst), "v"(voff), "s"(rs), "s"(soff) : "memory");
}
template <int N> DI void wait_vm() { asm volatile("s_waitcnt vmcnt(%0)" ::"i"(N) : "memory"); }
DI void drain_vm() { wait_vm<0>(); __builtin_amdgcn_s_waitcnt(0x0F70); }
DI void wait_lgkm0() { asm volatile("s_waitcnt lgkmcnt(0)" ::: "memory"); }
DI void bar() { asm volatile("" ::: "memory"); __builtin_amdgcn_s_barrier(); asm volatile("" ::: "memory"); }
DI void lbar() { wait_lgkm0(); bar(); }
template <class V> DI V lds_ld(unsigned a) { return *(LAS V*)((LAS char*)smem + a); }
template <class V> DI void lds_st(unsigned a, V v) { *(LAS V*)((LAS char*)smem + a) = v; }
DI bf16x4 lds_tr(unsigned a) { return __builtin_amdgcn_ds_read_tr16_b64_v4bf16((LAS bf16x4*)((LAS char*)smem + a)); }
DI f32x4 mfma(bf16x8 a, bf16x8 b, f32x4 c) { return __builtin_amdgcn_mfma_f32_16x16x32_bf16(a, b, c, 0, 0, 0); }
DI unsigned pk2(float a, float b) { f32x2 v = {a, b}; return __builtin_bit_cast(unsigned, __builtin_convertvector(v, bf16x2)); }
DI u32x2 pk4(f32x4 v) { u32x2 r = {pk2(v[0], v[1]), pk2(v[2], v[3])}; return r; }
DI bf16x8 frag(u32x2 lo, u32x2 hi) { u32x4 v = {lo[0], lo[1], hi[0], hi[1]}; return __builtin_bit_cast(bf16x8, v); }
DI float bflo(unsigned v) { return __builtin_bit_cast(float, v << 16); }
DI float bfhi(unsigned v) { return __builtin_bit_cast(float, v & 0xffff0000u); }
DI float rbf(float v) { return bflo(pk2(v, 0.f)); }
DI float relu2(float v) { float o; asm("v_add_f32 %0, %1, |%1|" : "=v"(o) : "v"(v)); return o; }
DI f32x4 phi4(f32x4 u, f32x4 rl, f32x4 rh) {
  const f32x4 l = u * rl;
  f32x4 e, a;
#pragma unroll
  for (int j = 0; j < 4; ++j) {
    e[j] = __builtin_amdgcn_fmed3f(__builtin_amdgcn_exp2f(l[j]), 0.f, 1.f);
    a[j] = relu2(u[j]);
  }
  return a * rh + e + 1e-6f;
}
DI f32x4 splat(float v) { return f32x4{v, v, v, v}; }
template <int CTRL> DI float dpp_add(float v) {
  return v + __builtin_bit_cast(float, __builtin_amdgcn_update_dpp(0, __builtin_bit_cast(int, v), CTRL, 0xf, 0xf, true));
}
DI float dot2(unsigned a, unsigned b, float c) {
  return __builtin_amdgcn_fdot2_f32_bf16(__builtin_bit_cast(bf16x2, a), __builtin_bit_cast(bf16x2, b), c, false);
}
DI void mfma_ip(f32x4& c, const u32x4& a, const u32x4& b) { asm volatile("v_mfma_f32_16x16x32_bf16 %0, %1, %2, %0" : "+v"(c) : "v"(a), "v"(b)); }
DI void mfma_c(f32x4& d, const u32x4& a, const u32x4& b, const f32x4& c) { asm volatile("v_mfma_f32_16x16x32_bf16 %0, %1, %2, %3" : "=&v"(d) : "v"(a), "v"(b), "v"(c)); }
DI void mfma_z(f32x4& c, const u32x4& a, const u32x4& b) { asm volatile("v_mfma_f32_16x16x32_bf16 %0, %1, %2, 0" : "=&v"(c) : "v"(a), "v"(b)); }
template <int N> DI void acc_fence(f32x4* c) {
  asm volatile("s_nop 7" ::: "memory");
#pragma unroll
  for (int i = 0; i < N; ++i) asm volatile("" : "+v"(c[i]));
}
DI u32x4 cat2(u32x2 a, u32x2 b) { return u32x4{a[0], a[1], b[0], b[1]}; }
DI u32x2 ldtr1(unsigned a) { return __builtin_bit_cast(u32x2, lds_tr(a)); }
template <int IMM> DI void set_m0i(unsigned v) { asm volatile("s_add_i32 m0, %0, %1" ::"s"(v), "i"(IMM) : "memory", "scc"); }
template <int OFF> DI void dma_goi(unsigned voff, rsrc_t rs, unsigned soff) {
  asm volatile("buffer_load_dwordx4 %0, %1, %2 offen offset:%3 lds" ::"v"(voff), "s"(rs), "s"(soff), "i"(OFF) : "memory");
}
#define STAMP_DECL
#define STAMP_ARG
#define STAMP_PASS
#define STAMPX(c, i)
#define STAMP_END
#define STAMP(i) STAMPX(__builtin_amdgcn_workitem_id_x() == 0, i)

DI unsigned poll_flags(gu32* word, bool active, unsigned tag) {
  unsigned v = 0;
#pragma unroll 1
  for (unsigned spins = 0; spins < SPIN_CAP; ++spins) {
    v = __hip_atomic_load(word, RLX_AGENT);
    if (all64(!active || v == tag)) break;
    __builtin_amdgcn_s_sleep(2);
  }
  asm volatile("" ::: "memory");
  return v;
}

DI void lds_set(unsigned a) { __hip_atomic_store((LAS unsigned*)smem + a / 4, 1u, __ATOMIC_RELAXED, __HIP_MEMORY_SCOPE_WORKGROUP); }
DI unsigned lds_get(unsigned a) { return __hip_atomic_load((LAS unsigned*)smem + a / 4, __ATOMIC_RELAXED, __HIP_MEMORY_SCOPE_WORKGROUP); }
DI void lds_wait(unsigned a) {
#pragma unroll 1
  for (unsigned spins = 0; spins < (SPIN_CAP << 4); ++spins) {
    if (rfl(lds_get(a))) break;
    __builtin_amdgcn_s_sleep(1);
  }
  asm volatile("" ::: "memory");
}

template <int I> struct IC { static constexpr int v = I; };
template <int B, int E, class Fn> DI void static_for(Fn&& f) {
  if constexpr (B < E) { f(IC<B>{}); static_for<B + 1, E>(f); }
}

enum { J_MLP = 0, J_KV = 1, J_Q = 2, J_SPARE = 3 };

struct Dma {
  rsrc_t rsA, rsB;
  unsigned vA0, vA1, vB0, vB1, vB2;
  unsigned sA[4], sB, advB;
  unsigned wa, wq;
  unsigned mA, mB, mW;
};

constexpr unsigned a_slot(unsigned s3) { return s3 == 0 ? 126976u : (s3 - 1) * 32768u; }
#define SB __builtin_amdgcn_sched_barrier(0)

template <int T> DI unsigned bofs(const unsigned (&boff)[T], int t) { return T == 6 && t >= 3 ? boff[t - 3] + 6144 : boff[t]; }
template <int T, bool TR, int KS, class Extra>
DI void kstep(f32x4 (&acc)[4][T], const u32x4 (&ac)[4], const u32x2 (&bc)[T][2], u32x4 (&an)[4], u32x2 (&bn)[T][2],
              const unsigned (&aoff)[4], const unsigned (&boff)[T], const Dma& d, Extra&& extra) {
  constexpr int s = KS >> 1, KIND = KS & 1, NP = KIND == 0 ? 4 : 3, ST = KIND == 0 ? (4 * T - 1) / NP : 2;
  constexpr int sn = (KS + 1) >> 1;
  constexpr unsigned asl = a_slot(sn % 3), bsl = ((KS + 1) % 5) * 12288u;
  static_for<0, 4 * T>([&](auto E) {
    constexpr int e = E.v, t = e >> 2, i = e & 3;
    const u32x4 bf = cat2(bc[t][0], bc[t][1]);
    if constexpr (KS == 0) { if (TR) mfma_z(acc[i][t], bf, ac[i]); else mfma_z(acc[i][t], ac[i], bf); }
    else { if (TR) mfma_ip(acc[i][t], bf, ac[i]); else mfma_ip(acc[i][t], ac[i], bf); }
    SB;
    if constexpr (KS < 19) {
      if constexpr (e < 4) an[e] = lds_ld<u32x4>(aoff[((KS + 1) & 1) + (sn % 3 == 0 ? 2 : 0)] + (sn % 3 == 0 ? 0u : asl) + e * 2048);
      else if constexpr (e < 4 + 2 * T) bn[(e - 4) >> 1][e & 1] = ldtr1(bofs<T>(boff, (e - 4) >> 1) + bsl + (e & 1) * 768);
    }
    constexpr int pc = e / ST;
    if constexpr (KIND == 0) {
      constexpr int adv = s < 8 ? (s + 2) * 128 : 0, dst = (int)a_slot((s + 2) % 3) + pc * 1024, off = dst >= adv ? adv : 0;
      if constexpr (pc < NP && e % ST == ST - 2) set_m0i<dst - off>(d.mA);
      if constexpr (pc < NP && e % ST == ST - 1) dma_goi<off>((pc & 1) ? d.vA1 : d.vA0, d.rsA, d.sA[pc] + (adv - off));
    } else {
      constexpr int h0 = (2 * s) % 5;
      if constexpr (pc < NP && e % ST == ST - 2) set_m0i<(h0 == 4 ? 0 : h0 * 12288) + pc * 1024>(h0 == 4 ? d.mW : d.mB);
      if constexpr (pc < NP && e % ST == ST - 1) dma_goi<0>(pc == 0 ? d.vB0 : (pc == 1 ? d.vB1 : d.vB2), d.rsB, d.sB);
    }
    extra(E);
    SB;
  });
}

struct RowStat { u32x4 xa; unsigned xb, ep, row; float s, q; };
DI void row_load(RowStat& R, rsrc_t rsX, char* ws, unsigned lane) {
  asm volatile("buffer_load_dwordx4 %0, %3, %5, %6 offen\n\tbuffer_load_dword %1, %4, %5, %6 offen offset:1024\n\tglobal_load_dword %2, %7, %8 sc1"
               : "=&v"(R.xa), "=&v"(R.xb), "=&v"(R.ep) : "v"(lane * 16), "v"(lane * 4), "s"(rsX), "s"(R.row * 1280), "v"(0u), "s"(ws) : "memory");
}
template <int NY> DI unsigned row_wait(const RowStat& R) {
  unsigned e;
  asm volatile("s_waitcnt vmcnt(%2)\n\tv_readfirstlane_b32 %0, %1\n\ts_nop 3" : "=s"(e) : "v"(R.ep), "i"(NY) : "memory");
  return rfl(e) + 1;
}
template <int K> DI void row_step(RowStat& R, unsigned lane) {
  if constexpr (K < 5) {
    if constexpr (K == 0) { R.s = 0.f; R.q = 0.f; }
    const unsigned v = K < 4 ? R.xa[K & 3] : R.xb;
    asm volatile("v_dot2c_f32_bf16 %0, %2, %3\n\tv_dot2c_f32_bf16 %1, %2, %2" : "+v"(R.s), "+v"(R.q) : "v"(v), "v"(0x3f803f80u));
  } else if constexpr (K < 11) {
    constexpr int C = K == 5 ? 0xB1 : (K == 6 ? 0x4E : (K == 7 ? 0x141 : (K == 8 ? 0x140 : (K == 9 ? 0x142 : 0x143))));
    if constexpr (K == 5) asm volatile("s_nop 2" : "+v"(R.s), "+v"(R.q));
    R.s = dpp_add<C>(R.s); R.q = dpp_add<C>(R.q);
  } else if constexpr (K == 11) {
    R.s *= 1.0f / D;
    R.q = __builtin_fmaxf(R.q * (1.0f / D) - R.s * R.s, 0.f);
  } else if constexpr (K == 12) {
    R.q = __builtin_amdgcn_sqrtf(rbf(rbf(R.q) + 1e-5f));
  } else {
    lds_st<unsigned>(lane == 63 ? L_GO + 32 + (R.row & 7) * 4 : L_GO + 64, pk2(R.s, R.q));
  }
  if constexpr (K >= 5 && K < 13) asm volatile("" : "+v"(R.s), "+v"(R.q));
}
DI void row_publish(const RowStat& R, char* ws, unsigned tag, unsigned tid) {
  if (tid < 8) __hip_atomic_store((gu64*)(ws + WS_STAT) + R.row + tid, ((unsigned long long)tag << 32) | lds_ld<unsigned>(L_GO + 32 + tid * 4), RLX_AGENT);
}

struct Gran { u32x2 g0, g1, g2; unsigned off, step; };
DI void gran_issue(Gran& G, rsrc_t rs) {
  asm volatile("buffer_load_dwordx2 %0, %3, %4, 0 offen sc1\n\tbuffer_load_dwordx2 %1, %3, %4, %5 offen sc1\n\t"
               "buffer_load_dwordx2 %2, %3, %4, %6 offen sc1"
               : "=&v"(G.g0), "=&v"(G.g1), "=&v"(G.g2) : "v"(G.off), "s"(rs), "s"(G.step), "s"(2 * G.step) : "memory");
}
DI void gran_load(Gran& G, rsrc_t rs) {
  asm volatile("buffer_load_dwordx2 %0, %3, %4, 0 offen sc1\n\tbuffer_load_dwordx2 %1, %3, %4, %5 offen sc1\n\t"
               "buffer_load_dwordx2 %2, %3, %4, %6 offen sc1\n\ts_waitcnt vmcnt(0)"
               : "=&v"(G.g0), "=&v"(G.g1), "=&v"(G.g2) : "v"(G.off), "s"(rs), "s"(G.step), "s"(2 * G.step) : "memory");
}

DI unsigned urow(unsigned pos) { const unsigned u = pos + ROT; return (u >= 54 ? u - 54 : u) * 40960u; }

constexpr unsigned wo_tile(unsigned v) { return (v / 5) * 10 + (v % 5 == 4 ? 4 : (v % 5 < 2 ? 2 * (v % 5) : 2 * (v % 5) + 1)); }
struct W2 { rsrc_t wd, wo; unsigned col; };
DI unsigned w2_off(const W2& W, int u, unsigned lw) {
  if (u < 54) return urow(u) + lw * 10240u + W.col;
  return wo_tile(u - 54) * 20480u + (lw >> 1) * ((u - 54) % 5 == 4 ? 102400u : 20480u) + (lw & 1) * 10240u + W.col;
}
DI u32x4 w2_load(const W2& W, int u, unsigned lw, unsigned voff) {
  return __builtin_amdgcn_raw_buffer_load_b128(u < 54 ? W.wd : W.wo, voff, w2_off(W, u, lw), 0);
}

template <int T, bool TR, bool PF, bool LASTA, class Pre>
DI void main_loop(Pre&& pre, RowStat& R, char* ws, f32x4 (&acc)[4][T], unsigned (&boff)[T], Dma& d, unsigned w, unsigned lane, unsigned tid,
                  unsigned& tag, unsigned rb, unsigned ccol, rsrc_t rsB2, rsrc_t rsWS, unsigned cb, u32x4 (&pre2)[36] STAMP_ARG) {
  const unsigned g = lane >> 4, r = lane & 15, m = w & 3;
  unsigned aoff[4];
  aoff[0] = (64 * m + r) * 128 + ((g ^ (r >> 1)) << 4);
  aoff[1] = aoff[0] ^ 64;
  aoff[2] = aoff[0] + a_slot(0); aoff[3] = aoff[1] + a_slot(0);
  const unsigned wq = d.wq, wa = d.wa;
  asm volatile("" : "+v"(aoff[0]), "+v"(aoff[1]), "+v"(aoff[2]), "+v"(aoff[3]), "+v"(boff[0]), "+v"(boff[1]), "+v"(boff[2]));
  if (T == 5) asm volatile("" : "+v"(boff[3]), "+v"(boff[4]));
  Gran G;
  unsigned v2, vb2, vpf, tA[3], tB[3];
  row_load(R, d.rsA, ws, lane);
  wait_vm<6 + NA1>();
  lbar();
  STAMP(1);
  u32x4 a0[4], a1[4];
  u32x2 b0[T][2], b1[T][2];
#pragma unroll
  for (int i = 0; i < 4; ++i) a0[i] = lds_ld<u32x4>(aoff[2] + i * 2048);
#pragma unroll
  for (int t = 0; t < T; ++t) { b0[t][0] = ldtr1(bofs<T>(boff, t)); b0[t][1] = ldtr1(bofs<T>(boff, t) + 768); }
  SB;
#pragma unroll
  for (int i = NA1; i < 4; ++i) dma16(a_slot(1) + d.mA + i * 1024, (i & 1) ? d.vA1 : d.vA0, d.rsA, d.sA[i] + 128);
  const bool y = w >= 4;
  bool ok = true;
  auto ln_store = [&] {
    const float v0 = y ? -bflo(G.g0[0]) : __builtin_bit_cast(float, G.g0[0]) + __builtin_bit_cast(float, G.g1[0]) + __builtin_bit_cast(float, G.g2[0]);
    const unsigned la = L_MISC + (y ? tid * 4 - 1024 : 2048 + tid * 4);
    lds_st<float>(la, v0);
    lds_st<float>(y ? la + 1024 : L_GO + 64 + (lane & 7) * 4, __builtin_amdgcn_rcpf(bfhi(G.g0[0])));
  };
  static_for<0, 10>([&](auto S_) {
    constexpr int s = S_.v, L = 4 * T;
    constexpr int NX = (PF && s >= 1 && 2 * s <= NPRE) ? 2 : 0;
    kstep<T, TR, 2 * s>(acc, a0, b0, a1, b1, aoff, boff, d, [&](auto E) {
      constexpr int e = E.v;
      if constexpr (s == 2) {
        if constexpr (e == 2) { vb2 = v2 + (3 * wq) * 10240u; asm volatile("" : "+v"(vb2)); }
        if constexpr (e == 5) { G.off = w < 4 ? WS_CSUM + ccol * 8 : WS_STAT + (rb * 256 + tid - 256) * 8; asm volatile("" : "+v"(G.off)); }
        if constexpr (e == 8) { G.step = w < 4 ? NCOL * 8 : 0; tA[0] = urow(wa) + cb * 128u; asm volatile("" : "+s"(G.step), "+s"(tA[0])); }
        if constexpr (e == 10) { tA[1] = urow(8 + wa) + cb * 128u; asm volatile("" : "+s"(tA[1])); }
        if constexpr (e == 12) { tA[2] = urow(31 + wa) + cb * 128u; asm volatile("" : "+s"(tA[2])); }
        if constexpr (e == 14) {
          tB[0] = urow(16) + cb * 128u; tB[1] = urow(w < 4 ? 19u : 22u) + cb * 128u; tB[2] = urow(w < 4 ? 25u : 28u) + cb * 128u;
          asm volatile("" : "+s"(tB[0]), "+s"(tB[1]), "+s"(tB[2]));
        }
      }
      if constexpr (s == (TR ? 1 : 3) && e == 2) pre(0);
      if constexpr (s == 0 && e == 2) pre(1);
      if constexpr (NX && e >= L - 2) {
        constexpr int u = 2 * (s - 1) + e - (L - 2);
        pre2[u] = __builtin_amdgcn_raw_buffer_load_b128(rsB2, vpf, urow(36 + u) + (w & 3) * 10240u + cb * 128u, 0);
      }
      if constexpr (s < 7) { if constexpr (e == L - 3) d.sB += s == 0 ? d.advB * (1 + (w >> 2)) : d.advB; }
      else if constexpr (s == 7) {
        if constexpr (e == L - 6) d.vB0 = y ? vb2 : d.vB0;
        if constexpr (e == L - 4) d.vB1 = y ? vb2 + 10240u : d.vB1;
        if constexpr (e == L - 3) d.vB2 = y ? vb2 + 20480u : d.vB2;
        if constexpr (e == L - 2) d.sB = y ? tB[0] : d.sB + d.advB;
        if constexpr (e == L - 1) d.rsB = y ? rsB2 : d.rsB;
      } else {
        if constexpr (e == L - 3) { d.sB = tB[s - 7]; d.rsB = rsB2; d.vB0 = vb2; d.vB1 = vb2 + 10240u; d.vB2 = vb2 + 20480u; }
      }
      if constexpr (s == 9 && e == L - 4) {
        asm volatile("s_waitcnt vmcnt(7)" : "+v"(G.g0), "+v"(G.g1), "+v"(G.g2)::"memory");
        ok = all64(G.g0[1] == tag && G.g1[1] == tag && G.g2[1] == tag);
      }
      if constexpr (s == 9 && e == L - 3) ln_store();
    });
    if constexpr (s == 0) {
      tag = row_wait<8 - NA1>(R);
      static_for<0, 14>([&](auto K) { row_step<K.v>(R, lane); });
    }
    if constexpr (s == 9) {
      if (__builtin_expect(!ok, 0)) {
#pragma unroll 1
        for (unsigned spins = 0; spins < SPIN_CAP; ++spins) {
          __builtin_amdgcn_s_sleep(2);
          gran_load(G, rsWS);
          if (all64(G.g0[1] == tag && G.g1[1] == tag && G.g2[1] == tag)) break;
        }
        ln_store();
      }
    }
    wait_lgkm0();
    if constexpr (s < 9) wait_vm<4 + NX>();
    bar();
    if constexpr (s == 0) { STAMP(12); }
    SB;
    kstep<T, TR, 2 * s + 1>(acc, a1, b1, a0, b0, aoff, boff, d, [&](auto E) {
      constexpr int e = E.v;
      if constexpr (s == 0 && e == 6) {
        if (y) {
          const unsigned so = d.sB - d.advB;
          dma16(d.mW + 49152, d.vB0, d.rsB, so); dma16(d.mW + 49152 + 1024, d.vB1, d.rsB, so); dma16(d.mW + 49152 + 2048, d.vB2, d.rsB, so);
        }
      }
      if constexpr (s == 0 && e == L - 4) {
        v2 = (lane >> 3) * 1280 + (((lane & 7) ^ (((lane >> 4) & 3) << 1)) << 4);
        vpf = y ? v2 : 0x7ffffff0u;
        asm volatile("" : "+v"(v2), "+v"(vpf));
      }
      if constexpr (s == 0 && e == L - 6) row_publish(R, ws, tag, tid);
      if constexpr (s == 8 && e == 0) gran_issue(G, rsWS);
      if constexpr (s >= 7 && s < 9 && e >= L - 4) {
        d.rsA = rsB2; d.vA0 = v2; d.vA1 = v2;
        d.sA[e - (L - 4)] = tA[s - 7] + (e - (L - 4)) * 10240;
      }
      if constexpr (s == 9 && LASTA && e == 7) {
        if (wa < 5) {
#pragma unroll
          for (int i = 0; i < 4; ++i) dma16(a_slot(0) + d.mA + i * 1024, v2, rsB2, tA[2] + i * 10240);
        }
      }
    });
    if constexpr (s == 8) { STAMP(13); }
    if constexpr (s == 2) { STAMP(26); }
    if constexpr (s == 6) { STAMP(27); }
  });
  acc_fence<4 * T>(&acc[0][0]);
  STAMP(14);
}

DI void st16(u32x4 v, rsrc_t rs, unsigned voff, unsigned soff) {
  __builtin_amdgcn_raw_buffer_store_b128(v, rs, voff, soff, 16);
  asm volatile("s_nop 1" ::"v"(v) : "memory");
}
DI void publish_flag(char* ws, unsigned flag_off, unsigned tag, unsigned tid) {
  drain_vm();
  bar();
  if (tid == 0) __hip_atomic_store((gu32*)(ws + flag_off), tag, RLX_AGENT);
}
DI void st_frag(rsrc_t rs, unsigned lane, unsigned off, u32x2 lo, u32x2 hi) { st16(cat2(lo, hi), rs, lane * 16, off); }
DI void shared_send(unsigned w, unsigned lane, const u32x2 (&sh)[4]) {
  if (w >= 4) lds_st<u32x4>(L_SCR + w * 1024 + lane * 16, cat2(sh[0], sh[1]));
  else lds_st<u32x4>(L_SCR + w * 1024 + lane * 16, cat2(sh[2], sh[3]));
  asm volatile("" ::: "memory");
}
DI void shared_recv(rsrc_t rs, unsigned w, unsigned lane, unsigned off, unsigned str, const u32x2 (&sh)[4]) {
  lbar();
  u32x4 rcv = lds_ld<u32x4>(L_SCR + (w ^ 4) * 1024 + lane * 16);
  u32x2 r0 = {rcv[0], rcv[1]}, r1 = {rcv[2], rcv[3]};
  if (w >= 4) { st_frag(rs, lane, off + 2 * str, r0, sh[2]); st_frag(rs, lane, off + 3 * str, r1, sh[3]); }
  else        { st_frag(rs, lane, off, sh[0], r0); st_frag(rs, lane, off + str, sh[1], r1); }
}

__global__ __launch_bounds__(512, 2) void fused_block(const void* px, const void* pwqkv, const void* pwo, const void* pwg, const void* pwu,
                                                         const void* pwd, void* pout, char* wsraw) {
  const unsigned long long wsr = (unsigned long long)wsraw, wsm = ((wsr & 1) - 1) & (WS_ALIGN - 2);
  char* const ws = (char*)((wsr + wsm) & ~(wsm | 1));
  unsigned tid = __builtin_amdgcn_workitem_id_x(), lane = tid & 63;
  const unsigned w = rfl(tid >> 6);
  const unsigned b = __builtin_amdgcn_workgroup_id_x(), xcd = b & 7, slot = b >> 3;
  const unsigned rb = 4 * (xcd & 1) + (slot & 3), jq = xcd >> 1, ji = slot >> 2;
  const unsigned spi = (xcd - 4) * 4 + (slot & 3);
  const unsigned long long jl0 = (jq & 1) ? 0x4123220908070605ull : 0x4021200403020100ull, jl1 = (jq & 1) ? 0x4327266011100f0eull : 0x422524600d0c0b0aull;
  const unsigned jb = (unsigned)(((jq & 2) ? jl1 : jl0) >> (8 * ji)) & 0xff, jtype = jb >> 5, jidx = jb & 31;
  STAMP_DECL;
  STAMP(0);

  Dma d;
  RowStat rs_;
  const bool mlp = jtype == J_MLP;
  const unsigned pitch = mlp ? 3456u : 3840u, wq = (w + slot) & 3, wa = (w + ji) & 7;
  d.wa = wa; d.wq = wq;
  const rsrc_t rsX = mk_rsrc(px, S * D * 2);
  d.vA0 = (lane >> 3) * 1280 + (((lane & 7) ^ (lane >> 4)) << 4); d.vA1 = d.vA0 ^ 64;
  d.rsA = rsX; d.mA = wa * 4096u;
#pragma unroll
  for (int i = 0; i < 4; ++i) d.sA[i] = (rb * 256 + 32 * wa + 8 * i) * 1280;
  unsigned tag;
  asm volatile("s_nop 4" :: "s"(rsX) : "memory");
  STAMP(6);
#pragma unroll
  for (int i = 0; i < 4; ++i) dma16(a_slot(0) + d.mA + i * 1024, (i & 1) ? d.vA1 : d.vA0, d.rsA, d.sA[i]);
  SB;
  {
    unsigned vb[3];
    const unsigned lim = mlp ? 176u : 144u;
    {
      const unsigned r0 = (lane * 43) >> 9, p0 = lane - 12 * r0;
      const bool w1 = p0 >= 8, w2 = p0 >= 4;
      const unsigned r1 = r0 + (w1 ? 6 : 5), p1 = p0 + (w1 ? -8 : 4), r2 = r0 + (w2 ? 11 : 10), p2 = p0 + (w2 ? -4 : 8);
      vb[0] = r0 * pitch + umin(p0 * 16, lim);
      vb[1] = r1 * pitch + umin((p1 * 16) ^ ((r1 & 8) << 2), lim);
      vb[2] = r2 * pitch + umin((p2 * 16) ^ 32, lim);
    }
    d.vB0 = vb[0]; d.vB1 = vb[1]; d.vB2 = vb[2];
    const unsigned mh = wq >> 1;
    const void* bw = mlp ? (mh ? pwu : pwg) : pwqkv;
    const unsigned cj = mlp ? 192u : 160u * jtype, cm = mlp ? 0u : (jtype == J_KV ? 1280u : 160u), c0 = jtype == J_KV ? 1280u : 0u;
    d.rsB = mk_rsrc(bw, D * pitch);
    d.sB = (16 * (wq & 1) + (w < 4 ? 32 : 0)) * pitch + c0 + cj * jidx + cm * mh; d.advB = 64 * pitch;
    d.mB = L_B + wq * 3072u + (w < 4 ? 0u : 12288u); d.mW = L_B + wq * 3072u + (w < 4 ? 49152u : 0u);
    const unsigned bdst = L_B + (w < 4 ? 12288u : 0u) + wq * 3072u;
    rs_.row = rfl(b * 8 + w);
    if (jtype != J_SPARE) {
      dma16(bdst, d.vB0, d.rsB, d.sB); dma16(bdst + 1024, d.vB1, d.rsB, d.sB); dma16(bdst + 2048, d.vB2, d.rsB, d.sB);
      const unsigned so = d.sB + d.advB;
      dma16(bdst + 24576, d.vB0, d.rsB, so); dma16(bdst + 24576 + 1024, d.vB1, d.rsB, so); dma16(bdst + 24576 + 2048, d.vB2, d.rsB, so);
#pragma unroll
      for (int i = 0; i < NA1; ++i) dma16(a_slot(1) + d.mA + i * 1024, (i & 1) ? d.vA1 : d.vA0, d.rsA, d.sA[i] + 128);
    }
    d.sB += d.advB;
  }
  unsigned long long pc0; unsigned codeb;
  asm volatile("s_getpc_b64 %0\n.Lfb_code_mark:\n\ts_mov_b32 %1, .Lfb_code_end0-.Lfb_code_mark\n\ts_max_u32 %1, %1, .Lfb_code_end1-.Lfb_code_mark"
               : "=s"(pc0), "=s"(codeb) :: "scc");
  STAMP(10);
  lds_st<unsigned>(L_GO + (tid & 1) * 4, 0u);

  const unsigned t2 = 28 * xcd + slot;
  const bool has2 = slot < 28 && t2 < 220;
  const unsigned G = t2 / 10, cb = t2 % 10;
  unsigned g = lane >> 4, r = lane & 15, q4 = r >> 2, p4 = lane & 3;
  const rsrc_t rsWd = mk_rsrc(pwd, has2 ? F * D * 2 : 0);
  const rsrc_t rsWS = mk_rsrc(ws, WS_END);
  unsigned v2 = (lane >> 3) * 1280 + (((lane & 7) ^ (((lane >> 4) & 3) << 1)) << 4);
  auto relane = [&] {
    asm volatile("" : "+v"(tid));
    lane = tid & 63; g = lane >> 4; r = lane & 15; q4 = r >> 2; p4 = lane & 3;
    v2 = (lane >> 3) * 1280 + (((lane & 7) ^ (((lane >> 4) & 3) << 1)) << 4);
  };
  const unsigned m = w & 3, n = w >> 2;
  const unsigned brow = L_B + (8 * g + q4) * 192;
  const unsigned bx = (g & 1) << 5;

  u32x4 pre2[36];
  W2 W;
  W.wd = rsWd; W.wo = mk_rsrc(pwo, has2 ? D * D * 2 : 0); W.col = cb * 128u;
  if (jtype == J_MLP) {
    f32x4 acc[4][6];
    unsigned boff[6];
#pragma unroll
    for (int t = 0; t < 3; ++t) {
      const unsigned gt = n ? (t == 2 ? 3 : 4 + t) : t;
      boff[t] = brow + ((32 * gt + 8 * p4) ^ bx); boff[t + 3] = boff[t] + 6144;
    }
    const unsigned ccol = tid < 96 ? 1920 + 96 * jidx + tid : 3648 + 96 * jidx + (tid < 192 ? tid - 96 : 0);
    main_loop<6, true, true, true>([](int) {}, rs_, ws, acc, boff, d, w, lane, tid, tag, rb, ccol, rsWd, rsWS, cb, pre2 STAMP_PASS);
    relane();
    float mm[4];
    f32x4 cg[3], cu[3];
#pragma unroll
    for (int i = 0; i < 4; ++i) mm[i] = lds_ld<float>(L_MISC + (64 * m + 16 * i + r) * 4);
#pragma unroll
    for (int t = 0; t < 3; ++t) {
      const unsigned gt = n ? (t == 2 ? 3 : 4 + t) : t;
      cg[t] = lds_ld<f32x4>(L_MISC + 2048 + (16 * gt + 4 * g) * 4); cu[t] = lds_ld<f32x4>(L_MISC + 2048 + (96 + 16 * gt + 4 * g) * 4);
    }
    auto hval = [&](int i, int t) {
      f32x4 gg = acc[i][t] + mm[i] * cg[t];
      const f32x4 uu = acc[i][t + 3] + mm[i] * cu[t];
#pragma unroll
      for (int j = 0; j < 4; ++j) gg[j] = relu2(gg[j]);
      return pk4(gg * uu);
    };
    const unsigned ho = WS_HA + rb * RBSZ + jidx * 49152 + m * 4 * 3072;
#pragma unroll
    for (int i = 0; i < 4; ++i) { st_frag(rsWS, lane, ho + i * 3072 + n * 2048, hval(i, 0), hval(i, 1)); SB; }
    const u32x2 sh[4] = {hval(0, 2), hval(1, 2), hval(2, 2), hval(3, 2)};
    shared_send(w, lane, sh);
    shared_recv(rsWS, w, lane, ho + 1024, 3072, sh);
    STAMP(15);
    publish_flag(ws, WS_FLAGH + (rb * 32 + jidx) * FS, tag, tid);
    STAMP(3);
  } else if (jtype == J_KV) {
    f32x4 acc[4][5];
    unsigned boff[5];
    boff[0] = brow + ((80 * n + 8 * p4) ^ bx); boff[1] = boff[0] + 6144;
    boff[2] = brow + ((80 * n + 40 + 8 * p4) ^ bx); boff[3] = boff[2] + 6144;
    boff[4] = brow + ((80 * n + 32 + 40 * (p4 >> 1)) ^ bx) + (p4 & 1) * 6144;
    const unsigned kb = 126976u + m * 6720 + n * 3360, dump = L_MISC - 2048 + lane * 16;
    const unsigned ea = (q4 == 1 || r == 0) ? kb + (r == 0 ? 20 : 12 + r) * 80 : 0u, eb = (q4 == 3 || r == 0) ? kb + 1680 + (r == 0 ? 20 : 4 + r) * 80 : 0u;
    unsigned ka[6] = {kb + r * 80 + g * 16, ea ? ea + g * 16 : dump, eb ? eb + g * 16 : dump, g == 0 ? kb + r * 80 + 64 : dump,
                      g == 2 ? kb + 1680 + r * 80 + 64 : dump, (g == 0 && ea) ? ea + 64 : ((g == 2 && eb) ? eb + 64 : dump)};
    auto pin = [&](int P) { if (P == 0) asm volatile("" : "+v"(ka[0]), "+v"(ka[1]), "+v"(ka[2]), "+v"(ka[3]), "+v"(ka[4]), "+v"(ka[5])); };
    const unsigned ccol = tid < 80 ? 640 + 80 * jidx + tid : 1280 + 80 * jidx + (tid < 160 ? tid - 80 : 0);
    main_loop<5, false, false, false>(pin, rs_, ws, acc, boff, d, w, lane, tid, tag, rb, ccol, rsWd, rsWS, cb, pre2 STAMP_PASS);
    relane();
    float cs[5];
    cs[0] = lds_ld<float>(L_MISC + 2048 + (40 * n + r) * 4);
    cs[1] = lds_ld<float>(L_MISC + 2048 + (80 + 40 * n + r) * 4);
    cs[2] = lds_ld<float>(L_MISC + 2048 + (40 * n + 20 + r) * 4);
    cs[3] = lds_ld<float>(L_MISC + 2048 + (80 + 40 * n + 20 + r) * 4);
    cs[4] = lds_ld<float>(L_MISC + 2048 + ((q4 & 1) * 80 + 40 * n + 16 + 20 * (q4 >> 1) + (r & 3)) * 4);
    u32x2 pk[4][3], vv[4][3];
    unsigned vmask = 0u - (q4 & 1);
    asm volatile("" : "+v"(vmask));
    f32x4 mm4[4], rr4[4];
#pragma unroll
    for (int i = 0; i < 4; ++i) {
      mm4[i] = lds_ld<f32x4>(L_MISC + (64 * m + 16 * i + 4 * g) * 4); rr4[i] = lds_ld<f32x4>(L_MISC + 1024 + (64 * m + 16 * i + 4 * g) * 4);
    }
    SB;
#pragma unroll
    for (int i = 0; i < 4; ++i) {
      const f32x4 mm = mm4[i], rr = rr4[i];
      const f32x4 rl = rr * 1.4426950408889634f, rh = rr * 0.5f;
      f32x4 val[5];
#pragma unroll
      for (int t = 0; t < 5; ++t) val[t] = acc[i][t] + mm * cs[t];
#pragma unroll
      for (int t = 0; t < 3; ++t) pk[i][t] = pk4(phi4(val[2 * t], rl, rh));
      vv[i][0] = pk4(val[1] * rr); vv[i][1] = pk4(val[3] * rr);
      const u32x2 v4 = pk4(val[4] * rr);
      vv[i][2] = u32x2{bsel(vmask, v4[0], 0x3f803f80u), bsel(vmask, v4[1], 0x3f803f80u)};
    }
    STAMP(25);
    f32x4 P[7] = {};
#pragma unroll
    for (int i = 0; i < 4; i += 2) {
      const bf16x8 k0 = frag(pk[i][0], pk[i + 1][0]), k2 = frag(pk[i][1], pk[i + 1][1]), k4 = frag(pk[i][2], pk[i + 1][2]);
      const bf16x8 v1 = frag(vv[i][0], vv[i + 1][0]), v3 = frag(vv[i][1], vv[i + 1][1]), v4 = frag(vv[i][2], vv[i + 1][2]);
      P[0] = mfma(k0, v1, P[0]); P[1] = mfma(k0, v4, P[1]); P[2] = mfma(k4, v1, P[2]);
      P[3] = mfma(k2, v3, P[3]); P[4] = mfma(k2, v4, P[4]); P[5] = mfma(k4, v3, P[5]); P[6] = mfma(k4, v4, P[6]);
    }
    lds_st<f32x4>(ka[0], P[0]); lds_st<f32x4>(ka[0] + 1680, P[3]); lds_st<f32x4>(ka[1], P[1]); lds_st<f32x4>(ka[2], P[4]);
    lds_st<f32x4>(ka[3], P[2]); lds_st<f32x4>(ka[4], P[5]); lds_st<f32x4>(ka[5], P[6]);
    lbar();
    if (tid < 210) {
      const unsigned a = 126976u + tid * 32;
      const f32x4 t0 = (lds_ld<f32x4>(a) + lds_ld<f32x4>(a + 6720)) + (lds_ld<f32x4>(a + 13440) + lds_ld<f32x4>(a + 20160));
      const f32x4 t1 = (lds_ld<f32x4>(a + 16) + lds_ld<f32x4>(a + 6736)) + (lds_ld<f32x4>(a + 13456) + lds_ld<f32x4>(a + 20176));
      st16(cat2(pk4(t0), pk4(t1)), rsWS, tid * 16, WS_KVP + (rb * 32 + 4 * jidx) * 840);
    }
    wait_lgkm0();
    STAMP(15);
    publish_flag(ws, WS_FLAGKV + ((jidx >> 1) * 32 + (jidx & 1) * 8 + rb) * FS, tag, tid);
    STAMP(3);
  } else if (jtype == J_Q) {
    f32x4 acc[4][5];
    unsigned boff[5];
#pragma unroll
    for (int t = 0; t < 5; ++t) boff[t] = brow + n * 6144 + ((32 * t + 8 * p4) ^ bx);
    const unsigned ccol = 160 * jidx + (tid < 160 ? tid : 0);
    unsigned tch[4];
    auto warm = [&](int P) {
      if (P == 1) {
        asm volatile("buffer_load_dword %0, %1, %2, 0 offen" : "=&v"(tch[3]) : "v"((((rb & 3) * 8 + w) * 16 + (lane & 15)) * 128),
                     "s"(mk_rsrc((const void*)pc0, codeb)) : "memory");
        return;
      }
      const unsigned gq = (rb * 4 + jidx) * 8 + w;
      asm volatile("buffer_load_dword %0, %3, %5, 0 offen\n\tbuffer_load_dword %1, %3, %5, %7 offen\n\tbuffer_load_dword %2, %4, %6, 0 offen"
                   : "=&v"(tch[0]), "=&v"(tch[1]), "=&v"(tch[2])
                   : "v"((gq * 34 + (lane < 34 ? lane : 0)) * 128), "v"((gq * 25 + (lane < 25 ? lane : 0)) * 128), "s"(mk_rsrc(pwd, F * D * 2)),
                     "s"(mk_rsrc(pwo, D * D * 2)), "s"(8576u * 128u) : "memory");
    };
    main_loop<5, true, false, false>(warm, rs_, ws, acc, boff, d, w, lane, tid, tag, rb, ccol, rsWd, rsWS, cb, pre2 STAMP_PASS);
    relane();
    asm volatile("" :: "v"(tch[0]), "v"(tch[1]), "v"(tch[2]), "v"(tch[3]));
    u32x2 pq[4][5];
    {
      float mm[4], rr[4];
      f32x4 c[5];
#pragma unroll
      for (int i = 0; i < 4; ++i) { mm[i] = lds_ld<float>(L_MISC + (64 * m + 16 * i + r) * 4); rr[i] = lds_ld<float>(L_MISC + 1024 + (64 * m + 16 * i + r) * 4); }
#pragma unroll
      for (int t = 0; t < 5; ++t) c[t] = lds_ld<f32x4>(L_MISC + 2048 + (80 * n + 16 * t + 4 * g) * 4);
      SB;
#pragma unroll
      for (int i = 0; i < 4; ++i)
#pragma unroll
        for (int t = 0; t < 5; ++t) pq[i][t] = pk4(phi4(acc[i][t] + mm[i] * c[t], splat(rr[i] * 1.4426950408889634f), splat(rr[i] * 0.5f)));
#pragma unroll
      for (int i = 0; i < 4; ++i) asm volatile("" : "+v"(pq[i][0]), "+v"(pq[i][1]), "+v"(pq[i][2]), "+v"(pq[i][3]), "+v"(pq[i][4]));
    }
    constexpr unsigned KP = 208, KQ = 84 * KP;
    unsigned wa[2];
#pragma unroll
    for (int k = 0; k < 2; ++k) {
      const unsigned qd = 2 * (tid < 420 ? tid : 0) + k, hd = qd / 105, e = (qd % 105) / 5, dq = qd % 5, hl = hd & 3;
      wa[k] = (hd >> 2) * KQ + (e < 20 ? hl * 20 + e : 80 + hl) * KP + hl * 40 + dq * 8;
    }
    unsigned fk = n * KQ + r * KP + g * 8, fz = n * KQ + (80 + (r & 3)) * KP + g * 8;
    unsigned gm[3] = {g < 1 ? ~0u : 0u, g < 2 ? ~0u : 0u, g < 3 ? ~0u : 0u};
    u32x4 B[4][3];
    f32x4 eps4 = splat(1e-6f);
#pragma unroll
    for (int i = 0; i < 4; ++i) {
      B[i][0] = cat2(pq[i][0], pq[i][1]); B[i][1] = cat2(pq[i][2], pq[i][3]); B[i][2] = cat2(pq[i][4], u32x2{0u, 0u});
      asm volatile("" : "+v"(B[i][0]), "+v"(B[i][1]), "+v"(B[i][2]));
    }
    asm volatile("" : "+v"(wa[0]), "+v"(wa[1]), "+v"(fk), "+v"(fz), "+v"(gm[0]), "+v"(gm[1]), "+v"(gm[2]), "+v"(eps4));
    STAMP(25);
    wait_vm<0>();
    bar();
#pragma unroll
    for (int k = 0; k < 5; ++k) lds_st<u32x4>(k * 8192 + tid * 16, u32x4{0u, 0u, 0u, 0u});
    if (w == 0) poll_flags((gu32*)(ws + WS_FLAGKV + (jidx * 32 + (lane & 15)) * FS), true, tag);
    lbar();
    STAMP(4);
    if (w < 7) {
      u32x4 v[8];
#pragma unroll
      for (int j = 0; j < 8; ++j)
        v[j] = __builtin_amdgcn_raw_buffer_load_b128(rsWS, (tid < 420 ? tid : 0) * 16, WS_KVP + (j * 32 + 8 * jidx) * 840, 16);
      SB;
      f32x4 t0 = {0.f, 0.f, 0.f, 0.f}, t1 = t0;
#pragma unroll
      for (int j = 0; j < 8; ++j) {
        t0 += f32x4{bflo(v[j][0]), bfhi(v[j][0]), bflo(v[j][1]), bfhi(v[j][1])};
        t1 += f32x4{bflo(v[j][2]), bfhi(v[j][2]), bflo(v[j][3]), bfhi(v[j][3])};
      }
      lds_st<u32x2>(wa[0], pk4(t0)); lds_st<u32x2>(wa[1], pk4(t1));
    }
    lbar();
    STAMP(16);
    u32x4 A[12];
#pragma unroll
    for (int P = 0; P < 3; ++P) A[P] = cat2(lds_ld<u32x2>(fz + P * 64), lds_ld<u32x2>(fz + P * 64 + 32));
#pragma unroll
    for (int u = 0; u < 5; ++u) {
      const unsigned a0 = fk + u * 16 * KP + (u < 3 ? 0 : 64);
      A[3 + u] = cat2(lds_ld<u32x2>(a0), lds_ld<u32x2>(a0 + 32));
    }
#pragma unroll
    for (int u = 1; u < 5; ++u) {
      const unsigned a0 = fk + u * 16 * KP + (u < 3 ? 64 : 128);
      A[7 + u] = cat2(lds_ld<u32x2>(a0), lds_ld<u32x2>(a0 + 32));
    }
    SB;
    u32x2 sh[4];
    f32x4 den[4], num[2][5];
    static_for<0, 12>([&](auto E) {
      constexpr int P = E.v >> 2, i = E.v & 3;
      if constexpr (P == 0) mfma_c(den[i], A[0], B[i][0], eps4); else mfma_ip(den[i], A[P], B[i][P]);
    });
    u32x2 o[5];
    const unsigned oo = WS_HA + rb * RBSZ + 18 * 49152 + jidx * 81920 + m * 4 * 5120;
    auto fin = [&](auto I, auto K) {
      constexpr int i = I.v, k = K.v, u = k < 8 ? k >> 1 : 4;
      if constexpr (k == 8 || !(k & 1)) {
        asm volatile("" : "+v"(num[i & 1][u]));
        const float rr = u == 0 ? den[i][0] : (u == 4 ? den[i][3] : bself(gm[(u + 2) % 3], den[i][(u + 3) & 3], den[i][u & 3]));
#pragma unroll
        for (int j = 0; j < 4; ++j) num[i & 1][u][j] = smul(num[i & 1][u][j], rr);
      }
      if constexpr (k == 8 || (k & 1)) o[u] = pk4(num[i & 1][u]);
      if constexpr (k == 3) st_frag(rsWS, lane, oo + i * 5120 + n * 2048, o[0], o[1]);
      if constexpr (k == 7) st_frag(rsWS, lane, oo + i * 5120 + n * 2048 + 1024, o[2], o[3]);
      if constexpr (k == 8) { sh[i] = o[4]; asm volatile("" : "+v"(sh[i])); }
    };
    static_for<0, 4>([&](auto I) {
      constexpr int i = I.v;
      static_for<0, 9>([&](auto K) {
        constexpr int k = K.v, u = k < 5 ? k : k - 4;
        if constexpr (k < 5) mfma_z(num[i & 1][u], A[3 + u], B[i][u < 3 ? 0 : 1]); else mfma_ip(num[i & 1][u], A[7 + u], B[i][u < 3 ? 1 : 2]);
        SB;
        if constexpr (i == 0) {
          if constexpr (k >= 5) {
            asm volatile("" : "+v"(den[k - 5]));
#pragma unroll
            for (int j = 0; j < 4; ++j) den[k - 5][j] = __builtin_amdgcn_rcpf(den[k - 5][j]);
            asm volatile("" : "+v"(den[k - 5]));
          }
        } else {
          fin(IC<i - 1>{}, K);
        }
        SB;
      });
    });
    static_for<0, 9>([&](auto K) { fin(IC<3>{}, K); SB; });
    shared_send(w, lane, sh);
    shared_recv(rsWS, w, lane, oo + 4096, 5120, sh);
    STAMP(15);
    publish_flag(ws, WS_FLAGO + (rb * 32 + jidx) * FS, tag, tid);
    STAMP(3);
    if (b == 224) {
      poll_flags((gu32*)(ws + WS_FLAGH + tid * FS), (tid & 31) < (tid < 256 ? 18u : 4u), tag);
      bar();
      if (tid == 0) __hip_atomic_store((gu32*)(ws + WS_EPOCH), tag, RLX_AGENT);
    }
  } else {
    const unsigned rk = spi * 4 + (w & 3), p0 = w < 4 ? rk : 128 + rk, p1 = p0 + 64;
    auto unit = [](unsigned q) { return q < 60 ? q + 30 : (q < 90 ? q - 60 : q); };
    const unsigned u0 = unit(p0), u1 = unit(p1);
    unsigned k10 = 0x00003f80u, k01 = 0x3f800000u;
    asm volatile("" : "+v"(k10), "+v"(k01));
    auto cs_src = [&](unsigned u, rsrc_t& rs, unsigned& cv, unsigned& st) {
      const unsigned cline = u / 3, third = u % 3;
      const void* cw = cline < 30 ? pwqkv : (cline < 57 ? pwg : pwu);
      const unsigned cpitch = cline < 30 ? 3840u : 3456u;
      const unsigned clocal = cline < 30 ? cline : (cline < 57 ? cline - 30 : cline - 57);
      rs = mk_rsrc(cw, u < 252 ? D * cpitch : 0);
      cv = (third * 216 + (lane >> 3)) * cpitch + clocal * 128 + (lane & 7) * 16;
      st = 8 * cpitch;
    };
    auto cs_publish = [&](unsigned u, float (&cs)[8]) {
#pragma unroll
      for (int k = 0; k < 4; ++k) { const bool hi = lane & 32; const float o = shfl_x(hi ? cs[k] : cs[k + 4], lane, 32); cs[k] = (hi ? cs[k + 4] : cs[k]) + o; }
#pragma unroll
      for (int k = 0; k < 2; ++k) { const bool hi = lane & 16; const float o = shfl_x(hi ? cs[k] : cs[k + 2], lane, 16); cs[k] = (hi ? cs[k + 2] : cs[k]) + o; }
      { const bool hi = lane & 8; const float o = shfl_x(hi ? cs[0] : cs[1], lane, 8); cs[0] = (hi ? cs[1] : cs[0]) + o; }
      if (u < 252)
        __hip_atomic_store((gu64*)(ws + WS_CSUM) + (u % 3) * NCOL + (u / 3) * 64 + (lane & 7) * 8 + (lane >> 3),
                           ((unsigned long long)tag << 32) | __builtin_bit_cast(unsigned, cs[0]), RLX_AGENT);
    };
    row_load(rs_, rsX, ws, lane);
    constexpr int CW = CWIN;
    u32x4 c[CW];
    rsrc_t rs0, rs1; unsigned cv0, cv1, st0, st1;
    cs_src(u0, rs0, cv0, st0); cs_src(u1, rs1, cv1, st1);
    auto cld = [&](int j) { return j < 27 ? __builtin_amdgcn_raw_buffer_load_b128(rs0, cv0 + j * st0, 0, 0) : __builtin_amdgcn_raw_buffer_load_b128(rs1, cv1 + (j - 27) * st1, 0, 0); };
#pragma unroll
    for (int j = 0; j < CW; ++j) c[j] = cld(j);
    SB;
    tag = row_wait<CW>(rs_);
    static_for<0, 14>([&](auto K) { row_step<K.v>(rs_, lane); });
    lbar();
    row_publish(rs_, ws, tag, tid);
    float cs[8] = {};
    static_for<0, 54>([&](auto J) {
      constexpr int j = J.v;
#pragma unroll
      for (int k = 0; k < 4; ++k) { cs[2 * k] = dot2(c[j % CW][k], k10, cs[2 * k]); cs[2 * k + 1] = dot2(c[j % CW][k], k01, cs[2 * k + 1]); }
      if constexpr (j + CW < 54) c[j % CW] = cld(j + CW);
      SB;
      if constexpr (j == 26) {
        cs_publish(u0, cs);
        STAMP(11);
#pragma unroll
        for (int k = 0; k < 8; ++k) cs[k] = 0.f;
      }
    });
    cs_publish(u1, cs);
    STAMP(12);
    bar();
#pragma unroll 1
    for (unsigned i = w; i < 144; i += 8) dma16(i * 1024, v2, rsWd, urow(i >> 2) + (i & 3) * 10240u + cb * 128u);
    drain_vm();
    bar();
  }
  if (!has2) { STAMP_END; return; }
  relane();

  const unsigned rbA = (G * 6) >> 4, rbB = (G * 6 + 5 > 127 ? 127 : G * 6 + 5) >> 4;
  if (w >= 4) {
    const unsigned lw = w - 4, ld0 = lw * 1024;
    gu32* fo = (gu32*)(ws + WS_FLAGO + ((lane < 32 ? rbA : rbB) * 32 + (lane & 3)) * FS);
    gu32* fh = (gu32*)(ws + WS_FLAGH + ((lane < 32 ? rbA : rbB) * 32 + ((lane & 31) < 18 ? lane & 31 : 17)) * FS);
    __builtin_amdgcn_s_sleep(4);
    if (jtype == J_KV) {
#pragma unroll
      for (int u = 31; u < 36; ++u) dma16(u * 4096 + ld0, v2, rsWd, w2_off(W, u, lw));
    }
    if (jtype != J_MLP) {
#pragma unroll
      for (int u = 0; u < NPRE; ++u) { pre2[u] = w2_load(W, 36 + u, lw, v2); SB; }
    }
#pragma unroll
    for (int u = 36 + NPRE; u < PSPLIT; ++u) { pre2[u - 36] = w2_load(W, u, lw, v2); SB; }
    if (w == 7) {
      __builtin_amdgcn_s_setprio(3);
      const unsigned v = poll_flags(fh, (lane & 31) < 18 && (lane & 31) >= 10, tag);
      lds_set(L_GO);
      if (!all64((lane & 31) >= 18 || v == tag)) poll_flags(fh, (lane & 31) < 18, tag);
      lds_set(L_GO + 4);
      __builtin_amdgcn_s_setprio(0);
    }
#pragma unroll
    for (int u = PSPLIT; u < HSPLIT; ++u) { pre2[u - 36] = w2_load(W, u, lw, v2); SB; }
    STAMP(8);
    unsigned la[3], wb[3], xo[3], sto[3];
    u32x2 st[3];
    u32x4 xr[3];
    const unsigned ch = lane & 7;
#pragma unroll
    for (int i = 0; i < 3; ++i) {
      const unsigned row = lw * 24 + i * 8 + (lane >> 3), rl = row >> 4, rr = row & 15;
      la[i] = (rl >= 3 ? 24576u + (rl - 3) * 4096u : rl * 4096u) + (ch >> 1) * 1024 + (ch & 1) * 512 + ((rr + 2 * ch) & 15) * 16;
      xo[i] = (G * 96 + row) * 1280 + cb * 128 + ch * 16;
      wb[i] = ld0 + lane * 16 + i * 49152;
      asm volatile("" : "+v"(la[i]), "+v"(wb[i]), "+v"(xo[i]));
    }
    float ri[3], rl[3];
    unsigned of = 0;
    f32x4 xf[3][2], dd[3][2], d2[3][2];
#pragma unroll
    for (int k = 2; k <= 6; ++k) {
      if (k == 5 && w == 7 && !all64((lane & 31) >= 4 || of == tag)) poll_flags(fo, (lane & 31) < 4, tag);
      if (k == 6) {
        wait_vm<0>();
#pragma unroll 1
        for (unsigned spins = 0; spins < SPIN_CAP && !all64(st[0][1] == tag && st[1][1] == tag && st[2][1] == tag); ++spins) {
#pragma unroll
          for (int h = 0; h < 3; ++h) st[h] = __builtin_amdgcn_raw_buffer_load_b64(rsWS, sto[h], 0, 16);
          asm volatile("" ::: "memory");
        }
#pragma unroll
        for (int h = 0; h < 3; ++h) {
          ri[h] = 2.0f * bfhi(st[h][0]) * bfhi(st[h][0]);
          rl[h] = -1.4426950408889634f * __builtin_amdgcn_rcpf(ri[h]);
          xf[h][0] = f32x4{bflo(xr[h][0]), bfhi(xr[h][0]), bflo(xr[h][1]), bfhi(xr[h][1])};
          xf[h][1] = f32x4{bflo(xr[h][2]), bfhi(xr[h][2]), bflo(xr[h][3]), bfhi(xr[h][3])};
          asm volatile("" : "+v"(ri[h]), "+v"(rl[h]), "+v"(xf[h][0]), "+v"(xf[h][1]));
        }
      }
      lbar();
      if (k == 4) { of = __hip_atomic_load(fo, RLX_AGENT); SB; }
      if (k < 6) {
        const int kk = k - 1;
#pragma unroll
        for (int u = 9 * (kk - 1) + ((kk - 1) & 1); u < 9 * kk + (kk & 1); ++u) {
          lds_st<u32x4>(wb[u / 12] + (u % 12) * 4096, pre2[u]);
          SB;
        }
      }
      if (k == 2 || k == 3) {
        constexpr int mid = (HSPLIT + 74) / 2 < 72 ? (HSPLIT + 74) / 2 : 72;
#pragma unroll
        for (int u = (k == 2 ? HSPLIT : mid); u < (k == 2 ? mid : 72); ++u) { pre2[u - 36] = w2_load(W, u, lw, v2); SB; }
        if (k == 3) { dma16(36 * 4096 + ld0, v2, W.wo, w2_off(W, 72, lw)); dma16(37 * 4096 + ld0, v2, W.wo, w2_off(W, 73, lw)); }
      }
      if (k == 2) {
#pragma unroll
        for (int h = 0; h < 3; ++h) {
          const unsigned row = G * 96 + lw * 24 + h * 8 + (lane >> 3);
          sto[h] = WS_STAT + (row < S ? row : 0) * 8;
          st[h] = __builtin_amdgcn_raw_buffer_load_b64(rsWS, sto[h], 0, 16);
          xr[h] = __builtin_amdgcn_raw_buffer_load_b128(rsX, xo[h], 0, 0);
        }
      }
    }
    lbar();
#pragma unroll
    for (int i = 0; i < 3; ++i) {
      dd[i][0] = lds_ld<f32x4>(la[i]); d2[i][0] = lds_ld<f32x4>(la[i] + 12288);
      dd[i][1] = lds_ld<f32x4>(la[i] + 256); d2[i][1] = lds_ld<f32x4>(la[i] + 12288 + 256);
      la[i] += 49152;
      asm volatile("" : "+v"(la[i]));
    }
#pragma unroll
    for (int i = 0; i < 3; ++i) {
      float v[8], e[8];
#pragma unroll
      for (int j = 0; j < 8; ++j) v[j] = sadd(dd[i][j >> 2][j & 3], d2[i][j >> 2][j & 3]);
      SB;
#pragma unroll
      for (int j = 0; j < 8; ++j) e[j] = smul(v[j], rl[i]);
      SB;
#pragma unroll
      for (int j = 0; j < 8; ++j) e[j] = __builtin_amdgcn_exp2f(e[j]);
      SB; asm volatile("s_nop 0"); SB;
#pragma unroll
      for (int j = 0; j < 8; ++j) e[j] = sfma(e[j], ri[i], ri[i]);
      SB;
#pragma unroll
      for (int j = 0; j < 8; ++j) e[j] = __builtin_amdgcn_rcpf(e[j]);
      SB; asm volatile("s_nop 0"); SB;
#pragma unroll
      for (int j = 0; j < 8; ++j) dd[i][j >> 2][j & 3] = smul(v[j], e[j]);
      asm volatile("" : "+v"(dd[i][0]), "+v"(dd[i][1]));
      SB;
    }
    const rsrc_t rsO = mk_rsrc(pout, S * D * 2);
    STAMP(9);
    bar();
    f32x4 ss[3][2];
#pragma unroll
    for (int i = 0; i < 3; ++i) {
      ss[i][0] = lds_ld<f32x4>(la[i]) + lds_ld<f32x4>(la[i] + 12288);
      ss[i][1] = lds_ld<f32x4>(la[i] + 256) + lds_ld<f32x4>(la[i] + 12288 + 256);
    }
#pragma unroll
    for (int i = 0; i < 3; ++i)
      __builtin_amdgcn_raw_buffer_store_b128(cat2(pk4(xf[i][0] + ss[i][0] * dd[i][0]), pk4(xf[i][1] + ss[i][1] * dd[i][1])), rsO, xo[i], 0, 0);
    STAMP(5);
    STAMP_END;
    asm volatile(".Lfb_code_end0:");
    return;
  }
  {
    const unsigned kh = w & 1, rt = G * 6 + 3 * (w >> 1);
    const rsrc_t rsH = mk_rsrc(ws + WS_HA, 8 * RBSZ);
    unsigned tb[3][4];
#pragma unroll
    for (int t = 0; t < 4; ++t) {
      tb[0][t] = (4 * g + q4) * 128 + ((t ^ ((2 * g + (q4 >> 1)) & 3)) << 5) + 8 * p4 + kh * 4096;
      tb[1][t] = tb[0][t] + 49152; tb[2][t] = tb[0][t] + 98304;
      asm volatile("" : "+v"(tb[0][t]), "+v"(tb[1][t]), "+v"(tb[2][t]));
    }
    auto rd1 = [&](int i, int t, int hf) {
      const int sl = i == 36 ? 36 : (2 * i) % 36, bi = sl / 12 > 2 ? 2 : sl / 12;
      return __builtin_bit_cast(u32x2, lds_tr(tb[bi][t] + (sl - 12 * bi) * 4096 + hf * 2048));
    };
    constexpr int NW = 7, W0 = 6;
    constexpr int OI = 22;
    f32x4 acc[2][3][4];
    u32x4 a[NW][3];
    u32x2 bw[2][4][2];
    unsigned va[2][3];
#pragma unroll
    for (int h = 0; h < 3; ++h) {
      va[0][h] = lane * 16 + ((rt + h) >> 4) * RBSZ + ((rt + h) & 15) * 3072;
      va[1][h] = va[0][h] + ((rt + h) & 15) * 2048;
      asm volatile("" : "+v"(va[0][h]), "+v"(va[1][h]));
    }
    unsigned xa[4];
#pragma unroll
    for (int t = 0; t < 4; ++t) {
      xa[t] = w * 12288u + t * 1024 + g * 256 + ((r + 4 * t + (g & 2)) & 15) * 16;
      asm volatile("" : "+v"(xa[t]));
    }
    auto put = [&](auto Z, auto E) {
      constexpr int z = Z.v, t = E.v / 3, h = 2 - E.v % 3;
      asm volatile("" : "+v"(acc[z][h][t]));
      lds_st<f32x4>(xa[t] + z * 49152 + h * 4096, acc[z][h][t]);
    };
    auto lda = [&](auto I, auto H) {
      constexpr int i = I.v, h = H.v;
      constexpr unsigned s0 = i < 27 ? (2 * i + ROT) % 54 : 2 * i, s1 = i < 27 ? (2 * i + 1 + ROT) % 54 : 2 * i + 1;
      a[i % NW][h] = __builtin_amdgcn_raw_buffer_load_b128(rsH, va[i >= 27][h], step_off(s0) + kh * (step_off(s1) - step_off(s0)), 16);
      SB;
    };
    lds_wait(L_GO);
    unsigned go2 = 0;
    STAMP(8);
    static_for<0, W0>([&](auto I) { lda(I, IC<0>{}); lda(I, IC<1>{}); lda(I, IC<2>{}); });
#pragma unroll
    for (int t = 0; t < 4; ++t) { bw[0][t][0] = rd1(0, t, 0); bw[0][t][1] = rd1(0, t, 1); }
    static_for<0, 37>([&](auto I) {
      constexpr int i = I.v;
      constexpr int lim = i + NW - 1;
      constexpr int nxt = i <= OI ? (W0 + 2 * i < lim ? W0 + 2 * i : lim) : (27 + 2 * (i - OI - 1) < lim ? 27 + 2 * (i - OI - 1) : lim);
      constexpr int end = i <= OI ? 27 : 37;
      constexpr int l0 = nxt < end ? nxt : -1, l1 = (nxt + 1 <= lim && nxt + 1 < end) ? nxt + 1 : -1;
      SB;
      static_for<0, 12>([&](auto E) {
        constexpr int e = E.v, t = e / 3, h = e % 3;
        const u32x4 bf = cat2(bw[i & 1][t][0], bw[i & 1][t][1]);
        if constexpr (i == 0 || i == 27) mfma_z(acc[i >= 27][2 - h][t], bf, a[i % NW][2 - h]); else mfma_ip(acc[i >= 27][2 - h][t], bf, a[i % NW][2 - h]);
        SB;
        if constexpr (i == 4 && e == 2) go2 = lds_get(L_GO + 4);
        if constexpr ((i == 27 || i == 28) && (e & 1)) put(IC<0>{}, IC<(i - 27) * 6 + e / 2>{});
        if constexpr (i == 36 && e >= 8) put(IC<1>{}, IC<e - 8>{});
        if constexpr (i < 36 && h < 2) bw[(i + 1) & 1][t][h] = rd1(i + 1, t, h);
        if constexpr (l1 < 0) { if constexpr (h == 2 && t < 3 && l0 >= 0) lda(IC<l0>{}, IC<t>{}); }
        else if constexpr (e & 1) lda(IC<(e < 6 ? l0 : l1)>{}, IC<(e >> 1) % 3>{});
        SB;
      });
      if constexpr (i == 29) { asm volatile("s_waitcnt lgkmcnt(8)" ::: "memory"); bar(); }
      if constexpr (i == 4) { if (__builtin_expect(!rfl(go2), 0)) lds_wait(L_GO + 4); asm volatile("" ::: "memory"); STAMP(17); }
      if constexpr (i > 4 && i < 27 && (i % 9 == 4 || i % 9 == 8)) {
        bar();
        STAMP(17 + (2 * i) / 9);
      }
    });
    STAMP(24);
    static_for<4, 12>([&](auto E) { if constexpr (E.v == 8) asm volatile("s_nop 3" ::: "memory"); put(IC<1>{}, E); SB; });
    lbar();
    STAMP(5);
    STAMP_END;
    asm volatile(".Lfb_code_end1:");
  }
}

extern "C" void kernel_launch(void* const* d_in, const int* in_sizes, int n_in, void* d_out, int out_size, void* d_ws,
                              size_t ws_size, hipStream_t stream) {
  fused_block<<<256, 512, 0, stream>>>(d_in[0], d_in[1], d_in[2], d_in[4], d_in[5], d_in[6], d_out,
                                       (char*)d_ws + (ws_size >= WS_ALIGN + WS_END ? 0 : 1));
}
```

```cpp
#include <hip/hip_runtime.h>
#include <stdint.h>

typedef __attribute__((ext_vector_type(8))) __bf16 bf16x8;
typedef __attribute__((ext_vector_type(4))) __bf16 bf16x4;
typedef __attribute__((ext_vector_type(2))) __bf16 bf16x2;
typedef __attribute__((ext_vector_type(4))) float f32x4;
typedef __attribute__((ext_vector_type(2))) float f32x2;
typedef __attribute__((ext_vector_type(4))) unsigned u32x4;
typedef __attribute__((ext_vector_type(2))) unsigned u32x2;
typedef __amdgpu_buffer_rsrc_t rsrc_t;
#define LAS __attribute__((address_space(3)))
#define GAS __attribute__((address_space(1)))
#define DI __device__ __forceinline__
#define RLX_AGENT __ATOMIC_RELAXED, __HIP_MEMORY_SCOPE_AGENT
typedef GAS unsigned gu32;
typedef GAS unsigned long long gu64;

constexpr int S = 2048, D = 640, F = 1728;
constexpr int NCOL = 5376;
constexpr int NSTEP = 74;
constexpr unsigned WS_EPOCH = 0;
constexpr unsigned FS = 128;
constexpr unsigned WS_FLAGH = 768u << 10;
constexpr unsigned WS_FLAGO = WS_FLAGH + 8 * 32 * FS;
constexpr unsigned WS_FLAGKV = WS_FLAGO + 8 * 32 * FS;
constexpr unsigned WS_STAT = 4096;
constexpr unsigned WS_CSUM = WS_STAT + S * 8;
constexpr unsigned WS_KVP = WS_CSUM + 3 * NCOL * 8;
constexpr unsigned WS_HA = 1u << 20;
constexpr unsigned RBSZ = 16 * NSTEP * 1024;
constexpr unsigned step_off(unsigned st) { return st < 54 ? (st / 3) * 49152 + (st % 3) * 1024 : 18 * 49152 + ((st - 54) / 5) * 81920 + ((st - 54) % 5) * 1024; }
constexpr unsigned WS_END = 11u << 20;
constexpr unsigned long long WS_ALIGN = 64ull << 20;
constexpr unsigned L_B = 65536;
constexpr unsigned L_SCR = 147456;
constexpr unsigned L_MISC = 159744;
constexpr unsigned L_GO = L_MISC + 3072;
constexpr unsigned SPIN_CAP = 1u << 18;
constexpr int NA1 = 2;
constexpr int NPRE = 12;
constexpr int PSPLIT = 48;
constexpr int HSPLIT = 64;
constexpr int CWIN = 40;
constexpr unsigned ROT = 32;

__shared__ __attribute__((aligned(16))) char smem[163840];

DI float sadd(float a, float b) { float o; asm("v_add_f32 %0, %1, %2" : "=v"(o) : "v"(a), "v"(b)); return o; }
DI float smul(float a, float b) { float o; asm("v_mul_f32 %0, %1, %2" : "=v"(o) : "v"(a), "v"(b)); return o; }
DI float sfma(float a, float b, float c) { float o; asm("v_fma_f32 %0, %1, %2, %3" : "=v"(o) : "v"(a), "v"(b), "v"(c)); return o; }
DI unsigned bsel(unsigned m, unsigned a, unsigned b) { return (m & a) | (~m & b); }
#define SC_MUL "v_mul_f32 %0, %4, %8\n\tv_mul_f32 %2, %5, %8\n\tv_mul_f32 %1, %6, %8\n\tv_mul_f32 %3, %7, %8\n\t"
#define SC_CVT "v_cvt_pk_bf16_f32 %0, %0, %2\n\tv_cvt_pk_bf16_f32 %1, %1, %3"
DI u32x2 scale_pk4(const f32x4& v, float a) {
  u32x2 o; float t0, t1;
  asm volatile(SC_MUL SC_CVT : "=&v"(o[0]), "=&v"(o[1]), "=&v"(t0), "=&v"(t1) : "v"(v[0]), "v"(v[1]), "v"(v[2]), "v"(v[3]), "v"(a));
  return o;
}
template <bool Z0, bool Z1>
DI u32x2 mfma2_scale(f32x4& d0, const u32x4& a0, const u32x4& b0, f32x4& d1, const u32x4& a1, const u32x4& b1, const f32x4& v, float a) {
  u32x2 o; float t0, t1;
#define M2S(C0, C1, K0, K1) \
  asm volatile("v_mfma_f32_16x16x32_bf16 %4, %11, %12, " C0 "\n\tv_mul_f32 %0, %6, %10\n\tv_mul_f32 %2, %7, %10\n\tv_mul_f32 %1, %8, %10\n\t" \
               "v_mfma_f32_16x16x32_bf16 %5, %13, %14, " C1 "\n\tv_mul_f32 %3, %9, %10\n\tv_cvt_pk_bf16_f32 %0, %0, %2\n\tv_cvt_pk_bf16_f32 %1, %1, %3" \
               : "=&v"(o[0]), "=&v"(o[1]), "=&v"(t0), "=&v"(t1), K0(d0), K1(d1) \
               : "v"(v[0]), "v"(v[1]), "v"(v[2]), "v"(v[3]), "v"(a), "v"(a0), "v"(b0), "v"(a1), "v"(b1))
  if constexpr (Z0 && Z1) M2S("0", "0", "=&v", "=&v");
  else if constexpr (Z0) M2S("0", "%5", "=&v", "+v");
  else M2S("%4", "%5", "+v", "+v");
#undef M2S
  return o;
}
DI void rcp4(f32x4& v) {
  float a, b, c, d;
  asm volatile("v_rcp_f32 %0, %4\n\tv_rcp_f32 %1, %5\n\tv_rcp_f32 %2, %6\n\tv_rcp_f32 %3, %7" : "=&v"(a), "=&v"(b), "=&v"(c), "=&v"(d) : "v"(v[0]), "v"(v[1]), "v"(v[2]), "v"(v[3]));
  v = f32x4{a, b, c, d};
}
DI bool all64(bool c) { return __builtin_amdgcn_ballot_w64(!c) == 0; }
DI float shfl_x(float v, unsigned lane, unsigned m) {
  return __builtin_bit_cast(float, __builtin_amdgcn_ds_bpermute((int)((lane ^ m) << 2), __builtin_bit_cast(int, v)));
}
DI unsigned umin(unsigned a, unsigned b) { return a < b ? a : b; }
DI unsigned rfl(unsigned v) { return __builtin_amdgcn_readfirstlane(v); }
DI rsrc_t mk_rsrc(const void* p, unsigned bytes) { return __builtin_amdgcn_make_buffer_rsrc((void*)p, (short)0, (int)bytes, 0x00020000); }
DI void dma16(unsigned ldsdst, unsigned voff, rsrc_t rs, unsigned soff) {
  asm volatile("s_mov_b32 m0, %0\n\ts_nop 0\n\tbuffer_load_dwordx4 %1, %2, %3 offen lds" ::"s"(ldsdst), "v"(voff), "s"(rs), "s"(soff) : "memory");
}
template <int N> DI void wait_vm() { asm volatile("s_waitcnt vmcnt(%0)" ::"i"(N) : "memory"); }
DI void drain_vm() { wait_vm<0>(); __builtin_amdgcn_s_waitcnt(0x0F70); }
DI void wait_lgkm0() { asm volatile("s_waitcnt lgkmcnt(0)" ::: "memory"); }
DI void bar() { asm volatile("" ::: "memory"); __builtin_amdgcn_s_barrier(); asm volatile("" ::: "memory"); }
DI void lbar() { wait_lgkm0(); bar(); }
template <class V> DI V lds_ld(unsigned a) { return *(LAS V*)((LAS char*)smem + a); }
template <class V> DI void lds_st(unsigned a, V v) { *(LAS V*)((LAS char*)smem + a) = v; }
DI bf16x4 lds_tr(unsigned a) { return __builtin_amdgcn_ds_read_tr16_b64_v4bf16((LAS bf16x4*)((LAS char*)smem + a)); }
DI f32x4 mfma(bf16x8 a, bf16x8 b, f32x4 c) { return __builtin_amdgcn_mfma_f32_16x16x32_bf16(a, b, c, 0, 0, 0); }
DI unsigned pk2(float a, float b) { f32x2 v = {a, b}; return __builtin_bit_cast(unsigned, __builtin_convertvector(v, bf16x2)); }
DI u32x2 pk4(f32x4 v) { u32x2 r = {pk2(v[0], v[1]), pk2(v[2], v[3])}; return r; }
DI bf16x8 frag(u32x2 lo, u32x2 hi) { u32x4 v = {lo[0], lo[1], hi[0], hi[1]}; return __builtin_bit_cast(bf16x8, v); }
DI float bflo(unsigned v) { return __builtin_bit_cast(float, v << 16); }
DI float bfhi(unsigned v) { return __builtin_bit_cast(float, v & 0xffff0000u); }
DI float rbf(float v) { return bflo(pk2(v, 0.f)); }
DI float relu2(float v) { float o; asm("v_add_f32 %0, %1, |%1|" : "=v"(o) : "v"(v)); return o; }
DI f32x4 phi4(f32x4 u, f32x4 rl, f32x4 rh) {
  const f32x4 l = u * rl;
  f32x4 e, a;
#pragma unroll
  for (int j = 0; j < 4; ++j) {
    e[j] = __builtin_amdgcn_fmed3f(__builtin_amdgcn_exp2f(l[j]), 0.f, 1.f);
    a[j] = relu2(u[j]);
  }
  return a * rh + e + 1e-6f;
}
DI f32x4 splat(float v) { return f32x4{v, v, v, v}; }
template <int CTRL> DI float dpp_add(float v) {
  return v + __builtin_bit_cast(float, __builtin_amdgcn_update_dpp(0, __builtin_bit_cast(int, v), CTRL, 0xf, 0xf, true));
}
DI float dot2(unsigned a, unsigned b, float c) {
  return __builtin_amdgcn_fdot2_f32_bf16(__builtin_bit_cast(bf16x2, a), __builtin_bit_cast(bf16x2, b), c, false);
}
DI void mfma_ip(f32x4& c, const u32x4& a, const u32x4& b) { asm volatile("v_mfma_f32_16x16x32_bf16 %0, %1, %2, %0" : "+v"(c) : "v"(a), "v"(b)); }
DI void mfma_c(f32x4& d, const u32x4& a, const u32x4& b, const f32x4& c) { asm volatile("v_mfma_f32_16x16x32_bf16 %0, %1, %2, %3" : "=&v"(d) : "v"(a), "v"(b), "v"(c)); }
DI void mfma_z(f32x4& c, const u32x4& a, const u32x4& b) { asm volatile("v_mfma_f32_16x16x32_bf16 %0, %1, %2, 0" : "=&v"(c) : "v"(a), "v"(b)); }
template <int N> DI void acc_fence(f32x4* c) {
  asm volatile("s_nop 7" ::: "memory");
#pragma unroll
  for (int i = 0; i < N; ++i) asm volatile("" : "+v"(c[i]));
}
DI u32x4 cat2(u32x2 a, u32x2 b) { return u32x4{a[0], a[1], b[0], b[1]}; }
DI u32x2 ldtr1(unsigned a) { return __builtin_bit_cast(u32x2, lds_tr(a)); }
template <int IMM> DI void set_m0i(unsigned v) { asm volatile("s_add_i32 m0, %0, %1" ::"s"(v), "i"(IMM) : "memory", "scc"); }
template <int OFF> DI void dma_goi(unsigned voff, rsrc_t rs, unsigned soff) {
  asm volatile("buffer_load_dwordx4 %0, %1, %2 offen offset:%3 lds" ::"v"(voff), "s"(rs), "s"(soff), "i"(OFF) : "memory");
}

DI unsigned poll_flags(gu32* word, bool active, unsigned tag) {
  unsigned v = 0;
#pragma unroll 1
  for (unsigned spins = 0; spins < SPIN_CAP; ++spins) {
    v = __hip_atomic_load(word, RLX_AGENT);
    if (all64(!active || v == tag)) break;
    __builtin_amdgcn_s_sleep(2);
  }
  asm volatile("" ::: "memory");
  return v;
}

DI void lds_set(unsigned a) { __hip_atomic_store((LAS unsigned*)smem + a / 4, 1u, __ATOMIC_RELAXED, __HIP_MEMORY_SCOPE_WORKGROUP); }
DI unsigned lds_get(unsigned a) { return __hip_atomic_load((LAS unsigned*)smem + a / 4, __ATOMIC_RELAXED, __HIP_MEMORY_SCOPE_WORKGROUP); }
DI void lds_wait(unsigned a) {
#pragma unroll 1
  for (unsigned spins = 0; spins < (SPIN_CAP << 4); ++spins) {
    if (rfl(lds_get(a))) break;
    __builtin_amdgcn_s_sleep(1);
  }
  asm volatile("" ::: "memory");
}

template <int I> struct IC { static constexpr int v = I; };
template <int B, int E, class Fn> DI void static_for(Fn&& f) {
  if constexpr (B < E) { f(IC<B>{}); static_for<B + 1, E>(f); }
}

enum { J_MLP = 0, J_KV = 1, J_Q = 2, J_SPARE = 3 };

struct Dma {
  rsrc_t rsA, rsB;
  unsigned vA0, vA1, vB0, vB1, vB2;
  unsigned sA[4], sB, advB;
  unsigned wa, wq;
  unsigned mA, mB, mW;
};

constexpr unsigned a_slot(unsigned s3) { return s3 == 0 ? 126976u : (s3 - 1) * 32768u; }
#define SB __builtin_amdgcn_sched_barrier(0)

template <int T> DI unsigned bofs(const unsigned (&boff)[T], int t) { return T == 6 && t >= 3 ? boff[t - 3] + 6144 : boff[t]; }
template <int T, bool TR, int KS, class Extra>
DI void kstep(f32x4 (&acc)[4][T], const u32x4 (&ac)[4], const u32x2 (&bc)[T][2], u32x4 (&an)[4], u32x2 (&bn)[T][2],
              const unsigned (&aoff)[4], const unsigned (&boff)[T], const Dma& d, Extra&& extra) {
  constexpr int s = KS >> 1, KIND = KS & 1, NP = KIND == 0 ? 4 : 3, ST = KIND == 0 ? (4 * T - 1) / NP : 2;
  constexpr int sn = (KS + 1) >> 1;
  constexpr unsigned asl = a_slot(sn % 3), bsl = ((KS + 1) % 5) * 12288u;
  static_for<0, 4 * T>([&](auto E) {
    constexpr int e = E.v, t = e >> 2, i = e & 3;
    const u32x4 bf = cat2(bc[t][0], bc[t][1]);
    if constexpr (KS == 0) { if (TR) mfma_z(acc[i][t], bf, ac[i]); else mfma_z(acc[i][t], ac[i], bf); }
    else { if (TR) mfma_ip(acc[i][t], bf, ac[i]); else mfma_ip(acc[i][t], ac[i], bf); }
    SB;
    if constexpr (KS < 19) {
      if constexpr (e < 4) an[e] = lds_ld<u32x4>(aoff[((KS + 1) & 1) + (sn % 3 == 0 ? 2 : 0)] + (sn % 3 == 0 ? 0u : asl) + e * 2048);
      else if constexpr (e < 4 + 2 * T) bn[(e - 4) >> 1][e & 1] = ldtr1(bofs<T>(boff, (e - 4) >> 1) + bsl + (e & 1) * 768);
    }
    constexpr int pc = e / ST;
    if constexpr (KIND == 0) {
      constexpr int adv = s < 8 ? (s + 2) * 128 : 0, dst = (int)a_slot((s + 2) % 3) + pc * 1024, off = dst >= adv ? adv : 0;
      if constexpr (pc < NP && e % ST == ST - 2) set_m0i<dst - off>(d.mA);
      if constexpr (pc < NP && e % ST == ST - 1) dma_goi<off>((pc & 1) ? d.vA1 : d.vA0, d.rsA, d.sA[pc] + (adv - off));
    } else {
      constexpr int h0 = (2 * s) % 5;
      if constexpr (pc < NP && e % ST == ST - 2) set_m0i<(h0 == 4 ? 0 : h0 * 12288) + pc * 1024>(h0 == 4 ? d.mW : d.mB);
      if constexpr (pc < NP && e % ST == ST - 1) dma_goi<0>(pc == 0 ? d.vB0 : (pc == 1 ? d.vB1 : d.vB2), d.rsB, d.sB);
    }
    extra(E);
    SB;
  });
}

struct RowStat { u32x4 xa; unsigned xb, ep, row; float s, q; };
DI void row_load(RowStat& R, rsrc_t rsX, char* ws, unsigned lane) {
  asm volatile("buffer_load_dwordx4 %0, %3, %5, %6 offen\n\tbuffer_load_dword %1, %4, %5, %6 offen offset:1024\n\tglobal_load_dword %2, %7, %8 sc1"
               : "=&v"(R.xa), "=&v"(R.xb), "=&v"(R.ep) : "v"(lane * 16), "v"(lane * 4), "s"(rsX), "s"(R.row * 1280), "v"(0u), "s"(ws) : "memory");
}
template <int NY> DI unsigned row_wait(const RowStat& R) {
  unsigned e;
  asm volatile("s_waitcnt vmcnt(%2)\n\tv_readfirstlane_b32 %0, %1\n\ts_nop 3" : "=s"(e) : "v"(R.ep), "i"(NY) : "memory");
  return rfl(e) + 1;
}
template <int K> DI void row_step(RowStat& R, unsigned lane) {
  if constexpr (K < 5) {
    if constexpr (K == 0) { R.s = 0.f; R.q = 0.f; }
    const unsigned v = K < 4 ? R.xa[K & 3] : R.xb;
    asm volatile("v_dot2c_f32_bf16 %0, %2, %3\n\tv_dot2c_f32_bf16 %1, %2, %2" : "+v"(R.s), "+v"(R.q) : "v"(v), "v"(0x3f803f80u));
  } else if constexpr (K < 11) {
    constexpr int C = K == 5 ? 0xB1 : (K == 6 ? 0x4E : (K == 7 ? 0x141 : (K == 8 ? 0x140 : (K == 9 ? 0x142 : 0x143))));
    if constexpr (K == 5) asm volatile("s_nop 2" : "+v"(R.s), "+v"(R.q));
    R.s = dpp_add<C>(R.s); R.q = dpp_add<C>(R.q);
  } else if constexpr (K == 11) {
    R.s *= 1.0f / D;
    R.q = __builtin_fmaxf(R.q * (1.0f / D) - R.s * R.s, 0.f);
  } else if constexpr (K == 12) {
    R.q = __builtin_amdgcn_sqrtf(rbf(rbf(R.q) + 1e-5f));
  } else {
    lds_st<unsigned>(lane == 63 ? L_GO + 32 + (R.row & 7) * 4 : L_GO + 64, pk2(R.s, R.q));
  }
  if constexpr (K >= 5 && K < 13) asm volatile("" : "+v"(R.s), "+v"(R.q));
}
DI void row_publish(const RowStat& R, char* ws, unsigned tag, unsigned tid) {
  if (tid < 8) __hip_atomic_store((gu64*)(ws + WS_STAT) + R.row + tid, ((unsigned long long)tag << 32) | lds_ld<unsigned>(L_GO + 32 + tid * 4), RLX_AGENT);
}

struct Gran { u32x2 g0, g1, g2; unsigned off, step; };
DI void gran_issue(Gran& G, rsrc_t rs) {
  asm volatile("buffer_load_dwordx2 %0, %3, %4, 0 offen sc1\n\tbuffer_load_dwordx2 %1, %3, %4, %5 offen sc1\n\t"
               "buffer_load_dwordx2 %2, %3, %4, %6 offen sc1"
               : "=&v"(G.g0), "=&v"(G.g1), "=&v"(G.g2) : "v"(G.off), "s"(rs), "s"(G.step), "s"(2 * G.step) : "memory");
}
DI void gran_load(Gran& G, rsrc_t rs) {
  asm volatile("buffer_load_dwordx2 %0, %3, %4, 0 offen sc1\n\tbuffer_load_dwordx2 %1, %3, %4, %5 offen sc1\n\t"
               "buffer_load_dwordx2 %2, %3, %4, %6 offen sc1\n\ts_waitcnt vmcnt(0)"
               : "=&v"(G.g0), "=&v"(G.g1), "=&v"(G.g2) : "v"(G.off), "s"(rs), "s"(G.step), "s"(2 * G.step) : "memory");
}

DI unsigned urow(unsigned pos) { const unsigned u = pos + ROT; return (u >= 54 ? u - 54 : u) * 40960u; }

constexpr unsigned wo_tile(unsigned v) { return (v / 5) * 10 + (v % 5 == 4 ? 4 : (v % 5 < 2 ? 2 * (v % 5) : 2 * (v % 5) + 1)); }
struct W2 { rsrc_t wd, wo; unsigned col; };
DI unsigned w2_off(const W2& W, int u, unsigned lw) {
  if (u < 54) return urow(u) + lw * 10240u + W.col;
  return wo_tile(u - 54) * 20480u + (lw >> 1) * ((u - 54) % 5 == 4 ? 102400u : 20480u) + (lw & 1) * 10240u + W.col;
}
DI u32x4 w2_load(const W2& W, int u, unsigned lw, unsigned voff) {
  return __builtin_amdgcn_raw_buffer_load_b128(u < 54 ? W.wd : W.wo, voff, w2_off(W, u, lw), 0);
}

template <int T, bool TR, bool PF, bool LASTA, class Pre>
DI void main_loop(Pre&& pre, RowStat& R, char* ws, f32x4 (&acc)[4][T], unsigned (&boff)[T], Dma& d, unsigned w, unsigned lane, unsigned tid,
                  unsigned& tag, unsigned rb, unsigned ccol, rsrc_t rsB2, rsrc_t rsWS, unsigned cb, u32x4 (&pre2)[36]) {
  const unsigned g = lane >> 4, r = lane & 15, m = w & 3;
  unsigned aoff[4];
  aoff[0] = (64 * m + r) * 128 + ((g ^ (r >> 1)) << 4);
  aoff[1] = aoff[0] ^ 64;
  aoff[2] = aoff[0] + a_slot(0); aoff[3] = aoff[1] + a_slot(0);
  const unsigned wq = d.wq, wa = d.wa;
  asm volatile("" : "+v"(aoff[0]), "+v"(aoff[1]), "+v"(aoff[2]), "+v"(aoff[3]), "+v"(boff[0]), "+v"(boff[1]), "+v"(boff[2]));
  if (T == 5) asm volatile("" : "+v"(boff[3]), "+v"(boff[4]));
  Gran G;
  unsigned v2, vb2, vpf, tA[3], tB[3];
  row_load(R, d.rsA, ws, lane);
  wait_vm<6 + NA1>();
  lbar();
  u32x4 a0[4], a1[4];
  u32x2 b0[T][2], b1[T][2];
#pragma unroll
  for (int i = 0; i < 4; ++i) a0[i] = lds_ld<u32x4>(aoff[2] + i * 2048);
#pragma unroll
  for (int t = 0; t < T; ++t) { b0[t][0] = ldtr1(bofs<T>(boff, t)); b0[t][1] = ldtr1(bofs<T>(boff, t) + 768); }
  SB;
#pragma unroll
  for (int i = NA1; i < 4; ++i) dma16(a_slot(1) + d.mA + i * 1024, (i & 1) ? d.vA1 : d.vA0, d.rsA, d.sA[i] + 128);
  const bool y = w >= 4;
  bool ok = true;
  auto ln_store = [&] {
    const float v0 = y ? -bflo(G.g0[0]) : __builtin_bit_cast(float, G.g0[0]) + __builtin_bit_cast(float, G.g1[0]) + __builtin_bit_cast(float, G.g2[0]);
    const unsigned la = L_MISC + (y ? tid * 4 - 1024 : 2048 + tid * 4);
    lds_st<float>(la, v0);
    lds_st<float>(y ? la + 1024 : L_GO + 64 + (lane & 7) * 4, __builtin_amdgcn_rcpf(bfhi(G.g0[0])));
  };
  static_for<0, 10>([&](auto S_) {
    constexpr int s = S_.v, L = 4 * T;
    constexpr bool WU = TR && !PF && s % 2 == 0 && s < 8;
    constexpr int NX = (PF && s >= 1 && 2 * s <= NPRE) ? 2 : 0;
    kstep<T, TR, 2 * s>(acc, a0, b0, a1, b1, aoff, boff, d, [&](auto E) {
      constexpr int e = E.v;
      if constexpr (s == 2) {
        if constexpr (e == 2) { vb2 = v2 + (3 * wq) * 10240u; asm volatile("" : "+v"(vb2)); }
        if constexpr (e == 5) { G.off = w < 4 ? WS_CSUM + ccol * 8 : WS_STAT + (rb * 256 + tid - 256) * 8; asm volatile("" : "+v"(G.off)); }
        if constexpr (e == 8) { G.step = w < 4 ? NCOL * 8 : 0; tA[0] = urow(wa) + cb * 128u; asm volatile("" : "+s"(G.step), "+s"(tA[0])); }
        if constexpr (e == 10) { tA[1] = urow(8 + wa) + cb * 128u; asm volatile("" : "+s"(tA[1])); }
        if constexpr (e == 12) { tA[2] = urow(31 + wa) + cb * 128u; asm volatile("" : "+s"(tA[2])); }
        if constexpr (e == 14) {
          tB[0] = urow(16) + cb * 128u; tB[1] = urow(w < 4 ? 19u : 22u) + cb * 128u; tB[2] = urow(w < 4 ? 25u : 28u) + cb * 128u;
          asm volatile("" : "+s"(tB[0]), "+s"(tB[1]), "+s"(tB[2]));
        }
      }
      if constexpr (!TR && s == 3 && e == 2) pre(0);
      if constexpr (WU && e == L - 1) pre(s / 2);
      if constexpr (NX && e >= L - 2) {
        constexpr int u = 2 * (s - 1) + e - (L - 2);
        pre2[u] = __builtin_amdgcn_raw_buffer_load_b128(rsB2, vpf, urow(36 + u) + (w & 3) * 10240u + cb * 128u, 0);
      }
      if constexpr (s < 7) { if constexpr (e == L - 3) d.sB += s == 0 ? d.advB * (1 + (w >> 2)) : d.advB; }
      else if constexpr (s == 7) {
        if constexpr (e == L - 6) d.vB0 = y ? vb2 : d.vB0;
        if constexpr (e == L - 4) d.vB1 = y ? vb2 + 10240u : d.vB1;
        if constexpr (e == L - 3) d.vB2 = y ? vb2 + 20480u : d.vB2;
        if constexpr (e == L - 2) d.sB = y ? tB[0] : d.sB + d.advB;
        if constexpr (e == L - 1) d.rsB = y ? rsB2 : d.rsB;
      } else {
        if constexpr (e == L - 3) { d.sB = tB[s - 7]; d.rsB = rsB2; d.vB0 = vb2; d.vB1 = vb2 + 10240u; d.vB2 = vb2 + 20480u; }
      }
      if constexpr (s == 9 && e == L - 4) {
        asm volatile("s_waitcnt vmcnt(7)" : "+v"(G.g0), "+v"(G.g1), "+v"(G.g2)::"memory");
        ok = all64(G.g0[1] == tag && G.g1[1] == tag && G.g2[1] == tag);
      }
      if constexpr (s == 9 && e == L - 3) ln_store();
    });
    if constexpr (s == 0) {
      tag = row_wait<8 - NA1>(R);
      static_for<0, 14>([&](auto K) { row_step<K.v>(R, lane); });
    }
    if constexpr (s == 9) {
      if (__builtin_expect(!ok, 0)) {
#pragma unroll 1
        for (unsigned spins = 0; spins < SPIN_CAP; ++spins) {
          __builtin_amdgcn_s_sleep(2);
          gran_load(G, rsWS);
          if (all64(G.g0[1] == tag && G.g1[1] == tag && G.g2[1] == tag)) break;
        }
        ln_store();
      }
    }
    wait_lgkm0();
    if constexpr (s < 9) wait_vm<4 + NX + WU>();
    bar();
    if constexpr (s == 9) { if (y) __builtin_amdgcn_s_setprio(1); }
    SB;
    kstep<T, TR, 2 * s + 1>(acc, a1, b1, a0, b0, aoff, boff, d, [&](auto E) {
      constexpr int e = E.v;
      if constexpr (s == 0 && e == 6) {
        if (y) {
          const unsigned so = d.sB - d.advB;
          dma16(d.mW + 49152, d.vB0, d.rsB, so); dma16(d.mW + 49152 + 1024, d.vB1, d.rsB, so); dma16(d.mW + 49152 + 2048, d.vB2, d.rsB, so);
        }
      }
      if constexpr (s == 0 && e == L - 4) {
        v2 = (lane >> 3) * 1280 + (((lane & 7) ^ (((lane >> 4) & 3) << 1)) << 4);
        vpf = y ? v2 : 0x7ffffff0u;
        asm volatile("" : "+v"(v2), "+v"(vpf));
      }
      if constexpr (s == 0 && e == L - 6) row_publish(R, ws, tag, tid);
      if constexpr (s == 8 && e == 0) gran_issue(G, rsWS);
      if constexpr (s >= 7 && s < 9 && e >= L - 4) {
        d.rsA = rsB2; d.vA0 = v2; d.vA1 = v2;
        d.sA[e - (L - 4)] = tA[s - 7] + (e - (L - 4)) * 10240;
      }
      if constexpr (s == 9 && LASTA && e == 7) {
        if (wa < 5) {
#pragma unroll
          for (int i = 0; i < 4; ++i) dma16(a_slot(0) + d.mA + i * 1024, v2, rsB2, tA[2] + i * 10240);
        }
      }
    });
  });
  __builtin_amdgcn_s_setprio(0);
  acc_fence<4 * T>(&acc[0][0]);
}

DI void st16(u32x4 v, rsrc_t rs, unsigned voff, unsigned soff) {
  __builtin_amdgcn_raw_buffer_store_b128(v, rs, voff, soff, 16);
  asm volatile("s_nop 1" ::"v"(v) : "memory");
}
DI void publish_flag(char* ws, unsigned flag_off, unsigned tag, unsigned tid) {
  drain_vm();
  bar();
  if (tid == 0) __hip_atomic_store((gu32*)(ws + flag_off), tag, RLX_AGENT);
}
DI void st_frag(rsrc_t rs, unsigned lane, unsigned off, u32x2 lo, u32x2 hi) { st16(cat2(lo, hi), rs, lane * 16, off); }
DI void shared_send(unsigned w, unsigned lane, const u32x2 (&sh)[4]) {
  if (w >= 4) lds_st<u32x4>(L_SCR + w * 1024 + lane * 16, cat2(sh[0], sh[1]));
  else lds_st<u32x4>(L_SCR + w * 1024 + lane * 16, cat2(sh[2], sh[3]));
  asm volatile("" ::: "memory");
}
DI void shared_recv(rsrc_t rs, unsigned w, unsigned lane, unsigned off, unsigned str, const u32x2 (&sh)[4]) {
  lbar();
  u32x4 rcv = lds_ld<u32x4>(L_SCR + (w ^ 4) * 1024 + lane * 16);
  u32x2 r0 = {rcv[0], rcv[1]}, r1 = {rcv[2], rcv[3]};
  if (w >= 4) { st_frag(rs, lane, off + 2 * str, r0, sh[2]); st_frag(rs, lane, off + 3 * str, r1, sh[3]); }
  else        { st_frag(rs, lane, off, sh[0], r0); st_frag(rs, lane, off + str, sh[1], r1); }
}

__global__ __launch_bounds__(512, 2) void fused_block(const void* px, const void* pwqkv, const void* pwo, const void* pwg, const void* pwu,
                                                         const void* pwd, void* pout, char* wsraw) {
  const unsigned long long wsr = (unsigned long long)wsraw, wsm = ((wsr & 1) - 1) & (WS_ALIGN - 2);
  char* const ws = (char*)((wsr + wsm) & ~(wsm | 1));
  unsigned tid = __builtin_amdgcn_workitem_id_x(), lane = tid & 63;
  const unsigned w = rfl(tid >> 6);
  const unsigned b = __builtin_amdgcn_workgroup_id_x(), xcd = b & 7, slot = b >> 3;
  const unsigned rb = 4 * (xcd & 1) + (slot & 3), jq = xcd >> 1, ji = slot >> 2;
  const unsigned spi = xcd * 2 + (slot & 1);
  const unsigned long long jl0 = (jq & 1) ? 0x4123220908070605ull : 0x4021200403020100ull, jl1 = (jq & 1) ? 0x4327266011100f0eull : 0x422524600d0c0b0aull;
  const unsigned jsel = (jq ^ ((slot & 30) == 18 ? 2 : 0)) & 2;
  const unsigned jb = (unsigned)((jsel ? jl1 : jl0) >> (8 * ji)) & 0xff, jtype = jb >> 5, jidx = jb & 31;

  Dma d;
  RowStat rs_;
  const bool mlp = jtype == J_MLP;
  const unsigned pitch = mlp ? 3456u : 3840u, wq = (w + slot) & 3, wa = (w + ji) & 7;
  d.wa = wa; d.wq = wq;
  const rsrc_t rsX = mk_rsrc(px, S * D * 2);
  d.vA0 = (lane >> 3) * 1280 + (((lane & 7) ^ (lane >> 4)) << 4); d.vA1 = d.vA0 ^ 64;
  d.rsA = rsX; d.mA = wa * 4096u;
#pragma unroll
  for (int i = 0; i < 4; ++i) d.sA[i] = (rb * 256 + 32 * wa + 8 * i) * 1280;
  unsigned tag;
  asm volatile("s_nop 4" :: "s"(rsX) : "memory");
#pragma unroll
  for (int i = 0; i < 4; ++i) dma16(a_slot(0) + d.mA + i * 1024, (i & 1) ? d.vA1 : d.vA0, d.rsA, d.sA[i]);
  SB;
  {
    unsigned vb[3];
    const unsigned lim = mlp ? 176u : 144u;
    {
      const unsigned r0 = (lane * 43) >> 9, p0 = lane - 12 * r0;
      const bool w1 = p0 >= 8, w2 = p0 >= 4;
      const unsigned r1 = r0 + (w1 ? 6 : 5), p1 = p0 + (w1 ? -8 : 4), r2 = r0 + (w2 ? 11 : 10), p2 = p0 + (w2 ? -4 : 8);
      vb[0] = r0 * pitch + umin(p0 * 16, lim);
      vb[1] = r1 * pitch + umin((p1 * 16) ^ ((r1 & 8) << 2), lim);
      vb[2] = r2 * pitch + umin((p2 * 16) ^ 32, lim);
    }
    d.vB0 = vb[0]; d.vB1 = vb[1]; d.vB2 = vb[2];
    const unsigned mh = wq >> 1;
    const void* bw = mlp ? (mh ? pwu : pwg) : pwqkv;
    const unsigned cj = mlp ? 192u : 160u * jtype, cm = mlp ? 0u : (jtype == J_KV ? 1280u : 160u), c0 = jtype == J_KV ? 1280u : 0u;
    d.rsB = mk_rsrc(bw, D * pitch);
    d.sB = (16 * (wq & 1) + (w < 4 ? 32 : 0)) * pitch + c0 + cj * jidx + cm * mh; d.advB = 64 * pitch;
    d.mB = L_B + wq * 3072u + (w < 4 ? 0u : 12288u); d.mW = L_B + wq * 3072u + (w < 4 ? 49152u : 0u);
    const unsigned bdst = L_B + (w < 4 ? 12288u : 0u) + wq * 3072u;
    rs_.row = rfl(b * 8 + w);
    if (jtype != J_SPARE) {
      dma16(bdst, d.vB0, d.rsB, d.sB); dma16(bdst + 1024, d.vB1, d.rsB, d.sB); dma16(bdst + 2048, d.vB2, d.rsB, d.sB);
      const unsigned so = d.sB + d.advB;
      dma16(bdst + 24576, d.vB0, d.rsB, so); dma16(bdst + 24576 + 1024, d.vB1, d.rsB, so); dma16(bdst + 24576 + 2048, d.vB2, d.rsB, so);
#pragma unroll
      for (int i = 0; i < NA1; ++i) dma16(a_slot(1) + d.mA + i * 1024, (i & 1) ? d.vA1 : d.vA0, d.rsA, d.sA[i] + 128);
    }
    d.sB += d.advB;
  }
  unsigned long long pc0; unsigned codeb;
  asm volatile("s_getpc_b64 %0\n.Lfb_code_mark:\n\ts_mov_b32 %1, .Lfb_code_end0-.Lfb_code_mark\n\ts_max_u32 %1, %1, .Lfb_code_end1-.Lfb_code_mark"
               : "=s"(pc0), "=s"(codeb) :: "scc");
  lds_st<unsigned>(L_GO + (tid & 1) * 4, 0u);

  const unsigned t2 = 28 * xcd + slot;
  const bool has2 = slot < 28 && t2 < 220;
  const unsigned G = t2 / 10, cb = t2 % 10;
  unsigned g = lane >> 4, r = lane & 15, q4 = r >> 2, p4 = lane & 3;
  const rsrc_t rsWd = mk_rsrc(pwd, has2 ? F * D * 2 : 0);
  const rsrc_t rsWS = mk_rsrc(ws, WS_END);
  unsigned v2 = (lane >> 3) * 1280 + (((lane & 7) ^ (((lane >> 4) & 3) << 1)) << 4);
  auto relane = [&] {
    asm volatile("" : "+v"(tid));
    lane = tid & 63; g = lane >> 4; r = lane & 15; q4 = r >> 2; p4 = lane & 3;
    v2 = (lane >> 3) * 1280 + (((lane & 7) ^ (((lane >> 4) & 3) << 1)) << 4);
  };
  const unsigned m = w & 3, n = w >> 2;
  const unsigned brow = L_B + (8 * g + q4) * 192;
  const unsigned bx = (g & 1) << 5;

  u32x4 pre2[36];
  W2 W;
  W.wd = rsWd; W.wo = mk_rsrc(pwo, has2 ? D * D * 2 : 0); W.col = cb * 128u;
  if (jtype == J_MLP) {
    f32x4 acc[4][6];
    unsigned boff[6];
#pragma unroll
    for (int t = 0; t < 3; ++t) {
      const unsigned gt = n ? (t == 2 ? 3 : 4 + t) : t;
      boff[t] = brow + ((32 * gt + 8 * p4) ^ bx); boff[t + 3] = boff[t] + 6144;
    }
    const unsigned ccol = tid < 96 ? 1920 + 96 * jidx + tid : 3648 + 96 * jidx + (tid < 192 ? tid - 96 : 0);
    main_loop<6, true, true, true>([](int) {}, rs_, ws, acc, boff, d, w, lane, tid, tag, rb, ccol, rsWd, rsWS, cb, pre2);
    relane();
    float mm[4];
    f32x4 cg[3], cu[3];
#pragma unroll
    for (int i = 0; i < 4; ++i) mm[i] = lds_ld<float>(L_MISC + (64 * m + 16 * i + r) * 4);
#pragma unroll
    for (int t = 0; t < 3; ++t) {
      const unsigned gt = n ? (t == 2 ? 3 : 4 + t) : t;
      cg[t] = lds_ld<f32x4>(L_MISC + 2048 + (16 * gt + 4 * g) * 4); cu[t] = lds_ld<f32x4>(L_MISC + 2048 + (96 + 16 * gt + 4 * g) * 4);
    }
    auto hval = [&](int i, int t) {
      f32x4 gg = acc[i][t] + mm[i] * cg[t];
      const f32x4 uu = acc[i][t + 3] + mm[i] * cu[t];
#pragma unroll
      for (int j = 0; j < 4; ++j) gg[j] = relu2(gg[j]);
      return pk4(gg * uu);
    };
    const unsigned ho = WS_HA + rb * RBSZ + jidx * 49152 + m * 4 * 3072;
#pragma unroll
    for (int i = 0; i < 4; ++i) { st_frag(rsWS, lane, ho + i * 3072 + n * 2048, hval(i, 0), hval(i, 1)); SB; }
    const u32x2 sh[4] = {hval(0, 2), hval(1, 2), hval(2, 2), hval(3, 2)};
    shared_send(w, lane, sh);
    shared_recv(rsWS, w, lane, ho + 1024, 3072, sh);
    publish_flag(ws, WS_FLAGH + (rb * 32 + jidx) * FS, tag, tid);
  } else if (jtype == J_KV) {
    f32x4 acc[4][5];
    unsigned boff[5];
    boff[0] = brow + ((80 * n + 8 * p4) ^ bx); boff[1] = boff[0] + 6144;
    boff[2] = brow + ((80 * n + 40 + 8 * p4) ^ bx); boff[3] = boff[2] + 6144;
    boff[4] = brow + ((80 * n + 32 + 40 * (p4 >> 1)) ^ bx) + (p4 & 1) * 6144;
    const unsigned kb = 126976u + m * 6720 + n * 3360, dump = L_MISC - 2048 + lane * 16;
    const unsigned ea = (q4 == 1 || r == 0) ? kb + (r == 0 ? 20 : 12 + r) * 80 : 0u, eb = (q4 == 3 || r == 0) ? kb + 1680 + (r == 0 ? 20 : 4 + r) * 80 : 0u;
    unsigned ka[6] = {kb + r * 80 + g * 16, ea ? ea + g * 16 : dump, eb ? eb + g * 16 : dump, g == 0 ? kb + r * 80 + 64 : dump,
                      g == 2 ? kb + 1680 + r * 80 + 64 : dump, (g == 0 && ea) ? ea + 64 : ((g == 2 && eb) ? eb + 64 : dump)};
    auto pin = [&](int P) { if (P == 0) asm volatile("" : "+v"(ka[0]), "+v"(ka[1]), "+v"(ka[2]), "+v"(ka[3]), "+v"(ka[4]), "+v"(ka[5])); };
    const unsigned ccol = tid < 80 ? 640 + 80 * jidx + tid : 1280 + 80 * jidx + (tid < 160 ? tid - 80 : 0);
    main_loop<5, false, false, false>(pin, rs_, ws, acc, boff, d, w, lane, tid, tag, rb, ccol, rsWd, rsWS, cb, pre2);
    relane();
    float cs[5];
    cs[0] = lds_ld<float>(L_MISC + 2048 + (40 * n + r) * 4);
    cs[1] = lds_ld<float>(L_MISC + 2048 + (80 + 40 * n + r) * 4);
    cs[2] = lds_ld<float>(L_MISC + 2048 + (40 * n + 20 + r) * 4);
    cs[3] = lds_ld<float>(L_MISC + 2048 + (80 + 40 * n + 20 + r) * 4);
    cs[4] = lds_ld<float>(L_MISC + 2048 + ((q4 & 1) * 80 + 40 * n + 16 + 20 * (q4 >> 1) + (r & 3)) * 4);
    u32x2 pk[4][3], vv[4][3];
    unsigned vmask = 0u - (q4 & 1);
    asm volatile("" : "+v"(vmask));
    f32x4 mm4[4], rr4[4];
#pragma unroll
    for (int i = 0; i < 4; ++i) {
      mm4[i] = lds_ld<f32x4>(L_MISC + (64 * m + 16 * i + 4 * g) * 4); rr4[i] = lds_ld<f32x4>(L_MISC + 1024 + (64 * m + 16 * i + 4 * g) * 4);
    }
    SB;
#pragma unroll
    for (int i = 0; i < 4; ++i) {
      const f32x4 mm = mm4[i], rr = rr4[i];
      const f32x4 rl = rr * 1.4426950408889634f, rh = rr * 0.5f;
      f32x4 val[5];
#pragma unroll
      for (int t = 0; t < 5; ++t) val[t] = acc[i][t] + mm * cs[t];
#pragma unroll
      for (int t = 0; t < 3; ++t) pk[i][t] = pk4(phi4(val[2 * t], rl, rh));
      vv[i][0] = pk4(val[1] * rr); vv[i][1] = pk4(val[3] * rr);
      const u32x2 v4 = pk4(val[4] * rr);
      vv[i][2] = u32x2{bsel(vmask, v4[0], 0x3f803f80u), bsel(vmask, v4[1], 0x3f803f80u)};
    }
    SB;
    f32x4 P[7] = {};
#pragma unroll
    for (int i = 0; i < 4; i += 2) {
      const bf16x8 k0 = frag(pk[i][0], pk[i + 1][0]), k2 = frag(pk[i][1], pk[i + 1][1]), k4 = frag(pk[i][2], pk[i + 1][2]);
      const bf16x8 v1 = frag(vv[i][0], vv[i + 1][0]), v3 = frag(vv[i][1], vv[i + 1][1]), v4 = frag(vv[i][2], vv[i + 1][2]);
      P[0] = mfma(k0, v1, P[0]); P[1] = mfma(k0, v4, P[1]); P[2] = mfma(k4, v1, P[2]);
      P[3] = mfma(k2, v3, P[3]); P[4] = mfma(k2, v4, P[4]); P[5] = mfma(k4, v3, P[5]); P[6] = mfma(k4, v4, P[6]);
    }
    lds_st<f32x4>(ka[0], P[0]); lds_st<f32x4>(ka[0] + 1680, P[3]); lds_st<f32x4>(ka[1], P[1]); lds_st<f32x4>(ka[2], P[4]);
    lds_st<f32x4>(ka[3], P[2]); lds_st<f32x4>(ka[4], P[5]); lds_st<f32x4>(ka[5], P[6]);
    lbar();
    if (tid < 210) {
      const unsigned a = 126976u + tid * 32;
      const f32x4 t0 = (lds_ld<f32x4>(a) + lds_ld<f32x4>(a + 6720)) + (lds_ld<f32x4>(a + 13440) + lds_ld<f32x4>(a + 20160));
      const f32x4 t1 = (lds_ld<f32x4>(a + 16) + lds_ld<f32x4>(a + 6736)) + (lds_ld<f32x4>(a + 13456) + lds_ld<f32x4>(a + 20176));
      st16(cat2(pk4(t0), pk4(t1)), rsWS, tid * 16, WS_KVP + (rb * 32 + 4 * jidx) * 840);
    }
    wait_lgkm0();
    publish_flag(ws, WS_FLAGKV + ((jidx >> 1) * 32 + (jidx & 1) * 8 + rb) * FS, tag, tid);
  } else if (jtype == J_Q) {
    f32x4 acc[4][5];
    unsigned boff[5];
#pragma unroll
    for (int t = 0; t < 5; ++t) boff[t] = brow + n * 6144 + ((32 * t + 8 * p4) ^ bx);
    const unsigned ccol = 160 * jidx + (tid < 160 ? tid : 0);
    unsigned tch[4] = {0u, 0u, 0u, 0u};
    auto warm = [&](int P) {
      const unsigned gq = (rb * 4 + jidx) * 8 + w;
      const unsigned vo = P == 0 ? ((rb & 3) * 8 + w) * 16 + (lane & 15) : (P == 3 ? gq * 25 + (lane < 25 ? lane : 0) : gq * 34 + (lane < 34 ? lane : 0) + (P - 1) * 8576);
      const rsrc_t rs = P == 0 ? mk_rsrc((const void*)pc0, codeb) : (P == 3 ? mk_rsrc(pwo, D * D * 2) : mk_rsrc(pwd, F * D * 2));
      asm volatile("buffer_load_dword %0, %1, %2, 0 offen" : "=&v"(tch[P]) : "v"(vo * 128), "s"(rs) : "memory");
    };
    main_loop<5, true, false, false>(warm, rs_, ws, acc, boff, d, w, lane, tid, tag, rb, ccol, rsWd, rsWS, cb, pre2);
    relane();
    asm volatile("" :: "v"(tch[0]), "v"(tch[1]), "v"(tch[2]), "v"(tch[3]));
    u32x2 pq[4][5];
    {
      float mm[4], rr[4];
      f32x4 c[5];
#pragma unroll
      for (int i = 0; i < 4; ++i) { mm[i] = lds_ld<float>(L_MISC + (64 * m + 16 * i + r) * 4); rr[i] = lds_ld<float>(L_MISC + 1024 + (64 * m + 16 * i + r) * 4); }
#pragma unroll
      for (int t = 0; t < 5; ++t) c[t] = lds_ld<f32x4>(L_MISC + 2048 + (80 * n + 16 * t + 4 * g) * 4);
      SB;
#pragma unroll
      for (int i = 0; i < 4; ++i)
#pragma unroll
        for (int t = 0; t < 5; ++t) pq[i][t] = pk4(phi4(acc[i][t] + mm[i] * c[t], splat(rr[i] * 1.4426950408889634f), splat(rr[i] * 0.5f)));
#pragma unroll
      for (int i = 0; i < 4; ++i) asm volatile("" : "+v"(pq[i][0]), "+v"(pq[i][1]), "+v"(pq[i][2]), "+v"(pq[i][3]), "+v"(pq[i][4]));
    }
    constexpr unsigned KP = 208, KQ = 84 * KP;
    unsigned wa[2];
#pragma unroll
    for (int k = 0; k < 2; ++k) {
      const unsigned qd = 2 * (tid < 420 ? tid : 0) + k, hd = qd / 105, e = (qd % 105) / 5, dq = qd % 5, hl = hd & 3;
      const unsigned Q = hl * 5 + dq;
      wa[k] = (hd >> 2) * KQ + (e < 20 ? hl * 20 + e : 80 + hl) * KP + (Q >> 3) * 64 + (Q & 3) * 16 + ((Q >> 2) & 1) * 8;
    }
    unsigned fk = n * KQ + r * KP + g * 16, fz = n * KQ + (80 + (r & 3)) * KP + g * 16;
    unsigned gm[3] = {g < 1 ? ~0u : 0u, g < 2 ? ~0u : 0u, g < 3 ? ~0u : 0u};
    u32x4 B[4][3];
    f32x4 eps4 = splat(1e-6f);
#pragma unroll
    for (int i = 0; i < 4; ++i) {
      B[i][0] = cat2(pq[i][0], pq[i][1]); B[i][1] = cat2(pq[i][2], pq[i][3]); B[i][2] = cat2(pq[i][4], u32x2{0u, 0u});
      asm volatile("" : "+v"(B[i][0]), "+v"(B[i][1]), "+v"(B[i][2]));
    }
    asm volatile("" : "+v"(wa[0]), "+v"(wa[1]), "+v"(fk), "+v"(fz), "+v"(gm[0]), "+v"(gm[1]), "+v"(gm[2]), "+v"(eps4));
    wait_vm<0>();
    bar();
#pragma unroll
    for (int k = 0; k < 5; ++k) lds_st<u32x4>(k * 8192 + tid * 16, u32x4{0u, 0u, 0u, 0u});
    if (w == 0) poll_flags((gu32*)(ws + WS_FLAGKV + (jidx * 32 + (lane & 15)) * FS), true, tag);
    lbar();
    if (w < 7) {
      u32x4 v[8];
#pragma unroll
      for (int j = 0; j < 8; ++j)
        v[j] = __builtin_amdgcn_raw_buffer_load_b128(rsWS, (tid < 420 ? tid : 0) * 16, WS_KVP + (j * 32 + 8 * jidx) * 840, 16);
      SB;
      f32x4 t0 = {0.f, 0.f, 0.f, 0.f}, t1 = t0;
#pragma unroll
      for (int j = 0; j < 8; ++j) {
        t0 += f32x4{bflo(v[j][0]), bfhi(v[j][0]), bflo(v[j][1]), bfhi(v[j][1])};
        t1 += f32x4{bflo(v[j][2]), bfhi(v[j][2]), bflo(v[j][3]), bfhi(v[j][3])};
      }
      lds_st<u32x2>(wa[0], pk4(t0)); lds_st<u32x2>(wa[1], pk4(t1));
    }
    lbar();
    u32x4 A[12];
#pragma unroll
    for (int P = 0; P < 3; ++P) A[P] = lds_ld<u32x4>(fz + P * 64);
#pragma unroll
    for (int u = 0; u < 5; ++u) A[3 + u] = lds_ld<u32x4>(fk + u * 16 * KP + (u < 3 ? 0 : 64));
#pragma unroll
    for (int u = 1; u < 5; ++u) A[7 + u] = lds_ld<u32x4>(fk + u * 16 * KP + (u < 3 ? 64 : 128));
    SB;
    u32x2 sh[4];
    f32x4 den[4], num[2][5];
    static_for<0, 12>([&](auto E) {
      constexpr int P = E.v >> 2, i = E.v & 3;
      if constexpr (P == 0) mfma_c(den[i], A[0], B[i][0], eps4); else mfma_ip(den[i], A[P], B[i][P]);
    });
    u32x2 o[5];
    const unsigned oo = WS_HA + rb * RBSZ + 18 * 49152 + jidx * 81920 + m * 4 * 5120;
    float rr[4][5];
    constexpr auto mu = [](int k) { return k < 5 ? k : k - 4; };
    constexpr auto ma = [](int k) { return 3 + k; };
    constexpr auto mb = [](int k) { return k < 3 ? 0 : (k < 7 ? 1 : 2); };
    static_for<0, 9>([&](auto K) {
      constexpr int k = K.v;
      if constexpr (k < 5) mfma_z(num[0][mu(k)], A[ma(k)], B[0][mb(k)]); else mfma_ip(num[0][mu(k)], A[ma(k)], B[0][mb(k)]);
      if constexpr (k >= 5) rcp4(den[k - 5]);
      SB;
    });
    asm volatile("s_nop 0");
    SB;
#pragma unroll
    for (int i = 0; i < 4; ++i) {
      rr[i][0] = den[i][0]; rr[i][4] = den[i][3];
#pragma unroll
      for (int u = 1; u < 4; ++u) {
        const u32x4 db = __builtin_bit_cast(u32x4, den[i]);
        rr[i][u] = __builtin_bit_cast(float, bsel(gm[u - 1], db[u - 1], db[u]));
      }
    }
    SB;
    static_for<0, 4>([&](auto I) {
      constexpr int i = I.v, c = i & 1, x = c ^ 1;
      static_for<0, 5>([&](auto U) {
        constexpr int u = U.v, k = 2 * u;
        if constexpr (i == 3) o[u] = scale_pk4(num[c][u], rr[i][u]);
        else if constexpr (u < 4)
          o[u] = mfma2_scale<(k < 5), (k + 1 < 5)>(num[x][mu(k)], A[ma(k)], B[i + 1][mb(k)], num[x][mu(k + 1)], A[ma(k + 1)], B[i + 1][mb(k + 1)], num[c][u], rr[i][u]);
        else { mfma_ip(num[x][4], A[11], B[i + 1][2]); o[4] = scale_pk4(num[c][4], rr[i][4]); }
        if constexpr (u == 1) st_frag(rsWS, lane, oo + i * 5120 + n * 2048, o[0], o[1]);
        if constexpr (u == 3) st_frag(rsWS, lane, oo + i * 5120 + n * 2048 + 1024, o[2], o[3]);
        if constexpr (u == 4) sh[i] = o[4];
        SB;
      });
    });
    shared_send(w, lane, sh);
    shared_recv(rsWS, w, lane, oo + 4096, 5120, sh);
    publish_flag(ws, WS_FLAGO + (rb * 32 + jidx) * FS, tag, tid);
    if (b == 224) {
      poll_flags((gu32*)(ws + WS_FLAGH + tid * FS), (tid & 31) < (tid < 256 ? 18u : 4u), tag);
      bar();
      if (tid == 0) __hip_atomic_store((gu32*)(ws + WS_EPOCH), tag, RLX_AGENT);
    }
  } else {
    const unsigned rk = spi * 4 + (w & 3), p0 = w < 4 ? rk : 128 + rk, p1 = p0 + 64;
    auto unit = [](unsigned q) { return q < 60 ? q + 30 : (q < 90 ? q - 60 : q); };
    const unsigned u0 = unit(p0), u1 = unit(p1);
    unsigned k10 = 0x00003f80u, k01 = 0x3f800000u;
    asm volatile("" : "+v"(k10), "+v"(k01));
    auto cs_src = [&](unsigned u, rsrc_t& rs, unsigned& cv, unsigned& st) {
      const unsigned cline = u / 3, third = u % 3;
      const void* cw = cline < 30 ? pwqkv : (cline < 57 ? pwg : pwu);
      const unsigned cpitch = cline < 30 ? 3840u : 3456u;
      const unsigned clocal = cline < 30 ? cline : (cline < 57 ? cline - 30 : cline - 57);
      rs = mk_rsrc(cw, u < 252 ? D * cpitch : 0);
      cv = (third * 216 + (lane >> 3)) * cpitch + clocal * 128 + (lane & 7) * 16;
      st = 8 * cpitch;
    };
    auto cs_publish = [&](unsigned u, float (&cs)[8]) {
#pragma unroll
      for (int k = 0; k < 4; ++k) { const bool hi = lane & 32; const float o = shfl_x(hi ? cs[k] : cs[k + 4], lane, 32); cs[k] = (hi ? cs[k + 4] : cs[k]) + o; }
#pragma unroll
      for (int k = 0; k < 2; ++k) { const bool hi = lane & 16; const float o = shfl_x(hi ? cs[k] : cs[k + 2], lane, 16); cs[k] = (hi ? cs[k + 2] : cs[k]) + o; }
      { const bool hi = lane & 8; const float o = shfl_x(hi ? cs[0] : cs[1], lane, 8); cs[0] = (hi ? cs[1] : cs[0]) + o; }
      if (u < 252)
        __hip_atomic_store((gu64*)(ws + WS_CSUM) + (u % 3) * NCOL + (u / 3) * 64 + (lane & 7) * 8 + (lane >> 3),
                           ((unsigned long long)tag << 32) | __builtin_bit_cast(unsigned, cs[0]), RLX_AGENT);
    };
    row_load(rs_, rsX, ws, lane);
    constexpr int CW = CWIN;
    u32x4 c[CW];
    rsrc_t rs0, rs1; unsigned cv0, cv1, st0, st1;
    cs_src(u0, rs0, cv0, st0); cs_src(u1, rs1, cv1, st1);
    auto cld = [&](int j) { return j < 27 ? __builtin_amdgcn_raw_buffer_load_b128(rs0, cv0 + j * st0, 0, 0) : __builtin_amdgcn_raw_buffer_load_b128(rs1, cv1 + (j - 27) * st1, 0, 0); };
#pragma unroll
    for (int j = 0; j < CW; ++j) c[j] = cld(j);
    SB;
    tag = row_wait<CW>(rs_);
    static_for<0, 14>([&](auto K) { row_step<K.v>(rs_, lane); });
    lbar();
    row_publish(rs_, ws, tag, tid);
    float cs[8] = {};
    static_for<0, 54>([&](auto J) {
      constexpr int j = J.v;
#pragma unroll
      for (int k = 0; k < 4; ++k) { cs[2 * k] = dot2(c[j % CW][k], k10, cs[2 * k]); cs[2 * k + 1] = dot2(c[j % CW][k], k01, cs[2 * k + 1]); }
      if constexpr (j + CW < 54) c[j % CW] = cld(j + CW);
      SB;
      if constexpr (j == 26) {
        cs_publish(u0, cs);
#pragma unroll
        for (int k = 0; k < 8; ++k) cs[k] = 0.f;
      }
    });
    cs_publish(u1, cs);
    bar();
#pragma unroll 1
    for (unsigned i = w; i < 144; i += 8) dma16(i * 1024, v2, rsWd, urow(i >> 2) + (i & 3) * 10240u + cb * 128u);
    drain_vm();
    bar();
  }
  if (!has2) return;
  relane();

  const unsigned rbA = (G * 6) >> 4, rbB = (G * 6 + 5 > 127 ? 127 : G * 6 + 5) >> 4;
  if (w >= 4) {
    const unsigned lw = w - 4, ld0 = lw * 1024;
    gu32* fo = (gu32*)(ws + WS_FLAGO + ((lane < 32 ? rbA : rbB) * 32 + (lane & 3)) * FS);
    gu32* fh = (gu32*)(ws + WS_FLAGH + ((lane < 32 ? rbA : rbB) * 32 + ((lane & 31) < 18 ? lane & 31 : 17)) * FS);
    __builtin_amdgcn_s_sleep(4);
    if (jtype == J_KV) {
#pragma unroll
      for (int u = 31; u < 36; ++u) dma16(u * 4096 + ld0, v2, rsWd, w2_off(W, u, lw));
    }
    if (jtype != J_MLP) {
#pragma unroll
      for (int u = 0; u < NPRE; ++u) { pre2[u] = w2_load(W, 36 + u, lw, v2); SB; }
    }
#pragma unroll
    for (int u = 36 + NPRE; u < PSPLIT; ++u) { pre2[u - 36] = w2_load(W, u, lw, v2); SB; }
    if (w == 7) {
      __builtin_amdgcn_s_setprio(3);
      const unsigned v = poll_flags(fh, (lane & 31) < 18 && (lane & 31) >= 10, tag);
      lds_set(L_GO);
      if (!all64((lane & 31) >= 18 || v == tag)) poll_flags(fh, (lane & 31) < 18, tag);
      lds_set(L_GO + 4);
      __builtin_amdgcn_s_setprio(0);
    }
#pragma unroll
    for (int u = PSPLIT; u < HSPLIT; ++u) { pre2[u - 36] = w2_load(W, u, lw, v2); SB; }
    unsigned la[3], wb[3], xo[3], sto[3];
    u32x2 st[3];
    u32x4 xr[3];
    const unsigned ch = lane & 7;
#pragma unroll
    for (int i = 0; i < 3; ++i) {
      const unsigned row = lw * 24 + i * 8 + (lane >> 3), rl = row >> 4, rr = row & 15;
      la[i] = (rl >= 3 ? 24576u + (rl - 3) * 4096u : rl * 4096u) + (ch >> 1) * 1024 + (ch & 1) * 512 + ((rr + 2 * ch) & 15) * 16;
      xo[i] = (G * 96 + row) * 1280 + cb * 128 + ch * 16;
      wb[i] = ld0 + lane * 16 + i * 49152;
      asm volatile("" : "+v"(la[i]), "+v"(wb[i]), "+v"(xo[i]));
    }
    float ri[3], rl[3];
    unsigned of = 0;
    f32x4 xf[3][2], dd[3][2], d2[3][2];
#pragma unroll
    for (int k = 2; k <= 6; ++k) {
      if (k == 5 && w == 7 && !all64((lane & 31) >= 4 || of == tag)) poll_flags(fo, (lane & 31) < 4, tag);
      if (k == 6) {
        wait_vm<0>();
#pragma unroll 1
        for (unsigned spins = 0; spins < SPIN_CAP && !all64(st[0][1] == tag && st[1][1] == tag && st[2][1] == tag); ++spins) {
#pragma unroll
          for (int h = 0; h < 3; ++h) st[h] = __builtin_amdgcn_raw_buffer_load_b64(rsWS, sto[h], 0, 16);
          asm volatile("" ::: "memory");
        }
#pragma unroll
        for (int h = 0; h < 3; ++h) {
          ri[h] = 2.0f * bfhi(st[h][0]) * bfhi(st[h][0]);
          rl[h] = -1.4426950408889634f * __builtin_amdgcn_rcpf(ri[h]);
          xf[h][0] = f32x4{bflo(xr[h][0]), bfhi(xr[h][0]), bflo(xr[h][1]), bfhi(xr[h][1])};
          xf[h][1] = f32x4{bflo(xr[h][2]), bfhi(xr[h][2]), bflo(xr[h][3]), bfhi(xr[h][3])};
          asm volatile("" : "+v"(ri[h]), "+v"(rl[h]), "+v"(xf[h][0]), "+v"(xf[h][1]));
        }
      }
      lbar();
      if (k == 4) { of = __hip_atomic_load(fo, RLX_AGENT); SB; }
      if (k < 6) {
        const int kk = k - 1;
#pragma unroll
        for (int u = 9 * (kk - 1) + ((kk - 1) & 1); u < 9 * kk + (kk & 1); ++u) {
          lds_st<u32x4>(wb[u / 12] + (u % 12) * 4096, pre2[u]);
          SB;
        }
      }
      if (k == 2 || k == 3) {
        constexpr int mid = (HSPLIT + 74) / 2 < 72 ? (HSPLIT + 74) / 2 : 72;
#pragma unroll
        for (int u = (k == 2 ? HSPLIT : mid); u < (k == 2 ? mid : 72); ++u) { pre2[u - 36] = w2_load(W, u, lw, v2); SB; }
        if (k == 3) { dma16(36 * 4096 + ld0, v2, W.wo, w2_off(W, 72, lw)); dma16(37 * 4096 + ld0, v2, W.wo, w2_off(W, 73, lw)); }
      }
      if (k == 2) {
#pragma unroll
        for (int h = 0; h < 3; ++h) {
          const unsigned row = G * 96 + lw * 24 + h * 8 + (lane >> 3);
          sto[h] = WS_STAT + (row < S ? row : 0) * 8;
          st[h] = __builtin_amdgcn_raw_buffer_load_b64(rsWS, sto[h], 0, 16);
          xr[h] = __builtin_amdgcn_raw_buffer_load_b128(rsX, xo[h], 0, 0);
        }
      }
    }
    lbar();
#pragma unroll
    for (int i = 0; i < 3; ++i) {
      dd[i][0] = lds_ld<f32x4>(la[i]); d2[i][0] = lds_ld<f32x4>(la[i] + 12288);
      dd[i][1] = lds_ld<f32x4>(la[i] + 256); d2[i][1] = lds_ld<f32x4>(la[i] + 12288 + 256);
      la[i] += 49152;
      asm volatile("" : "+v"(la[i]));
    }
#pragma unroll
    for (int i = 0; i < 3; ++i) {
      float v[8], e[8];
#pragma unroll
      for (int j = 0; j < 8; ++j) v[j] = sadd(dd[i][j >> 2][j & 3], d2[i][j >> 2][j & 3]);
      SB;
#pragma unroll
      for (int j = 0; j < 8; ++j) e[j] = smul(v[j], rl[i]);
      SB;
#pragma unroll
      for (int j = 0; j < 8; ++j) e[j] = __builtin_amdgcn_exp2f(e[j]);
      SB; asm volatile("s_nop 0"); SB;
#pragma unroll
      for (int j = 0; j < 8; ++j) e[j] = sfma(e[j], ri[i], ri[i]);
      SB;
#pragma unroll
      for (int j = 0; j < 8; ++j) e[j] = __builtin_amdgcn_rcpf(e[j]);
      SB; asm volatile("s_nop 0"); SB;
#pragma unroll
      for (int j = 0; j < 8; ++j) dd[i][j >> 2][j & 3] = smul(v[j], e[j]);
      asm volatile("" : "+v"(dd[i][0]), "+v"(dd[i][1]));
      SB;
    }
    const rsrc_t rsO = mk_rsrc(pout, S * D * 2);
    bar();
    f32x4 ss[3][2];
#pragma unroll
    for (int i = 0; i < 3; ++i) {
      ss[i][0] = lds_ld<f32x4>(la[i]) + lds_ld<f32x4>(la[i] + 12288);
      ss[i][1] = lds_ld<f32x4>(la[i] + 256) + lds_ld<f32x4>(la[i] + 12288 + 256);
    }
#pragma unroll
    for (int i = 0; i < 3; ++i)
      __builtin_amdgcn_raw_buffer_store_b128(cat2(pk4(xf[i][0] + ss[i][0] * dd[i][0]), pk4(xf[i][1] + ss[i][1] * dd[i][1])), rsO, xo[i], 0, 0);
    asm volatile(".Lfb_code_end0:");
    return;
  }
  {
    const unsigned kh = w & 1, rt = G * 6 + 3 * (w >> 1);
    const rsrc_t rsH = mk_rsrc(ws + WS_HA, 8 * RBSZ);
    unsigned tb[3][4];
#pragma unroll
    for (int t = 0; t < 4; ++t) {
      tb[0][t] = (4 * g + q4) * 128 + ((t ^ ((2 * g + (q4 >> 1)) & 3)) << 5) + 8 * p4 + kh * 4096;
      tb[1][t] = tb[0][t] + 49152; tb[2][t] = tb[0][t] + 98304;
      asm volatile("" : "+v"(tb[0][t]), "+v"(tb[1][t]), "+v"(tb[2][t]));
    }
    auto rd1 = [&](int i, int t, int hf) {
      const int sl = i == 36 ? 36 : (2 * i) % 36, bi = sl / 12 > 2 ? 2 : sl / 12;
      return __builtin_bit_cast(u32x2, lds_tr(tb[bi][t] + (sl - 12 * bi) * 4096 + hf * 2048));
    };
    constexpr int NW = 7, W0 = 6;
    constexpr int OI = 22;
    f32x4 acc[2][3][4];
    u32x4 a[NW][3];
    u32x2 bw[2][4][2];
    unsigned va[2][3];
#pragma unroll
    for (int h = 0; h < 3; ++h) {
      va[0][h] = lane * 16 + ((rt + h) >> 4) * RBSZ + ((rt + h) & 15) * 3072;
      va[1][h] = va[0][h] + ((rt + h) & 15) * 2048;
      asm volatile("" : "+v"(va[0][h]), "+v"(va[1][h]));
    }
    unsigned xa[4];
#pragma unroll
    for (int t = 0; t < 4; ++t) {
      xa[t] = w * 12288u + t * 1024 + g * 256 + ((r + 4 * t + (g & 2)) & 15) * 16;
      asm volatile("" : "+v"(xa[t]));
    }
    auto put = [&](auto Z, auto E) {
      constexpr int z = Z.v, t = E.v / 3, h = 2 - E.v % 3;
      asm volatile("" : "+v"(acc[z][h][t]));
      lds_st<f32x4>(xa[t] + z * 49152 + h * 4096, acc[z][h][t]);
    };
    auto lda = [&](auto I, auto H) {
      constexpr int i = I.v, h = H.v;
      constexpr unsigned s0 = i < 27 ? (2 * i + ROT) % 54 : 2 * i, s1 = i < 27 ? (2 * i + 1 + ROT) % 54 : 2 * i + 1;
      a[i % NW][h] = __builtin_amdgcn_raw_buffer_load_b128(rsH, va[i >= 27][h], step_off(s0) + kh * (step_off(s1) - step_off(s0)), 16);
      SB;
    };
    lds_wait(L_GO);
    unsigned go2 = 0;
    static_for<0, W0>([&](auto I) { lda(I, IC<0>{}); lda(I, IC<1>{}); lda(I, IC<2>{}); });
#pragma unroll
    for (int t = 0; t < 4; ++t) { bw[0][t][0] = rd1(0, t, 0); bw[0][t][1] = rd1(0, t, 1); }
    static_for<0, 37>([&](auto I) {
      constexpr int i = I.v;
      constexpr int lim = i + NW - 1;
      constexpr int nxt = i <= OI ? (W0 + 2 * i < lim ? W0 + 2 * i : lim) : (27 + 2 * (i - OI - 1) < lim ? 27 + 2 * (i - OI - 1) : lim);
      constexpr int end = i <= OI ? 27 : 37;
      constexpr int l0 = nxt < end ? nxt : -1, l1 = (nxt + 1 <= lim && nxt + 1 < end) ? nxt + 1 : -1;
      SB;
      static_for<0, 12>([&](auto E) {
        constexpr int e = E.v, t = e / 3, h = e % 3;
        const u32x4 bf = cat2(bw[i & 1][t][0], bw[i & 1][t][1]);
        if constexpr (i == 0 || i == 27) mfma_z(acc[i >= 27][2 - h][t], bf, a[i % NW][2 - h]); else mfma_ip(acc[i >= 27][2 - h][t], bf, a[i % NW][2 - h]);
        SB;
        if constexpr (i == 4 && e == 2) go2 = lds_get(L_GO + 4);
        if constexpr ((i == 27 || i == 28) && (e & 1)) put(IC<0>{}, IC<(i - 27) * 6 + e / 2>{});
        if constexpr (i == 36 && e >= 8) put(IC<1>{}, IC<e - 8>{});
        if constexpr (i < 36 && h < 2) bw[(i + 1) & 1][t][h] = rd1(i + 1, t, h);
        if constexpr (l1 < 0) { if constexpr (h == 2 && t < 3 && l0 >= 0) lda(IC<l0>{}, IC<t>{}); }
        else if constexpr (e & 1) lda(IC<(e < 6 ? l0 : l1)>{}, IC<(e >> 1) % 3>{});
        SB;
      });
      if constexpr (i == 29) { asm volatile("s_waitcnt lgkmcnt(8)" ::: "memory"); bar(); }
      if constexpr (i == 4) { if (__builtin_expect(!rfl(go2), 0)) lds_wait(L_GO + 4); asm volatile("" ::: "memory"); }
      if constexpr (i > 4 && i < 27 && (i % 9 == 4 || i % 9 == 8)) bar();
    });
    static_for<4, 12>([&](auto E) { if constexpr (E.v == 8) asm volatile("s_nop 3" ::: "memory"); put(IC<1>{}, E); SB; });
    lbar();
    asm volatile(".Lfb_code_end1:");
  }
}

extern "C" void kernel_launch(void* const* d_in, const int* in_sizes, int n_in, void* d_out, int out_size, void* d_ws,
                              size_t ws_size, hipStream_t stream) {
  fused_block<<<256, 512, 0, stream>>>(d_in[0], d_in[1], d_in[2], d_in[4], d_in[5], d_in[6], d_out,
                                       (char*)d_ws + (ws_size >= WS_ALIGN + WS_END ? 0 : 1));
}
```
